```python
import torch

def _softmax(x, axis=-1):
    return torch.softmax(x, dim=axis)
def _gelu(x):
    return torch.nn.functional.gelu(x, approximate="tanh")
def _silu(x):
    return torch.nn.functional.silu(x)
def _sigmoid(x):
    return torch.sigmoid(x)
def _layernorm(x, axis=-1, eps=1e-5):
    m = x.mean(dim=axis, keepdim=True)
    v = x.var(dim=axis, keepdim=True, unbiased=False)
    return (x - m) / torch.sqrt(v + eps)
def _scatter_add(tgt, idx, src, axis=0):
    return tgt.clone().index_add_(axis, idx, src)
def _segment_sum(src, seg, n):
    out = torch.zeros((n,) + src.shape[1:], dtype=src.dtype)
    return out.index_add_(0, seg, src)
def _pad(x, pw):
    flat = tuple(v for pair in reversed(pw) for v in pair)
    return torch.nn.functional.pad(x, flat)

kernel_name = 'gated_mix__ssm_conv_h3__sliding_local_dilated__prompt'

def setup_inputs():

    _w = (lambda a: (lambda t: t.to(torch.bfloat16) if t.is_floating_point() else t)(torch.as_tensor(a)))
    def _A_setup():
        B, S, D = 1, 2048, 640
        ks = 4
        return {
            "q": _w((lambda *s: torch.randn(*s, dtype=torch.bfloat16) * 0.05)(B, S, D)),
            "k": _w((lambda *s: torch.randn(*s, dtype=torch.bfloat16) * 0.8)(B, S, D)),
            "v": _w((lambda *s: torch.randn(*s, dtype=torch.bfloat16) * 0.8)(B, S, D)),
            "shift_k": _w((lambda *s: torch.randn(*s, dtype=torch.bfloat16) * 0.8)(D, ks) * 0.5),
            "ssm_k": _w((lambda *s: torch.randn(*s, dtype=torch.bfloat16) * 3.2)(D, S) * (1.0 / S ** 0.5)),
            "ssm_D": _w((lambda *s: torch.randn(*s, dtype=torch.bfloat16) * 0.8)(D)),
        }
    _A_in = _A_setup()

    def _B_setup():
        B, S, D = 1, 2048, 640
        return {
            "x": _w((lambda *s: torch.randn(*s, dtype=torch.bfloat16) * 0.05)(B, S, D)),
            "wqkv": _w((lambda *s: torch.randn(*s, dtype=torch.bfloat16) * 0.8)(D, 3 * D)),
            "wo": _w((lambda *s: torch.randn(*s, dtype=torch.bfloat16) * 0.05)(D, D)),
        }
    _B_in = _B_setup()

    return {
        "A_q": _A_in["q"],
        "A_k": _A_in["k"],
        "A_v": _A_in["v"],
        "A_shift_k": _A_in["shift_k"],
        "A_ssm_k": _A_in["ssm_k"],
        "A_ssm_D": _A_in["ssm_D"],
        "B_x": _B_in["x"],
        "B_wqkv": _B_in["wqkv"],
        "B_wo": _B_in["wo"],
        "gate_w": _w((lambda *s: torch.randn(*s, dtype=torch.bfloat16) * 0.05)(640) * 16.0),
    }

def reference(A_q, A_k, A_v, A_shift_k, A_ssm_k, A_ssm_D, B_x, B_wqkv, B_wo, gate_w):
    def _A_ref(q, k, v, shift_k, ssm_k, ssm_D):
        B, S, D = q.shape
        ks = shift_k.shape[-1]
        k_pad = _pad(k, ((0, 0), (ks - 1, 0), (0, 0)))
        k_shift = torch.zeros_like(k)
        for t in range(ks):
            k_shift = k_shift + k_pad[:, t:t + S, :] * shift_k[:, ks - 1 - t][None, None, :]
        kv = k_shift * v
        L = 2 * S
        kv_pad = _pad(kv, ((0, 0), (0, S), (0, 0)))
        kf_pad = _pad(ssm_k, ((0, 0), (0, S)))
        kv_f = (lambda x, axes=(-2,-1): torch.fft.rfft2(x.float(), dim=axes))(kv_pad[:, None, :, :], axes=(1, 2))
        kf_f = (lambda x, axes=(-2,-1): torch.fft.rfft2(x.float(), dim=axes))(kf_pad[:, None, :], axes=(1, 2))
        kf_f = (lambda x, *d: x.permute(*d))(kf_f, 1, 2, 0)
        y_f = kv_f * kf_f[None, :, :, :]
        y = (lambda x, s, axes=(-2,-1): torch.fft.irfft2(x, s=s, dim=axes).to(torch.bfloat16))(y_f, (1, L), axes=(1, 2))[:, 0, :S, :]
        y = y + kv * ssm_D[None, None, :]
        return q * y

    def _B_ref(x, wqkv, wo):
        B, S, D = x.shape
        H = 32; Dh = D // H
        W, dil = 8, 2
        qkv = (lambda x, *d: x.permute(*d))((x @ wqkv).reshape(B, S, 3, H, Dh), 2, 0, 3, 1, 4)
        q, k, v = qkv[0], qkv[1], qkv[2]
        scale = Dh ** -0.5
        pos = torch.arange(S)
        off = torch.arange(W) * dil
        idx = pos[:, None] - off[None, :]
        mask = idx >= 0
        idx_c = torch.where(mask, idx, 0).reshape(-1)
        kg = k[:, :, idx_c, :].reshape(B, H, S, W, Dh)
        vg = v[:, :, idx_c, :].reshape(B, H, S, W, Dh)
        logits = torch.einsum("bhsd,bhswd->bhsw", q, kg) * scale
        logits = torch.where(mask[None, None, :, :], logits, -1e9)
        attn = _softmax(logits, axis=-1)
        o = torch.einsum("bhsw,bhswd->bhsd", attn, vg)
        return (lambda x, *d: x.permute(*d))(o, 0, 2, 1, 3).reshape(B, S, D) @ wo

    x = A_q
    g = _sigmoid((x * gate_w).sum(-1, keepdims=True))
    a = _A_ref(x, A_k, A_v, A_shift_k, A_ssm_k, A_ssm_D)
    b = _B_ref(x, B_wqkv, B_wo)
    return g * a + (1 - g) * b
```

```cpp
#define V_DELAY 6
#define MAP1 1
#define HINT_AT 9
#define VY_DELAY 16
#define GY_DELAY 12
#define O_ROT 0
#define G_OWN 0
#define OUT_AUX 2
#define CODE_LINES 381
#include <hip/hip_runtime.h>
#include <hip/hip_bf16.h>
#include <stdint.h>

#define S_LEN 2048
#define D_DIM 640
#define NQKV 1920

typedef unsigned short u16;
typedef unsigned long long u64;
typedef __attribute__((ext_vector_type(8))) short bf16x8;
typedef __attribute__((ext_vector_type(4))) short s16x4;
typedef __attribute__((ext_vector_type(4))) float f32x4;
typedef __attribute__((ext_vector_type(2))) float f32x2;
typedef __attribute__((ext_vector_type(4))) unsigned u32x4;
typedef __attribute__((ext_vector_type(2))) unsigned u32x2;
typedef __attribute__((ext_vector_type(2))) __bf16 bf16x2_t;
typedef __attribute__((address_space(3))) s16x4 lds_s16x4;
typedef __attribute__((address_space(3))) const s16x4 lds_cs16x4;
typedef __attribute__((address_space(3))) const bf16x8 lds_cbf16x8;
typedef __attribute__((address_space(3))) char lds_char;

#define __float_as_uint(f) __builtin_bit_cast(unsigned, (float)(f))
#define __uint_as_float(u) __builtin_bit_cast(float, (unsigned)(u))
#define __any(c) (__builtin_amdgcn_ballot_w64(c) != 0)
#define __all(c) (__builtin_amdgcn_ballot_w64(!(c)) == 0)
extern "C" __device__ unsigned long long llvm_dispatch_id() __asm("llvm.amdgcn.dispatch.id");

__device__ __forceinline__ float bf2f(u16 x) { return __uint_as_float(((unsigned)x) << 16); }
__device__ __forceinline__ u16 f2bf(float f) {
    __hip_bfloat16 b = __float2bfloat16(f);
    return *reinterpret_cast<u16*>(&b);
}
__device__ __forceinline__ float rbf(float f) { return bf2f(f2bf(f)); }
__device__ __forceinline__ float lo_f(unsigned u) { return __uint_as_float(u << 16); }
__device__ __forceinline__ float hi_f(unsigned u) { return __uint_as_float(u & 0xffff0000u); }
__device__ __forceinline__ unsigned pack2(float a, float b) {
    return __builtin_bit_cast(unsigned, __builtin_convertvector((f32x2){a, b}, bf16x2_t));
}
__device__ __forceinline__ float dot2(unsigned a, unsigned b, float c) {
    return __builtin_amdgcn_fdot2_f32_bf16(__builtin_bit_cast(bf16x2_t, a), __builtin_bit_cast(bf16x2_t, b), c, false);
}

#define SB() __builtin_amdgcn_sched_barrier(0)
#define WAIT_VM(n) asm volatile("s_waitcnt vmcnt(" #n ")" ::: "memory")
#define RAW_BARRIER() do { asm volatile("s_waitcnt lgkmcnt(0)" ::: "memory"); __builtin_amdgcn_s_barrier(); } while (0)

#define KV_PITCH 4224
#define WS_KV 4096
#define WS_O (WS_KV + D_DIM * KV_PITCH)
#define WS_Y (WS_O + D_DIM * S_LEN * 2)
#define WS_G (WS_Y + D_DIM * S_LEN * 2)
#define WS_XF (WS_G + S_LEN * 4)
#define WS_SF (WS_XF + 128 * 128)
#define WS_HF (WS_SF + 128 * 128)
#define WS_END (WS_HF + 128 * 128)
#define WS_DBG (WS_END + 4096)

#define L_A 0
#define A_SLOT 18432
#define L_W 55296
#define W_SLOT 16384
#define W_PITCH 512
#define NW 6
#define G_XCH 71680
#define G_Y 0
#define G_Q (14 * 10240)
#define G_VX 153600
#define G_HX 158208
#define G_VXD (G_HX + 4608)
#define KV_BYTES 4352
#define V_F (4 * KV_BYTES)
#define XC_ROW 288
#define XC_B 12
#define XC_COPY 4944
#define XC_BYTES (XC_COPY + 17 * XC_ROW)
#define V_KV4 (V_F + 5 * XC_BYTES)
#define V_KV(c) ((c) < 4 ? (c) * KV_BYTES : V_KV4)
#define V_WO (V_KV4 + KV_BYTES - 10240)
#define V_KST (V_WO + 10240 + 1312)
#define V_VST (V_KST + 20608)
#define V_STG 113536
#define V_QS (V_WO + 10240)
#define V_SCR 130560
#define V_XCH 0
#define V_Q 20480
#define V_Y (V_Q + 10240)
#define Y_PITCH 160
#define L_TOUCH 163328
#define L_CTRL 163584
#define LDS_TOTAL 163840
static_assert(V_VST + 20608 <= V_STG && V_STG % 128 == 0 && V_STG + 20480 <= L_TOUCH && V_WO >= V_F + 4 * XC_BYTES && V_WO + 10 * 10240 <= L_TOUCH &&
              V_WO + 6 * 10240 <= V_SCR && V_SCR + 32768 <= L_TOUCH && V_Y + 80 * Y_PITCH <= V_WO && V_WO % 16 == 0, "lds");
static_assert(L_W + NW * W_SLOT <= G_VX && G_VX + 4608 <= G_HX && G_VXD + 520 <= L_CTRL && G_Y + 80 * Y_PITCH <= A_SLOT, "lds");
#define C_FREE (L_CTRL + 16)
#define C_OST (L_CTRL + 48)
#define C_SENT (L_CTRL + 52)
#define C_WO (L_CTRL + 56)
#define C_KV (L_CTRL + 60)
#define C_KVST (L_CTRL + 64)
#define C_CONVN (L_CTRL + 68)
#define C_Y (L_CTRL + 72)
#define C_KVR (L_CTRL + 76)
#define C_XD (L_CTRL + 80)
#define C_RED (L_CTRL + 84)
#define C_ORDY (L_CTRL + 88)
#define C_QR (L_CTRL + 92)
#define C_XCH (L_CTRL + 96)
#define C_PRE (L_CTRL + 112)
#define C_SFC (L_CTRL + 136)
#define C_GO (L_CTRL + 140)
#define C_HR (L_CTRL + 144)
#define C_RED2 (L_CTRL + 148)
#define C_KVD (L_CTRL + 152)
#define C_WT (L_CTRL + 156)
#define C_HX (L_CTRL + 160)
#define C_OST1 (L_CTRL + 176)

#define KNOB(n, v)
#define SPIN_CAP 4000000
#define GH 0
#define WO_PRIO 0
#define T_PRIO 3
#define C0_PRIO 2
#define C1_PRIO 1
#define L_PRIO 3
#define O_SLEEP 4
#define O_DELAY 0
#define POLL_SLEEP 2
#define Y_SLEEP 8
#define W_SLEEP 4
#define X_SLEEP 2
#define X_DELAY 0
#define CONV_PRIO 2
#define NBUF 4
#define WATCH_RT 2
#define JL 17
#define RING_E0 70
#define RING_F 105
#define WOT_SPLIT 0
#define GO_W C_KVD
#define GO_N 11
#define SENT_PACE 8
#define GATE_LATE 0
#define WOT_PACE 3
#define SENT_W C_KVD
#define SENT_N 11
#define NSENT 20
#define T0_PRIO T_PRIO
#define G_WST 4
#define V_WST 4
#define V_WST0 0
#define WR_X 2
#define GW_DELAY 0
#define O_AHEAD 4
#define PASS_PRIO 1
#define G_OST 8
#define V_ODELAY 0
#define W_LATE 1
#define Y_SL0 0
#define SF_DELAY 8
#define WO_MALL 1
#define T4_EARLY 0
#define POLL_DELAY 2
#define LATE_AT C_LOOP
#define LATE_N 4
#define KROT ((0x27057250u >> (xcd * 4)) & 15)

#define STAMP(id)
#define FLUSH()
#define CSTAMP(i)

__device__ __forceinline__ unsigned lds_ld32(unsigned a) {
    unsigned v;
    asm volatile("ds_read_b32 %0, %1\n\ts_waitcnt lgkmcnt(0)" : "=v"(v) : "v"(a) : "memory");
    return v;
}
__device__ __forceinline__ unsigned lds_min4(unsigned a) {
    u32x4 v;
    asm volatile("ds_read_b128 %0, %1\n\ts_waitcnt lgkmcnt(0)" : "=v"(v) : "v"(a) : "memory");
    return min(min(v[0], v[1]), min(v[2], v[3]));
}
__device__ __forceinline__ unsigned lds_min8(unsigned a) {
    u32x4 v, u;
    asm volatile("ds_read_b128 %0, %2\n\tds_read_b128 %1, %2 offset:16\n\ts_waitcnt lgkmcnt(0)" : "=&v"(v), "=&v"(u) : "v"(a) : "memory");
    return min(min(min(v[0], v[1]), min(v[2], v[3])), min(min(u[0], u[1]), min(u[2], u[3])));
}
__device__ __forceinline__ void lds_st32(unsigned a, unsigned v) {
    asm volatile("ds_write_b32 %0, %1" ::"v"(a), "v"(v) : "memory");
}
__device__ __forceinline__ unsigned lds_add_rtn(unsigned a, int lane) {
    unsigned old = 0;
    if (lane == 0) {
        unsigned one = 1;
        asm volatile("ds_add_rtn_u32 %0, %1, %2\n\ts_waitcnt lgkmcnt(0)" : "=v"(old) : "v"(a), "v"(one) : "memory");
    }
    return __builtin_amdgcn_readfirstlane(old);
}
__device__ __forceinline__ void lds_add(unsigned a, int lane, unsigned n = 1) {
    if (lane == 0) asm volatile("ds_add_u32 %0, %1" ::"v"(a), "v"(n) : "memory");
}
__device__ __forceinline__ void spin_min4(unsigned a, unsigned target) {
#pragma nounroll
    for (int i = 0; i < SPIN_CAP; ++i) {
        if (__builtin_amdgcn_readfirstlane(lds_min4(a)) >= target) break;
        __builtin_amdgcn_s_sleep(1);
    }
}
__device__ __forceinline__ void spin_min8(unsigned a, unsigned target) {
#pragma nounroll
    for (int i = 0; i < SPIN_CAP; ++i) {
        if (__builtin_amdgcn_readfirstlane(lds_min8(a)) >= target) break;
        __builtin_amdgcn_s_sleep(1);
    }
}
__device__ __forceinline__ void spin_ge(unsigned a, unsigned target) {
#pragma nounroll
    for (int i = 0; i < SPIN_CAP; ++i) {
        if (__builtin_amdgcn_readfirstlane(lds_ld32(a)) >= target) break;
        __builtin_amdgcn_s_sleep(1);
    }
}
typedef __attribute__((address_space(3))) volatile u32x4 lds_vu32x4;
typedef __attribute__((address_space(3))) volatile unsigned lds_vu32_t;
__device__ __forceinline__ u32x4 lds_v128(unsigned a) { return *(lds_vu32x4*)(size_t)a; }
__device__ __forceinline__ unsigned lds_v32(unsigned a) { return *(lds_vu32_t*)(size_t)a; }
__device__ __forceinline__ float fmax_raw(float a, float b) { return __builtin_elementwise_maximum(a, b); }
__device__ __forceinline__ float fmax3_raw(float a, float b, float c) { return __builtin_elementwise_maximum(__builtin_elementwise_maximum(a, b), c); }
__device__ __forceinline__ unsigned opaque(unsigned v) { asm volatile("" : "+v"(v)); return v; }
__device__ __forceinline__ bf16x8 cat8(s16x4 a, s16x4 b) { return __builtin_shufflevector(a, b, 0, 1, 2, 3, 4, 5, 6, 7); }

__device__ __forceinline__ void poll_flags(__amdgpu_buffer_rsrc_t rs, unsigned off, unsigned tlo, unsigned thi) {
#pragma nounroll
    for (int i = 0; i < SPIN_CAP; ++i) {
        asm volatile("" ::: "memory");
        const u32x2 v = __builtin_amdgcn_raw_buffer_load_b64(rs, off, 0, 16);
        if (__all(v[0] == tlo && v[1] == thi)) break;
        __builtin_amdgcn_s_sleep(POLL_SLEEP);
    }
    __builtin_amdgcn_fence(__ATOMIC_ACQUIRE, "wavefront");
}

__device__ __forceinline__ void spin_slow(unsigned a, unsigned target) {
#pragma nounroll
    for (int i = 0; i < SPIN_CAP; ++i) {
        if (__builtin_amdgcn_readfirstlane(lds_ld32(a)) >= target) break;
        __builtin_amdgcn_s_sleep(6);
    }
}
__device__ __forceinline__ void spin_mid(unsigned a, unsigned target) {
#pragma nounroll
    for (int i = 0; i < SPIN_CAP; ++i) {
        if (__builtin_amdgcn_readfirstlane(lds_ld32(a)) >= target) break;
        __builtin_amdgcn_s_sleep(3);
    }
}
__device__ __forceinline__ void spin_all(unsigned a, unsigned target) {
#pragma nounroll
    for (int i = 0; i < SPIN_CAP; ++i) {
        if (__all(lds_ld32(a) >= target)) break;
        __builtin_amdgcn_s_sleep(1);
    }
}

template <int N> __device__ __forceinline__ void waitvm() { asm volatile("s_waitcnt vmcnt(%0)" ::"n"(N) : "memory"); }
#define BARRIER() asm volatile("s_barrier" ::: "memory")
#define GLS(voff, sbase, lw, ldsoff) asm volatile("s_add_u32 m0, %2, %3\n\ts_nop 0\n\tglobal_load_lds_dwordx4 %0, %1" ::"v"(voff), "s"(sbase), "s"(lw), "n"(ldsoff) : "memory", "scc")
#define GLT(voff, sbase) asm volatile("s_mov_b32 m0, %2\n\ts_nop 0\n\tglobal_load_lds_dword %0, %1" ::"v"(voff), "s"(sbase), "s"(lb + L_TOUCH) : "memory")
#define TRRD(p) __builtin_amdgcn_ds_read_tr16_b64_v4i16((lds_s16x4*)(p))

constexpr int ring_total(int NA) { return 10 * (8 + NA); }
constexpr int ring_need(int J, int NA) { return J < 0 ? NA + 8 : J >= JL ? 10 * (8 + NA) : (J & 1) ? (NA + 8) * ((J + 3) / 2) : (NA + 8) * ((J + 2) / 2) + 4 + NA; }
constexpr int ring_isA(int p, int NA) { return p < NA ? 1 : p < NA + 8 ? 0 : (p % (8 + NA) >= 4 && p % (8 + NA) < 4 + NA); }
constexpr int ring_tile(int p, int NA) { return p < NA ? 0 : p < NA + 8 ? (p - NA) / 4 : ring_isA(p, NA) ? p / (8 + NA) : 2 * (p / (8 + NA)) + (p % (8 + NA) >= 4); }
constexpr int ring_sub(int p, int NA) { return p < NA ? p : p < NA + 8 ? (p - NA) % 4 : p % (8 + NA) < 4 ? p % (8 + NA) : p % (8 + NA) < 4 + NA ? p % (8 + NA) - 4 : p % (8 + NA) - 4 - NA; }
constexpr int ring_cum(int J, int NA) {
    int c = J >= (JL < 16 ? JL : 16) ? ring_total(NA) : NA + 8 + (8 + NA) * (2 * RING_E0 + RING_F * (J + 1)) / 200;
    if (c > ring_total(NA)) c = ring_total(NA);
    if (c < ring_need(J, NA)) c = ring_need(J, NA);
    while (ring_isA(c - 1, NA) ? 2 * (ring_tile(c - 1, NA) - 3) + 1 > J - 1 : ring_tile(c - 1, NA) - NW > J - 1) --c;
    return c;
}
static_assert(ring_cum(16, 5) == ring_total(5) && ring_cum(16, 4) == ring_total(4), "ring");
template <int NA, int P0, int P1>
__device__ __forceinline__ void ring_issue(const unsigned (&ev)[9], const void* xp, const void* wp, int kr, unsigned lw) {
    if constexpr (P0 < P1) {
        constexpr int t = ring_tile(P0, NA), sub = ring_sub(P0, NA);
        if constexpr (ring_isA(P0, NA)) {
            const int c = __builtin_amdgcn_readfirstlane(kr + t >= 10 ? kr + t - 10 : kr + t);
            GLS(ev[4 + sub], (const char*)xp + c * 128, lw, L_A + (t % 3) * A_SLOT + sub * 4096);
        } else {
            const int c = __builtin_amdgcn_readfirstlane(2 * kr + t >= 20 ? 2 * kr + t - 20 : 2 * kr + t);
            GLS(ev[sub], (const char*)wp + (size_t)c * (32 * NQKV * 2), lw, L_W + (t % NW) * W_SLOT + sub * 4096);
        }
        ring_issue<NA, P0 + 1, P1>(ev, xp, wp, kr, lw);
    }
}
template <int NA, int J>
__device__ __forceinline__ void ring_run(const unsigned (&ev)[9], const void* xp, const void* wp, int kr, unsigned lw) {
    ring_issue<NA, (J < 0 ? 0 : ring_cum(J - 1, NA)), ring_cum(J, NA)>(ev, xp, wp, kr, lw);
    if constexpr (J < 16 && J <= JL) {
        waitvm<ring_cum(J, NA) - ring_need(J, NA)>();
        BARRIER();
        if constexpr (J < JL) ring_run<NA, J + 1>(ev, xp, wp, kr, lw);
    }
}

#define CONV_ASM \
    "s_mov_b32 m0, %[kvl]\n" \
    "v_mov_b32 v50, 1\n" \
    "global_load_lds_dwordx4 %[xo], %[wsp] offset:0 sc1\n" \
    "global_load_lds_dwordx4 %[xo], %[wsp] offset:1024 sc1\n" \
    "global_load_lds_dwordx4 %[xo], %[wsp] offset:2048 sc1\n" \
    "global_load_lds_dwordx4 %[xo], %[wsp] offset:3072 sc1\n" \
    "s_mov_b64 exec, 1\n" \
    "ds_add_u32 %[ctl], v50 offset:80\n" \
    "s_mov_b64 exec, -1\n" \
    "v_add_u32 v84, %[sx4], %[xp]\n" \
    "v_add_u32 v51, %[sh4], %[hp]\n" \
    "v_add_u32 v160, 0, %[xp]\n" \
    "v_add_u32 v161, 1152, %[xp]\n" \
    "v_add_u32 v85, 1152, v84\n" \
    "v_add_u32 v162, 2304, %[xp]\n" \
    "v_add_u32 v86, 2304, v84\n" \
    "v_add_u32 v163, 3456, %[xp]\n" \
    "v_add_u32 v87, 3456, v84\n" \
    "ds_read2_b32 v[88:89], v87 offset0:144 offset1:145\n" \
    "ds_read2_b32 v[90:91], v87 offset0:146 offset1:147\n" \
    "ds_read2_b32 v[92:93], v87 offset0:0 offset1:1\n" \
    "ds_read2_b32 v[94:95], v87 offset0:2 offset1:3\n" \
    "ds_read2_b32 v[96:97], v86 offset0:144 offset1:145\n" \
    "ds_read2_b32 v[98:99], v86 offset0:146 offset1:147\n" \
    "ds_read2_b32 v[100:101], v86 offset0:0 offset1:1\n" \
    "ds_read2_b32 v[102:103], v86 offset0:2 offset1:3\n" \
    "ds_read2_b32 v[104:105], v85 offset0:144 offset1:145\n" \
    "ds_read2_b32 v[106:107], v85 offset0:146 offset1:147\n" \
    "ds_read2_b32 v[108:109], v85 offset0:0 offset1:1\n" \
    "ds_read2_b32 v[110:111], v85 offset0:2 offset1:3\n" \
    "ds_read2_b32 v[112:113], v84 offset0:144 offset1:145\n" \
    "ds_read2_b32 v[114:115], v84 offset0:146 offset1:147\n" \
    "ds_read2_b32 v[116:117], v84 offset0:0 offset1:1\n" \
    "ds_read2_b32 v[118:119], v84 offset0:2 offset1:3\n" \
    "s_mov_b32 %[cnt], 0\n" \
    "Lsp_%=:\n" \
    "ds_read_b32 v50, %[ctl] offset:144\n" \
    "s_add_u32 %[cnt], %[cnt], 1\n" \
    "s_waitcnt lgkmcnt(0)\n" \
    "v_readfirstlane_b32 %[tmp], v50\n" \
    "s_cmp_ge_u32 %[cnt], %[cap]\n" \
    "s_cbranch_scc1 Lgo_%=\n" \
    "s_cmp_lt_u32 %[tmp], 4\n" \
    "s_cbranch_scc1 Lsp_%=\n" \
    "Lgo_%=:\n" \
    "ds_read_b128 v[148:151], v51 offset:3584\n" \
    "ds_read_b128 v[144:147], v51 offset:3072\n" \
    "ds_read_b128 v[140:143], v51 offset:2560\n" \
    "ds_read_b128 v[136:139], v51 offset:2048\n" \
    "ds_read_b128 v[132:135], v51 offset:1536\n" \
    "ds_read_b128 v[128:131], v51 offset:1024\n" \
    "ds_read_b128 v[124:127], v51 offset:512\n" \
    "ds_read_b128 v[120:123], v51 offset:0\n" \
    "s_waitcnt lgkmcnt(7)\n" \
    "v_mfma_f32_16x16x32_bf16 v[80:83], v[88:91], v[148:151], 0\n" \
    "ds_read_b128 v[148:151], v51 offset:3648\n" \
    "s_waitcnt lgkmcnt(7)\n" \
    "v_mfma_f32_16x16x32_bf16 v[76:79], v[88:91], v[144:147], 0\n" \
    "ds_read_b128 v[156:159], v51 offset:576\n" \
    "s_waitcnt lgkmcnt(7)\n" \
    "v_mfma_f32_16x16x32_bf16 v[72:75], v[88:91], v[140:143], 0\n" \
    "ds_read_b128 v[152:155], v51 offset:64\n" \
    "s_waitcnt lgkmcnt(7)\n" \
    "v_mfma_f32_16x16x32_bf16 v[68:71], v[88:91], v[136:139], 0\n" \
    "s_waitcnt lgkmcnt(6)\n" \
    "v_mfma_f32_16x16x32_bf16 v[64:67], v[88:91], v[132:135], 0\n" \
    "s_waitcnt lgkmcnt(5)\n" \
    "v_mfma_f32_16x16x32_bf16 v[60:63], v[88:91], v[128:131], 0\n" \
    "s_waitcnt lgkmcnt(4)\n" \
    "v_mfma_f32_16x16x32_bf16 v[56:59], v[88:91], v[124:127], 0\n" \
    "s_waitcnt lgkmcnt(3)\n" \
    "v_mfma_f32_16x16x32_bf16 v[52:55], v[88:91], v[120:123], 0\n" \
    "ds_read2_b32 v[88:89], v87 offset0:160 offset1:161\n" \
    "v_mfma_f32_16x16x32_bf16 v[80:83], v[92:95], v[144:147], v[80:83]\n" \
    "ds_read2_b32 v[90:91], v87 offset0:162 offset1:163\n" \
    "v_mfma_f32_16x16x32_bf16 v[76:79], v[92:95], v[140:143], v[76:79]\n" \
    "ds_read_b128 v[144:147], v51 offset:3136\n" \
    "v_mfma_f32_16x16x32_bf16 v[72:75], v[92:95], v[136:139], v[72:75]\n" \
    "v_mfma_f32_16x16x32_bf16 v[68:71], v[92:95], v[132:135], v[68:71]\n" \
    "v_mfma_f32_16x16x32_bf16 v[64:67], v[92:95], v[128:131], v[64:67]\n" \
    "v_mfma_f32_16x16x32_bf16 v[60:63], v[92:95], v[124:127], v[60:63]\n" \
    "v_mfma_f32_16x16x32_bf16 v[56:59], v[92:95], v[120:123], v[56:59]\n" \
    "ds_read2_b32 v[92:93], v87 offset0:16 offset1:17\n" \
    "v_mfma_f32_16x16x32_bf16 v[80:83], v[96:99], v[140:143], v[80:83]\n" \
    "ds_read_b128 v[140:143], v51 offset:2624\n" \
    "v_mfma_f32_16x16x32_bf16 v[76:79], v[96:99], v[136:139], v[76:79]\n" \
    "ds_read2_b32 v[94:95], v87 offset0:18 offset1:19\n" \
    "v_mfma_f32_16x16x32_bf16 v[72:75], v[96:99], v[132:135], v[72:75]\n" \
    "v_mfma_f32_16x16x32_bf16 v[68:71], v[96:99], v[128:131], v[68:71]\n" \
    "v_mfma_f32_16x16x32_bf16 v[64:67], v[96:99], v[124:127], v[64:67]\n" \
    "v_mfma_f32_16x16x32_bf16 v[60:63], v[96:99], v[120:123], v[60:63]\n" \
    "ds_read2_b32 v[96:97], v86 offset0:160 offset1:161\n" \
    "v_mfma_f32_16x16x32_bf16 v[80:83], v[100:103], v[136:139], v[80:83]\n" \
    "ds_read_b128 v[136:139], v51 offset:2112\n" \
    "v_mfma_f32_16x16x32_bf16 v[76:79], v[100:103], v[132:135], v[76:79]\n" \
    "ds_read2_b32 v[98:99], v86 offset0:162 offset1:163\n" \
    "v_mfma_f32_16x16x32_bf16 v[72:75], v[100:103], v[128:131], v[72:75]\n" \
    "v_mfma_f32_16x16x32_bf16 v[68:71], v[100:103], v[124:127], v[68:71]\n" \
    "v_mfma_f32_16x16x32_bf16 v[64:67], v[100:103], v[120:123], v[64:67]\n" \
    "ds_read2_b32 v[100:101], v86 offset0:16 offset1:17\n" \
    "v_mfma_f32_16x16x32_bf16 v[80:83], v[104:107], v[132:135], v[80:83]\n" \
    "ds_read_b128 v[132:135], v51 offset:1600\n" \
    "v_mfma_f32_16x16x32_bf16 v[76:79], v[104:107], v[128:131], v[76:79]\n" \
    "ds_read2_b32 v[102:103], v86 offset0:18 offset1:19\n" \
    "v_mfma_f32_16x16x32_bf16 v[72:75], v[104:107], v[124:127], v[72:75]\n" \
    "v_mfma_f32_16x16x32_bf16 v[68:71], v[104:107], v[120:123], v[68:71]\n" \
    "ds_read2_b32 v[104:105], v85 offset0:160 offset1:161\n" \
    "v_mfma_f32_16x16x32_bf16 v[80:83], v[108:111], v[128:131], v[80:83]\n" \
    "ds_read_b128 v[128:131], v51 offset:1088\n" \
    "v_mfma_f32_16x16x32_bf16 v[76:79], v[108:111], v[124:127], v[76:79]\n" \
    "ds_read2_b32 v[106:107], v85 offset0:162 offset1:163\n" \
    "v_mfma_f32_16x16x32_bf16 v[72:75], v[108:111], v[120:123], v[72:75]\n" \
    "ds_read2_b32 v[108:109], v85 offset0:16 offset1:17\n" \
    "v_mfma_f32_16x16x32_bf16 v[80:83], v[112:115], v[124:127], v[80:83]\n" \
    "ds_read2_b32 v[110:111], v85 offset0:18 offset1:19\n" \
    "v_mfma_f32_16x16x32_bf16 v[76:79], v[112:115], v[120:123], v[76:79]\n" \
    "ds_read2_b32 v[112:113], v84 offset0:160 offset1:161\n" \
    "v_mfma_f32_16x16x32_bf16 v[80:83], v[116:119], v[120:123], v[80:83]\n" \
    "ds_read2_b32 v[114:115], v84 offset0:162 offset1:163\n" \
    "s_waitcnt lgkmcnt(15)\n" \
    "v_mfma_f32_16x16x32_bf16 v[80:83], v[88:91], v[148:151], v[80:83]\n" \
    "ds_read2_b32 v[116:117], v84 offset0:16 offset1:17\n" \
    "v_mfma_f32_16x16x32_bf16 v[76:79], v[88:91], v[144:147], v[76:79]\n" \
    "ds_read2_b32 v[118:119], v84 offset0:18 offset1:19\n" \
    "s_waitcnt lgkmcnt(15)\n" \
    "v_mfma_f32_16x16x32_bf16 v[72:75], v[88:91], v[140:143], v[72:75]\n" \
    "s_waitcnt vmcnt(0)\n" \
    "ds_read_b128 v[148:151], %[hp] offset:3584\n" \
    "s_waitcnt lgkmcnt(14)\n" \
    "v_mfma_f32_16x16x32_bf16 v[68:71], v[88:91], v[136:139], v[68:71]\n" \
    "ds_read_b128 v[124:127], %[hp] offset:512\n" \
    "s_waitcnt lgkmcnt(12)\n" \
    "v_mfma_f32_16x16x32_bf16 v[64:67], v[88:91], v[132:135], v[64:67]\n" \
    "ds_read_b128 v[120:123], %[hp] offset:0\n" \
    "s_waitcnt lgkmcnt(10)\n" \
    "v_mfma_f32_16x16x32_bf16 v[60:63], v[88:91], v[128:131], v[60:63]\n" \
    "v_mfma_f32_16x16x32_bf16 v[56:59], v[88:91], v[156:159], v[56:59]\n" \
    "v_mfma_f32_16x16x32_bf16 v[52:55], v[88:91], v[152:155], v[52:55]\n" \
    "ds_read2_b32 v[88:89], v163 offset0:144 offset1:145\n" \
    "v_mfma_f32_16x16x32_bf16 v[80:83], v[92:95], v[144:147], v[80:83]\n" \
    "ds_read2_b32 v[90:91], v163 offset0:146 offset1:147\n" \
    "v_mfma_f32_16x16x32_bf16 v[76:79], v[92:95], v[140:143], v[76:79]\n" \
    "ds_read_b128 v[144:147], %[hp] offset:3072\n" \
    "v_mfma_f32_16x16x32_bf16 v[72:75], v[92:95], v[136:139], v[72:75]\n" \
    "v_mfma_f32_16x16x32_bf16 v[68:71], v[92:95], v[132:135], v[68:71]\n" \
    "ds_write_b128 %[scr], v[52:55] offset:0\n" \
    "v_mfma_f32_16x16x32_bf16 v[64:67], v[92:95], v[128:131], v[64:67]\n" \
    "ds_read_b64 v[86:87], %[kvr] offset:0\n" \
    "v_mfma_f32_16x16x32_bf16 v[60:63], v[92:95], v[156:159], v[60:63]\n" \
    "v_mfma_f32_16x16x32_bf16 v[56:59], v[92:95], v[152:155], v[56:59]\n" \
    "ds_read2_b32 v[92:93], v163 offset0:0 offset1:1\n" \
    "v_mfma_f32_16x16x32_bf16 v[80:83], v[96:99], v[140:143], v[80:83]\n" \
    "ds_read_b128 v[140:143], %[hp] offset:2560\n" \
    "v_mfma_f32_16x16x32_bf16 v[76:79], v[96:99], v[136:139], v[76:79]\n" \
    "ds_read2_b32 v[94:95], v163 offset0:2 offset1:3\n" \
    "v_mfma_f32_16x16x32_bf16 v[72:75], v[96:99], v[132:135], v[72:75]\n" \
    "v_mfma_f32_16x16x32_bf16 v[68:71], v[96:99], v[128:131], v[68:71]\n" \
    "ds_write_b128 %[scr], v[56:59] offset:1024\n" \
    "v_mfma_f32_16x16x32_bf16 v[64:67], v[96:99], v[156:159], v[64:67]\n" \
    "v_mfma_f32_16x16x32_bf16 v[60:63], v[96:99], v[152:155], v[60:63]\n" \
    "ds_read2_b32 v[96:97], v162 offset0:144 offset1:145\n" \
    "v_mfma_f32_16x16x32_bf16 v[80:83], v[100:103], v[136:139], v[80:83]\n" \
    "ds_read_b128 v[136:139], %[hp] offset:2048\n" \
    "v_mfma_f32_16x16x32_bf16 v[76:79], v[100:103], v[132:135], v[76:79]\n" \
    "ds_read2_b32 v[98:99], v162 offset0:146 offset1:147\n" \
    "v_mfma_f32_16x16x32_bf16 v[72:75], v[100:103], v[128:131], v[72:75]\n" \
    "v_mfma_f32_16x16x32_bf16 v[68:71], v[100:103], v[156:159], v[68:71]\n" \
    "s_waitcnt lgkmcnt(7)\n" \
    "v_lshlrev_b32 v84, 16, v86\n" \
    "v_mfma_f32_16x16x32_bf16 v[64:67], v[100:103], v[152:155], v[64:67]\n" \
    "ds_read2_b32 v[100:101], v162 offset0:0 offset1:1\n" \
    "v_mfma_f32_16x16x32_bf16 v[80:83], v[104:107], v[132:135], v[80:83]\n" \
    "ds_read_b128 v[132:135], %[hp] offset:1536\n" \
    "v_and_b32 v85, 0xffff0000, v86\n" \
    "v_mfma_f32_16x16x32_bf16 v[76:79], v[104:107], v[128:131], v[76:79]\n" \
    "ds_read2_b32 v[102:103], v162 offset0:2 offset1:3\n" \
    "v_mfma_f32_16x16x32_bf16 v[72:75], v[104:107], v[156:159], v[72:75]\n" \
    "v_lshlrev_b32 v86, 16, v87\n" \
    "v_mfma_f32_16x16x32_bf16 v[68:71], v[104:107], v[152:155], v[68:71]\n" \
    "ds_read2_b32 v[104:105], v161 offset0:144 offset1:145\n" \
    "v_mfma_f32_16x16x32_bf16 v[80:83], v[108:111], v[128:131], v[80:83]\n" \
    "ds_read_b128 v[128:131], %[hp] offset:1024\n" \
    "v_and_b32 v87, 0xffff0000, v87\n" \
    "v_mfma_f32_16x16x32_bf16 v[76:79], v[108:111], v[156:159], v[76:79]\n" \
    "ds_read2_b32 v[106:107], v161 offset0:146 offset1:147\n" \
    "v_mfma_f32_16x16x32_bf16 v[72:75], v[108:111], v[152:155], v[72:75]\n" \
    "ds_read2_b32 v[108:109], v161 offset0:0 offset1:1\n" \
    "v_mfma_f32_16x16x32_bf16 v[80:83], v[112:115], v[156:159], v[80:83]\n" \
    "ds_read2_b32 v[110:111], v161 offset0:2 offset1:3\n" \
    "v_mfma_f32_16x16x32_bf16 v[76:79], v[112:115], v[152:155], v[76:79]\n" \
    "ds_read2_b32 v[112:113], v160 offset0:144 offset1:145\n" \
    "v_mfma_f32_16x16x32_bf16 v[80:83], v[116:119], v[152:155], v[80:83]\n" \
    "ds_read2_b32 v[114:115], v160 offset0:146 offset1:147\n" \
    "v_mfma_f32_16x16x32_bf16 v[44:47], v[88:91], v[148:151], 0\n" \
    "ds_read2_b32 v[116:117], v160 offset0:0 offset1:1\n" \
    "v_mfma_f32_16x16x32_bf16 v[40:43], v[88:91], v[144:147], 0\n" \
    "ds_read2_b32 v[118:119], v160 offset0:2 offset1:3\n" \
    "s_waitcnt lgkmcnt(15)\n" \
    "v_mfma_f32_16x16x32_bf16 v[36:39], v[88:91], v[140:143], 0\n" \
    "ds_read_b128 v[148:151], %[hp] offset:3648\n" \
    "s_waitcnt lgkmcnt(14)\n" \
    "v_mfma_f32_16x16x32_bf16 v[32:35], v[88:91], v[136:139], 0\n" \
    "ds_read_b128 v[156:159], %[hp] offset:576\n" \
    "s_waitcnt lgkmcnt(12)\n" \
    "v_mfma_f32_16x16x32_bf16 v[28:31], v[88:91], v[132:135], 0\n" \
    "ds_read_b128 v[152:155], %[hp] offset:64\n" \
    "s_waitcnt lgkmcnt(10)\n" \
    "v_mfma_f32_16x16x32_bf16 v[24:27], v[88:91], v[128:131], 0\n" \
    "ds_write_b128 %[scr], v[60:63] offset:2048\n" \
    "v_mfma_f32_16x16x32_bf16 v[20:23], v[88:91], v[124:127], 0\n" \
    "v_mfma_f32_16x16x32_bf16 v[16:19], v[88:91], v[120:123], 0\n" \
    "ds_read2_b32 v[88:89], v163 offset0:160 offset1:161\n" \
    "v_mfma_f32_16x16x32_bf16 v[44:47], v[92:95], v[144:147], v[44:47]\n" \
    "ds_read2_b32 v[90:91], v163 offset0:162 offset1:163\n" \
    "v_mfma_f32_16x16x32_bf16 v[40:43], v[92:95], v[140:143], v[40:43]\n" \
    "ds_read_b128 v[144:147], %[hp] offset:3136\n" \
    "v_mfma_f32_16x16x32_bf16 v[36:39], v[92:95], v[136:139], v[36:39]\n" \
    "v_fmac_f32 v16, %[dd], v84\n" \
    "v_fmac_f32 v17, %[dd], v85\n" \
    "v_mfma_f32_16x16x32_bf16 v[32:35], v[92:95], v[132:135], v[32:35]\n" \
    "v_fmac_f32 v18, %[dd], v86\n" \
    "v_fmac_f32 v19, %[dd], v87\n" \
    "v_mfma_f32_16x16x32_bf16 v[28:31], v[92:95], v[128:131], v[28:31]\n" \
    "ds_read_b64 v[86:87], %[kvr] offset:512\n" \
    "v_mfma_f32_16x16x32_bf16 v[24:27], v[92:95], v[124:127], v[24:27]\n" \
    "ds_write_b128 %[scr], v[64:67] offset:3072\n" \
    "v_mfma_f32_16x16x32_bf16 v[20:23], v[92:95], v[120:123], v[20:23]\n" \
    "ds_read2_b32 v[92:93], v163 offset0:16 offset1:17\n" \
    "v_mfma_f32_16x16x32_bf16 v[44:47], v[96:99], v[140:143], v[44:47]\n" \
    "ds_read_b128 v[140:143], %[hp] offset:2624\n" \
    "v_mfma_f32_16x16x32_bf16 v[40:43], v[96:99], v[136:139], v[40:43]\n" \
    "ds_read2_b32 v[94:95], v163 offset0:18 offset1:19\n" \
    "v_mfma_f32_16x16x32_bf16 v[36:39], v[96:99], v[132:135], v[36:39]\n" \
    "ds_write_b128 %[scr], v[68:71] offset:4096\n" \
    "v_mfma_f32_16x16x32_bf16 v[32:35], v[96:99], v[128:131], v[32:35]\n" \
    "v_mfma_f32_16x16x32_bf16 v[28:31], v[96:99], v[124:127], v[28:31]\n" \
    "v_mfma_f32_16x16x32_bf16 v[24:27], v[96:99], v[120:123], v[24:27]\n" \
    "ds_read2_b32 v[96:97], v162 offset0:160 offset1:161\n" \
    "v_mfma_f32_16x16x32_bf16 v[44:47], v[100:103], v[136:139], v[44:47]\n" \
    "ds_read_b128 v[136:139], %[hp] offset:2112\n" \
    "v_mfma_f32_16x16x32_bf16 v[40:43], v[100:103], v[132:135], v[40:43]\n" \
    "ds_read2_b32 v[98:99], v162 offset0:162 offset1:163\n" \
    "v_mfma_f32_16x16x32_bf16 v[36:39], v[100:103], v[128:131], v[36:39]\n" \
    "ds_write_b128 %[scr], v[72:75] offset:5120\n" \
    "v_mfma_f32_16x16x32_bf16 v[32:35], v[100:103], v[124:127], v[32:35]\n" \
    "s_waitcnt lgkmcnt(9)\n" \
    "v_lshlrev_b32 v84, 16, v86\n" \
    "v_mfma_f32_16x16x32_bf16 v[28:31], v[100:103], v[120:123], v[28:31]\n" \
    "ds_read2_b32 v[100:101], v162 offset0:16 offset1:17\n" \
    "v_mfma_f32_16x16x32_bf16 v[44:47], v[104:107], v[132:135], v[44:47]\n" \
    "ds_read_b128 v[132:135], %[hp] offset:1600\n" \
    "v_and_b32 v85, 0xffff0000, v86\n" \
    "v_mfma_f32_16x16x32_bf16 v[40:43], v[104:107], v[128:131], v[40:43]\n" \
    "ds_read2_b32 v[102:103], v162 offset0:18 offset1:19\n" \
    "v_mfma_f32_16x16x32_bf16 v[36:39], v[104:107], v[124:127], v[36:39]\n" \
    "v_lshlrev_b32 v86, 16, v87\n" \
    "v_mfma_f32_16x16x32_bf16 v[32:35], v[104:107], v[120:123], v[32:35]\n" \
    "ds_read2_b32 v[104:105], v161 offset0:160 offset1:161\n" \
    "v_mfma_f32_16x16x32_bf16 v[44:47], v[108:111], v[128:131], v[44:47]\n" \
    "ds_read_b128 v[128:131], %[hp] offset:1088\n" \
    "v_and_b32 v87, 0xffff0000, v87\n" \
    "v_mfma_f32_16x16x32_bf16 v[40:43], v[108:111], v[124:127], v[40:43]\n" \
    "ds_read2_b32 v[106:107], v161 offset0:162 offset1:163\n" \
    "v_mfma_f32_16x16x32_bf16 v[36:39], v[108:111], v[120:123], v[36:39]\n" \
    "ds_read2_b32 v[108:109], v161 offset0:16 offset1:17\n" \
    "v_mfma_f32_16x16x32_bf16 v[44:47], v[112:115], v[124:127], v[44:47]\n" \
    "ds_read2_b32 v[110:111], v161 offset0:18 offset1:19\n" \
    "v_mfma_f32_16x16x32_bf16 v[40:43], v[112:115], v[120:123], v[40:43]\n" \
    "ds_read2_b32 v[112:113], v160 offset0:160 offset1:161\n" \
    "v_mfma_f32_16x16x32_bf16 v[44:47], v[116:119], v[120:123], v[44:47]\n" \
    "ds_read2_b32 v[114:115], v160 offset0:162 offset1:163\n" \
    "v_mfma_f32_16x16x32_bf16 v[44:47], v[88:91], v[148:151], v[44:47]\n" \
    "ds_read2_b32 v[116:117], v160 offset0:16 offset1:17\n" \
    "v_mfma_f32_16x16x32_bf16 v[40:43], v[88:91], v[144:147], v[40:43]\n" \
    "ds_read2_b32 v[118:119], v160 offset0:18 offset1:19\n" \
    "s_waitcnt lgkmcnt(15)\n" \
    "v_mfma_f32_16x16x32_bf16 v[36:39], v[88:91], v[140:143], v[36:39]\n" \
    "ds_read_b128 v[148:151], %[hp] offset:3712\n" \
    "s_waitcnt lgkmcnt(15)\n" \
    "v_mfma_f32_16x16x32_bf16 v[32:35], v[88:91], v[136:139], v[32:35]\n" \
    "ds_read_b128 v[124:127], %[hp] offset:640\n" \
    "s_waitcnt lgkmcnt(12)\n" \
    "v_mfma_f32_16x16x32_bf16 v[28:31], v[88:91], v[132:135], v[28:31]\n" \
    "ds_read_b128 v[120:123], %[hp] offset:128\n" \
    "s_waitcnt lgkmcnt(10)\n" \
    "v_mfma_f32_16x16x32_bf16 v[24:27], v[88:91], v[128:131], v[24:27]\n" \
    "ds_write_b128 %[scr], v[76:79] offset:6144\n" \
    "v_mfma_f32_16x16x32_bf16 v[20:23], v[88:91], v[156:159], v[20:23]\n" \
    "v_mfma_f32_16x16x32_bf16 v[16:19], v[88:91], v[152:155], v[16:19]\n" \
    "ds_read2_b32 v[88:89], v163 offset0:176 offset1:177\n" \
    "v_mfma_f32_16x16x32_bf16 v[44:47], v[92:95], v[144:147], v[44:47]\n" \
    "ds_read2_b32 v[90:91], v163 offset0:178 offset1:179\n" \
    "v_mfma_f32_16x16x32_bf16 v[40:43], v[92:95], v[140:143], v[40:43]\n" \
    "ds_read_b128 v[144:147], %[hp] offset:3200\n" \
    "v_fmac_f32 v20, %[dd], v84\n" \
    "v_fmac_f32 v21, %[dd], v85\n" \
    "v_mfma_f32_16x16x32_bf16 v[36:39], v[92:95], v[136:139], v[36:39]\n" \
    "v_fmac_f32 v22, %[dd], v86\n" \
    "v_fmac_f32 v23, %[dd], v87\n" \
    "v_mfma_f32_16x16x32_bf16 v[32:35], v[92:95], v[132:135], v[32:35]\n" \
    "ds_write_b128 %[scr], v[80:83] offset:7168\n" \
    "v_mov_b32 v50, 1\n" \
    "s_mov_b64 exec, 1\n" \
    "ds_add_u32 %[ctl], v50 offset:84\n" \
    "s_mov_b64 exec, -1\n" \
    "v_mfma_f32_16x16x32_bf16 v[28:31], v[92:95], v[128:131], v[28:31]\n" \
    "ds_read_b64 v[86:87], %[kvr] offset:1024\n" \
    "v_mfma_f32_16x16x32_bf16 v[24:27], v[92:95], v[156:159], v[24:27]\n" \
    "v_mfma_f32_16x16x32_bf16 v[20:23], v[92:95], v[152:155], v[20:23]\n" \
    "ds_read2_b32 v[92:93], v163 offset0:32 offset1:33\n" \
    "v_mfma_f32_16x16x32_bf16 v[44:47], v[96:99], v[140:143], v[44:47]\n" \
    "ds_read_b128 v[140:143], %[hp] offset:2688\n" \
    "v_mfma_f32_16x16x32_bf16 v[40:43], v[96:99], v[136:139], v[40:43]\n" \
    "ds_read2_b32 v[94:95], v163 offset0:34 offset1:35\n" \
    "v_mfma_f32_16x16x32_bf16 v[36:39], v[96:99], v[132:135], v[36:39]\n" \
    "v_mfma_f32_16x16x32_bf16 v[32:35], v[96:99], v[128:131], v[32:35]\n" \
    "v_mfma_f32_16x16x32_bf16 v[28:31], v[96:99], v[156:159], v[28:31]\n" \
    "v_mfma_f32_16x16x32_bf16 v[24:27], v[96:99], v[152:155], v[24:27]\n" \
    "ds_read2_b32 v[96:97], v162 offset0:176 offset1:177\n" \
    "v_mfma_f32_16x16x32_bf16 v[44:47], v[100:103], v[136:139], v[44:47]\n" \
    "ds_read_b128 v[136:139], %[hp] offset:2176\n" \
    "v_mfma_f32_16x16x32_bf16 v[40:43], v[100:103], v[132:135], v[40:43]\n" \
    "ds_read2_b32 v[98:99], v162 offset0:178 offset1:179\n" \
    "v_mfma_f32_16x16x32_bf16 v[36:39], v[100:103], v[128:131], v[36:39]\n" \
    "v_mfma_f32_16x16x32_bf16 v[32:35], v[100:103], v[156:159], v[32:35]\n" \
    "s_waitcnt lgkmcnt(6)\n" \
    "v_lshlrev_b32 v84, 16, v86\n" \
    "v_mfma_f32_16x16x32_bf16 v[28:31], v[100:103], v[152:155], v[28:31]\n" \
    "ds_read2_b32 v[100:101], v162 offset0:32 offset1:33\n" \
    "v_mfma_f32_16x16x32_bf16 v[44:47], v[104:107], v[132:135], v[44:47]\n" \
    "ds_read_b128 v[132:135], %[hp] offset:1664\n" \
    "v_and_b32 v85, 0xffff0000, v86\n" \
    "v_mfma_f32_16x16x32_bf16 v[40:43], v[104:107], v[128:131], v[40:43]\n" \
    "ds_read2_b32 v[102:103], v162 offset0:34 offset1:35\n" \
    "v_mfma_f32_16x16x32_bf16 v[36:39], v[104:107], v[156:159], v[36:39]\n" \
    "v_lshlrev_b32 v86, 16, v87\n" \
    "v_mfma_f32_16x16x32_bf16 v[32:35], v[104:107], v[152:155], v[32:35]\n" \
    "ds_read2_b32 v[104:105], v161 offset0:176 offset1:177\n" \
    "v_mfma_f32_16x16x32_bf16 v[44:47], v[108:111], v[128:131], v[44:47]\n" \
    "ds_read_b128 v[128:131], %[hp] offset:1152\n" \
    "v_and_b32 v87, 0xffff0000, v87\n" \
    "v_mfma_f32_16x16x32_bf16 v[40:43], v[108:111], v[156:159], v[40:43]\n" \
    "ds_read2_b32 v[106:107], v161 offset0:178 offset1:179\n" \
    "v_mfma_f32_16x16x32_bf16 v[36:39], v[108:111], v[152:155], v[36:39]\n" \
    "ds_read2_b32 v[108:109], v161 offset0:32 offset1:33\n" \
    "v_mfma_f32_16x16x32_bf16 v[44:47], v[112:115], v[156:159], v[44:47]\n" \
    "ds_read2_b32 v[110:111], v161 offset0:34 offset1:35\n" \
    "v_mfma_f32_16x16x32_bf16 v[40:43], v[112:115], v[152:155], v[40:43]\n" \
    "ds_read2_b32 v[112:113], v160 offset0:176 offset1:177\n" \
    "v_mfma_f32_16x16x32_bf16 v[44:47], v[116:119], v[152:155], v[44:47]\n" \
    "ds_read2_b32 v[114:115], v160 offset0:178 offset1:179\n" \
    "v_mfma_f32_16x16x32_bf16 v[44:47], v[88:91], v[148:151], v[44:47]\n" \
    "ds_read2_b32 v[116:117], v160 offset0:32 offset1:33\n" \
    "v_mfma_f32_16x16x32_bf16 v[40:43], v[88:91], v[144:147], v[40:43]\n" \
    "ds_read2_b32 v[118:119], v160 offset0:34 offset1:35\n" \
    "s_waitcnt lgkmcnt(15)\n" \
    "v_mfma_f32_16x16x32_bf16 v[36:39], v[88:91], v[140:143], v[36:39]\n" \
    "ds_read_b128 v[148:151], %[hp] offset:3776\n" \
    "s_waitcnt lgkmcnt(14)\n" \
    "v_mfma_f32_16x16x32_bf16 v[32:35], v[88:91], v[136:139], v[32:35]\n" \
    "ds_read_b128 v[156:159], %[hp] offset:704\n" \
    "s_waitcnt lgkmcnt(12)\n" \
    "v_mfma_f32_16x16x32_bf16 v[28:31], v[88:91], v[132:135], v[28:31]\n" \
    "ds_read_b128 v[152:155], %[hp] offset:192\n" \
    "s_waitcnt lgkmcnt(10)\n" \
    "v_mfma_f32_16x16x32_bf16 v[24:27], v[88:91], v[128:131], v[24:27]\n" \
    "v_mfma_f32_16x16x32_bf16 v[20:23], v[88:91], v[124:127], v[20:23]\n" \
    "v_mfma_f32_16x16x32_bf16 v[16:19], v[88:91], v[120:123], v[16:19]\n" \
    "ds_read2_b32 v[88:89], v163 offset0:192 offset1:193\n" \
    "v_mfma_f32_16x16x32_bf16 v[44:47], v[92:95], v[144:147], v[44:47]\n" \
    "ds_read2_b32 v[90:91], v163 offset0:194 offset1:195\n" \
    "v_fmac_f32 v24, %[dd], v84\n" \
    "v_fmac_f32 v25, %[dd], v85\n" \
    "v_mfma_f32_16x16x32_bf16 v[40:43], v[92:95], v[140:143], v[40:43]\n" \
    "ds_read_b128 v[144:147], %[hp] offset:3264\n" \
    "v_fmac_f32 v26, %[dd], v86\n" \
    "v_fmac_f32 v27, %[dd], v87\n" \
    "v_mfma_f32_16x16x32_bf16 v[36:39], v[92:95], v[136:139], v[36:39]\n" \
    "v_mfma_f32_16x16x32_bf16 v[32:35], v[92:95], v[132:135], v[32:35]\n" \
    "v_mfma_f32_16x16x32_bf16 v[28:31], v[92:95], v[128:131], v[28:31]\n" \
    "ds_read_b64 v[86:87], %[kvr] offset:1536\n" \
    "v_mfma_f32_16x16x32_bf16 v[24:27], v[92:95], v[124:127], v[24:27]\n" \
    "v_mfma_f32_16x16x32_bf16 v[20:23], v[92:95], v[120:123], v[20:23]\n" \
    "ds_read2_b32 v[92:93], v163 offset0:48 offset1:49\n" \
    "v_mfma_f32_16x16x32_bf16 v[44:47], v[96:99], v[140:143], v[44:47]\n" \
    "ds_read_b128 v[140:143], %[hp] offset:2752\n" \
    "v_mfma_f32_16x16x32_bf16 v[40:43], v[96:99], v[136:139], v[40:43]\n" \
    "ds_read2_b32 v[94:95], v163 offset0:50 offset1:51\n" \
    "v_mfma_f32_16x16x32_bf16 v[36:39], v[96:99], v[132:135], v[36:39]\n" \
    "v_mfma_f32_16x16x32_bf16 v[32:35], v[96:99], v[128:131], v[32:35]\n" \
    "v_mfma_f32_16x16x32_bf16 v[28:31], v[96:99], v[124:127], v[28:31]\n" \
    "v_mfma_f32_16x16x32_bf16 v[24:27], v[96:99], v[120:123], v[24:27]\n" \
    "ds_read2_b32 v[96:97], v162 offset0:192 offset1:193\n" \
    "v_mfma_f32_16x16x32_bf16 v[44:47], v[100:103], v[136:139], v[44:47]\n" \
    "ds_read_b128 v[136:139], %[hp] offset:2240\n" \
    "v_mfma_f32_16x16x32_bf16 v[40:43], v[100:103], v[132:135], v[40:43]\n" \
    "ds_read2_b32 v[98:99], v162 offset0:194 offset1:195\n" \
    "v_mfma_f32_16x16x32_bf16 v[36:39], v[100:103], v[128:131], v[36:39]\n" \
    "v_mfma_f32_16x16x32_bf16 v[32:35], v[100:103], v[124:127], v[32:35]\n" \
    "s_waitcnt lgkmcnt(6)\n" \
    "v_lshlrev_b32 v84, 16, v86\n" \
    "v_mfma_f32_16x16x32_bf16 v[28:31], v[100:103], v[120:123], v[28:31]\n" \
    "ds_read2_b32 v[100:101], v162 offset0:48 offset1:49\n" \
    "v_mfma_f32_16x16x32_bf16 v[44:47], v[104:107], v[132:135], v[44:47]\n" \
    "ds_read_b128 v[132:135], %[hp] offset:1728\n" \
    "v_and_b32 v85, 0xffff0000, v86\n" \
    "v_mfma_f32_16x16x32_bf16 v[40:43], v[104:107], v[128:131], v[40:43]\n" \
    "ds_read2_b32 v[102:103], v162 offset0:50 offset1:51\n" \
    "v_mfma_f32_16x16x32_bf16 v[36:39], v[104:107], v[124:127], v[36:39]\n" \
    "v_lshlrev_b32 v86, 16, v87\n" \
    "v_mfma_f32_16x16x32_bf16 v[32:35], v[104:107], v[120:123], v[32:35]\n" \
    "ds_read2_b32 v[104:105], v161 offset0:192 offset1:193\n" \
    "v_mfma_f32_16x16x32_bf16 v[44:47], v[108:111], v[128:131], v[44:47]\n" \
    "ds_read_b128 v[128:131], %[hp] offset:1216\n" \
    "v_and_b32 v87, 0xffff0000, v87\n" \
    "v_mfma_f32_16x16x32_bf16 v[40:43], v[108:111], v[124:127], v[40:43]\n" \
    "ds_read2_b32 v[106:107], v161 offset0:194 offset1:195\n" \
    "v_mfma_f32_16x16x32_bf16 v[36:39], v[108:111], v[120:123], v[36:39]\n" \
    "ds_read2_b32 v[108:109], v161 offset0:48 offset1:49\n" \
    "v_mfma_f32_16x16x32_bf16 v[44:47], v[112:115], v[124:127], v[44:47]\n" \
    "ds_read2_b32 v[110:111], v161 offset0:50 offset1:51\n" \
    "v_mfma_f32_16x16x32_bf16 v[40:43], v[112:115], v[120:123], v[40:43]\n" \
    "ds_read2_b32 v[112:113], v160 offset0:192 offset1:193\n" \
    "v_mfma_f32_16x16x32_bf16 v[44:47], v[116:119], v[120:123], v[44:47]\n" \
    "ds_read2_b32 v[114:115], v160 offset0:194 offset1:195\n" \
    "v_mfma_f32_16x16x32_bf16 v[44:47], v[88:91], v[148:151], v[44:47]\n" \
    "ds_read2_b32 v[116:117], v160 offset0:48 offset1:49\n" \
    "v_mfma_f32_16x16x32_bf16 v[40:43], v[88:91], v[144:147], v[40:43]\n" \
    "ds_read2_b32 v[118:119], v160 offset0:50 offset1:51\n" \
    "s_waitcnt lgkmcnt(15)\n" \
    "v_mfma_f32_16x16x32_bf16 v[36:39], v[88:91], v[140:143], v[36:39]\n" \
    "ds_read_b128 v[148:151], %[hp] offset:3840\n" \
    "s_waitcnt lgkmcnt(14)\n" \
    "v_mfma_f32_16x16x32_bf16 v[32:35], v[88:91], v[136:139], v[32:35]\n" \
    "ds_read_b128 v[124:127], %[hp] offset:768\n" \
    "s_waitcnt lgkmcnt(12)\n" \
    "v_mfma_f32_16x16x32_bf16 v[28:31], v[88:91], v[132:135], v[28:31]\n" \
    "ds_read_b128 v[120:123], %[hp] offset:256\n" \
    "s_waitcnt lgkmcnt(10)\n" \
    "v_mfma_f32_16x16x32_bf16 v[24:27], v[88:91], v[128:131], v[24:27]\n" \
    "v_mfma_f32_16x16x32_bf16 v[20:23], v[88:91], v[156:159], v[20:23]\n" \
    "v_mfma_f32_16x16x32_bf16 v[16:19], v[88:91], v[152:155], v[16:19]\n" \
    "ds_read2_b32 v[88:89], v163 offset0:216 offset1:217\n" \
    "v_fmac_f32 v28, %[dd], v84\n" \
    "v_fmac_f32 v29, %[dd], v85\n" \
    "v_mfma_f32_16x16x32_bf16 v[44:47], v[92:95], v[144:147], v[44:47]\n" \
    "ds_read2_b32 v[90:91], v163 offset0:218 offset1:219\n" \
    "v_fmac_f32 v30, %[dd], v86\n" \
    "v_fmac_f32 v31, %[dd], v87\n" \
    "v_mfma_f32_16x16x32_bf16 v[40:43], v[92:95], v[140:143], v[40:43]\n" \
    "ds_read_b128 v[144:147], %[hp] offset:3328\n" \
    "v_mfma_f32_16x16x32_bf16 v[36:39], v[92:95], v[136:139], v[36:39]\n" \
    "v_mfma_f32_16x16x32_bf16 v[32:35], v[92:95], v[132:135], v[32:35]\n" \
    "v_mfma_f32_16x16x32_bf16 v[28:31], v[92:95], v[128:131], v[28:31]\n" \
    "ds_read_b64 v[86:87], %[kvr] offset:2048\n" \
    "v_mfma_f32_16x16x32_bf16 v[24:27], v[92:95], v[156:159], v[24:27]\n" \
    "v_mfma_f32_16x16x32_bf16 v[20:23], v[92:95], v[152:155], v[20:23]\n" \
    "ds_read2_b32 v[92:93], v163 offset0:72 offset1:73\n" \
    "v_mfma_f32_16x16x32_bf16 v[44:47], v[96:99], v[140:143], v[44:47]\n" \
    "ds_read_b128 v[140:143], %[hp] offset:2816\n" \
    "v_mfma_f32_16x16x32_bf16 v[40:43], v[96:99], v[136:139], v[40:43]\n" \
    "ds_read2_b32 v[94:95], v163 offset0:74 offset1:75\n" \
    "v_mfma_f32_16x16x32_bf16 v[36:39], v[96:99], v[132:135], v[36:39]\n" \
    "v_mfma_f32_16x16x32_bf16 v[32:35], v[96:99], v[128:131], v[32:35]\n" \
    "v_mfma_f32_16x16x32_bf16 v[28:31], v[96:99], v[156:159], v[28:31]\n" \
    "v_mfma_f32_16x16x32_bf16 v[24:27], v[96:99], v[152:155], v[24:27]\n" \
    "ds_read2_b32 v[96:97], v162 offset0:216 offset1:217\n" \
    "v_mfma_f32_16x16x32_bf16 v[44:47], v[100:103], v[136:139], v[44:47]\n" \
    "ds_read_b128 v[136:139], %[hp] offset:2304\n" \
    "v_mfma_f32_16x16x32_bf16 v[40:43], v[100:103], v[132:135], v[40:43]\n" \
    "ds_read2_b32 v[98:99], v162 offset0:218 offset1:219\n" \
    "v_mfma_f32_16x16x32_bf16 v[36:39], v[100:103], v[128:131], v[36:39]\n" \
    "v_mfma_f32_16x16x32_bf16 v[32:35], v[100:103], v[156:159], v[32:35]\n" \
    "s_waitcnt lgkmcnt(6)\n" \
    "v_lshlrev_b32 v84, 16, v86\n" \
    "v_mfma_f32_16x16x32_bf16 v[28:31], v[100:103], v[152:155], v[28:31]\n" \
    "ds_read2_b32 v[100:101], v162 offset0:72 offset1:73\n" \
    "v_mfma_f32_16x16x32_bf16 v[44:47], v[104:107], v[132:135], v[44:47]\n" \
    "ds_read_b128 v[132:135], %[hp] offset:1792\n" \
    "v_and_b32 v85, 0xffff0000, v86\n" \
    "v_mfma_f32_16x16x32_bf16 v[40:43], v[104:107], v[128:131], v[40:43]\n" \
    "ds_read2_b32 v[102:103], v162 offset0:74 offset1:75\n" \
    "v_mfma_f32_16x16x32_bf16 v[36:39], v[104:107], v[156:159], v[36:39]\n" \
    "v_lshlrev_b32 v86, 16, v87\n" \
    "v_mfma_f32_16x16x32_bf16 v[32:35], v[104:107], v[152:155], v[32:35]\n" \
    "ds_read2_b32 v[104:105], v161 offset0:216 offset1:217\n" \
    "v_mfma_f32_16x16x32_bf16 v[44:47], v[108:111], v[128:131], v[44:47]\n" \
    "ds_read_b128 v[128:131], %[hp] offset:1280\n" \
    "v_and_b32 v87, 0xffff0000, v87\n" \
    "v_mfma_f32_16x16x32_bf16 v[40:43], v[108:111], v[156:159], v[40:43]\n" \
    "ds_read2_b32 v[106:107], v161 offset0:218 offset1:219\n" \
    "v_mfma_f32_16x16x32_bf16 v[36:39], v[108:111], v[152:155], v[36:39]\n" \
    "ds_read2_b32 v[108:109], v161 offset0:72 offset1:73\n" \
    "v_mfma_f32_16x16x32_bf16 v[44:47], v[112:115], v[156:159], v[44:47]\n" \
    "ds_read2_b32 v[110:111], v161 offset0:74 offset1:75\n" \
    "v_mfma_f32_16x16x32_bf16 v[40:43], v[112:115], v[152:155], v[40:43]\n" \
    "ds_read2_b32 v[112:113], v160 offset0:216 offset1:217\n" \
    "v_mfma_f32_16x16x32_bf16 v[44:47], v[116:119], v[152:155], v[44:47]\n" \
    "ds_read2_b32 v[114:115], v160 offset0:218 offset1:219\n" \
    "v_add_u32 v160, 288, v160\n" \
    "v_add_u32 v161, 288, v161\n" \
    "v_add_u32 v162, 288, v162\n" \
    "v_add_u32 v163, 288, v163\n" \
    "v_mfma_f32_16x16x32_bf16 v[44:47], v[88:91], v[148:151], v[44:47]\n" \
    "ds_read2_b32 v[116:117], v160 offset0:0 offset1:1\n" \
    "v_mfma_f32_16x16x32_bf16 v[40:43], v[88:91], v[144:147], v[40:43]\n" \
    "ds_read2_b32 v[118:119], v160 offset0:2 offset1:3\n" \
    "s_waitcnt lgkmcnt(15)\n" \
    "v_mfma_f32_16x16x32_bf16 v[36:39], v[88:91], v[140:143], v[36:39]\n" \
    "ds_read_b128 v[148:151], %[hp] offset:3904\n" \
    "s_waitcnt lgkmcnt(14)\n" \
    "v_mfma_f32_16x16x32_bf16 v[32:35], v[88:91], v[136:139], v[32:35]\n" \
    "ds_read_b128 v[156:159], %[hp] offset:832\n" \
    "s_waitcnt lgkmcnt(12)\n" \
    "v_mfma_f32_16x16x32_bf16 v[28:31], v[88:91], v[132:135], v[28:31]\n" \
    "ds_read_b128 v[152:155], %[hp] offset:320\n" \
    "s_waitcnt lgkmcnt(10)\n" \
    "v_mfma_f32_16x16x32_bf16 v[24:27], v[88:91], v[128:131], v[24:27]\n" \
    "v_mfma_f32_16x16x32_bf16 v[20:23], v[88:91], v[124:127], v[20:23]\n" \
    "v_fmac_f32 v32, %[dd], v84\n" \
    "v_fmac_f32 v33, %[dd], v85\n" \
    "v_mfma_f32_16x16x32_bf16 v[16:19], v[88:91], v[120:123], v[16:19]\n" \
    "ds_read2_b32 v[88:89], v163 offset0:160 offset1:161\n" \
    "v_fmac_f32 v34, %[dd], v86\n" \
    "v_fmac_f32 v35, %[dd], v87\n" \
    "v_mfma_f32_16x16x32_bf16 v[44:47], v[92:95], v[144:147], v[44:47]\n" \
    "ds_read2_b32 v[90:91], v163 offset0:162 offset1:163\n" \
    "v_mfma_f32_16x16x32_bf16 v[40:43], v[92:95], v[140:143], v[40:43]\n" \
    "ds_read_b128 v[144:147], %[hp] offset:3392\n" \
    "v_mfma_f32_16x16x32_bf16 v[36:39], v[92:95], v[136:139], v[36:39]\n" \
    "v_mfma_f32_16x16x32_bf16 v[32:35], v[92:95], v[132:135], v[32:35]\n" \
    "v_mfma_f32_16x16x32_bf16 v[28:31], v[92:95], v[128:131], v[28:31]\n" \
    "ds_read_b64 v[86:87], %[kvr] offset:2560\n" \
    "v_mfma_f32_16x16x32_bf16 v[24:27], v[92:95], v[124:127], v[24:27]\n" \
    "v_mfma_f32_16x16x32_bf16 v[20:23], v[92:95], v[120:123], v[20:23]\n" \
    "ds_read2_b32 v[92:93], v163 offset0:16 offset1:17\n" \
    "v_mfma_f32_16x16x32_bf16 v[44:47], v[96:99], v[140:143], v[44:47]\n" \
    "ds_read_b128 v[140:143], %[hp] offset:2880\n" \
    "v_mfma_f32_16x16x32_bf16 v[40:43], v[96:99], v[136:139], v[40:43]\n" \
    "ds_read2_b32 v[94:95], v163 offset0:18 offset1:19\n" \
    "v_mfma_f32_16x16x32_bf16 v[36:39], v[96:99], v[132:135], v[36:39]\n" \
    "v_mfma_f32_16x16x32_bf16 v[32:35], v[96:99], v[128:131], v[32:35]\n" \
    "v_mfma_f32_16x16x32_bf16 v[28:31], v[96:99], v[124:127], v[28:31]\n" \
    "v_mfma_f32_16x16x32_bf16 v[24:27], v[96:99], v[120:123], v[24:27]\n" \
    "ds_read2_b32 v[96:97], v162 offset0:160 offset1:161\n" \
    "v_mfma_f32_16x16x32_bf16 v[44:47], v[100:103], v[136:139], v[44:47]\n" \
    "ds_read_b128 v[136:139], %[hp] offset:2368\n" \
    "v_mfma_f32_16x16x32_bf16 v[40:43], v[100:103], v[132:135], v[40:43]\n" \
    "ds_read2_b32 v[98:99], v162 offset0:162 offset1:163\n" \
    "v_mfma_f32_16x16x32_bf16 v[36:39], v[100:103], v[128:131], v[36:39]\n" \
    "v_mfma_f32_16x16x32_bf16 v[32:35], v[100:103], v[124:127], v[32:35]\n" \
    "s_waitcnt lgkmcnt(6)\n" \
    "v_lshlrev_b32 v84, 16, v86\n" \
    "v_mfma_f32_16x16x32_bf16 v[28:31], v[100:103], v[120:123], v[28:31]\n" \
    "ds_read2_b32 v[100:101], v162 offset0:16 offset1:17\n" \
    "v_mfma_f32_16x16x32_bf16 v[44:47], v[104:107], v[132:135], v[44:47]\n" \
    "ds_read_b128 v[132:135], %[hp] offset:1856\n" \
    "v_and_b32 v85, 0xffff0000, v86\n" \
    "v_mfma_f32_16x16x32_bf16 v[40:43], v[104:107], v[128:131], v[40:43]\n" \
    "ds_read2_b32 v[102:103], v162 offset0:18 offset1:19\n" \
    "v_mfma_f32_16x16x32_bf16 v[36:39], v[104:107], v[124:127], v[36:39]\n" \
    "v_lshlrev_b32 v86, 16, v87\n" \
    "v_mfma_f32_16x16x32_bf16 v[32:35], v[104:107], v[120:123], v[32:35]\n" \
    "ds_read2_b32 v[104:105], v161 offset0:160 offset1:161\n" \
    "v_mfma_f32_16x16x32_bf16 v[44:47], v[108:111], v[128:131], v[44:47]\n" \
    "ds_read_b128 v[128:131], %[hp] offset:1344\n" \
    "v_and_b32 v87, 0xffff0000, v87\n" \
    "v_mfma_f32_16x16x32_bf16 v[40:43], v[108:111], v[124:127], v[40:43]\n" \
    "ds_read2_b32 v[106:107], v161 offset0:162 offset1:163\n" \
    "v_mfma_f32_16x16x32_bf16 v[36:39], v[108:111], v[120:123], v[36:39]\n" \
    "ds_read2_b32 v[108:109], v161 offset0:16 offset1:17\n" \
    "v_mfma_f32_16x16x32_bf16 v[44:47], v[112:115], v[124:127], v[44:47]\n" \
    "ds_read2_b32 v[110:111], v161 offset0:18 offset1:19\n" \
    "v_mfma_f32_16x16x32_bf16 v[40:43], v[112:115], v[120:123], v[40:43]\n" \
    "ds_read2_b32 v[112:113], v160 offset0:160 offset1:161\n" \
    "v_mfma_f32_16x16x32_bf16 v[44:47], v[116:119], v[120:123], v[44:47]\n" \
    "ds_read2_b32 v[114:115], v160 offset0:162 offset1:163\n" \
    "v_mfma_f32_16x16x32_bf16 v[44:47], v[88:91], v[148:151], v[44:47]\n" \
    "ds_read2_b32 v[116:117], v160 offset0:16 offset1:17\n" \
    "v_mfma_f32_16x16x32_bf16 v[40:43], v[88:91], v[144:147], v[40:43]\n" \
    "ds_read2_b32 v[118:119], v160 offset0:18 offset1:19\n" \
    "s_waitcnt lgkmcnt(15)\n" \
    "v_mfma_f32_16x16x32_bf16 v[36:39], v[88:91], v[140:143], v[36:39]\n" \
    "ds_read_b128 v[148:151], %[hp] offset:3968\n" \
    "s_waitcnt lgkmcnt(14)\n" \
    "v_mfma_f32_16x16x32_bf16 v[32:35], v[88:91], v[136:139], v[32:35]\n" \
    "ds_read_b128 v[124:127], %[hp] offset:896\n" \
    "s_waitcnt lgkmcnt(12)\n" \
    "v_mfma_f32_16x16x32_bf16 v[28:31], v[88:91], v[132:135], v[28:31]\n" \
    "ds_read_b128 v[120:123], %[hp] offset:384\n" \
    "s_waitcnt lgkmcnt(10)\n" \
    "v_mfma_f32_16x16x32_bf16 v[24:27], v[88:91], v[128:131], v[24:27]\n" \
    "v_fmac_f32 v36, %[dd], v84\n" \
    "v_fmac_f32 v37, %[dd], v85\n" \
    "v_mfma_f32_16x16x32_bf16 v[20:23], v[88:91], v[156:159], v[20:23]\n" \
    "v_fmac_f32 v38, %[dd], v86\n" \
    "v_fmac_f32 v39, %[dd], v87\n" \
    "v_mfma_f32_16x16x32_bf16 v[16:19], v[88:91], v[152:155], v[16:19]\n" \
    "ds_read2_b32 v[88:89], v163 offset0:176 offset1:177\n" \
    "v_mfma_f32_16x16x32_bf16 v[44:47], v[92:95], v[144:147], v[44:47]\n" \
    "ds_read2_b32 v[90:91], v163 offset0:178 offset1:179\n" \
    "v_mfma_f32_16x16x32_bf16 v[40:43], v[92:95], v[140:143], v[40:43]\n" \
    "ds_read_b128 v[144:147], %[hp] offset:3456\n" \
    "v_mfma_f32_16x16x32_bf16 v[36:39], v[92:95], v[136:139], v[36:39]\n" \
    "v_mfma_f32_16x16x32_bf16 v[32:35], v[92:95], v[132:135], v[32:35]\n" \
    "v_mfma_f32_16x16x32_bf16 v[28:31], v[92:95], v[128:131], v[28:31]\n" \
    "ds_read_b64 v[86:87], %[kvr] offset:3072\n" \
    "v_mfma_f32_16x16x32_bf16 v[24:27], v[92:95], v[156:159], v[24:27]\n" \
    "v_mfma_f32_16x16x32_bf16 v[20:23], v[92:95], v[152:155], v[20:23]\n" \
    "ds_read2_b32 v[92:93], v163 offset0:32 offset1:33\n" \
    "v_mfma_f32_16x16x32_bf16 v[44:47], v[96:99], v[140:143], v[44:47]\n" \
    "ds_read_b128 v[140:143], %[hp] offset:2944\n" \
    "v_mfma_f32_16x16x32_bf16 v[40:43], v[96:99], v[136:139], v[40:43]\n" \
    "ds_read2_b32 v[94:95], v163 offset0:34 offset1:35\n" \
    "v_mfma_f32_16x16x32_bf16 v[36:39], v[96:99], v[132:135], v[36:39]\n" \
    "v_mfma_f32_16x16x32_bf16 v[32:35], v[96:99], v[128:131], v[32:35]\n" \
    "v_mfma_f32_16x16x32_bf16 v[28:31], v[96:99], v[156:159], v[28:31]\n" \
    "v_mfma_f32_16x16x32_bf16 v[24:27], v[96:99], v[152:155], v[24:27]\n" \
    "ds_read2_b32 v[96:97], v162 offset0:176 offset1:177\n" \
    "v_mfma_f32_16x16x32_bf16 v[44:47], v[100:103], v[136:139], v[44:47]\n" \
    "ds_read_b128 v[136:139], %[hp] offset:2432\n" \
    "v_mfma_f32_16x16x32_bf16 v[40:43], v[100:103], v[132:135], v[40:43]\n" \
    "ds_read2_b32 v[98:99], v162 offset0:178 offset1:179\n" \
    "v_mfma_f32_16x16x32_bf16 v[36:39], v[100:103], v[128:131], v[36:39]\n" \
    "v_mfma_f32_16x16x32_bf16 v[32:35], v[100:103], v[156:159], v[32:35]\n" \
    "s_waitcnt lgkmcnt(6)\n" \
    "v_lshlrev_b32 v84, 16, v86\n" \
    "v_mfma_f32_16x16x32_bf16 v[28:31], v[100:103], v[152:155], v[28:31]\n" \
    "ds_read2_b32 v[100:101], v162 offset0:32 offset1:33\n" \
    "v_mfma_f32_16x16x32_bf16 v[44:47], v[104:107], v[132:135], v[44:47]\n" \
    "ds_read_b128 v[132:135], %[hp] offset:1920\n" \
    "v_and_b32 v85, 0xffff0000, v86\n" \
    "v_mfma_f32_16x16x32_bf16 v[40:43], v[104:107], v[128:131], v[40:43]\n" \
    "ds_read2_b32 v[102:103], v162 offset0:34 offset1:35\n" \
    "v_mfma_f32_16x16x32_bf16 v[36:39], v[104:107], v[156:159], v[36:39]\n" \
    "v_lshlrev_b32 v86, 16, v87\n" \
    "v_mfma_f32_16x16x32_bf16 v[32:35], v[104:107], v[152:155], v[32:35]\n" \
    "ds_read2_b32 v[104:105], v161 offset0:176 offset1:177\n" \
    "v_mfma_f32_16x16x32_bf16 v[44:47], v[108:111], v[128:131], v[44:47]\n" \
    "ds_read_b128 v[128:131], %[hp] offset:1408\n" \
    "v_and_b32 v87, 0xffff0000, v87\n" \
    "v_mfma_f32_16x16x32_bf16 v[40:43], v[108:111], v[156:159], v[40:43]\n" \
    "ds_read2_b32 v[106:107], v161 offset0:178 offset1:179\n" \
    "v_mfma_f32_16x16x32_bf16 v[36:39], v[108:111], v[152:155], v[36:39]\n" \
    "ds_read2_b32 v[108:109], v161 offset0:32 offset1:33\n" \
    "v_mfma_f32_16x16x32_bf16 v[44:47], v[112:115], v[156:159], v[44:47]\n" \
    "ds_read2_b32 v[110:111], v161 offset0:34 offset1:35\n" \
    "v_mfma_f32_16x16x32_bf16 v[40:43], v[112:115], v[152:155], v[40:43]\n" \
    "ds_read2_b32 v[112:113], v160 offset0:176 offset1:177\n" \
    "v_mfma_f32_16x16x32_bf16 v[44:47], v[116:119], v[152:155], v[44:47]\n" \
    "ds_read2_b32 v[114:115], v160 offset0:178 offset1:179\n" \
    "v_mfma_f32_16x16x32_bf16 v[44:47], v[88:91], v[148:151], v[44:47]\n" \
    "ds_read2_b32 v[116:117], v160 offset0:32 offset1:33\n" \
    "v_mfma_f32_16x16x32_bf16 v[40:43], v[88:91], v[144:147], v[40:43]\n" \
    "ds_read2_b32 v[118:119], v160 offset0:34 offset1:35\n" \
    "s_waitcnt lgkmcnt(15)\n" \
    "v_mfma_f32_16x16x32_bf16 v[36:39], v[88:91], v[140:143], v[36:39]\n" \
    "ds_read_b128 v[152:155], %[hp] offset:448\n" \
    "s_waitcnt lgkmcnt(14)\n" \
    "v_mfma_f32_16x16x32_bf16 v[32:35], v[88:91], v[136:139], v[32:35]\n" \
    "ds_read_b128 v[156:159], %[hp] offset:960\n" \
    "s_waitcnt lgkmcnt(12)\n" \
    "v_mfma_f32_16x16x32_bf16 v[28:31], v[88:91], v[132:135], v[28:31]\n" \
    "ds_read_b128 v[148:151], %[hp] offset:4032\n" \
    "v_fmac_f32 v40, %[dd], v84\n" \
    "v_fmac_f32 v41, %[dd], v85\n" \
    "s_waitcnt lgkmcnt(10)\n" \
    "v_mfma_f32_16x16x32_bf16 v[24:27], v[88:91], v[128:131], v[24:27]\n" \
    "v_fmac_f32 v42, %[dd], v86\n" \
    "v_fmac_f32 v43, %[dd], v87\n" \
    "v_mfma_f32_16x16x32_bf16 v[20:23], v[88:91], v[124:127], v[20:23]\n" \
    "v_mfma_f32_16x16x32_bf16 v[16:19], v[88:91], v[120:123], v[16:19]\n" \
    "ds_read2_b32 v[88:89], v163 offset0:192 offset1:193\n" \
    "v_mfma_f32_16x16x32_bf16 v[44:47], v[92:95], v[144:147], v[44:47]\n" \
    "ds_read2_b32 v[90:91], v163 offset0:194 offset1:195\n" \
    "v_mfma_f32_16x16x32_bf16 v[40:43], v[92:95], v[140:143], v[40:43]\n" \
    "ds_read_b128 v[144:147], %[hp] offset:3520\n" \
    "v_mfma_f32_16x16x32_bf16 v[36:39], v[92:95], v[136:139], v[36:39]\n" \
    "v_mfma_f32_16x16x32_bf16 v[32:35], v[92:95], v[132:135], v[32:35]\n" \
    "v_mfma_f32_16x16x32_bf16 v[28:31], v[92:95], v[128:131], v[28:31]\n" \
    "ds_read_b64 v[86:87], %[kvr] offset:3584\n" \
    "v_mfma_f32_16x16x32_bf16 v[24:27], v[92:95], v[124:127], v[24:27]\n" \
    "v_mfma_f32_16x16x32_bf16 v[20:23], v[92:95], v[120:123], v[20:23]\n" \
    "ds_read2_b32 v[92:93], v163 offset0:48 offset1:49\n" \
    "v_mfma_f32_16x16x32_bf16 v[44:47], v[96:99], v[140:143], v[44:47]\n" \
    "ds_read2_b32 v[94:95], v163 offset0:50 offset1:51\n" \
    "v_mfma_f32_16x16x32_bf16 v[40:43], v[96:99], v[136:139], v[40:43]\n" \
    "ds_read_b128 v[140:143], %[hp] offset:3008\n" \
    "v_mfma_f32_16x16x32_bf16 v[36:39], v[96:99], v[132:135], v[36:39]\n" \
    "v_mfma_f32_16x16x32_bf16 v[32:35], v[96:99], v[128:131], v[32:35]\n" \
    "v_mfma_f32_16x16x32_bf16 v[28:31], v[96:99], v[124:127], v[28:31]\n" \
    "v_mfma_f32_16x16x32_bf16 v[24:27], v[96:99], v[120:123], v[24:27]\n" \
    "ds_read2_b32 v[96:97], v162 offset0:192 offset1:193\n" \
    "v_mfma_f32_16x16x32_bf16 v[44:47], v[100:103], v[136:139], v[44:47]\n" \
    "ds_read2_b32 v[98:99], v162 offset0:194 offset1:195\n" \
    "v_mfma_f32_16x16x32_bf16 v[40:43], v[100:103], v[132:135], v[40:43]\n" \
    "ds_read_b128 v[136:139], %[hp] offset:2496\n" \
    "v_mfma_f32_16x16x32_bf16 v[36:39], v[100:103], v[128:131], v[36:39]\n" \
    "v_mfma_f32_16x16x32_bf16 v[32:35], v[100:103], v[124:127], v[32:35]\n" \
    "s_waitcnt lgkmcnt(6)\n" \
    "v_lshlrev_b32 v84, 16, v86\n" \
    "v_mfma_f32_16x16x32_bf16 v[28:31], v[100:103], v[120:123], v[28:31]\n" \
    "ds_read2_b32 v[100:101], v162 offset0:48 offset1:49\n" \
    "v_mfma_f32_16x16x32_bf16 v[44:47], v[104:107], v[132:135], v[44:47]\n" \
    "ds_read_b128 v[132:135], %[hp] offset:1984\n" \
    "v_and_b32 v85, 0xffff0000, v86\n" \
    "v_mfma_f32_16x16x32_bf16 v[40:43], v[104:107], v[128:131], v[40:43]\n" \
    "ds_read2_b32 v[102:103], v162 offset0:50 offset1:51\n" \
    "v_mfma_f32_16x16x32_bf16 v[36:39], v[104:107], v[124:127], v[36:39]\n" \
    "v_lshlrev_b32 v86, 16, v87\n" \
    "v_mfma_f32_16x16x32_bf16 v[32:35], v[104:107], v[120:123], v[32:35]\n" \
    "ds_read2_b32 v[104:105], v161 offset0:192 offset1:193\n" \
    "v_mfma_f32_16x16x32_bf16 v[44:47], v[108:111], v[128:131], v[44:47]\n" \
    "ds_read_b128 v[128:131], %[hp] offset:1472\n" \
    "v_and_b32 v87, 0xffff0000, v87\n" \
    "v_mfma_f32_16x16x32_bf16 v[40:43], v[108:111], v[124:127], v[40:43]\n" \
    "ds_read2_b32 v[106:107], v161 offset0:194 offset1:195\n" \
    "v_mfma_f32_16x16x32_bf16 v[36:39], v[108:111], v[120:123], v[36:39]\n" \
    "ds_read2_b32 v[108:109], v161 offset0:48 offset1:49\n" \
    "v_mfma_f32_16x16x32_bf16 v[44:47], v[112:115], v[124:127], v[44:47]\n" \
    "ds_read2_b32 v[110:111], v161 offset0:50 offset1:51\n" \
    "v_mfma_f32_16x16x32_bf16 v[40:43], v[112:115], v[120:123], v[40:43]\n" \
    "ds_read2_b32 v[112:113], v160 offset0:192 offset1:193\n" \
    "v_mfma_f32_16x16x32_bf16 v[44:47], v[116:119], v[120:123], v[44:47]\n" \
    "ds_read2_b32 v[114:115], v160 offset0:194 offset1:195\n" \
    "v_mfma_f32_16x16x32_bf16 v[16:19], v[88:91], v[152:155], v[16:19]\n" \
    "ds_read2_b32 v[116:117], v160 offset0:48 offset1:49\n" \
    "v_mfma_f32_16x16x32_bf16 v[20:23], v[88:91], v[156:159], v[20:23]\n" \
    "ds_read2_b32 v[118:119], v160 offset0:50 offset1:51\n" \
    "s_waitcnt lgkmcnt(15)\n" \
    "v_mfma_f32_16x16x32_bf16 v[20:23], v[92:95], v[152:155], v[20:23]\n" \
    "ds_read_b32 v50, %[ctl] offset:52\n" \
    "s_waitcnt lgkmcnt(8)\n" \
    "v_mfma_f32_16x16x32_bf16 v[24:27], v[88:91], v[128:131], v[24:27]\n" \
    "v_fmac_f32 v44, %[dd], v84\n" \
    "v_fmac_f32 v45, %[dd], v85\n" \
    "v_mfma_f32_16x16x32_bf16 v[24:27], v[92:95], v[156:159], v[24:27]\n" \
    "v_fmac_f32 v46, %[dd], v86\n" \
    "v_fmac_f32 v47, %[dd], v87\n" \
    "v_mfma_f32_16x16x32_bf16 v[24:27], v[96:99], v[152:155], v[24:27]\n" \
    "v_mfma_f32_16x16x32_bf16 v[28:31], v[88:91], v[132:135], v[28:31]\n" \
    "v_cvt_pk_bf16_f32 v52, v16, v17\n" \
    "v_cvt_pk_bf16_f32 v54, v20, v21\n" \
    "v_mfma_f32_16x16x32_bf16 v[28:31], v[92:95], v[128:131], v[28:31]\n" \
    "v_cvt_pk_bf16_f32 v53, v18, v19\n" \
    "v_cvt_pk_bf16_f32 v55, v22, v23\n" \
    "v_mfma_f32_16x16x32_bf16 v[28:31], v[96:99], v[156:159], v[28:31]\n" \
    "v_min_u32 v52, -2, v52\n" \
    "v_min_u32 v54, -2, v54\n" \
    "v_mfma_f32_16x16x32_bf16 v[28:31], v[100:103], v[152:155], v[28:31]\n" \
    "v_min_u32 v53, -2, v53\n" \
    "v_min_u32 v55, -2, v55\n" \
    "v_mfma_f32_16x16x32_bf16 v[32:35], v[88:91], v[136:139], v[32:35]\n" \
    "v_permlane16_swap_b32 v52, v54\n" \
    "v_add_u32 v68, 0, %[yo]\n" \
    "v_mfma_f32_16x16x32_bf16 v[32:35], v[92:95], v[132:135], v[32:35]\n" \
    "v_permlane16_swap_b32 v53, v55\n" \
    "v_mfma_f32_16x16x32_bf16 v[32:35], v[96:99], v[128:131], v[32:35]\n" \
    "s_waitcnt lgkmcnt(0)\n" \
    "v_readfirstlane_b32 %[tmp], v50\n" \
    "s_mov_b32 %[cnt], 0\n" \
    "s_cmp_lg_u32 %[tmp], 0\n" \
    "s_cbranch_scc1 Lok_%=\n" \
    "Ls2_%=:\n" \
    "ds_read_b32 v50, %[ctl] offset:52\n" \
    "s_add_u32 %[cnt], %[cnt], 1\n" \
    "s_waitcnt lgkmcnt(0)\n" \
    "v_readfirstlane_b32 %[tmp], v50\n" \
    "s_cmp_ge_u32 %[cnt], %[cap]\n" \
    "s_cbranch_scc1 Lok_%=\n" \
    "s_cmp_eq_u32 %[tmp], 0\n" \
    "s_cbranch_scc1 Ls2_%=\n" \
    "Lok_%=:\n" \
    "global_store_dwordx4 v68, v[52:55], %[wsp] sc1\n" \
    "v_mfma_f32_16x16x32_bf16 v[32:35], v[100:103], v[156:159], v[32:35]\n" \
    "v_cvt_pk_bf16_f32 v56, v24, v25\n" \
    "v_cvt_pk_bf16_f32 v58, v28, v29\n" \
    "v_mfma_f32_16x16x32_bf16 v[32:35], v[104:107], v[152:155], v[32:35]\n" \
    "v_cvt_pk_bf16_f32 v57, v26, v27\n" \
    "v_cvt_pk_bf16_f32 v59, v30, v31\n" \
    "v_mfma_f32_16x16x32_bf16 v[36:39], v[88:91], v[140:143], v[36:39]\n" \
    "v_min_u32 v56, -2, v56\n" \
    "v_min_u32 v58, -2, v58\n" \
    "v_mfma_f32_16x16x32_bf16 v[36:39], v[92:95], v[136:139], v[36:39]\n" \
    "v_min_u32 v57, -2, v57\n" \
    "v_min_u32 v59, -2, v59\n" \
    "v_mfma_f32_16x16x32_bf16 v[36:39], v[96:99], v[132:135], v[36:39]\n" \
    "v_permlane16_swap_b32 v56, v58\n" \
    "v_add_u32 v69, 655360, %[yo]\n" \
    "v_mfma_f32_16x16x32_bf16 v[36:39], v[100:103], v[128:131], v[36:39]\n" \
    "v_permlane16_swap_b32 v57, v59\n" \
    "v_mfma_f32_16x16x32_bf16 v[36:39], v[104:107], v[156:159], v[36:39]\n" \
    "global_store_dwordx4 v69, v[56:59], %[wsp] sc1\n" \
    "v_mfma_f32_16x16x32_bf16 v[36:39], v[108:111], v[152:155], v[36:39]\n" \
    "v_mfma_f32_16x16x32_bf16 v[40:43], v[88:91], v[144:147], v[40:43]\n" \
    "v_mfma_f32_16x16x32_bf16 v[40:43], v[92:95], v[140:143], v[40:43]\n" \
    "v_mfma_f32_16x16x32_bf16 v[40:43], v[96:99], v[136:139], v[40:43]\n" \
    "v_mfma_f32_16x16x32_bf16 v[40:43], v[100:103], v[132:135], v[40:43]\n" \
    "v_cvt_pk_bf16_f32 v60, v32, v33\n" \
    "v_cvt_pk_bf16_f32 v62, v36, v37\n" \
    "v_mfma_f32_16x16x32_bf16 v[40:43], v[104:107], v[128:131], v[40:43]\n" \
    "v_cvt_pk_bf16_f32 v61, v34, v35\n" \
    "v_cvt_pk_bf16_f32 v63, v38, v39\n" \
    "v_mfma_f32_16x16x32_bf16 v[40:43], v[108:111], v[156:159], v[40:43]\n" \
    "v_min_u32 v60, -2, v60\n" \
    "v_min_u32 v62, -2, v62\n" \
    "v_mfma_f32_16x16x32_bf16 v[40:43], v[112:115], v[152:155], v[40:43]\n" \
    "v_min_u32 v61, -2, v61\n" \
    "v_min_u32 v63, -2, v63\n" \
    "v_mfma_f32_16x16x32_bf16 v[44:47], v[88:91], v[148:151], v[44:47]\n" \
    "v_permlane16_swap_b32 v60, v62\n" \
    "v_add_u32 v70, 1310720, %[yo]\n" \
    "v_mfma_f32_16x16x32_bf16 v[44:47], v[92:95], v[144:147], v[44:47]\n" \
    "v_permlane16_swap_b32 v61, v63\n" \
    "v_mfma_f32_16x16x32_bf16 v[44:47], v[96:99], v[140:143], v[44:47]\n" \
    "global_store_dwordx4 v70, v[60:63], %[wsp] sc1\n" \
    "v_mfma_f32_16x16x32_bf16 v[44:47], v[100:103], v[136:139], v[44:47]\n" \
    "v_mfma_f32_16x16x32_bf16 v[44:47], v[104:107], v[132:135], v[44:47]\n" \
    "v_mfma_f32_16x16x32_bf16 v[44:47], v[108:111], v[128:131], v[44:47]\n" \
    "v_mfma_f32_16x16x32_bf16 v[44:47], v[112:115], v[156:159], v[44:47]\n" \
    "v_mfma_f32_16x16x32_bf16 v[44:47], v[116:119], v[152:155], v[44:47]\n" \
    "s_nop 7\n" \
    "v_cvt_pk_bf16_f32 v64, v40, v41\n" \
    "v_cvt_pk_bf16_f32 v66, v44, v45\n" \
    "v_cvt_pk_bf16_f32 v65, v42, v43\n" \
    "v_cvt_pk_bf16_f32 v67, v46, v47\n" \
    "v_min_u32 v64, -2, v64\n" \
    "v_min_u32 v66, -2, v66\n" \
    "v_min_u32 v65, -2, v65\n" \
    "v_min_u32 v67, -2, v67\n" \
    "v_permlane16_swap_b32 v64, v66\n" \
    "v_add_u32 v71, 1966080, %[yo]\n" \
    "v_permlane16_swap_b32 v65, v67\n" \
    "global_store_dwordx4 v71, v[64:67], %[wsp] sc1\n" \
    "s_nop 1\n"
#define CONV_CLOB "v16", "v17", "v18", "v19", "v20", "v21", "v22", "v23", "v24", "v25", "v26", "v27", "v28", "v29", "v30", "v31", "v32", "v33", "v34", "v35", "v36", "v37", "v38", "v39", "v40", "v41", "v42", "v43", "v44", "v45", "v46", "v47", "v50", "v51", "v52", "v53", "v54", "v55", "v56", "v57", "v58", "v59", "v60", "v61", "v62", "v63", "v64", "v65", "v66", "v67", "v68", "v69", "v70", "v71", "v72", "v73", "v74", "v75", "v76", "v77", "v78", "v79", "v80", "v81", "v82", "v83", "v84", "v85", "v86", "v87", "v88", "v89", "v90", "v91", "v92", "v93", "v94", "v95", "v96", "v97", "v98", "v99", "v100", "v101", "v102", "v103", "v104", "v105", "v106", "v107", "v108", "v109", "v110", "v111", "v112", "v113", "v114", "v115", "v116", "v117", "v118", "v119", "v120", "v121", "v122", "v123", "v124", "v125", "v126", "v127", "v128", "v129", "v130", "v131", "v132", "v133", "v134", "v135", "v136", "v137", "v138", "v139", "v140", "v141", "v142", "v143", "v144", "v145", "v146", "v147", "v148", "v149", "v150", "v151", "v152", "v153", "v154", "v155", "v156", "v157", "v158", "v159", "v160", "v161", "v162", "v163", "scc", "memory"

static_assert(C_HR - L_CTRL == 144 && C_RED - L_CTRL == 84 && C_XD - L_CTRL == 80 && C_SENT - L_CTRL == 52, "offsets in the conv stream");
typedef __attribute__((ext_vector_type(16))) unsigned u32x16;
typedef __attribute__((address_space(3))) const unsigned lds_cu32;
typedef __attribute__((address_space(3))) u32x2 lds_u32x2;
typedef __attribute__((address_space(3))) u32x4 lds_u32x4;
typedef __attribute__((address_space(3))) f32x4 lds_f32x4;
#define GPTR(T, lo, hi) ((T*)(__attribute__((address_space(1))) T*)(((u64)(hi) << 32) | (u64)(lo)))
#if defined(__HIP_DEVICE_COMPILE__)
#define NOPK __attribute__((target("no-packed-fp32-ops")))
#else
#define NOPK
#endif
__global__ NOPK __launch_bounds__(768, 3) void fused_kernel(const u16* x_, const u16* kin_, const u16* vin_, const u16* shift_k_,
                                                       const u16* ssm_k_, const u16* ssm_D_, const u16* wqkv_, const u16* wo_,
                                                       const u16* gw_, char* ws_, u16* out_) {
    u32x16 ka0;
    u32x4 ka1;
    u32x2 ka2;
    asm volatile("s_load_dwordx16 %0, %3, 0\n\ts_load_dwordx4 %1, %3, 64\n\ts_load_dwordx2 %2, %3, 80"
                 : "=&s"(ka0), "=&s"(ka1), "=&s"(ka2) : "s"(__builtin_amdgcn_kernarg_segment_ptr()));
#define x GPTR(const u16, ka0[0], ka0[1])
#define kin GPTR(const u16, ka0[2], ka0[3])
#define vin GPTR(const u16, ka0[4], ka0[5])
#define shift_k GPTR(const u16, ka0[6], ka0[7])
#define ssm_k GPTR(const u16, ka0[8], ka0[9])
#define ssm_D GPTR(const u16, ka0[10], ka0[11])
#define wqkv GPTR(const u16, ka0[12], ka0[13])
#define wo GPTR(const u16, ka0[14], ka0[15])
#define gw GPTR(const u16, ka1[0], ka1[1])
#define ws GPTR(char, ka1[2], ka1[3])
#define out GPTR(u16, ka2[0], ka2[1])
#define wsr __builtin_amdgcn_make_buffer_rsrc(ws, 0, 0x4000000, 0x00020000)
    __shared__ __attribute__((aligned(16))) char smem[LDS_TOTAL];
    const int bid = __builtin_amdgcn_workgroup_id_x();
    const int tid = __builtin_amdgcn_workitem_id_x();
    const int lane = tid & 63;
    const int wave = __builtin_amdgcn_readfirstlane(tid >> 6);
    const int xcd = bid & 7, r5 = bid >> 3, r = r5 & 15;
    const bool isG = r5 >= 16;
    const int hg = (xcd & 1) * 4 + (r & 3);
    const int rb2 = (xcd >> 1) * 4 + (r >> 2);
    const int cq = (xcd & 1) * 2 + (r & 1), rbv = (xcd >> 1) * 8 + (r >> 1);
    const int vid = cq * 32 + rbv;
    const int sid = rbv * 4 + cq;
    const int rbo = xcd * 4 + (r >> 3) * 2 + ((r5 >> 4) ^ GH ^ 1), oc = r5 & 7;
    const int w = wave & 3;
    const int wr = wave < 4 ? w ^ WR_X : w;
    const int rto = (rbo >> 1) * 8 + (wr >> 1) * 4 + (rbo & 1) * 2 + (wr & 1);
    const int rtq = (rbo >> 1) * 8 + (w >> 1) * 4 + (rbo & 1) * 2 + (w & 1);
    const int g = lane >> 4, n16 = lane & 15, q4 = n16 >> 2, p4 = lane & 3;
    const unsigned lb = (unsigned)(size_t)(lds_char*)smem;
    u64 tag;
    {
        u64 id = llvm_dispatch_id();
        u64 dp = (u64)(size_t)__builtin_amdgcn_dispatch_ptr();
        tag = ((u64)((unsigned)id ^ 0x5bd1e995u) << 32) | (u64)((unsigned)(dp >> 6) ^ (unsigned)(id >> 32) ^ 0x9e3779b9u);
    }
    const unsigned tlo = (unsigned)tag, thi = (unsigned)(tag >> 32);
    if (wave == 0) {
        u64 pc;
        asm volatile("s_getpc_b64 %0" : "=s"(pc));
        pc &= ~255ull;
        const unsigned cl = lane * 32 + r5;
        if (cl < CODE_LINES) GLT(cl * 128, pc);
    }
    const u32x4 ones = {0xffffffffu, 0xffffffffu, 0xffffffffu, 0xffffffffu};

#define WO_OFF(c) ({ const int rho_ = (c) / 10, ch_ = (c) - rho_ * 10; (unsigned)((rho_ ^ ((rho_ & 8) >> 1)) * (D_DIM * 2) + oc * 160 + ch_ * 16); })
    const int kr = KROT;
#define T_C(i) __builtin_amdgcn_readfirstlane((kr + (i)) >= 10 ? kr + (i) - 10 : kr + (i))
#define T_WV() ({ const int j_ = min(n16, 14); (unsigned)((r * 4 + g) * (NQKV * 2) + j_ * 128 + ((j_ >= 5) + (j_ >= 10) + (xcd & 1)) * 640); })
#define T_XV() ((unsigned)(((xcd >> 1) * 512 + r * 32 + (lane & 31)) * (D_DIM * 2)))
#define T_W(wv, i) GLT(wv, (const char*)wqkv + (size_t)T_C(i) * (64 * NQKV * 2))
#define T_X(xv, i) { const int c0_ = T_C(i), c1_ = T_C((i) + 1); GLT((xv) + (lane < 32 ? c0_ : c1_) * 128, x); }
#define T_PAIR(i) { T_W(wv, i); T_X(xv_, i); T_W(wv, (i) + 1); }

    unsigned e0;
    if (isG) {
        const unsigned t0_ = T_WV() + (kr + (w >> 1)) * (64 * NQKV * 2);
        const unsigned t1_ = ((xcd >> 1) * 512 + (w == 1 ? r * 32 + (lane & 31) : r - ((xcd >> 1) ? 16 : 0))) * (D_DIM * 2) + (w == 1 ? kr + (lane >> 5) : min(lane, 9)) * 128;
        e0 = (w & 1) ? t1_ : t0_;
    } else {
        const unsigned c = (wave >= 5 ? wave - 5 : wave) * 64 + lane, row = (c * 3277) >> 16;
        e0 = rbv * (64 * D_DIM * 2) + cq * 320 + row * (D_DIM * 2 - 320) + c * 16;
    }
    asm volatile("" : "+v"(e0));
    asm volatile("s_waitcnt lgkmcnt(0)" : "+s"(ka0), "+s"(ka1), "+s"(ka2) :: "memory");
    if (isG) {
        if ((wave >> 2) == 1) GLT(e0, (w & 1) ? x : wqkv);
        BARRIER();
    }
    u32x2 skw;
    if (!isG && wave < 10) {
        const int tens = wave >= 5, i5 = wave - 5 * tens;
        const unsigned lw = lb + (tens ? V_VST : V_KST) + i5 * 1024;
        const char* kb = (const char*)(tens ? vin : kin);
        __builtin_amdgcn_s_sleep(V_DELAY);
        GLS(e0, kb, lw, 0);
        GLS(e0, kb + 16 * D_DIM * 2, lw, 5152);
        GLS(e0, kb + 32 * D_DIM * 2, lw, 2 * 5152);
        GLS(e0, kb + 48 * D_DIM * 2, lw, 3 * 5152);
        asm volatile("global_load_dwordx2 %0, %1, %2" : "=v"(skw) : "v"((cq * 160 + wave * 16 + n16) * 8), "s"(shift_k) : "memory");
        if (wave == 0) { if (lane < 60) GLS(e0, kb - (rbv ? 3 * D_DIM * 2 : 0), lb, V_KST - 32 - 960); }
    }
    if (wave == 11) {
        if (lane < 16) *(u32x4*)(smem + L_CTRL + lane * 16) = (u32x4){0u, 0u, 0u, 0u};
        asm volatile("s_waitcnt lgkmcnt(0)" ::: "memory");
    }
    STAMP(1);
#define Y_OFF (WS_Y + rbo * (D_DIM * 128) + oc * 10240 + w * 1024)
#define Y_FIRST() u32x4 yv[3]; float g1; { yv[0] = __builtin_amdgcn_raw_buffer_load_b128(wsr, lane * 16, Y_OFF, 16); yv[1] = __builtin_amdgcn_raw_buffer_load_b128(wsr, lane * 16, Y_OFF + 4096, 16); \
        yv[2] = __builtin_amdgcn_raw_buffer_load_b128(wsr, lane * 16, Y_OFF + (w < 2 ? 8192 : 0), 16); \
        g1 = __uint_as_float(__builtin_amdgcn_raw_buffer_load_b32(wsr, (lane & 15) * 4, WS_G + rto * 64, 16)); }
#define RELANE() const int lane_ = opaque(lane); const int g = lane_ >> 4, n16 = lane_ & 15, q4 = n16 >> 2, p4 = lane_ & 3; \
        const unsigned qa = lb + (isG ? G_Q : V_Q) + w * 2560 + n16 * 160 + 8 * g
#define SENT4(v) __any(max(max((v)[0], (v)[1]), max((v)[2], (v)[3])) == 0xffffffffu)
#define Q_READ(base) f32x4 gq[5]; { const int l_ = opaque(lane); _Pragma("unroll") for (int ct = 0; ct < 5; ++ct) { \
            const u32x2 q2 = *(const lds_u32x2*)(size_t)(lb + (base) + w * 2560 + (l_ & 15) * 160 + 8 * (l_ >> 4) + ct * 32); \
            gq[ct] = (f32x4){lo_f(q2[0]), hi_f(q2[0]), lo_f(q2[1]), hi_f(q2[1])}; } \
        asm volatile("" : "+v"(gq[0]), "+v"(gq[1]), "+v"(gq[2]), "+v"(gq[3]), "+v"(gq[4]) :: "memory"); }
#define P_STAGE() { \
        RELANE(); \
        const unsigned l_y = lb + (isG ? G_Y : V_Y); \
        const unsigned yw = l_y + (w * 8 + (lane >> 3)) * Y_PITCH + (lane & 7) * 16; \
        const unsigned yr = l_y + (4 * g + q4) * Y_PITCH + w * 32 + p4 * 8; \
_Pragma("nounroll") \
        for (int it = 0; it < SPIN_CAP; ++it) { \
            const bool b0 = SENT4(yv[0]), b1 = SENT4(yv[1]), b2 = SENT4(yv[2]); \
            if (!(b0 | b1 | b2)) break; \
            if (it || Y_SL0) __builtin_amdgcn_s_sleep(Y_SLEEP); \
            asm volatile("" ::: "memory"); \
            if (b0) yv[0] = __builtin_amdgcn_raw_buffer_load_b128(wsr, lane * 16, Y_OFF, 16); \
            if (b1) yv[1] = __builtin_amdgcn_raw_buffer_load_b128(wsr, lane * 16, Y_OFF + 4096, 16); \
            if (b2) yv[2] = __builtin_amdgcn_raw_buffer_load_b128(wsr, lane * 16, Y_OFF + (w < 2 ? 8192 : 0), 16); \
        } \
        STAMP(8); \
        *(lds_u32x4*)(size_t)yw = yv[0]; \
        *(lds_u32x4*)(size_t)(yw + 32 * Y_PITCH) = yv[1]; \
        if (w < 2) *(lds_u32x4*)(size_t)(yw + 64 * Y_PITCH) = yv[2]; \
        lds_add(lb + C_Y, lane); \
_Pragma("unroll") \
        for (int ct = 0; ct < 5; ++ct) gq[ct] *= g1; \
        asm volatile("" : "+v"(gq[0]), "+v"(gq[1]), "+v"(gq[2]), "+v"(gq[3]), "+v"(gq[4])); \
        spin_ge(lb + C_Y, 4); \
        u32x2 y2[5]; \
_Pragma("unroll") \
        for (int ct = 0; ct < 5; ++ct) y2[ct] = __builtin_bit_cast(u32x2, TRRD((lds_char*)(size_t)(yr + ct * 16 * Y_PITCH))); \
_Pragma("unroll") \
        for (int ct = 0; ct < 5; ++ct) \
            *(lds_u32x2*)(size_t)(qa + ct * 32) = (u32x2){pack2(gq[ct][0] * lo_f(y2[ct][0]), gq[ct][1] * hi_f(y2[ct][0])), pack2(gq[ct][2] * lo_f(y2[ct][1]), gq[ct][3] * hi_f(y2[ct][1]))}; \
        lds_add(lb + C_XCH + w * 4, lane); \
        STAMP(9); \
        FLUSH(); \
        return; }

    if (isG) {
      if (wave >= 4) {
        const int half = (wave >> 2) - 1;
        if (half) __builtin_amdgcn_s_setprio(C1_PRIO); else __builtin_amdgcn_s_setprio(C0_PRIO);
        const int rb = rb2 * 2 + half;
        f32x4 acc[4][5];
        unsigned aofs[2];
#pragma unroll
        for (int sub = 0; sub < 2; ++sub) aofs[sub] = lb + L_A + half * 8192 + n16 * 128 + (((sub * 4 + g) ^ ((n16 >> 1) & 7)) * 16);
        unsigned wofs[4];
#pragma unroll
        for (int i = 0; i < 4; ++i) {
            const int part = i < 3 ? i : (p4 < 3 ? p4 : 2);
            const int Q = i < 3 ? 5 * w + (w & 1) + p4 : 5 * w + ((w & 1) ? 0 : 4);
            const int c2 = (Q >> 1) - 2 * (hg & 1);
            const int cc = ((unsigned)c2 < 8u ? part * 8 + c2 : 24 + part * 2 + (c2 & 1)) ^ (2 * ((g & 1) * 4 + q4));
            wofs[i] = lb + L_W + (8 * g + q4) * W_PITCH + cc * 16 + (Q & 1) * 8;
        }
        bf16x8 af[5], bA[4], bB[4];
        lds_char* wp[2][4];
        lds_char* ap[2];
#pragma unroll
        for (int i = 0; i < 4; ++i) { wp[0][i] = (lds_char*)(size_t)opaque(wofs[i]); wp[1][i] = (lds_char*)(size_t)opaque(wofs[i] + 3 * W_SLOT); }
        ap[0] = (lds_char*)(size_t)opaque(aofs[0]);
        ap[1] = (lds_char*)(size_t)opaque(aofs[1]);
#define WSL(J, i) (wp[((J) % NW) / 3][i] + (((J) % NW) % 3) * W_SLOT)
#define MFMA16A(c, a, b) asm volatile("v_mfma_f32_16x16x32_bf16 %0, %1, %2, %0" : "+v"(c) : "v"(a), "v"(b))
#define MFMA16Z(c, a, b) asm volatile("v_mfma_f32_16x16x32_bf16 %0, %1, %2, 0" : "=&v"(c) : "v"(a), "v"(b))
#define MFMA16(c, a, b) do { if (first_) MFMA16Z(c, a, b); else MFMA16A(c, a, b); } while (0)
#define BORD(rt) (((rt) + 1) & 3)
#define GST(J)
#define STEP(J, bc, bn, R0) { \
            const bool first_ = (J) == 0; \
            _Pragma("unroll") for (int rt = R0; rt < 5; ++rt) { \
                s16x4 b0_, b1_; \
                if (rt > 0) MFMA16(acc[0][rt], bc[0], af[rt]); SB(); \
                if ((J) < 19 && rt < 4 + R0) { b0_ = TRRD(WSL((J) + 1, BORD(rt - R0))); SB(); } \
                MFMA16(acc[1][rt], bc[1], af[rt]); SB(); \
                if ((J) < 19 && rt < 4 + R0) { b1_ = TRRD(WSL((J) + 1, BORD(rt - R0)) + 4 * W_PITCH); bn[BORD(rt - R0)] = cat8(b0_, b1_); SB(); } \
                MFMA16(acc[2][rt], af[rt], bc[2]); SB(); \
                MFMA16(acc[3][rt], bc[3], af[rt]); SB(); \
                if ((J) < 19) { af[rt] = *(lds_cbf16x8*)(ap[((J) + 1) & 1] + ((((J) + 1) >> 1) % 3) * A_SLOT + rt * 2048); SB(); } \
            } \
            if ((J) >= (JL < 16 ? JL - 1 : 16) && (J) < 19) lds_st32(lb + C_FREE + (wave - 4) * 4, (J));        \
            if ((J) <= JL) BARRIER(); \
            GST(J); \
            SB(); }
#define GEMM(R0) { \
        _Pragma("unroll") for (int i = 0; i < 4; ++i) bA[i] = cat8(TRRD(wp[0][i]), TRRD(wp[0][i] + 4 * W_PITCH)); \
        _Pragma("unroll") for (int rt = R0; rt < 5; ++rt) af[rt] = *(lds_cbf16x8*)(ap[0] + rt * 2048); \
        SB(); \
        STEP(0, bA, bB, R0) STEP(1, bB, bA, R0) STEP(2, bA, bB, R0) STEP(3, bB, bA, R0) STEP(4, bA, bB, R0) STEP(5, bB, bA, R0) \
        STEP(6, bA, bB, R0) STEP(7, bB, bA, R0) STEP(8, bA, bB, R0) STEP(9, bB, bA, R0) STAMP(14); STEP(10, bA, bB, R0) STEP(11, bB, bA, R0) \
        STEP(12, bA, bB, R0) STEP(13, bB, bA, R0) STEP(14, bA, bB, R0) STEP(15, bB, bA, R0) STEP(16, bA, bB, R0) STEP(17, bB, bA, R0) \
        STEP(18, bA, bB, R0) STEP(19, bB, bA, R0) }
        const int qi = n16, par = lane & 1;
        f32x4 cm0, cm1;
        u32x4 psel, vxa;
#pragma unroll
        for (int e = 0; e < 4; ++e) {
            const bool own = 4 * g + e <= qi, pm = (e & 1) == par;
            cm0[e] = pm && !own ? 0.f : -3.0e38f;
            cm1[e] = pm && own ? 0.f : -3.0e38f;
            const bool ow = 4 * g + par + 2 * (e & 1) <= qi;
            const unsigned by = (e & 1) ? 0x0302u : 0x0100u;
            psel[e] = (ow == (e >= 2)) ? (par ? (by << 16) | 0x0c0cu : 0x0c0c0000u | by) : 0x0c0c0c0cu;
            vxa[e] = p4 == e ? lb + G_VX + w * 1152 + (half * 4 + e) * 128 + (4 * g + q4) * 8 : lb + L_CTRL;
        }
        unsigned scw = g == 2 ? lb + G_VX + w * 1152 + half * 512 + qi * 8 : lb + G_VXD;
        const int cc = (hg * 80 + w * 20 + (w & 1) * 4) / 8 + (g >> 1), colx = hg * 80 + w * 20 + ((w & 1) ? 0 : 16);
        unsigned oo = WS_O + ((rb * 4 + (g & 1)) * 20 + (cc >> 2)) * 1024 + ((cc & 3) * 16 + qi) * 16;
        unsigned ox = (g * 20 + (colx >> 5)) * 1024 + (((colx >> 3) & 3) * 16 + qi) * 16 + (colx & 4) * 2;
        asm volatile("" : "+v"(cm0), "+v"(cm1), "+v"(psel), "+v"(vxa), "+v"(scw), "+v"(oo), "+v"(ox));
        BARRIER();
        STAMP(7);
        CSTAMP(0);
        if (half == 0) GEMM(0) else GEMM(1)
        asm volatile("s_nop 7\n\ts_nop 7" ::: "memory");
        STAMP(2);
        CSTAMP(1);

        if (half) __builtin_amdgcn_s_setprio(T_PRIO); else __builtin_amdgcn_s_setprio(T0_PRIO);
        typedef __attribute__((address_space(3))) volatile unsigned lds_vu32;
        const unsigned sent0 = *(lds_vu32*)(size_t)(lb + C_SENT);
        const unsigned hx0 = half ? *(lds_vu32*)(size_t)(lb + C_HX + w * 4) : 1u;
#define HINT() { if (wave == 11 - 4 * GH) { if (lane == 0) __hip_atomic_store((u64*)(ws + WS_HF + (rb2 * 8 + hg) * 128), tag, __ATOMIC_RELAXED, __HIP_MEMORY_SCOPE_AGENT); } }
        if (HINT_AT < 0) HINT();
        u32x4 kop[5], qop[5];
        u32x16 vv;
        const unsigned hx = lb + G_HX + w * 1152;
#pragma unroll
        for (int rt = 4; rt >= 0; --rt) {
            if (rt == 0 && half) break;
            const unsigned d0 = pack2(acc[3][rt][0], acc[3][rt][1]), d1 = pack2(acc[3][rt][2], acc[3][rt][3]);
            asm volatile("ds_write2_b32 %0, %1, %2 offset0:%3 offset1:%4" ::"v"(scw), "v"(d0), "v"(d1), "n"(rt * 32), "n"(rt * 32 + 1) : "memory");
            const auto sw2 = __builtin_amdgcn_permlane16_swap(d0, d1, false, false);
            kop[rt][0] = pack2(acc[1][rt][0], acc[1][rt][1]);
            kop[rt][1] = pack2(acc[1][rt][2], acc[1][rt][3]);
            kop[rt][2] = sw2[1];
            kop[rt][3] = 0u;
            vv[2 * rt] = pack2(acc[2][rt][0], acc[2][rt][1]);
            vv[2 * rt + 1] = pack2(acc[2][rt][2], acc[2][rt][3]);
            if (rt == 4 && half == 0) {
                asm volatile("ds_write2_b64 %0, %1, %2 offset1:1" ::"v"(hx + lane * 16), "v"((u32x2){kop[4][0], kop[4][1]}), "v"((u32x2){vv[8], vv[9]}) : "memory");
                asm volatile("ds_write_b32 %0, %1" ::"v"(g < 2 ? hx + 1024 + lane * 4 : lb + L_TOUCH), "v"(kop[4][2]) : "memory");
                lds_st32(lb + C_HX + w * 4, 1);
            }
            if (rt > 0) {
                qop[rt][0] = pack2(acc[0][rt][0], acc[0][rt][1]);
                qop[rt][1] = pack2(acc[0][rt][2], acc[0][rt][3]);
                qop[rt][2] = g < 2 ? sw2[0] : 0u;
                qop[rt][3] = 0u;
            }
        }
        if (half == 1) {
            if (!__builtin_amdgcn_readfirstlane(hx0)) spin_ge(lb + C_HX + w * 4, 1);
            const u32x4 a = lds_v128(hx + lane * 16);
            const unsigned b = lds_v32(hx + 1024 + (lane & 31) * 4);
            kop[0] = (u32x4){a[0], a[1], b, 0u};
            vv[0] = a[2];
            vv[1] = a[3];
        }
        bf16x8 vx[5];
#pragma unroll
        for (int rt = 4; rt >= 1; --rt) vx[rt] = cat8(TRRD((lds_char*)(size_t)vxa[rt - 1]), TRRD((lds_char*)(size_t)(vxa[rt - 1] + 128)));
        STAMP(3);
        if (HINT_AT == 0) HINT();
        const float scale = 0.22360679774997896f * 1.4426950408889634f;
        const f32x4 z4 = {0.f, 0.f, 0.f, 0.f};
        float l0[5], l1[5], mx[5];
#pragma unroll
        for (int rt = 4; rt >= 1; --rt) {
            const f32x4 s0 = __builtin_amdgcn_mfma_f32_16x16x32_bf16(__builtin_bit_cast(bf16x8, kop[rt - 1]), __builtin_bit_cast(bf16x8, qop[rt]), cm0, 0, 0, 0);
            const f32x4 s1 = __builtin_amdgcn_mfma_f32_16x16x32_bf16(__builtin_bit_cast(bf16x8, kop[rt]), __builtin_bit_cast(bf16x8, qop[rt]), cm1, 0, 0, 0);
            if (rt == 1 && rb == 0) {
                l0[rt] = fmax_raw(s1[0], s1[1]);
                l1[rt] = fmax_raw(s1[2], s1[3]);
            } else {
                l0[rt] = fmax_raw(fmax3_raw(s0[0], s0[1], s1[0]), s1[1]);
                l1[rt] = fmax_raw(fmax3_raw(s0[2], s0[3], s1[2]), s1[3]);
            }
            mx[rt] = fmax_raw(l0[rt], l1[rt]);
        }
#define FU(v) __float_as_uint(v)
#define UF(v) __uint_as_float(v)
#define RED4(dst, src, OP) { \
            const auto ra_ = __builtin_amdgcn_permlane16_swap(FU(src[1]), FU(src[2]), false, false); \
            const auto rb_ = __builtin_amdgcn_permlane16_swap(FU(src[3]), FU(src[4]), false, false); \
            const auto rc_ = __builtin_amdgcn_permlane32_swap(FU(OP(UF(ra_[0]), UF(ra_[1]))), FU(OP(UF(rb_[0]), UF(rb_[1]))), false, false); \
            dst = OP(UF(rc_[0]), UF(rc_[1])); }
#define BC4(dst, src) { \
            const auto ra_ = __builtin_amdgcn_permlane32_swap(FU(src), FU(src), false, false); \
            const auto rb_ = __builtin_amdgcn_permlane16_swap(ra_[0], ra_[0], false, false); \
            const auto rc_ = __builtin_amdgcn_permlane16_swap(ra_[1], ra_[1], false, false); \
            dst[1] = UF(rb_[0]); dst[2] = UF(rb_[1]); dst[3] = UF(rc_[0]); dst[4] = UF(rc_[1]); }
#define FADD(a, b) ((a) + (b))
        if (HINT_AT == 1) HINT();
        STAMP(5);
        float red;
        RED4(red, mx, fmax_raw);
        red *= scale;
        BC4(mx, red);
        f32x2 pp[5];
        float sm[5];
#pragma unroll
        for (int rt = 4; rt >= 1; --rt) {
            pp[rt] = (f32x2){__builtin_amdgcn_exp2f(__builtin_fmaf(l0[rt], scale, -mx[rt])), __builtin_amdgcn_exp2f(__builtin_fmaf(l1[rt], scale, -mx[rt]))};
            sm[rt] = pp[rt][0] + pp[rt][1];
        }
        RED4(red, sm, FADD);
        red = __builtin_amdgcn_rcpf(red);
        BC4(sm, red);
        if (HINT_AT == 2) HINT();
        STAMP(6);
        f32x4 o1[5], o2 = z4;
#pragma unroll
        for (int rt = 4; rt >= 1; --rt) {
            const unsigned pk = pack2(pp[rt][0] * sm[rt], pp[rt][1] * sm[rt]);
            const u32x4 pop = {__builtin_amdgcn_perm(pk, pk, psel[0]), __builtin_amdgcn_perm(pk, pk, psel[1]), __builtin_amdgcn_perm(pk, pk, psel[2]), __builtin_amdgcn_perm(pk, pk, psel[3])};
            const u32x4 vop = {vv[2 * rt - 2], vv[2 * rt - 1], vv[2 * rt], vv[2 * rt + 1]};
            o1[rt] = __builtin_amdgcn_mfma_f32_16x16x32_bf16(__builtin_bit_cast(bf16x8, vop), __builtin_bit_cast(bf16x8, pop), z4, 0, 0, 0);
            o2 = __builtin_amdgcn_mfma_f32_16x16x32_bf16(vx[rt], __builtin_bit_cast(bf16x8, pop), o2, 0, 0, 0);
        }
        if (!__builtin_amdgcn_readfirstlane(sent0)) spin_ge(lb + C_SENT, 1);
#pragma unroll
        for (int rt = 3; rt >= 1; rt -= 2) {
            const auto r0 = __builtin_amdgcn_permlane16_swap(min(pack2(o1[rt][0], o1[rt][1]), 0xfffffffeu), min(pack2(o1[rt + 1][0], o1[rt + 1][1]), 0xfffffffeu), false, false);
            const auto r1 = __builtin_amdgcn_permlane16_swap(min(pack2(o1[rt][2], o1[rt][3]), 0xfffffffeu), min(pack2(o1[rt + 1][2], o1[rt + 1][3]), 0xfffffffeu), false, false);
            __builtin_amdgcn_raw_buffer_store_b128((u32x4){r0[0], r1[0], r0[1], r1[1]}, wsr, oo + (rt - 1) * 20480, 0, 16);
        }
        __builtin_amdgcn_raw_buffer_store_b64((u32x2){min(pack2(o2[0], o2[1]), 0xfffffffeu), min(pack2(o2[2], o2[3]), 0xfffffffeu)}, wsr, ox, WS_O + rb * 4 * 20480, 16);
        if (HINT_AT == 3) HINT();
        STAMP(12);
        if (half) {
            if (GY_DELAY > 0) { lds_add(lb + C_OST1, lane); __builtin_amdgcn_s_sleep(GY_DELAY); }
            Y_FIRST();
            if (GY_DELAY == 0) lds_add(lb + C_OST1, lane);
            STAMP(4);
            spin_ge(lb + C_WO, 4);
            spin_all(lb + C_FREE + (lane & 7) * 4, 18);
            Q_READ(G_Q);
            P_STAGE();
        }
        lds_add(lb + C_OST, lane);
        STAMP(4);
      } else {
        __builtin_amdgcn_s_setprio(L_PRIO);
        unsigned ev[9];
        {
            const int hi = lane >> 5, pos = lane & 31;
#pragma unroll
            for (int v = 0; v < 2; ++v) {
                const int row = 2 * w + hi + 8 * v;
                int L = pos ^ (2 * (v * 4 + (row & 3)));
                L = L > 29 ? L - 2 : L;
                const int part = L < 24 ? L >> 3 : (L - 24) >> 1;
                const int chunk = L < 24 ? (L & 7) + 2 * (hg & 1) : (L & 1) + ((hg & 1) ? 0 : 8);
                ev[v] = row * (NQKV * 2) + chunk * 16 + part * 1280 + hg * 160;
                ev[v + 2] = ev[v] + 16 * NQKV * 2;
            }
            const int ch = (lane & 7) ^ ((4 * (w & 1) + g) & 7);
#pragma unroll
            for (int j = 0; j < 5; ++j) {
                int t = rb2 * 128 - 16 + 8 * w + (lane >> 3) + 32 * j;
                t = t < 0 ? 0 : t;
                t = t > S_LEN - 1 ? S_LEN - 1 : t;
                ev[4 + j] = t * (D_DIM * 2) + ch * 16;
            }
        }
        unsigned wf[2][3], qf[3];
#pragma unroll
        for (int v = 0; v < 2; ++v)
#pragma unroll
            for (int j = 0; j < 3; ++j) wf[v][j] = opaque(WO_OFF((((w + 2 * v) & 3) + 4 * j) * 64 + lane));
#pragma unroll
        for (int j = 0; j < 3; ++j) { const int c = min(j * 64 + lane, 159); const int row = c / 10, ch = c - row * 10; qf[j] = opaque(row * (D_DIM * 2) + ch * 16); }
        const unsigned sfo = opaque(WS_SF + (lane < 8 ? rb2 * 8 + lane : (rbo >> 1) * 8 + (lane & 7)) * 128);
        const unsigned fra = opaque(lb + C_FREE + (lane & 7) * 4);
        SB();
        STAMP(5);
        if (w < 2) ring_run<5, -1>(ev, x, wqkv, kr, lb + w * 1024); else ring_run<4, -1>(ev, x, wqkv, kr, lb + w * 1024);
        STAMP(2);
#define WO_PART(chunk, pos, v) { const char* wb_ = (const char*)wo + (size_t)(chunk) * (64 * D_DIM * 2); const unsigned sl_ = lb + ((w + 2 * (v)) & 3) * 1024; \
            GLS(wf[v][0], wb_, sl_, (pos) * 10240); GLS(wf[v][1], wb_, sl_, (pos) * 10240 + 4096); if (((w + 2 * (v)) & 3) < 2) GLS(wf[v][2], wb_, sl_, (pos) * 10240 + 8192); }
        if (JL == 14) spin_all(fra, 14);
        WO_PART(0, 9, 0);
        WO_PART(5, 2, 1);
        if (JL >= 16) {
            if (JL == 16) WAIT_VM(5); else WAIT_VM(9);
            BARRIER();
        } else {
            spin_all(fra, 15);
        }
        WO_PART(1, 10, 0);
        WO_PART(2, 11, 1);
        if (JL == 17) { WAIT_VM(10); BARRIER(); }
        __builtin_amdgcn_s_setprio(WO_PRIO);
        u32x2 fv;
        if (w == 3) asm volatile("global_load_dwordx2 %0, %1, %2 sc1" : "=v"(fv) : "v"(sfo), "s"(ws) : "memory");
        spin_all(fra, 16);
        WO_PART(6, 3, 0);
        WO_PART(3, 12, 1);
        if (w == 3) {
            asm volatile("s_waitcnt vmcnt(5)" : "+v"(fv) :: "memory");
            if (__all(fv[0] == tlo && fv[1] == thi)) lds_st32(lb + C_SENT, 1);
        }
        WO_PART(7, 4, 0);
        WO_PART(4, 13, 1);
        spin_all(fra, 17);
        WO_PART(8, 5, 0);
        WO_PART(9, 6, 1);
#pragma unroll
        for (int j = 0; j < 3; ++j)
            if (j < 2 || lane < 32) GLS(qf[j], (const char*)x + (size_t)(rtq * 16) * (D_DIM * 2) + oc * 160, lb + w * 2560, G_Q + j * 1024);
        STAMP(3);
        if (w == 3 && !__all(fv[0] == tlo && fv[1] == thi)) {
            poll_flags(wsr, sfo, tlo, thi);
            lds_st32(lb + C_SENT, 1);
        }
        WAIT_VM(0);
        lds_add(lb + C_WO, lane);
        STAMP(4);
      }
    } else {
        __builtin_amdgcn_s_setprio(3);
        const int c5 = wave < 4 ? wave : 4;
        const int d = vid * 5 + c5;
        unsigned Dw = *(const __attribute__((address_space(4))) unsigned*)(size_t)(ssm_D + (d & ~1));
        unsigned hp0 = *(const __attribute__((address_space(4))) unsigned*)(size_t)(ssm_k + (vid * 5 + 4) * S_LEN + w * 512 - 2);
        BARRIER();
        if (wave <= 4) hp0 = 0;
        asm volatile("" : "+s"(Dw), "+s"(hp0));
#define SF_DONE() do { WAIT_VM(0); if (lds_add_rtn(lb + C_SFC, lane) == 5) { \
            if (lane == 0) __hip_atomic_store((u64*)(ws + WS_SF + sid * 128), tag, __ATOMIC_RELAXED, __HIP_MEMORY_SCOPE_AGENT); lds_add(lb + C_SFC, lane); } } while (0)
#define WO_CHUNK(chunk, lds) { const char* wb_ = (const char*)wo + (size_t)(chunk) * (64 * D_DIM * 2); \
            _Pragma("unroll") for (int p = 0; p < 10; ++p) GLS(wof[p], wb_, lds, p * 1024); }
#define KV_DONE() do { if (lds_add_rtn(lb + C_KVD, lane) == 9) { \
            if (lane == 0) __hip_atomic_store((u64*)(ws + WS_XF + vid * 128), tag, __ATOMIC_RELAXED, __HIP_MEMORY_SCOPE_AGENT); lds_add(lb + C_KVD, lane); } } while (0)
        if (wave < 10) {
            const int m0 = (g & 1) * 2 + (g >> 1);
            const unsigned kp = opaque(lb + V_KST + (8 * m0 + q4) * 320 + (m0 >> 1) * 32 + wave * 32 + p4 * 8);
            const unsigned km = opaque(kp - 1280 - ((m0 & 1) ? 0 : 32));
            const unsigned sw_ = opaque(lb + V_STG + wave * 2048 + n16 * 128 + ((m0 + n16) & 7) * 16);
            const unsigned sr_ = opaque(lb + V_STG + wave * 2048 + (lane >> 3) * 128 + ((lane + (lane >> 3)) & 7) * 16);
            const unsigned e1 = opaque(WS_KV + (cq * 160 + wave * 16 + (lane >> 3)) * KV_PITCH + rbv * 128 + (lane & 7) * 16), e2 = opaque(e1 + 8 * KV_PITCH);
            asm volatile("s_waitcnt vmcnt(0)" : "+v"(skw) :: "memory");
            lds_add(lb + C_KV, lane);
            const f32x2 s0 = {lo_f(skw[0]), lo_f(skw[0])}, s1 = {hi_f(skw[0]), hi_f(skw[0])}, s2 = {lo_f(skw[1]), lo_f(skw[1])}, s3 = {hi_f(skw[1]), hi_f(skw[1])};
            spin_ge(lb + C_KV, 10);
            STAMP(2);
            u32x2 kq[2][3], vq[2][2];
#pragma unroll
            for (int i = 0; i < 2; ++i) {
                kq[i][0] = __builtin_bit_cast(u32x2, TRRD((lds_char*)(size_t)(km + i * 10304)));
#pragma unroll
                for (int e = 0; e < 2; ++e) kq[i][e + 1] = __builtin_bit_cast(u32x2, TRRD((lds_char*)(size_t)(kp + i * 10304 + e * 1280)));
#pragma unroll
                for (int e = 0; e < 2; ++e) vq[i][e] = __builtin_bit_cast(u32x2, TRRD((lds_char*)(size_t)(kp + i * 10304 + (V_VST - V_KST) + e * 1280)));
            }
            if (rbv == 0 && m0 == 0) kq[0][0] = (u32x2){0u, 0u};
            f32x2 kf[12], res[8];
#pragma unroll
            for (int e = 1; e < 12; ++e) {
                const unsigned u0 = kq[0][e >> 2][(e >> 1) & 1], u1 = kq[1][e >> 2][(e >> 1) & 1];
                kf[e] = (e & 1) ? (f32x2){hi_f(u0), hi_f(u1)} : (f32x2){lo_f(u0), lo_f(u1)};
            }
#pragma unroll
            for (int e = 0; e < 8; ++e) {
                const unsigned u0 = vq[0][e >> 2][(e >> 1) & 1], u1 = vq[1][e >> 2][(e >> 1) & 1];
                f32x2 a = kf[e + 1] * s3;
                a = kf[e + 2] * s2 + a;
                a = kf[e + 3] * s1 + a;
                a = kf[e + 4] * s0 + a;
                res[e] = a * ((e & 1) ? (f32x2){hi_f(u0), hi_f(u1)} : (f32x2){lo_f(u0), lo_f(u1)});
            }
#pragma unroll
            for (int i = 0; i < 2; ++i) {
                u32x4 pk;
#pragma unroll
                for (int e = 0; e < 4; ++e) pk[e] = pack2(res[2 * e][i], res[2 * e + 1][i]);
                *(lds_vu32x4*)(size_t)(sw_ ^ (i * 64)) = pk;
            }
            const u32x4 pk0 = lds_v128(sr_), pk1 = lds_v128(sr_ + 1024);
            __builtin_amdgcn_raw_buffer_store_b128(pk0, wsr, e1, 0, 16);
            __builtin_amdgcn_raw_buffer_store_b128(pk1, wsr, e2, 0, 16);
            lds_add(lb + C_KVST, lane);
            STAMP(3);
        }
        if (wave >= 8) {
            const unsigned wv = T_WV(), xv_ = T_XV();
            if (wave == 10) {
                T_PAIR(2);
                T_PAIR(4);
                if (WO_MALL && lane < 50) GLT((unsigned)((vid * 50 + lane) * 128), wo);
            } else if (wave == 11) {
                if (lane < 56) {
#pragma unroll
                    for (int c5 = 0; c5 < 5; ++c5)
                        *(lds_u32x4*)(size_t)(lb + (lane < 16 ? V_KV(c5) + lane * 16 : V_F + c5 * XC_BYTES + 15 * XC_ROW + 256 + (lane < 36 ? XC_COPY + (lane - 16) * 16 : XC_B + 4 + (lane - 36) * 16))) = (u32x4){0u, 0u, 0u, 0u};
                }
                const unsigned x4o = opaque(WS_KV + d * KV_PITCH + lane * 16);
                spin_ge(lb + C_KVD, 11);
                if (POLL_DELAY > 0) __builtin_amdgcn_s_sleep(POLL_DELAY);
                const unsigned xfo = WS_XF + (cq * 32 + (lane & 31)) * 128;
                const u32x2 fv = __builtin_amdgcn_raw_buffer_load_b64(wsr, xfo, 0, 16);
                if (!__all(fv[0] == tlo && fv[1] == thi)) {
                    lds_st32(lb + C_GO, 1);
                    poll_flags(wsr, xfo, tlo, thi);
                }
                asm volatile("s_mov_b32 m0, %2\n\ts_nop 0\n\tglobal_load_lds_dwordx4 %0, %1 sc1\n\tglobal_load_lds_dwordx4 %0, %1 offset:1024 sc1\n\t"
                             "global_load_lds_dwordx4 %0, %1 offset:2048 sc1\n\tglobal_load_lds_dwordx4 %0, %1 offset:3072 sc1" ::"v"(x4o), "s"(ws), "s"(lb + V_KV4 + 256) : "memory");
                lds_add(lb + C_KVR, lane);
                STAMP(2);
                WAIT_VM(0);
                lds_st32(lb + C_HR, 4);
                STAMP(3);
                spin_ge(lb + C_XD, 4);
                lds_st32(lb + C_GO, 1);
            } else {
                WAIT_VM(0);
                KV_DONE();
                STAMP(11);
                spin_ge(lb + SENT_W, SENT_N);
#pragma unroll
                for (int i = 0; i < NSENT; ++i) {
                    if (i >= SENT_PACE) waitvm<SENT_PACE - 1>();
                    __builtin_amdgcn_raw_buffer_store_b128(ones, wsr, (wave == 8 ? WS_O : WS_Y) + sid * 20480 + lane * 16 + i * 1024, 0, 16);
                }
                STAMP(4);
                SF_DONE();
                STAMP(12);
            }
            unsigned wof[10];
#pragma unroll
            for (int p = 0; p < 10; ++p) wof[p] = opaque(WO_OFF(p * 64 + lane));
            if (wave == 10) {
                spin_ge(lb + GO_W, GO_N);
                T_PAIR(6);
                if (T4_EARLY) WAIT_VM(1); else spin_ge(lb + C_XD, 4);
                T_PAIR(8);
                spin_ge(lb + C_XD, 4);
#pragma unroll
                for (int i = 0; i < 7; ++i) {
                    waitvm<WOT_PACE>();
                    if (i < 6 || lane < 16) GLT((unsigned)(lane * 128), (const char*)wo + (r * 400 + i * 64) * 128);
                }
                WAIT_VM(0);
                lds_st32(lb + C_WT, 1);
                STAMP(4);
            }
            {
                unsigned qf[3];
#pragma unroll
                for (int j = 0; j < 3; ++j) { const int c = min(j * 64 + lane, 159); const int row = c / 10, ch = c - row * 10; qf[j] = opaque(row * (D_DIM * 2) + ch * 16); }
                spin_mid(lb + C_XD, 4);
#pragma unroll
                for (int j = 0; j < 3; ++j)
                    if (j < 2 || lane < 32) GLS(qf[j], (const char*)x + (size_t)(rtq * 16) * (D_DIM * 2) + oc * 160, lb + w * 2560, V_QS + j * 1024);
                WAIT_VM(0);
            }
            Q_READ(V_QS);
            lds_add(lb + C_QR, lane);
            spin_mid(lb + C_WT, 1);
            STAMP(5);
            WO_CHUNK(2 + w, lb + V_WO + (2 + w) * 10240);
            if (wave == 11) {
                spin_mid(lb + C_SFC, 7);
                poll_flags(wsr, WS_SF + (lane < 32 ? lane * 4 + cq : (rbo >> 1) * 8 + (lane & 7)) * 128, tlo, thi);
                lds_st32(lb + C_SENT, 1);
                STAMP(10);
            } else if (w < 2) {
                spin_mid(lb + (w ? C_QR : C_RED2), 4);
                WO_CHUNK(w, lb + V_WO + w * 10240);
            }
            WAIT_VM(0);
            lds_add(lb + C_WO, lane);
            STAMP(6);
            spin_mid(lb + C_CONVN, 8);
            if (VY_DELAY > 0) { lds_add(lb + C_OST, lane); __builtin_amdgcn_s_sleep(VY_DELAY); }
            Y_FIRST();
            if (VY_DELAY == 0) lds_add(lb + C_OST, lane);
            STAMP(7);
            P_STAGE();
        } else {
            const unsigned xb = lb + V_F + c5 * XC_BYTES;
            const unsigned hb = lb + V_KV(c5) + 256;
            const float Dd = (d & 1) ? hi_f(Dw) : lo_f(Dw);
            u32x4 hv[4];
            const unsigned l16 = lane * 16;
            {
                const char* hsrc = (const char*)ssm_k + (size_t)d * (S_LEN * 2) + (wave < 4 ? 0 : w * 1024);
                spin_ge(lb + C_KVST, 10);
#define H_LD(i) asm volatile("global_load_dwordx4 %0, %1, %2 offset:%3" : "=v"(hv[i]) : "v"(l16), "s"(hsrc), "n"((i) * 1024) : "memory")
                H_LD(0);
                if (wave < 4) { H_LD(1); H_LD(2); H_LD(3); WAIT_VM(4); } else WAIT_VM(1);
                KV_DONE();
                STAMP(4);
            }
            {
                const unsigned da = xb + (3 - g) * XC_ROW + (15 - n16) * 16 + (wave < 4 ? 0 : -w * (4 * XC_ROW));
                u32x4 ra[4], rb[4];
                asm volatile("s_waitcnt vmcnt(0)" : "+v"(hv[0]), "+v"(hv[1]), "+v"(hv[2]), "+v"(hv[3]) :: "memory");
#define H_PREV(i) __builtin_amdgcn_update_dpp((i) == 0 ? hp0 : __builtin_amdgcn_readlane(hv[(i) == 0 ? 0 : (i) - 1][3], 63), hv[i][3], 0x138, 0xf, 0xf, false)
#define BFI16(lo, hi) (((lo) & 0xffffu) | ((hi) & 0xffff0000u))
#define H_PREP(i) { ra[i] = (u32x4){__builtin_amdgcn_alignbit(hv[i][3], hv[i][3], 16), __builtin_amdgcn_alignbit(hv[i][2], hv[i][2], 16), __builtin_amdgcn_alignbit(hv[i][1], hv[i][1], 16), __builtin_amdgcn_alignbit(hv[i][0], hv[i][0], 16)}; \
                    rb[i] = (u32x4){BFI16(hv[i][3], hv[i][2]), BFI16(hv[i][2], hv[i][1]), BFI16(hv[i][1], hv[i][0]), BFI16(hv[i][0], H_PREV(i))}; \
                    *(lds_u32x4*)(size_t)(da + (3 - (i)) * (4 * XC_ROW) + XC_COPY) = ra[i]; *(lds_u32x4*)(size_t)(da + (3 - (i)) * (4 * XC_ROW) + XC_B + 4) = rb[i]; }
#define H_DUP(i) { *(lds_u32x4*)(size_t)(da + (3 - (i)) * (4 * XC_ROW) + XC_COPY - 32) = ra[i]; *(lds_u32x4*)(size_t)(da + (3 - (i)) * (4 * XC_ROW) + XC_B + 4 - 32) = rb[i]; }
                H_PREP(0)
                if (wave < 4) {
                    H_PREP(1) H_PREP(2) H_PREP(3)
                    if (n16 == 15) { H_DUP(0) H_DUP(1) H_DUP(2) if (g < 3) H_DUP(3) }
                } else {
                    if (n16 == 15 && (w < 3 || g < 3)) H_DUP(0)
                }
            }
            STAMP(9);
            const int j0 = wave >= 4 ? 2 * w : 0;
            const int mm = (n16 & 3) | ((n16 & 4) << 1) | ((n16 & 8) >> 1);
            const unsigned kp = hb + 2 * (256 * j0 + 128 * (g >> 1) + 8 * mm + 4 * (g & 1));
            const int ts = 256 * (j0 + (g & 1)) + 128 * (g >> 1) + 8 * mm;
            const unsigned yo = WS_Y + d * 128 + ((ts >> 7) * 2 + ((ts >> 5) & 1)) * (D_DIM * 128) + (((ts >> 6) & 1) * 32 + (ts & 31)) * 2;
            if (wave < 4) {
                const int b1 = n16 >> 3, phi = n16 & 7;
                const unsigned xp = xb + ((phi & 1) ? XC_COPY - 4 : XC_B) + (1 - b1) * XC_ROW + 2 * (8 - 2 * (phi >> 1) + 8 * g);
                const unsigned hp = hb + 2 * (8 * (mm + g) - 120);
                const int sx4 = (4 - w) * XC_BYTES + (w >> 1) * XC_ROW + (w & 1) * 128, sh4 = V_KV4 - w * KV_BYTES + w * 128;
                unsigned c0_, c1_;
                __builtin_amdgcn_s_setprio(CONV_PRIO);
                spin_ge(lb + C_KVR, 5);
                STAMP(6);
                CSTAMP(0);
                asm volatile(CONV_ASM
                             : [cnt] "=&s"(c0_), [tmp] "=&s"(c1_)
                             : [xp] "v"(xp), [hp] "v"(hp), [sx4] "s"(sx4), [sh4] "s"(sh4), [kvl] "s"(hb), [kvr] "v"(kp), [scr] "v"(lb + V_SCR + w * 8192 + l16),
                               [ctl] "v"(lb + L_CTRL), [dd] "s"(Dd), [cap] "s"(SPIN_CAP), [xo] "v"(WS_KV + d * KV_PITCH + l16), [wsp] "s"(ws), [yo] "v"(yo)
                             : CONV_CLOB);
                __builtin_amdgcn_s_setprio(3);
                CSTAMP(1);
            } else {
                lds_add(lb + C_KVR, lane);
                {
                    const int grow = (xcd >> 1) * 512 + r * 32 + (xcd & 1) * 16 + w * 4 + g;
                    const unsigned go = opaque(grow * (D_DIM * 2) + n16 * 16);
                    if (GATE_LATE) spin_mid(lb + C_XD, 4);
                    u32x4 gx[5], gv[5];
#pragma unroll
                    for (int i = 0; i < 5; ++i) {
                        gx[i] = *(const u32x4*)((const char*)x + go + i * 256);
                        gv[i] = *(const u32x4*)((const char*)gw + n16 * 16 + i * 256);
                    }
                    float sa[4] = {0.f, 0.f, 0.f, 0.f};
#pragma unroll
                    for (int i = 0; i < 5; ++i)
#pragma unroll
                        for (int e = 0; e < 4; ++e) sa[e] = __builtin_fmaf(hi_f(gx[i][e]), hi_f(gv[i][e]), __builtin_fmaf(lo_f(gx[i][e]), lo_f(gv[i][e]), sa[e]));
                    float sacc = (sa[0] + sa[1]) + (sa[2] + sa[3]);
                    sacc += __builtin_amdgcn_update_dpp(0.f, sacc, 0x128, 0xf, 0xf, true);
                    sacc += __builtin_amdgcn_update_dpp(0.f, sacc, 0x124, 0xf, 0xf, true);
                    sacc += __builtin_amdgcn_update_dpp(0.f, sacc, 0x122, 0xf, 0xf, true);
                    sacc += __builtin_amdgcn_update_dpp(0.f, sacc, 0x121, 0xf, 0xf, true);
                    const float gres = rbf(__builtin_amdgcn_rcpf(1.f + __expf(-rbf(sacc))));
                    if (n16 == 0) __builtin_amdgcn_raw_buffer_store_b32(__float_as_uint(gres), wsr, WS_G + grow * 4, 0, 16);
                    SF_DONE();
                    STAMP(6);
                }
                unsigned wof[10];
#pragma unroll
                for (int p = 0; p < 10; ++p) wof[p] = opaque(WO_OFF(p * 64 + lane));
                spin_mid(lb + C_RED, 4);
                STAMP(5);
                f32x4 acc[2];
#pragma unroll
                for (int e = 0; e < 2; ++e) {
                    const u32x2 kk = *(const lds_u32x2*)(size_t)(kp + e * 512);
                    acc[e] = (f32x4){lo_f(kk[0]) * Dd, hi_f(kk[0]) * Dd, lo_f(kk[1]) * Dd, hi_f(kk[1]) * Dd};
#pragma unroll
                    for (int o = 0; o < 4; ++o) acc[e] += *(const lds_f32x4*)(size_t)(lb + V_SCR + o * 8192 + (2 * w + e) * 1024 + l16);
                }
                lds_add(lb + C_RED2, lane);
                const auto r0 = __builtin_amdgcn_permlane16_swap(min(pack2(acc[0][0], acc[0][1]), 0xfffffffeu), min(pack2(acc[1][0], acc[1][1]), 0xfffffffeu), false, false);
                const auto r1 = __builtin_amdgcn_permlane16_swap(min(pack2(acc[0][2], acc[0][3]), 0xfffffffeu), min(pack2(acc[1][2], acc[1][3]), 0xfffffffeu), false, false);
                STAMP(7);
                spin_ge(lb + C_RED2, 4);
                spin_mid(lb + C_WT, 1);
                WO_CHUNK(6 + w, lb + V_WO + (6 + w) * 10240);
                WAIT_VM(0);
                lds_add(lb + C_WO, lane);
                spin_mid(lb + C_SENT, 1);
                __builtin_amdgcn_raw_buffer_store_b128((u32x4){r0[0], r1[0], r0[1], r1[1]}, wsr, yo, 0, 16);
            }
            lds_add(lb + C_CONVN, lane);
            STAMP(12);
        }
    }

    __builtin_amdgcn_s_setprio(T_PRIO);
    {
        RELANE();
        const int kh = wave < 4;
        const int ofo = WS_O + (rto * 20 + kh * 10) * 1024 + lane * 16;
        const int hsw = (g & 1) * 4;
        const unsigned lofs = lb + (8 * g + hsw + q4) * 160 + p4 * 8;
        lds_char* pc = (lds_char*)(size_t)(lofs + (isG ? (kh ? 2 : 9) * 10240 : V_WO + kh * 51200));
        lds_char* pd = pc + (4 - 2 * hsw) * 160;
        const int rot = O_ROT;
#define KS(ks) __builtin_amdgcn_readfirstlane((ks) + rot >= 10 ? (ks) + rot - 10 : (ks) + rot)
#define WO_RD1(dst, ks, ct) dst = cat8(TRRD(pc + KS(ks) * 5120 + (ct) * 32), TRRD(pd + KS(ks) * 5120 + (ct) * 32))
        bf16x8 wB[NBUF][5];
        spin_mid(lb + C_WO, isG ? 4 : 8);
#pragma unroll
        for (int ks = 0; ks < NBUF; ++ks)
#pragma unroll
            for (int ct = 0; ct < 5; ++ct) WO_RD1(wB[ks][ct], ks, ct);
        spin_ge(lb + C_SENT, 1);
        {
            const int hh = wr >> 1;
            if (isG && (xcd & 1) == kh && G_OWN) {
                spin_ge(lb + C_OST + hh * (C_OST1 - C_OST), 4);
                if (O_DELAY > 0) __builtin_amdgcn_s_sleep(O_DELAY);
            } else if ((w & 1) == 0) {
                if (isG) spin_ge(lb + C_OST + hh * (C_OST1 - C_OST), G_WST); else spin_ge(lb + C_OST, hh ? V_WST : V_WST0);
                if (isG && GW_DELAY > 0) __builtin_amdgcn_s_sleep(GW_DELAY);
                const int pr = kh * 4 + (lane & 3);
                const unsigned wo_ = WS_O + (rto * 20 + ((pr * 80) >> 5)) * 1024 + (((pr * 10) & 3) * 16) * 16;
#pragma nounroll
                for (int it = 0; it < SPIN_CAP; ++it) {
                    asm volatile("" ::: "memory");
                    const unsigned v = __builtin_amdgcn_raw_buffer_load_b32(wsr, wo_, 0, 16);
                    if (!__any(v == 0xffffffffu)) break;
                    __builtin_amdgcn_s_sleep(W_SLEEP);
                }
                lds_st32(lb + C_PRE + kh * 8 + hh * 4, 1);
            } else {
                spin_ge(lb + C_PRE + kh * 8 + hh * 4, 1);
            }
        }
        __builtin_amdgcn_s_setprio(PASS_PRIO);
        STAMP(8);
        u32x4 of[10];
#pragma unroll
        for (int ks = 0; ks < O_AHEAD; ++ks) of[ks] = __builtin_amdgcn_raw_buffer_load_b128(wsr, ofo, KS(ks) * 1024, 16);
        SB();
        STAMP(9);
        f32x4 acc[5];
        float g0;
        const f32x4 z4 = {0.f, 0.f, 0.f, 0.f};
#define WO_PASS(first) { SB(); \
            _Pragma("unroll") for (int ks = 0; ks < 10; ++ks) \
                _Pragma("unroll") for (int ct = 0; ct < 5; ++ct) { \
                    acc[ct] = __builtin_amdgcn_mfma_f32_16x16x32_bf16(wB[ks % NBUF][ct], __builtin_bit_cast(bf16x8, of[ks]), ks ? acc[ct] : z4, 0, 0, 0); SB(); \
                    if (first && ct == 0 && ks + O_AHEAD < 10) { of[ks + O_AHEAD] = __builtin_amdgcn_raw_buffer_load_b128(wsr, ofo, KS(ks + O_AHEAD) * 1024, 16); SB(); } \
                    if (first && ct == 0 && ks + O_AHEAD == 10) { g0 = __uint_as_float(__builtin_amdgcn_raw_buffer_load_b32(wsr, WS_G + (rto * 16 + n16) * 4, 0, 16)); SB(); } \
                    if (ks + NBUF < 10) { WO_RD1(wB[ks % NBUF][ct], ks + NBUF, ct); SB(); } \
                } }
        WO_PASS(true);
        g0 = 1.f - g0;
        STAMP(10);
        if (__any(acc[0][0] != acc[0][0])) {
#pragma unroll
            for (int ks = 0; ks < NBUF; ++ks)
#pragma unroll
                for (int ct = 0; ct < 5; ++ct) WO_RD1(wB[ks][ct], ks, ct);
            SB();
#pragma nounroll
            for (int it = 0; it < SPIN_CAP; ++it) {
                bool any = false;
#pragma unroll
                for (int ks = 0; ks < 10; ++ks)
                    if (SENT4(of[ks])) {
                        asm volatile("global_load_dwordx4 %0, %1, %2 sc1" : "+v"(of[ks]) : "v"(ofo), "s"(ws + KS(ks) * 1024) : "memory");
                        any = true;
                    }
                if (!any) break;
                asm volatile("s_waitcnt vmcnt(0)" : "+v"(of[0]), "+v"(of[1]), "+v"(of[2]), "+v"(of[3]), "+v"(of[4]), "+v"(of[5]), "+v"(of[6]), "+v"(of[7]), "+v"(of[8]), "+v"(of[9]) :: "memory");
            }
            WO_PASS(false);
        }
        STAMP(11);
        const unsigned xa = lb + (isG ? G_XCH : V_XCH) + wr * 5120 + lane * 16;
        if (kh) {
            spin_all(isG ? lb + C_FREE + (lane & 7) * 4 : lb + C_CONVN, isG ? 18 : 8);
#pragma unroll
            for (int ct = 0; ct < 5; ++ct) *(lds_f32x4*)(size_t)(xa + ct * 1024) = acc[ct];
            lds_add(lb + C_XCH + wr * 4, lane);
        } else {
            spin_ge(lb + C_XCH + w * 4, 2);
            STAMP(13);
            unsigned pk[5][2];
#pragma unroll
            for (int ct = 0; ct < 5; ++ct) {
                const u32x2 p2 = *(const lds_u32x2*)(size_t)(qa + ct * 32);
                const f32x4 fin = (f32x4){lo_f(p2[0]), hi_f(p2[0]), lo_f(p2[1]), hi_f(p2[1])} + g0 * (acc[ct] + *(const lds_f32x4*)(size_t)(xa + ct * 1024));
                pk[ct][0] = pack2(fin[0], fin[1]);
                pk[ct][1] = pack2(fin[2], fin[3]);
            }
            const auto outr = __builtin_amdgcn_make_buffer_rsrc(out, 0, S_LEN * D_DIM * 2, 0x00020000);
            const unsigned ob = ((rto * 16 + n16) * D_DIM + oc * 80) * 2;
#pragma unroll
            for (int p = 0; p < 2; ++p) {
                const auto r0 = __builtin_amdgcn_permlane16_swap(pk[2 * p][0], pk[2 * p + 1][0], false, false);
                const auto r1 = __builtin_amdgcn_permlane16_swap(pk[2 * p][1], pk[2 * p + 1][1], false, false);
                __builtin_amdgcn_raw_buffer_store_b128((u32x4){r0[0], r1[0], r0[1], r1[1]}, outr, ob + ((2 * p + (g & 1)) * 16 + (g >> 1) * 8) * 2, 0, OUT_AUX);
            }
            __builtin_amdgcn_raw_buffer_store_b64((u32x2){pk[4][0], pk[4][1]}, outr, ob + (64 + 4 * g) * 2, 0, OUT_AUX);
        }
        FLUSH();
    }
}

#undef x
#undef kin
#undef vin
#undef shift_k
#undef ssm_k
#undef ssm_D
#undef wqkv
#undef wo
#undef gw
#undef ws
#undef out
#undef wsr
extern "C" void kernel_launch(void* const* d_in, const int* in_sizes, int n_in, void* d_out, int out_size,
                              void* d_ws, size_t ws_size, hipStream_t stream) {
    const u16* q = (const u16*)d_in[0];
    const u16* k = (const u16*)d_in[1];
    const u16* v = (const u16*)d_in[2];
    const u16* shift_k = (const u16*)d_in[3];
    const u16* ssm_k = (const u16*)d_in[4];
    const u16* ssm_D = (const u16*)d_in[5];
    const u16* wqkv = (const u16*)d_in[7];
    const u16* wo = (const u16*)d_in[8];
    const u16* gw = (const u16*)d_in[9];
    fused_kernel<<<256, 768, 0, stream>>>(q, k, v, shift_k, ssm_k, ssm_D, wqkv, wo, gw, (char*)d_ws, (u16*)d_out);
}
```

```cpp
#define V_DELAY 6
#define MAP1 1
#define HINT_AT 9
#define VY_DELAY 16
#define GY_DELAY 12
#define G_OWN 0
#define OUT_AUX 2
#define L_DELAY 0
#define O_ROT 0
#define WOT_PACE 6
#define SF_EARLY 2
#define CODE_LINES 391
#include <hip/hip_runtime.h>
#include <hip/hip_bf16.h>
#include <stdint.h>

#define S_LEN 2048
#define D_DIM 640
#define NQKV 1920

typedef unsigned short u16;
typedef unsigned long long u64;
typedef __attribute__((ext_vector_type(8))) short bf16x8;
typedef __attribute__((ext_vector_type(4))) short s16x4;
typedef __attribute__((ext_vector_type(4))) float f32x4;
typedef __attribute__((ext_vector_type(2))) float f32x2;
typedef __attribute__((ext_vector_type(4))) unsigned u32x4;
typedef __attribute__((ext_vector_type(2))) unsigned u32x2;
typedef __attribute__((ext_vector_type(2))) __bf16 bf16x2_t;
typedef __attribute__((address_space(3))) s16x4 lds_s16x4;
typedef __attribute__((address_space(3))) const s16x4 lds_cs16x4;
typedef __attribute__((address_space(3))) const bf16x8 lds_cbf16x8;
typedef __attribute__((address_space(3))) char lds_char;

#define __float_as_uint(f) __builtin_bit_cast(unsigned, (float)(f))
#define __uint_as_float(u) __builtin_bit_cast(float, (unsigned)(u))
#define __any(c) (__builtin_amdgcn_ballot_w64(c) != 0)
#define __all(c) (__builtin_amdgcn_ballot_w64(!(c)) == 0)
extern "C" __device__ unsigned long long llvm_dispatch_id() __asm("llvm.amdgcn.dispatch.id");

__device__ __forceinline__ float bf2f(u16 x) { return __uint_as_float(((unsigned)x) << 16); }
__device__ __forceinline__ u16 f2bf(float f) {
    __hip_bfloat16 b = __float2bfloat16(f);
    return *reinterpret_cast<u16*>(&b);
}
__device__ __forceinline__ float rbf(float f) { return bf2f(f2bf(f)); }
__device__ __forceinline__ float lo_f(unsigned u) { return __uint_as_float(u << 16); }
__device__ __forceinline__ float hi_f(unsigned u) { return __uint_as_float(u & 0xffff0000u); }
__device__ __forceinline__ unsigned pack2(float a, float b) {
    return __builtin_bit_cast(unsigned, __builtin_convertvector((f32x2){a, b}, bf16x2_t));
}
__device__ __forceinline__ float dot2(unsigned a, unsigned b, float c) {
    return __builtin_amdgcn_fdot2_f32_bf16(__builtin_bit_cast(bf16x2_t, a), __builtin_bit_cast(bf16x2_t, b), c, false);
}

#define SB() __builtin_amdgcn_sched_barrier(0)
#define WAIT_VM(n) asm volatile("s_waitcnt vmcnt(" #n ")" ::: "memory")
#define RAW_BARRIER() do { asm volatile("s_waitcnt lgkmcnt(0)" ::: "memory"); __builtin_amdgcn_s_barrier(); } while (0)

#define KV_PITCH 4224
#define WS_KV 4096
#define WS_O (WS_KV + D_DIM * KV_PITCH)
#define WS_Y (WS_O + D_DIM * S_LEN * 2)
#define WS_G (WS_Y + D_DIM * S_LEN * 2)
#define WS_XF (WS_G + S_LEN * 4)
#define WS_SF (WS_XF + 128 * 128)
#define WS_HF (WS_SF + 128 * 128)
#define WS_H (WS_HF + 128 * 128)
#define WS_END (WS_H + 128 * 4 * 3072)
#define WS_DBG (WS_END + 4096)

#define A_SLOT 18432
#define W_SLOT 16384
#define W_PITCH 512
#define NW 6
#define A_OFF(s) ((s) == 0 ? 53248 : ((s) - 1) * A_SLOT)
#define W_OFF(s) ((s) == 0 ? 36864 : 71680 + ((s) - 1) * W_SLOT)
#define G_R1 0
#define G_R0 88064
#define G_XCH 53248
#define G_Y (G_XCH + 20480)
#define G_Q (G_R0 + 51200)
#define G_HX (G_Q + 10240)
#define G_VX 153600
#define G_TAB (G_VX + 4608)
#define G_HXO (G_TAB + 3072)
#define G_ZR (G_HXO + 1024)
#define G_VXD (G_ZR + 64)
#define KV_BYTES 4352
#define V_F (4 * KV_BYTES)
#define XC_ROW 288
#define XC_B 12
#define XC_COPY 4944
#define XC_BYTES (XC_COPY + 17 * XC_ROW)
#define V_KV4 (V_F + 5 * XC_BYTES)
#define V_KV(c) ((c) < 4 ? (c) * KV_BYTES : V_KV4)
#define V_WO (V_KV4 + KV_BYTES - 10240)
#define V_KST (V_WO + 10240 + 1312)
#define V_VST (V_KST + 20608)
#define V_STG 113536
#define V_QS (V_WO + 10240)
#define V_SCR 130560
#define V_XCH 0
#define V_Q 20480
#define V_Y (V_Q + 10240)
#define Y_PITCH 160
#define L_TOUCH 163328
#define L_CTRL 163584
#define LDS_TOTAL 163840
static_assert(V_VST + 20608 <= V_STG && V_STG % 128 == 0 && V_STG + 20480 <= L_TOUCH && V_WO >= V_F + 4 * XC_BYTES && V_WO + 10 * 10240 <= L_TOUCH &&
              V_WO + 6 * 10240 <= V_SCR && V_SCR + 32768 <= L_TOUCH && V_Y + 80 * Y_PITCH <= V_WO && V_WO % 16 == 0, "lds");
static_assert(W_OFF(5) + W_SLOT == G_VX && G_HX + 4096 == G_VX && G_ZR + 656 <= L_TOUCH && G_Y + 80 * Y_PITCH <= G_R0 && G_TAB % 16 == 0, "lds");
#define C_FREE (L_CTRL + 16)
#define C_OST (L_CTRL + 48)
#define C_SENT (L_CTRL + 52)
#define C_WO (L_CTRL + 56)
#define C_KV (L_CTRL + 60)
#define C_KVST (L_CTRL + 64)
#define C_CONVN (L_CTRL + 68)
#define C_Y (L_CTRL + 72)
#define C_KVR (L_CTRL + 76)
#define C_XD (L_CTRL + 80)
#define C_RED (L_CTRL + 84)
#define C_ORDY (L_CTRL + 88)
#define C_QR (L_CTRL + 92)
#define C_XCH (L_CTRL + 96)
#define C_PRE (L_CTRL + 112)
#define C_SFC (L_CTRL + 136)
#define C_GO (L_CTRL + 140)
#define C_HR (L_CTRL + 144)
#define C_RED2 (L_CTRL + 148)
#define C_KVD (L_CTRL + 152)
#define C_WT (L_CTRL + 156)
#define C_HX (L_CTRL + 160)
#define C_OST1 (L_CTRL + 176)
#define C_HS (L_CTRL + 180)
#define C_BP C_XCH
#define C_PF (L_CTRL + 192)
#define PART(slot) (isG ? ((slot) < 2 ? G_R1 + (slot) * 20480 : G_R0 + ((slot) - 2) * 20480) : V_WO + (slot) * 20480)

#define KNOB(n, v)
#define SPIN_CAP 4000000
#define GH 0
#define WO_PRIO 0
#define T_PRIO 3
#define C0_PRIO 2
#define C1_PRIO 1
#define L_PRIO 3
#define O_SLEEP 4
#define O_DELAY 0
#define POLL_SLEEP 2
#define Y_SLEEP 8
#define W_SLEEP 4
#define X_SLEEP 2
#define X_DELAY 0
#define CONV_PRIO 2
#define NBUF 4
#define WATCH_RT 2
#define JL 17
#define RING_E0 70
#define RING_F 105
#define WOT_SPLIT 0
#define GO_W C_KVD
#define GO_N 11
#define SENT_PACE 8
#define GATE_LATE 0
#define SENT_W C_KVD
#define SENT_N 11
#define NSENT 20
#define T0_PRIO T_PRIO
#define G_WST 4
#define V_WST 4
#define V_WST0 0
#define WR_X 2
#define HALO_AT 0
#define H_SLEEP 2
#define HS_WAIT 1
#define H_DELAY 0
#define LAST_PRIO 3
#define WATCH_C 16
#define GO2_W C_XD
#define GO2_N 4
#define H_POL ""
#define KV_POL ""
#define WATCH_J 0
#define GW_DELAY 0
#define O_AHEAD 4
#define PASS_PRIO 1
#define G_OST 8
#define V_ODELAY 0
#define W_LATE 1
#define Y_SL0 0
#define SF_DELAY 8
#define WO_MALL 1
#define T4_EARLY 0
#define POLL_DELAY 2
#define LATE_AT C_LOOP
#define LATE_N 4
#define KROT ((0x27057250u >> (xcd * 4)) & 15)

#define STAMP(id)
#define FLUSH()
#define CSTAMP(i)

__device__ __forceinline__ unsigned lds_ld32(unsigned a) {
    unsigned v;
    asm volatile("ds_read_b32 %0, %1\n\ts_waitcnt lgkmcnt(0)" : "=v"(v) : "v"(a) : "memory");
    return v;
}
__device__ __forceinline__ unsigned lds_min4(unsigned a) {
    u32x4 v;
    asm volatile("ds_read_b128 %0, %1\n\ts_waitcnt lgkmcnt(0)" : "=v"(v) : "v"(a) : "memory");
    return min(min(v[0], v[1]), min(v[2], v[3]));
}
__device__ __forceinline__ unsigned lds_min8(unsigned a) {
    u32x4 v, u;
    asm volatile("ds_read_b128 %0, %2\n\tds_read_b128 %1, %2 offset:16\n\ts_waitcnt lgkmcnt(0)" : "=&v"(v), "=&v"(u) : "v"(a) : "memory");
    return min(min(min(v[0], v[1]), min(v[2], v[3])), min(min(u[0], u[1]), min(u[2], u[3])));
}
__device__ __forceinline__ void lds_st32(unsigned a, unsigned v) {
    asm volatile("ds_write_b32 %0, %1" ::"v"(a), "v"(v) : "memory");
}
__device__ __forceinline__ unsigned lds_add_rtn(unsigned a, int lane) {
    unsigned old = 0;
    if (lane == 0) {
        unsigned one = 1;
        asm volatile("ds_add_rtn_u32 %0, %1, %2\n\ts_waitcnt lgkmcnt(0)" : "=v"(old) : "v"(a), "v"(one) : "memory");
    }
    return __builtin_amdgcn_readfirstlane(old);
}
__device__ __forceinline__ void lds_add(unsigned a, int lane, unsigned n = 1) {
    if (lane == 0) asm volatile("ds_add_u32 %0, %1" ::"v"(a), "v"(n) : "memory");
}
__device__ __forceinline__ void spin_min4(unsigned a, unsigned target) {
#pragma nounroll
    for (int i = 0; i < SPIN_CAP; ++i) {
        if (__builtin_amdgcn_readfirstlane(lds_min4(a)) >= target) break;
        __builtin_amdgcn_s_sleep(1);
    }
}
__device__ __forceinline__ void spin_min8(unsigned a, unsigned target) {
#pragma nounroll
    for (int i = 0; i < SPIN_CAP; ++i) {
        if (__builtin_amdgcn_readfirstlane(lds_min8(a)) >= target) break;
        __builtin_amdgcn_s_sleep(1);
    }
}
__device__ __forceinline__ void spin_ge(unsigned a, unsigned target) {
#pragma nounroll
    for (int i = 0; i < SPIN_CAP; ++i) {
        if (__builtin_amdgcn_readfirstlane(lds_ld32(a)) >= target) break;
        __builtin_amdgcn_s_sleep(1);
    }
}
typedef __attribute__((address_space(3))) volatile u32x4 lds_vu32x4;
typedef __attribute__((address_space(3))) volatile unsigned lds_vu32_t;
__device__ __forceinline__ u32x4 lds_v128(unsigned a) { return *(lds_vu32x4*)(size_t)a; }
__device__ __forceinline__ unsigned lds_v32(unsigned a) { return *(lds_vu32_t*)(size_t)a; }
__device__ __forceinline__ float fmax_raw(float a, float b) { return __builtin_elementwise_maximum(a, b); }
__device__ __forceinline__ float fmax3_raw(float a, float b, float c) { return __builtin_elementwise_maximum(__builtin_elementwise_maximum(a, b), c); }
__device__ __forceinline__ unsigned opaque(unsigned v) { asm volatile("" : "+v"(v)); return v; }
__device__ __forceinline__ bf16x8 cat8(s16x4 a, s16x4 b) { return __builtin_shufflevector(a, b, 0, 1, 2, 3, 4, 5, 6, 7); }

__device__ __forceinline__ void poll_flags(__amdgpu_buffer_rsrc_t rs, unsigned off, unsigned tlo, unsigned thi) {
#pragma nounroll
    for (int i = 0; i < SPIN_CAP; ++i) {
        asm volatile("" ::: "memory");
        const u32x2 v = __builtin_amdgcn_raw_buffer_load_b64(rs, off, 0, 16);
        if (__all(v[0] == tlo && v[1] == thi)) break;
        __builtin_amdgcn_s_sleep(POLL_SLEEP);
    }
    __builtin_amdgcn_fence(__ATOMIC_ACQUIRE, "wavefront");
}

__device__ __forceinline__ void spin_slow(unsigned a, unsigned target) {
#pragma nounroll
    for (int i = 0; i < SPIN_CAP; ++i) {
        if (__builtin_amdgcn_readfirstlane(lds_ld32(a)) >= target) break;
        __builtin_amdgcn_s_sleep(6);
    }
}
__device__ __forceinline__ void spin_mid(unsigned a, unsigned target) {
#pragma nounroll
    for (int i = 0; i < SPIN_CAP; ++i) {
        if (__builtin_amdgcn_readfirstlane(lds_ld32(a)) >= target) break;
        __builtin_amdgcn_s_sleep(3);
    }
}
__device__ __forceinline__ void spin_all(unsigned a, unsigned target) {
#pragma nounroll
    for (int i = 0; i < SPIN_CAP; ++i) {
        if (__all(lds_ld32(a) >= target)) break;
        __builtin_amdgcn_s_sleep(1);
    }
}

template <int N> __device__ __forceinline__ void waitvm() { asm volatile("s_waitcnt vmcnt(%0)" ::"n"(N) : "memory"); }
#define BARRIER() asm volatile("s_barrier" ::: "memory")
#define GLS(voff, sbase, lw, ldsoff) asm volatile("s_add_u32 m0, %2, %3\n\ts_nop 0\n\tglobal_load_lds_dwordx4 %0, %1" ::"v"(voff), "s"(sbase), "s"(lw), "n"(ldsoff) : "memory", "scc")
#define GLSP(voff, sbase, lw, ldsoff) asm volatile("s_add_u32 m0, %2, %3\n\ts_nop 0\n\tglobal_load_lds_dwordx4 %0, %1" KV_POL ::"v"(voff), "s"(sbase), "s"(lw), "n"(ldsoff) : "memory", "scc")
#define GLT(voff, sbase) asm volatile("s_mov_b32 m0, %2\n\ts_nop 0\n\tglobal_load_lds_dword %0, %1" ::"v"(voff), "s"(sbase), "s"(lb + L_TOUCH) : "memory")
#define TRRD(p) __builtin_amdgcn_ds_read_tr16_b64_v4i16((lds_s16x4*)(p))

constexpr int ring_total(int NA) { return 10 * (8 + NA); }
constexpr int ring_need(int J, int NA) { return J < 0 ? NA + 8 : J >= JL ? 10 * (8 + NA) : (J & 1) ? (NA + 8) * ((J + 3) / 2) : (NA + 8) * ((J + 2) / 2) + 4 + NA; }
constexpr int ring_isA(int p, int NA) { return p < NA ? 1 : p < NA + 8 ? 0 : (p % (8 + NA) >= 4 && p % (8 + NA) < 4 + NA); }
constexpr int ring_tile(int p, int NA) { return p < NA ? 0 : p < NA + 8 ? (p - NA) / 4 : ring_isA(p, NA) ? p / (8 + NA) : 2 * (p / (8 + NA)) + (p % (8 + NA) >= 4); }
constexpr int ring_sub(int p, int NA) { return p < NA ? p : p < NA + 8 ? (p - NA) % 4 : p % (8 + NA) < 4 ? p % (8 + NA) : p % (8 + NA) < 4 + NA ? p % (8 + NA) - 4 : p % (8 + NA) - 4 - NA; }
constexpr int ring_cum(int J, int NA) {
    int c = J >= (JL < 16 ? JL : 16) ? ring_total(NA) : NA + 8 + (8 + NA) * (2 * RING_E0 + RING_F * (J + 1)) / 200;
    if (c > ring_total(NA)) c = ring_total(NA);
    if (c < ring_need(J, NA)) c = ring_need(J, NA);
    while (ring_isA(c - 1, NA) ? 2 * (ring_tile(c - 1, NA) - 3) + 1 > J - 1 : ring_tile(c - 1, NA) - NW > J - 1) --c;
    return c;
}
static_assert(ring_cum(16, 5) == ring_total(5) && ring_cum(16, 4) == ring_total(4), "ring");
template <int NA, int P0, int P1>
__device__ __forceinline__ void ring_issue(const unsigned (&ev)[9], const void* xp, const void* wp, int kr, unsigned lw) {
    if constexpr (P0 < P1) {
        constexpr int t = ring_tile(P0, NA), sub = ring_sub(P0, NA);
        if constexpr (ring_isA(P0, NA)) {
            const int c = __builtin_amdgcn_readfirstlane(kr + t >= 10 ? kr + t - 10 : kr + t);
            GLS(ev[4 + sub], (const char*)xp + c * 128, lw, A_OFF(t % 3) + sub * 4096);
        } else {
            const int c = __builtin_amdgcn_readfirstlane(2 * kr + t >= 20 ? 2 * kr + t - 20 : 2 * kr + t);
            GLS(ev[sub], (const char*)wp + (size_t)c * (32 * NQKV * 2), lw, W_OFF(t % NW) + sub * 4096);
        }
        ring_issue<NA, P0 + 1, P1>(ev, xp, wp, kr, lw);
    }
}
template <int NA, int J>
__device__ __forceinline__ void ring_run(const unsigned (&ev)[9], const void* xp, const void* wp, int kr, unsigned lw) {
    ring_issue<NA, (J < 0 ? 0 : ring_cum(J - 1, NA)), ring_cum(J, NA)>(ev, xp, wp, kr, lw);
    if constexpr (J < 16 && J <= JL) {
        waitvm<ring_cum(J, NA) - ring_need(J, NA)>();
        BARRIER();
        if constexpr (J < JL) ring_run<NA, J + 1>(ev, xp, wp, kr, lw);
    }
}

#define CONV_ASM \
    "s_mov_b32 m0, %[kvl]\n" \
    "v_mov_b32 v50, 1\n" \
    "global_load_lds_dwordx4 %[xo], %[wsp] offset:0 sc1\n" \
    "global_load_lds_dwordx4 %[xo], %[wsp] offset:1024 sc1\n" \
    "global_load_lds_dwordx4 %[xo], %[wsp] offset:2048 sc1\n" \
    "global_load_lds_dwordx4 %[xo], %[wsp] offset:3072 sc1\n" \
    "s_mov_b64 exec, 1\n" \
    "ds_add_u32 %[ctl], v50 offset:80\n" \
    "s_mov_b64 exec, -1\n" \
    "v_add_u32 v84, %[sx4], %[xp]\n" \
    "v_add_u32 v51, %[sh4], %[hp]\n" \
    "v_add_u32 v160, 0, %[xp]\n" \
    "v_add_u32 v161, 1152, %[xp]\n" \
    "v_add_u32 v85, 1152, v84\n" \
    "v_add_u32 v162, 2304, %[xp]\n" \
    "v_add_u32 v86, 2304, v84\n" \
    "v_add_u32 v163, 3456, %[xp]\n" \
    "v_add_u32 v87, 3456, v84\n" \
    "ds_read2_b32 v[88:89], v87 offset0:144 offset1:145\n" \
    "ds_read2_b32 v[90:91], v87 offset0:146 offset1:147\n" \
    "ds_read2_b32 v[92:93], v87 offset0:0 offset1:1\n" \
    "ds_read2_b32 v[94:95], v87 offset0:2 offset1:3\n" \
    "ds_read2_b32 v[96:97], v86 offset0:144 offset1:145\n" \
    "ds_read2_b32 v[98:99], v86 offset0:146 offset1:147\n" \
    "ds_read2_b32 v[100:101], v86 offset0:0 offset1:1\n" \
    "ds_read2_b32 v[102:103], v86 offset0:2 offset1:3\n" \
    "ds_read2_b32 v[104:105], v85 offset0:144 offset1:145\n" \
    "ds_read2_b32 v[106:107], v85 offset0:146 offset1:147\n" \
    "ds_read2_b32 v[108:109], v85 offset0:0 offset1:1\n" \
    "ds_read2_b32 v[110:111], v85 offset0:2 offset1:3\n" \
    "ds_read2_b32 v[112:113], v84 offset0:144 offset1:145\n" \
    "ds_read2_b32 v[114:115], v84 offset0:146 offset1:147\n" \
    "ds_read2_b32 v[116:117], v84 offset0:0 offset1:1\n" \
    "ds_read2_b32 v[118:119], v84 offset0:2 offset1:3\n" \
    "s_mov_b32 %[cnt], 0\n" \
    "Lsp_%=:\n" \
    "ds_read_b32 v50, %[ctl] offset:144\n" \
    "s_add_u32 %[cnt], %[cnt], 1\n" \
    "s_waitcnt lgkmcnt(0)\n" \
    "v_readfirstlane_b32 %[tmp], v50\n" \
    "s_cmp_ge_u32 %[cnt], %[cap]\n" \
    "s_cbranch_scc1 Lgo_%=\n" \
    "s_cmp_lt_u32 %[tmp], 4\n" \
    "s_cbranch_scc1 Lsp_%=\n" \
    "Lgo_%=:\n" \
    "ds_read_b128 v[148:151], v51 offset:3584\n" \
    "ds_read_b128 v[144:147], v51 offset:3072\n" \
    "ds_read_b128 v[140:143], v51 offset:2560\n" \
    "ds_read_b128 v[136:139], v51 offset:2048\n" \
    "ds_read_b128 v[132:135], v51 offset:1536\n" \
    "ds_read_b128 v[128:131], v51 offset:1024\n" \
    "ds_read_b128 v[124:127], v51 offset:512\n" \
    "ds_read_b128 v[120:123], v51 offset:0\n" \
    "s_waitcnt lgkmcnt(7)\n" \
    "v_mfma_f32_16x16x32_bf16 v[80:83], v[88:91], v[148:151], 0\n" \
    "ds_read_b128 v[148:151], v51 offset:3648\n" \
    "s_waitcnt lgkmcnt(7)\n" \
    "v_mfma_f32_16x16x32_bf16 v[76:79], v[88:91], v[144:147], 0\n" \
    "ds_read_b128 v[156:159], v51 offset:576\n" \
    "s_waitcnt lgkmcnt(7)\n" \
    "v_mfma_f32_16x16x32_bf16 v[72:75], v[88:91], v[140:143], 0\n" \
    "ds_read_b128 v[152:155], v51 offset:64\n" \
    "s_waitcnt lgkmcnt(7)\n" \
    "v_mfma_f32_16x16x32_bf16 v[68:71], v[88:91], v[136:139], 0\n" \
    "s_waitcnt lgkmcnt(6)\n" \
    "v_mfma_f32_16x16x32_bf16 v[64:67], v[88:91], v[132:135], 0\n" \
    "s_waitcnt lgkmcnt(5)\n" \
    "v_mfma_f32_16x16x32_bf16 v[60:63], v[88:91], v[128:131], 0\n" \
    "s_waitcnt lgkmcnt(4)\n" \
    "v_mfma_f32_16x16x32_bf16 v[56:59], v[88:91], v[124:127], 0\n" \
    "s_waitcnt lgkmcnt(3)\n" \
    "v_mfma_f32_16x16x32_bf16 v[52:55], v[88:91], v[120:123], 0\n" \
    "ds_read2_b32 v[88:89], v87 offset0:160 offset1:161\n" \
    "v_mfma_f32_16x16x32_bf16 v[80:83], v[92:95], v[144:147], v[80:83]\n" \
    "ds_read2_b32 v[90:91], v87 offset0:162 offset1:163\n" \
    "v_mfma_f32_16x16x32_bf16 v[76:79], v[92:95], v[140:143], v[76:79]\n" \
    "ds_read_b128 v[144:147], v51 offset:3136\n" \
    "v_mfma_f32_16x16x32_bf16 v[72:75], v[92:95], v[136:139], v[72:75]\n" \
    "v_mfma_f32_16x16x32_bf16 v[68:71], v[92:95], v[132:135], v[68:71]\n" \
    "v_mfma_f32_16x16x32_bf16 v[64:67], v[92:95], v[128:131], v[64:67]\n" \
    "v_mfma_f32_16x16x32_bf16 v[60:63], v[92:95], v[124:127], v[60:63]\n" \
    "v_mfma_f32_16x16x32_bf16 v[56:59], v[92:95], v[120:123], v[56:59]\n" \
    "ds_read2_b32 v[92:93], v87 offset0:16 offset1:17\n" \
    "v_mfma_f32_16x16x32_bf16 v[80:83], v[96:99], v[140:143], v[80:83]\n" \
    "ds_read_b128 v[140:143], v51 offset:2624\n" \
    "v_mfma_f32_16x16x32_bf16 v[76:79], v[96:99], v[136:139], v[76:79]\n" \
    "ds_read2_b32 v[94:95], v87 offset0:18 offset1:19\n" \
    "v_mfma_f32_16x16x32_bf16 v[72:75], v[96:99], v[132:135], v[72:75]\n" \
    "v_mfma_f32_16x16x32_bf16 v[68:71], v[96:99], v[128:131], v[68:71]\n" \
    "v_mfma_f32_16x16x32_bf16 v[64:67], v[96:99], v[124:127], v[64:67]\n" \
    "v_mfma_f32_16x16x32_bf16 v[60:63], v[96:99], v[120:123], v[60:63]\n" \
    "ds_read2_b32 v[96:97], v86 offset0:160 offset1:161\n" \
    "v_mfma_f32_16x16x32_bf16 v[80:83], v[100:103], v[136:139], v[80:83]\n" \
    "ds_read_b128 v[136:139], v51 offset:2112\n" \
    "v_mfma_f32_16x16x32_bf16 v[76:79], v[100:103], v[132:135], v[76:79]\n" \
    "ds_read2_b32 v[98:99], v86 offset0:162 offset1:163\n" \
    "v_mfma_f32_16x16x32_bf16 v[72:75], v[100:103], v[128:131], v[72:75]\n" \
    "v_mfma_f32_16x16x32_bf16 v[68:71], v[100:103], v[124:127], v[68:71]\n" \
    "v_mfma_f32_16x16x32_bf16 v[64:67], v[100:103], v[120:123], v[64:67]\n" \
    "ds_read2_b32 v[100:101], v86 offset0:16 offset1:17\n" \
    "v_mfma_f32_16x16x32_bf16 v[80:83], v[104:107], v[132:135], v[80:83]\n" \
    "ds_read_b128 v[132:135], v51 offset:1600\n" \
    "v_mfma_f32_16x16x32_bf16 v[76:79], v[104:107], v[128:131], v[76:79]\n" \
    "ds_read2_b32 v[102:103], v86 offset0:18 offset1:19\n" \
    "v_mfma_f32_16x16x32_bf16 v[72:75], v[104:107], v[124:127], v[72:75]\n" \
    "v_mfma_f32_16x16x32_bf16 v[68:71], v[104:107], v[120:123], v[68:71]\n" \
    "ds_read2_b32 v[104:105], v85 offset0:160 offset1:161\n" \
    "v_mfma_f32_16x16x32_bf16 v[80:83], v[108:111], v[128:131], v[80:83]\n" \
    "ds_read_b128 v[128:131], v51 offset:1088\n" \
    "v_mfma_f32_16x16x32_bf16 v[76:79], v[108:111], v[124:127], v[76:79]\n" \
    "ds_read2_b32 v[106:107], v85 offset0:162 offset1:163\n" \
    "v_mfma_f32_16x16x32_bf16 v[72:75], v[108:111], v[120:123], v[72:75]\n" \
    "ds_read2_b32 v[108:109], v85 offset0:16 offset1:17\n" \
    "v_mfma_f32_16x16x32_bf16 v[80:83], v[112:115], v[124:127], v[80:83]\n" \
    "ds_read2_b32 v[110:111], v85 offset0:18 offset1:19\n" \
    "v_mfma_f32_16x16x32_bf16 v[76:79], v[112:115], v[120:123], v[76:79]\n" \
    "ds_read2_b32 v[112:113], v84 offset0:160 offset1:161\n" \
    "v_mfma_f32_16x16x32_bf16 v[80:83], v[116:119], v[120:123], v[80:83]\n" \
    "ds_read2_b32 v[114:115], v84 offset0:162 offset1:163\n" \
    "s_waitcnt lgkmcnt(15)\n" \
    "v_mfma_f32_16x16x32_bf16 v[80:83], v[88:91], v[148:151], v[80:83]\n" \
    "ds_read2_b32 v[116:117], v84 offset0:16 offset1:17\n" \
    "v_mfma_f32_16x16x32_bf16 v[76:79], v[88:91], v[144:147], v[76:79]\n" \
    "ds_read2_b32 v[118:119], v84 offset0:18 offset1:19\n" \
    "s_waitcnt lgkmcnt(15)\n" \
    "v_mfma_f32_16x16x32_bf16 v[72:75], v[88:91], v[140:143], v[72:75]\n" \
    "s_waitcnt vmcnt(0)\n" \
    "ds_read_b128 v[148:151], %[hp] offset:3584\n" \
    "s_waitcnt lgkmcnt(14)\n" \
    "v_mfma_f32_16x16x32_bf16 v[68:71], v[88:91], v[136:139], v[68:71]\n" \
    "ds_read_b128 v[124:127], %[hp] offset:512\n" \
    "s_waitcnt lgkmcnt(12)\n" \
    "v_mfma_f32_16x16x32_bf16 v[64:67], v[88:91], v[132:135], v[64:67]\n" \
    "ds_read_b128 v[120:123], %[hp] offset:0\n" \
    "s_waitcnt lgkmcnt(10)\n" \
    "v_mfma_f32_16x16x32_bf16 v[60:63], v[88:91], v[128:131], v[60:63]\n" \
    "v_mfma_f32_16x16x32_bf16 v[56:59], v[88:91], v[156:159], v[56:59]\n" \
    "v_mfma_f32_16x16x32_bf16 v[52:55], v[88:91], v[152:155], v[52:55]\n" \
    "ds_read2_b32 v[88:89], v163 offset0:144 offset1:145\n" \
    "v_mfma_f32_16x16x32_bf16 v[80:83], v[92:95], v[144:147], v[80:83]\n" \
    "ds_read2_b32 v[90:91], v163 offset0:146 offset1:147\n" \
    "v_mfma_f32_16x16x32_bf16 v[76:79], v[92:95], v[140:143], v[76:79]\n" \
    "ds_read_b128 v[144:147], %[hp] offset:3072\n" \
    "v_mfma_f32_16x16x32_bf16 v[72:75], v[92:95], v[136:139], v[72:75]\n" \
    "v_mfma_f32_16x16x32_bf16 v[68:71], v[92:95], v[132:135], v[68:71]\n" \
    "ds_write_b128 %[scr], v[52:55] offset:0\n" \
    "v_mfma_f32_16x16x32_bf16 v[64:67], v[92:95], v[128:131], v[64:67]\n" \
    "ds_read_b64 v[86:87], %[kvr] offset:0\n" \
    "v_mfma_f32_16x16x32_bf16 v[60:63], v[92:95], v[156:159], v[60:63]\n" \
    "v_mfma_f32_16x16x32_bf16 v[56:59], v[92:95], v[152:155], v[56:59]\n" \
    "ds_read2_b32 v[92:93], v163 offset0:0 offset1:1\n" \
    "v_mfma_f32_16x16x32_bf16 v[80:83], v[96:99], v[140:143], v[80:83]\n" \
    "ds_read_b128 v[140:143], %[hp] offset:2560\n" \
    "v_mfma_f32_16x16x32_bf16 v[76:79], v[96:99], v[136:139], v[76:79]\n" \
    "ds_read2_b32 v[94:95], v163 offset0:2 offset1:3\n" \
    "v_mfma_f32_16x16x32_bf16 v[72:75], v[96:99], v[132:135], v[72:75]\n" \
    "v_mfma_f32_16x16x32_bf16 v[68:71], v[96:99], v[128:131], v[68:71]\n" \
    "ds_write_b128 %[scr], v[56:59] offset:1024\n" \
    "v_mfma_f32_16x16x32_bf16 v[64:67], v[96:99], v[156:159], v[64:67]\n" \
    "v_mfma_f32_16x16x32_bf16 v[60:63], v[96:99], v[152:155], v[60:63]\n" \
    "ds_read2_b32 v[96:97], v162 offset0:144 offset1:145\n" \
    "v_mfma_f32_16x16x32_bf16 v[80:83], v[100:103], v[136:139], v[80:83]\n" \
    "ds_read_b128 v[136:139], %[hp] offset:2048\n" \
    "v_mfma_f32_16x16x32_bf16 v[76:79], v[100:103], v[132:135], v[76:79]\n" \
    "ds_read2_b32 v[98:99], v162 offset0:146 offset1:147\n" \
    "v_mfma_f32_16x16x32_bf16 v[72:75], v[100:103], v[128:131], v[72:75]\n" \
    "v_mfma_f32_16x16x32_bf16 v[68:71], v[100:103], v[156:159], v[68:71]\n" \
    "s_waitcnt lgkmcnt(7)\n" \
    "v_lshlrev_b32 v84, 16, v86\n" \
    "v_mfma_f32_16x16x32_bf16 v[64:67], v[100:103], v[152:155], v[64:67]\n" \
    "ds_read2_b32 v[100:101], v162 offset0:0 offset1:1\n" \
    "v_mfma_f32_16x16x32_bf16 v[80:83], v[104:107], v[132:135], v[80:83]\n" \
    "ds_read_b128 v[132:135], %[hp] offset:1536\n" \
    "v_and_b32 v85, 0xffff0000, v86\n" \
    "v_mfma_f32_16x16x32_bf16 v[76:79], v[104:107], v[128:131], v[76:79]\n" \
    "ds_read2_b32 v[102:103], v162 offset0:2 offset1:3\n" \
    "v_mfma_f32_16x16x32_bf16 v[72:75], v[104:107], v[156:159], v[72:75]\n" \
    "v_lshlrev_b32 v86, 16, v87\n" \
    "v_mfma_f32_16x16x32_bf16 v[68:71], v[104:107], v[152:155], v[68:71]\n" \
    "ds_read2_b32 v[104:105], v161 offset0:144 offset1:145\n" \
    "v_mfma_f32_16x16x32_bf16 v[80:83], v[108:111], v[128:131], v[80:83]\n" \
    "ds_read_b128 v[128:131], %[hp] offset:1024\n" \
    "v_and_b32 v87, 0xffff0000, v87\n" \
    "v_mfma_f32_16x16x32_bf16 v[76:79], v[108:111], v[156:159], v[76:79]\n" \
    "ds_read2_b32 v[106:107], v161 offset0:146 offset1:147\n" \
    "v_mfma_f32_16x16x32_bf16 v[72:75], v[108:111], v[152:155], v[72:75]\n" \
    "ds_read2_b32 v[108:109], v161 offset0:0 offset1:1\n" \
    "v_mfma_f32_16x16x32_bf16 v[80:83], v[112:115], v[156:159], v[80:83]\n" \
    "ds_read2_b32 v[110:111], v161 offset0:2 offset1:3\n" \
    "v_mfma_f32_16x16x32_bf16 v[76:79], v[112:115], v[152:155], v[76:79]\n" \
    "ds_read2_b32 v[112:113], v160 offset0:144 offset1:145\n" \
    "v_mfma_f32_16x16x32_bf16 v[80:83], v[116:119], v[152:155], v[80:83]\n" \
    "ds_read2_b32 v[114:115], v160 offset0:146 offset1:147\n" \
    "v_mfma_f32_16x16x32_bf16 v[44:47], v[88:91], v[148:151], 0\n" \
    "ds_read2_b32 v[116:117], v160 offset0:0 offset1:1\n" \
    "v_mfma_f32_16x16x32_bf16 v[40:43], v[88:91], v[144:147], 0\n" \
    "ds_read2_b32 v[118:119], v160 offset0:2 offset1:3\n" \
    "s_waitcnt lgkmcnt(15)\n" \
    "v_mfma_f32_16x16x32_bf16 v[36:39], v[88:91], v[140:143], 0\n" \
    "ds_read_b128 v[148:151], %[hp] offset:3648\n" \
    "s_waitcnt lgkmcnt(14)\n" \
    "v_mfma_f32_16x16x32_bf16 v[32:35], v[88:91], v[136:139], 0\n" \
    "ds_read_b128 v[156:159], %[hp] offset:576\n" \
    "s_waitcnt lgkmcnt(12)\n" \
    "v_mfma_f32_16x16x32_bf16 v[28:31], v[88:91], v[132:135], 0\n" \
    "ds_read_b128 v[152:155], %[hp] offset:64\n" \
    "s_waitcnt lgkmcnt(10)\n" \
    "v_mfma_f32_16x16x32_bf16 v[24:27], v[88:91], v[128:131], 0\n" \
    "ds_write_b128 %[scr], v[60:63] offset:2048\n" \
    "v_mfma_f32_16x16x32_bf16 v[20:23], v[88:91], v[124:127], 0\n" \
    "v_mfma_f32_16x16x32_bf16 v[16:19], v[88:91], v[120:123], 0\n" \
    "ds_read2_b32 v[88:89], v163 offset0:160 offset1:161\n" \
    "v_mfma_f32_16x16x32_bf16 v[44:47], v[92:95], v[144:147], v[44:47]\n" \
    "ds_read2_b32 v[90:91], v163 offset0:162 offset1:163\n" \
    "v_mfma_f32_16x16x32_bf16 v[40:43], v[92:95], v[140:143], v[40:43]\n" \
    "ds_read_b128 v[144:147], %[hp] offset:3136\n" \
    "v_mfma_f32_16x16x32_bf16 v[36:39], v[92:95], v[136:139], v[36:39]\n" \
    "v_fmac_f32 v16, %[dd], v84\n" \
    "v_fmac_f32 v17, %[dd], v85\n" \
    "v_mfma_f32_16x16x32_bf16 v[32:35], v[92:95], v[132:135], v[32:35]\n" \
    "v_fmac_f32 v18, %[dd], v86\n" \
    "v_fmac_f32 v19, %[dd], v87\n" \
    "v_mfma_f32_16x16x32_bf16 v[28:31], v[92:95], v[128:131], v[28:31]\n" \
    "ds_read_b64 v[86:87], %[kvr] offset:512\n" \
    "v_mfma_f32_16x16x32_bf16 v[24:27], v[92:95], v[124:127], v[24:27]\n" \
    "ds_write_b128 %[scr], v[64:67] offset:3072\n" \
    "v_mfma_f32_16x16x32_bf16 v[20:23], v[92:95], v[120:123], v[20:23]\n" \
    "ds_read2_b32 v[92:93], v163 offset0:16 offset1:17\n" \
    "v_mfma_f32_16x16x32_bf16 v[44:47], v[96:99], v[140:143], v[44:47]\n" \
    "ds_read_b128 v[140:143], %[hp] offset:2624\n" \
    "v_mfma_f32_16x16x32_bf16 v[40:43], v[96:99], v[136:139], v[40:43]\n" \
    "ds_read2_b32 v[94:95], v163 offset0:18 offset1:19\n" \
    "v_mfma_f32_16x16x32_bf16 v[36:39], v[96:99], v[132:135], v[36:39]\n" \
    "ds_write_b128 %[scr], v[68:71] offset:4096\n" \
    "v_mfma_f32_16x16x32_bf16 v[32:35], v[96:99], v[128:131], v[32:35]\n" \
    "v_mfma_f32_16x16x32_bf16 v[28:31], v[96:99], v[124:127], v[28:31]\n" \
    "v_mfma_f32_16x16x32_bf16 v[24:27], v[96:99], v[120:123], v[24:27]\n" \
    "ds_read2_b32 v[96:97], v162 offset0:160 offset1:161\n" \
    "v_mfma_f32_16x16x32_bf16 v[44:47], v[100:103], v[136:139], v[44:47]\n" \
    "ds_read_b128 v[136:139], %[hp] offset:2112\n" \
    "v_mfma_f32_16x16x32_bf16 v[40:43], v[100:103], v[132:135], v[40:43]\n" \
    "ds_read2_b32 v[98:99], v162 offset0:162 offset1:163\n" \
    "v_mfma_f32_16x16x32_bf16 v[36:39], v[100:103], v[128:131], v[36:39]\n" \
    "ds_write_b128 %[scr], v[72:75] offset:5120\n" \
    "v_mfma_f32_16x16x32_bf16 v[32:35], v[100:103], v[124:127], v[32:35]\n" \
    "s_waitcnt lgkmcnt(9)\n" \
    "v_lshlrev_b32 v84, 16, v86\n" \
    "v_mfma_f32_16x16x32_bf16 v[28:31], v[100:103], v[120:123], v[28:31]\n" \
    "ds_read2_b32 v[100:101], v162 offset0:16 offset1:17\n" \
    "v_mfma_f32_16x16x32_bf16 v[44:47], v[104:107], v[132:135], v[44:47]\n" \
    "ds_read_b128 v[132:135], %[hp] offset:1600\n" \
    "v_and_b32 v85, 0xffff0000, v86\n" \
    "v_mfma_f32_16x16x32_bf16 v[40:43], v[104:107], v[128:131], v[40:43]\n" \
    "ds_read2_b32 v[102:103], v162 offset0:18 offset1:19\n" \
    "v_mfma_f32_16x16x32_bf16 v[36:39], v[104:107], v[124:127], v[36:39]\n" \
    "v_lshlrev_b32 v86, 16, v87\n" \
    "v_mfma_f32_16x16x32_bf16 v[32:35], v[104:107], v[120:123], v[32:35]\n" \
    "ds_read2_b32 v[104:105], v161 offset0:160 offset1:161\n" \
    "v_mfma_f32_16x16x32_bf16 v[44:47], v[108:111], v[128:131], v[44:47]\n" \
    "ds_read_b128 v[128:131], %[hp] offset:1088\n" \
    "v_and_b32 v87, 0xffff0000, v87\n" \
    "v_mfma_f32_16x16x32_bf16 v[40:43], v[108:111], v[124:127], v[40:43]\n" \
    "ds_read2_b32 v[106:107], v161 offset0:162 offset1:163\n" \
    "v_mfma_f32_16x16x32_bf16 v[36:39], v[108:111], v[120:123], v[36:39]\n" \
    "ds_read2_b32 v[108:109], v161 offset0:16 offset1:17\n" \
    "v_mfma_f32_16x16x32_bf16 v[44:47], v[112:115], v[124:127], v[44:47]\n" \
    "ds_read2_b32 v[110:111], v161 offset0:18 offset1:19\n" \
    "v_mfma_f32_16x16x32_bf16 v[40:43], v[112:115], v[120:123], v[40:43]\n" \
    "ds_read2_b32 v[112:113], v160 offset0:160 offset1:161\n" \
    "v_mfma_f32_16x16x32_bf16 v[44:47], v[116:119], v[120:123], v[44:47]\n" \
    "ds_read2_b32 v[114:115], v160 offset0:162 offset1:163\n" \
    "v_mfma_f32_16x16x32_bf16 v[44:47], v[88:91], v[148:151], v[44:47]\n" \
    "ds_read2_b32 v[116:117], v160 offset0:16 offset1:17\n" \
    "v_mfma_f32_16x16x32_bf16 v[40:43], v[88:91], v[144:147], v[40:43]\n" \
    "ds_read2_b32 v[118:119], v160 offset0:18 offset1:19\n" \
    "s_waitcnt lgkmcnt(15)\n" \
    "v_mfma_f32_16x16x32_bf16 v[36:39], v[88:91], v[140:143], v[36:39]\n" \
    "ds_read_b128 v[148:151], %[hp] offset:3712\n" \
    "s_waitcnt lgkmcnt(15)\n" \
    "v_mfma_f32_16x16x32_bf16 v[32:35], v[88:91], v[136:139], v[32:35]\n" \
    "ds_read_b128 v[124:127], %[hp] offset:640\n" \
    "s_waitcnt lgkmcnt(12)\n" \
    "v_mfma_f32_16x16x32_bf16 v[28:31], v[88:91], v[132:135], v[28:31]\n" \
    "ds_read_b128 v[120:123], %[hp] offset:128\n" \
    "s_waitcnt lgkmcnt(10)\n" \
    "v_mfma_f32_16x16x32_bf16 v[24:27], v[88:91], v[128:131], v[24:27]\n" \
    "ds_write_b128 %[scr], v[76:79] offset:6144\n" \
    "v_mfma_f32_16x16x32_bf16 v[20:23], v[88:91], v[156:159], v[20:23]\n" \
    "v_mfma_f32_16x16x32_bf16 v[16:19], v[88:91], v[152:155], v[16:19]\n" \
    "ds_read2_b32 v[88:89], v163 offset0:176 offset1:177\n" \
    "v_mfma_f32_16x16x32_bf16 v[44:47], v[92:95], v[144:147], v[44:47]\n" \
    "ds_read2_b32 v[90:91], v163 offset0:178 offset1:179\n" \
    "v_mfma_f32_16x16x32_bf16 v[40:43], v[92:95], v[140:143], v[40:43]\n" \
    "ds_read_b128 v[144:147], %[hp] offset:3200\n" \
    "v_fmac_f32 v20, %[dd], v84\n" \
    "v_fmac_f32 v21, %[dd], v85\n" \
    "v_mfma_f32_16x16x32_bf16 v[36:39], v[92:95], v[136:139], v[36:39]\n" \
    "v_fmac_f32 v22, %[dd], v86\n" \
    "v_fmac_f32 v23, %[dd], v87\n" \
    "v_mfma_f32_16x16x32_bf16 v[32:35], v[92:95], v[132:135], v[32:35]\n" \
    "ds_write_b128 %[scr], v[80:83] offset:7168\n" \
    "v_mov_b32 v50, 1\n" \
    "s_mov_b64 exec, 1\n" \
    "ds_add_u32 %[ctl], v50 offset:84\n" \
    "s_mov_b64 exec, -1\n" \
    "v_mfma_f32_16x16x32_bf16 v[28:31], v[92:95], v[128:131], v[28:31]\n" \
    "ds_read_b64 v[86:87], %[kvr] offset:1024\n" \
    "v_mfma_f32_16x16x32_bf16 v[24:27], v[92:95], v[156:159], v[24:27]\n" \
    "v_mfma_f32_16x16x32_bf16 v[20:23], v[92:95], v[152:155], v[20:23]\n" \
    "ds_read2_b32 v[92:93], v163 offset0:32 offset1:33\n" \
    "v_mfma_f32_16x16x32_bf16 v[44:47], v[96:99], v[140:143], v[44:47]\n" \
    "ds_read_b128 v[140:143], %[hp] offset:2688\n" \
    "v_mfma_f32_16x16x32_bf16 v[40:43], v[96:99], v[136:139], v[40:43]\n" \
    "ds_read2_b32 v[94:95], v163 offset0:34 offset1:35\n" \
    "v_mfma_f32_16x16x32_bf16 v[36:39], v[96:99], v[132:135], v[36:39]\n" \
    "v_mfma_f32_16x16x32_bf16 v[32:35], v[96:99], v[128:131], v[32:35]\n" \
    "v_mfma_f32_16x16x32_bf16 v[28:31], v[96:99], v[156:159], v[28:31]\n" \
    "v_mfma_f32_16x16x32_bf16 v[24:27], v[96:99], v[152:155], v[24:27]\n" \
    "ds_read2_b32 v[96:97], v162 offset0:176 offset1:177\n" \
    "v_mfma_f32_16x16x32_bf16 v[44:47], v[100:103], v[136:139], v[44:47]\n" \
    "ds_read_b128 v[136:139], %[hp] offset:2176\n" \
    "v_mfma_f32_16x16x32_bf16 v[40:43], v[100:103], v[132:135], v[40:43]\n" \
    "ds_read2_b32 v[98:99], v162 offset0:178 offset1:179\n" \
    "v_mfma_f32_16x16x32_bf16 v[36:39], v[100:103], v[128:131], v[36:39]\n" \
    "v_mfma_f32_16x16x32_bf16 v[32:35], v[100:103], v[156:159], v[32:35]\n" \
    "s_waitcnt lgkmcnt(6)\n" \
    "v_lshlrev_b32 v84, 16, v86\n" \
    "v_mfma_f32_16x16x32_bf16 v[28:31], v[100:103], v[152:155], v[28:31]\n" \
    "ds_read2_b32 v[100:101], v162 offset0:32 offset1:33\n" \
    "v_mfma_f32_16x16x32_bf16 v[44:47], v[104:107], v[132:135], v[44:47]\n" \
    "ds_read_b128 v[132:135], %[hp] offset:1664\n" \
    "v_and_b32 v85, 0xffff0000, v86\n" \
    "v_mfma_f32_16x16x32_bf16 v[40:43], v[104:107], v[128:131], v[40:43]\n" \
    "ds_read2_b32 v[102:103], v162 offset0:34 offset1:35\n" \
    "v_mfma_f32_16x16x32_bf16 v[36:39], v[104:107], v[156:159], v[36:39]\n" \
    "v_lshlrev_b32 v86, 16, v87\n" \
    "v_mfma_f32_16x16x32_bf16 v[32:35], v[104:107], v[152:155], v[32:35]\n" \
    "ds_read2_b32 v[104:105], v161 offset0:176 offset1:177\n" \
    "v_mfma_f32_16x16x32_bf16 v[44:47], v[108:111], v[128:131], v[44:47]\n" \
    "ds_read_b128 v[128:131], %[hp] offset:1152\n" \
    "v_and_b32 v87, 0xffff0000, v87\n" \
    "v_mfma_f32_16x16x32_bf16 v[40:43], v[108:111], v[156:159], v[40:43]\n" \
    "ds_read2_b32 v[106:107], v161 offset0:178 offset1:179\n" \
    "v_mfma_f32_16x16x32_bf16 v[36:39], v[108:111], v[152:155], v[36:39]\n" \
    "ds_read2_b32 v[108:109], v161 offset0:32 offset1:33\n" \
    "v_mfma_f32_16x16x32_bf16 v[44:47], v[112:115], v[156:159], v[44:47]\n" \
    "ds_read2_b32 v[110:111], v161 offset0:34 offset1:35\n" \
    "v_mfma_f32_16x16x32_bf16 v[40:43], v[112:115], v[152:155], v[40:43]\n" \
    "ds_read2_b32 v[112:113], v160 offset0:176 offset1:177\n" \
    "v_mfma_f32_16x16x32_bf16 v[44:47], v[116:119], v[152:155], v[44:47]\n" \
    "ds_read2_b32 v[114:115], v160 offset0:178 offset1:179\n" \
    "v_mfma_f32_16x16x32_bf16 v[44:47], v[88:91], v[148:151], v[44:47]\n" \
    "ds_read2_b32 v[116:117], v160 offset0:32 offset1:33\n" \
    "v_mfma_f32_16x16x32_bf16 v[40:43], v[88:91], v[144:147], v[40:43]\n" \
    "ds_read2_b32 v[118:119], v160 offset0:34 offset1:35\n" \
    "s_waitcnt lgkmcnt(15)\n" \
    "v_mfma_f32_16x16x32_bf16 v[36:39], v[88:91], v[140:143], v[36:39]\n" \
    "ds_read_b128 v[148:151], %[hp] offset:3776\n" \
    "s_waitcnt lgkmcnt(14)\n" \
    "v_mfma_f32_16x16x32_bf16 v[32:35], v[88:91], v[136:139], v[32:35]\n" \
    "ds_read_b128 v[156:159], %[hp] offset:704\n" \
    "s_waitcnt lgkmcnt(12)\n" \
    "v_mfma_f32_16x16x32_bf16 v[28:31], v[88:91], v[132:135], v[28:31]\n" \
    "ds_read_b128 v[152:155], %[hp] offset:192\n" \
    "s_waitcnt lgkmcnt(10)\n" \
    "v_mfma_f32_16x16x32_bf16 v[24:27], v[88:91], v[128:131], v[24:27]\n" \
    "v_mfma_f32_16x16x32_bf16 v[20:23], v[88:91], v[124:127], v[20:23]\n" \
    "v_mfma_f32_16x16x32_bf16 v[16:19], v[88:91], v[120:123], v[16:19]\n" \
    "ds_read2_b32 v[88:89], v163 offset0:192 offset1:193\n" \
    "v_mfma_f32_16x16x32_bf16 v[44:47], v[92:95], v[144:147], v[44:47]\n" \
    "ds_read2_b32 v[90:91], v163 offset0:194 offset1:195\n" \
    "v_fmac_f32 v24, %[dd], v84\n" \
    "v_fmac_f32 v25, %[dd], v85\n" \
    "v_mfma_f32_16x16x32_bf16 v[40:43], v[92:95], v[140:143], v[40:43]\n" \
    "ds_read_b128 v[144:147], %[hp] offset:3264\n" \
    "v_fmac_f32 v26, %[dd], v86\n" \
    "v_fmac_f32 v27, %[dd], v87\n" \
    "v_mfma_f32_16x16x32_bf16 v[36:39], v[92:95], v[136:139], v[36:39]\n" \
    "v_mfma_f32_16x16x32_bf16 v[32:35], v[92:95], v[132:135], v[32:35]\n" \
    "v_mfma_f32_16x16x32_bf16 v[28:31], v[92:95], v[128:131], v[28:31]\n" \
    "ds_read_b64 v[86:87], %[kvr] offset:1536\n" \
    "v_mfma_f32_16x16x32_bf16 v[24:27], v[92:95], v[124:127], v[24:27]\n" \
    "v_mfma_f32_16x16x32_bf16 v[20:23], v[92:95], v[120:123], v[20:23]\n" \
    "ds_read2_b32 v[92:93], v163 offset0:48 offset1:49\n" \
    "v_mfma_f32_16x16x32_bf16 v[44:47], v[96:99], v[140:143], v[44:47]\n" \
    "ds_read_b128 v[140:143], %[hp] offset:2752\n" \
    "v_mfma_f32_16x16x32_bf16 v[40:43], v[96:99], v[136:139], v[40:43]\n" \
    "ds_read2_b32 v[94:95], v163 offset0:50 offset1:51\n" \
    "v_mfma_f32_16x16x32_bf16 v[36:39], v[96:99], v[132:135], v[36:39]\n" \
    "v_mfma_f32_16x16x32_bf16 v[32:35], v[96:99], v[128:131], v[32:35]\n" \
    "v_mfma_f32_16x16x32_bf16 v[28:31], v[96:99], v[124:127], v[28:31]\n" \
    "v_mfma_f32_16x16x32_bf16 v[24:27], v[96:99], v[120:123], v[24:27]\n" \
    "ds_read2_b32 v[96:97], v162 offset0:192 offset1:193\n" \
    "v_mfma_f32_16x16x32_bf16 v[44:47], v[100:103], v[136:139], v[44:47]\n" \
    "ds_read_b128 v[136:139], %[hp] offset:2240\n" \
    "v_mfma_f32_16x16x32_bf16 v[40:43], v[100:103], v[132:135], v[40:43]\n" \
    "ds_read2_b32 v[98:99], v162 offset0:194 offset1:195\n" \
    "v_mfma_f32_16x16x32_bf16 v[36:39], v[100:103], v[128:131], v[36:39]\n" \
    "v_mfma_f32_16x16x32_bf16 v[32:35], v[100:103], v[124:127], v[32:35]\n" \
    "s_waitcnt lgkmcnt(6)\n" \
    "v_lshlrev_b32 v84, 16, v86\n" \
    "v_mfma_f32_16x16x32_bf16 v[28:31], v[100:103], v[120:123], v[28:31]\n" \
    "ds_read2_b32 v[100:101], v162 offset0:48 offset1:49\n" \
    "v_mfma_f32_16x16x32_bf16 v[44:47], v[104:107], v[132:135], v[44:47]\n" \
    "ds_read_b128 v[132:135], %[hp] offset:1728\n" \
    "v_and_b32 v85, 0xffff0000, v86\n" \
    "v_mfma_f32_16x16x32_bf16 v[40:43], v[104:107], v[128:131], v[40:43]\n" \
    "ds_read2_b32 v[102:103], v162 offset0:50 offset1:51\n" \
    "v_mfma_f32_16x16x32_bf16 v[36:39], v[104:107], v[124:127], v[36:39]\n" \
    "v_lshlrev_b32 v86, 16, v87\n" \
    "v_mfma_f32_16x16x32_bf16 v[32:35], v[104:107], v[120:123], v[32:35]\n" \
    "ds_read2_b32 v[104:105], v161 offset0:192 offset1:193\n" \
    "v_mfma_f32_16x16x32_bf16 v[44:47], v[108:111], v[128:131], v[44:47]\n" \
    "ds_read_b128 v[128:131], %[hp] offset:1216\n" \
    "v_and_b32 v87, 0xffff0000, v87\n" \
    "v_mfma_f32_16x16x32_bf16 v[40:43], v[108:111], v[124:127], v[40:43]\n" \
    "ds_read2_b32 v[106:107], v161 offset0:194 offset1:195\n" \
    "v_mfma_f32_16x16x32_bf16 v[36:39], v[108:111], v[120:123], v[36:39]\n" \
    "ds_read2_b32 v[108:109], v161 offset0:48 offset1:49\n" \
    "v_mfma_f32_16x16x32_bf16 v[44:47], v[112:115], v[124:127], v[44:47]\n" \
    "ds_read2_b32 v[110:111], v161 offset0:50 offset1:51\n" \
    "v_mfma_f32_16x16x32_bf16 v[40:43], v[112:115], v[120:123], v[40:43]\n" \
    "ds_read2_b32 v[112:113], v160 offset0:192 offset1:193\n" \
    "v_mfma_f32_16x16x32_bf16 v[44:47], v[116:119], v[120:123], v[44:47]\n" \
    "ds_read2_b32 v[114:115], v160 offset0:194 offset1:195\n" \
    "v_mfma_f32_16x16x32_bf16 v[44:47], v[88:91], v[148:151], v[44:47]\n" \
    "ds_read2_b32 v[116:117], v160 offset0:48 offset1:49\n" \
    "v_mfma_f32_16x16x32_bf16 v[40:43], v[88:91], v[144:147], v[40:43]\n" \
    "ds_read2_b32 v[118:119], v160 offset0:50 offset1:51\n" \
    "s_waitcnt lgkmcnt(15)\n" \
    "v_mfma_f32_16x16x32_bf16 v[36:39], v[88:91], v[140:143], v[36:39]\n" \
    "ds_read_b128 v[148:151], %[hp] offset:3840\n" \
    "s_waitcnt lgkmcnt(14)\n" \
    "v_mfma_f32_16x16x32_bf16 v[32:35], v[88:91], v[136:139], v[32:35]\n" \
    "ds_read_b128 v[124:127], %[hp] offset:768\n" \
    "s_waitcnt lgkmcnt(12)\n" \
    "v_mfma_f32_16x16x32_bf16 v[28:31], v[88:91], v[132:135], v[28:31]\n" \
    "ds_read_b128 v[120:123], %[hp] offset:256\n" \
    "s_waitcnt lgkmcnt(10)\n" \
    "v_mfma_f32_16x16x32_bf16 v[24:27], v[88:91], v[128:131], v[24:27]\n" \
    "v_mfma_f32_16x16x32_bf16 v[20:23], v[88:91], v[156:159], v[20:23]\n" \
    "v_mfma_f32_16x16x32_bf16 v[16:19], v[88:91], v[152:155], v[16:19]\n" \
    "ds_read2_b32 v[88:89], v163 offset0:216 offset1:217\n" \
    "v_fmac_f32 v28, %[dd], v84\n" \
    "v_fmac_f32 v29, %[dd], v85\n" \
    "v_mfma_f32_16x16x32_bf16 v[44:47], v[92:95], v[144:147], v[44:47]\n" \
    "ds_read2_b32 v[90:91], v163 offset0:218 offset1:219\n" \
    "v_fmac_f32 v30, %[dd], v86\n" \
    "v_fmac_f32 v31, %[dd], v87\n" \
    "v_mfma_f32_16x16x32_bf16 v[40:43], v[92:95], v[140:143], v[40:43]\n" \
    "ds_read_b128 v[144:147], %[hp] offset:3328\n" \
    "v_mfma_f32_16x16x32_bf16 v[36:39], v[92:95], v[136:139], v[36:39]\n" \
    "v_mfma_f32_16x16x32_bf16 v[32:35], v[92:95], v[132:135], v[32:35]\n" \
    "v_mfma_f32_16x16x32_bf16 v[28:31], v[92:95], v[128:131], v[28:31]\n" \
    "ds_read_b64 v[86:87], %[kvr] offset:2048\n" \
    "v_mfma_f32_16x16x32_bf16 v[24:27], v[92:95], v[156:159], v[24:27]\n" \
    "v_mfma_f32_16x16x32_bf16 v[20:23], v[92:95], v[152:155], v[20:23]\n" \
    "ds_read2_b32 v[92:93], v163 offset0:72 offset1:73\n" \
    "v_mfma_f32_16x16x32_bf16 v[44:47], v[96:99], v[140:143], v[44:47]\n" \
    "ds_read_b128 v[140:143], %[hp] offset:2816\n" \
    "v_mfma_f32_16x16x32_bf16 v[40:43], v[96:99], v[136:139], v[40:43]\n" \
    "ds_read2_b32 v[94:95], v163 offset0:74 offset1:75\n" \
    "v_mfma_f32_16x16x32_bf16 v[36:39], v[96:99], v[132:135], v[36:39]\n" \
    "v_mfma_f32_16x16x32_bf16 v[32:35], v[96:99], v[128:131], v[32:35]\n" \
    "v_mfma_f32_16x16x32_bf16 v[28:31], v[96:99], v[156:159], v[28:31]\n" \
    "v_mfma_f32_16x16x32_bf16 v[24:27], v[96:99], v[152:155], v[24:27]\n" \
    "ds_read2_b32 v[96:97], v162 offset0:216 offset1:217\n" \
    "v_mfma_f32_16x16x32_bf16 v[44:47], v[100:103], v[136:139], v[44:47]\n" \
    "ds_read_b128 v[136:139], %[hp] offset:2304\n" \
    "v_mfma_f32_16x16x32_bf16 v[40:43], v[100:103], v[132:135], v[40:43]\n" \
    "ds_read2_b32 v[98:99], v162 offset0:218 offset1:219\n" \
    "v_mfma_f32_16x16x32_bf16 v[36:39], v[100:103], v[128:131], v[36:39]\n" \
    "v_mfma_f32_16x16x32_bf16 v[32:35], v[100:103], v[156:159], v[32:35]\n" \
    "s_waitcnt lgkmcnt(6)\n" \
    "v_lshlrev_b32 v84, 16, v86\n" \
    "v_mfma_f32_16x16x32_bf16 v[28:31], v[100:103], v[152:155], v[28:31]\n" \
    "ds_read2_b32 v[100:101], v162 offset0:72 offset1:73\n" \
    "v_mfma_f32_16x16x32_bf16 v[44:47], v[104:107], v[132:135], v[44:47]\n" \
    "ds_read_b128 v[132:135], %[hp] offset:1792\n" \
    "v_and_b32 v85, 0xffff0000, v86\n" \
    "v_mfma_f32_16x16x32_bf16 v[40:43], v[104:107], v[128:131], v[40:43]\n" \
    "ds_read2_b32 v[102:103], v162 offset0:74 offset1:75\n" \
    "v_mfma_f32_16x16x32_bf16 v[36:39], v[104:107], v[156:159], v[36:39]\n" \
    "v_lshlrev_b32 v86, 16, v87\n" \
    "v_mfma_f32_16x16x32_bf16 v[32:35], v[104:107], v[152:155], v[32:35]\n" \
    "ds_read2_b32 v[104:105], v161 offset0:216 offset1:217\n" \
    "v_mfma_f32_16x16x32_bf16 v[44:47], v[108:111], v[128:131], v[44:47]\n" \
    "ds_read_b128 v[128:131], %[hp] offset:1280\n" \
    "v_and_b32 v87, 0xffff0000, v87\n" \
    "v_mfma_f32_16x16x32_bf16 v[40:43], v[108:111], v[156:159], v[40:43]\n" \
    "ds_read2_b32 v[106:107], v161 offset0:218 offset1:219\n" \
    "v_mfma_f32_16x16x32_bf16 v[36:39], v[108:111], v[152:155], v[36:39]\n" \
    "ds_read2_b32 v[108:109], v161 offset0:72 offset1:73\n" \
    "v_mfma_f32_16x16x32_bf16 v[44:47], v[112:115], v[156:159], v[44:47]\n" \
    "ds_read2_b32 v[110:111], v161 offset0:74 offset1:75\n" \
    "v_mfma_f32_16x16x32_bf16 v[40:43], v[112:115], v[152:155], v[40:43]\n" \
    "ds_read2_b32 v[112:113], v160 offset0:216 offset1:217\n" \
    "v_mfma_f32_16x16x32_bf16 v[44:47], v[116:119], v[152:155], v[44:47]\n" \
    "ds_read2_b32 v[114:115], v160 offset0:218 offset1:219\n" \
    "v_add_u32 v160, 288, v160\n" \
    "v_add_u32 v161, 288, v161\n" \
    "v_add_u32 v162, 288, v162\n" \
    "v_add_u32 v163, 288, v163\n" \
    "v_mfma_f32_16x16x32_bf16 v[44:47], v[88:91], v[148:151], v[44:47]\n" \
    "ds_read2_b32 v[116:117], v160 offset0:0 offset1:1\n" \
    "v_mfma_f32_16x16x32_bf16 v[40:43], v[88:91], v[144:147], v[40:43]\n" \
    "ds_read2_b32 v[118:119], v160 offset0:2 offset1:3\n" \
    "s_waitcnt lgkmcnt(15)\n" \
    "v_mfma_f32_16x16x32_bf16 v[36:39], v[88:91], v[140:143], v[36:39]\n" \
    "ds_read_b128 v[148:151], %[hp] offset:3904\n" \
    "s_waitcnt lgkmcnt(14)\n" \
    "v_mfma_f32_16x16x32_bf16 v[32:35], v[88:91], v[136:139], v[32:35]\n" \
    "ds_read_b128 v[156:159], %[hp] offset:832\n" \
    "s_waitcnt lgkmcnt(12)\n" \
    "v_mfma_f32_16x16x32_bf16 v[28:31], v[88:91], v[132:135], v[28:31]\n" \
    "ds_read_b128 v[152:155], %[hp] offset:320\n" \
    "s_waitcnt lgkmcnt(10)\n" \
    "v_mfma_f32_16x16x32_bf16 v[24:27], v[88:91], v[128:131], v[24:27]\n" \
    "v_mfma_f32_16x16x32_bf16 v[20:23], v[88:91], v[124:127], v[20:23]\n" \
    "v_fmac_f32 v32, %[dd], v84\n" \
    "v_fmac_f32 v33, %[dd], v85\n" \
    "v_mfma_f32_16x16x32_bf16 v[16:19], v[88:91], v[120:123], v[16:19]\n" \
    "ds_read2_b32 v[88:89], v163 offset0:160 offset1:161\n" \
    "v_fmac_f32 v34, %[dd], v86\n" \
    "v_fmac_f32 v35, %[dd], v87\n" \
    "v_mfma_f32_16x16x32_bf16 v[44:47], v[92:95], v[144:147], v[44:47]\n" \
    "ds_read2_b32 v[90:91], v163 offset0:162 offset1:163\n" \
    "v_mfma_f32_16x16x32_bf16 v[40:43], v[92:95], v[140:143], v[40:43]\n" \
    "ds_read_b128 v[144:147], %[hp] offset:3392\n" \
    "v_mfma_f32_16x16x32_bf16 v[36:39], v[92:95], v[136:139], v[36:39]\n" \
    "v_mfma_f32_16x16x32_bf16 v[32:35], v[92:95], v[132:135], v[32:35]\n" \
    "v_mfma_f32_16x16x32_bf16 v[28:31], v[92:95], v[128:131], v[28:31]\n" \
    "ds_read_b64 v[86:87], %[kvr] offset:2560\n" \
    "v_mfma_f32_16x16x32_bf16 v[24:27], v[92:95], v[124:127], v[24:27]\n" \
    "v_mfma_f32_16x16x32_bf16 v[20:23], v[92:95], v[120:123], v[20:23]\n" \
    "ds_read2_b32 v[92:93], v163 offset0:16 offset1:17\n" \
    "v_mfma_f32_16x16x32_bf16 v[44:47], v[96:99], v[140:143], v[44:47]\n" \
    "ds_read_b128 v[140:143], %[hp] offset:2880\n" \
    "v_mfma_f32_16x16x32_bf16 v[40:43], v[96:99], v[136:139], v[40:43]\n" \
    "ds_read2_b32 v[94:95], v163 offset0:18 offset1:19\n" \
    "v_mfma_f32_16x16x32_bf16 v[36:39], v[96:99], v[132:135], v[36:39]\n" \
    "v_mfma_f32_16x16x32_bf16 v[32:35], v[96:99], v[128:131], v[32:35]\n" \
    "v_mfma_f32_16x16x32_bf16 v[28:31], v[96:99], v[124:127], v[28:31]\n" \
    "v_mfma_f32_16x16x32_bf16 v[24:27], v[96:99], v[120:123], v[24:27]\n" \
    "ds_read2_b32 v[96:97], v162 offset0:160 offset1:161\n" \
    "v_mfma_f32_16x16x32_bf16 v[44:47], v[100:103], v[136:139], v[44:47]\n" \
    "ds_read_b128 v[136:139], %[hp] offset:2368\n" \
    "v_mfma_f32_16x16x32_bf16 v[40:43], v[100:103], v[132:135], v[40:43]\n" \
    "ds_read2_b32 v[98:99], v162 offset0:162 offset1:163\n" \
    "v_mfma_f32_16x16x32_bf16 v[36:39], v[100:103], v[128:131], v[36:39]\n" \
    "v_mfma_f32_16x16x32_bf16 v[32:35], v[100:103], v[124:127], v[32:35]\n" \
    "s_waitcnt lgkmcnt(6)\n" \
    "v_lshlrev_b32 v84, 16, v86\n" \
    "v_mfma_f32_16x16x32_bf16 v[28:31], v[100:103], v[120:123], v[28:31]\n" \
    "ds_read2_b32 v[100:101], v162 offset0:16 offset1:17\n" \
    "v_mfma_f32_16x16x32_bf16 v[44:47], v[104:107], v[132:135], v[44:47]\n" \
    "ds_read_b128 v[132:135], %[hp] offset:1856\n" \
    "v_and_b32 v85, 0xffff0000, v86\n" \
    "v_mfma_f32_16x16x32_bf16 v[40:43], v[104:107], v[128:131], v[40:43]\n" \
    "ds_read2_b32 v[102:103], v162 offset0:18 offset1:19\n" \
    "v_mfma_f32_16x16x32_bf16 v[36:39], v[104:107], v[124:127], v[36:39]\n" \
    "v_lshlrev_b32 v86, 16, v87\n" \
    "v_mfma_f32_16x16x32_bf16 v[32:35], v[104:107], v[120:123], v[32:35]\n" \
    "ds_read2_b32 v[104:105], v161 offset0:160 offset1:161\n" \
    "v_mfma_f32_16x16x32_bf16 v[44:47], v[108:111], v[128:131], v[44:47]\n" \
    "ds_read_b128 v[128:131], %[hp] offset:1344\n" \
    "v_and_b32 v87, 0xffff0000, v87\n" \
    "v_mfma_f32_16x16x32_bf16 v[40:43], v[108:111], v[124:127], v[40:43]\n" \
    "ds_read2_b32 v[106:107], v161 offset0:162 offset1:163\n" \
    "v_mfma_f32_16x16x32_bf16 v[36:39], v[108:111], v[120:123], v[36:39]\n" \
    "ds_read2_b32 v[108:109], v161 offset0:16 offset1:17\n" \
    "v_mfma_f32_16x16x32_bf16 v[44:47], v[112:115], v[124:127], v[44:47]\n" \
    "ds_read2_b32 v[110:111], v161 offset0:18 offset1:19\n" \
    "v_mfma_f32_16x16x32_bf16 v[40:43], v[112:115], v[120:123], v[40:43]\n" \
    "ds_read2_b32 v[112:113], v160 offset0:160 offset1:161\n" \
    "v_mfma_f32_16x16x32_bf16 v[44:47], v[116:119], v[120:123], v[44:47]\n" \
    "ds_read2_b32 v[114:115], v160 offset0:162 offset1:163\n" \
    "v_mfma_f32_16x16x32_bf16 v[44:47], v[88:91], v[148:151], v[44:47]\n" \
    "ds_read2_b32 v[116:117], v160 offset0:16 offset1:17\n" \
    "v_mfma_f32_16x16x32_bf16 v[40:43], v[88:91], v[144:147], v[40:43]\n" \
    "ds_read2_b32 v[118:119], v160 offset0:18 offset1:19\n" \
    "s_waitcnt lgkmcnt(15)\n" \
    "v_mfma_f32_16x16x32_bf16 v[36:39], v[88:91], v[140:143], v[36:39]\n" \
    "ds_read_b128 v[148:151], %[hp] offset:3968\n" \
    "s_waitcnt lgkmcnt(14)\n" \
    "v_mfma_f32_16x16x32_bf16 v[32:35], v[88:91], v[136:139], v[32:35]\n" \
    "ds_read_b128 v[124:127], %[hp] offset:896\n" \
    "s_waitcnt lgkmcnt(12)\n" \
    "v_mfma_f32_16x16x32_bf16 v[28:31], v[88:91], v[132:135], v[28:31]\n" \
    "ds_read_b128 v[120:123], %[hp] offset:384\n" \
    "s_waitcnt lgkmcnt(10)\n" \
    "v_mfma_f32_16x16x32_bf16 v[24:27], v[88:91], v[128:131], v[24:27]\n" \
    "v_fmac_f32 v36, %[dd], v84\n" \
    "v_fmac_f32 v37, %[dd], v85\n" \
    "v_mfma_f32_16x16x32_bf16 v[20:23], v[88:91], v[156:159], v[20:23]\n" \
    "v_fmac_f32 v38, %[dd], v86\n" \
    "v_fmac_f32 v39, %[dd], v87\n" \
    "v_mfma_f32_16x16x32_bf16 v[16:19], v[88:91], v[152:155], v[16:19]\n" \
    "ds_read2_b32 v[88:89], v163 offset0:176 offset1:177\n" \
    "v_mfma_f32_16x16x32_bf16 v[44:47], v[92:95], v[144:147], v[44:47]\n" \
    "ds_read2_b32 v[90:91], v163 offset0:178 offset1:179\n" \
    "v_mfma_f32_16x16x32_bf16 v[40:43], v[92:95], v[140:143], v[40:43]\n" \
    "ds_read_b128 v[144:147], %[hp] offset:3456\n" \
    "v_mfma_f32_16x16x32_bf16 v[36:39], v[92:95], v[136:139], v[36:39]\n" \
    "v_mfma_f32_16x16x32_bf16 v[32:35], v[92:95], v[132:135], v[32:35]\n" \
    "v_mfma_f32_16x16x32_bf16 v[28:31], v[92:95], v[128:131], v[28:31]\n" \
    "ds_read_b64 v[86:87], %[kvr] offset:3072\n" \
    "v_mfma_f32_16x16x32_bf16 v[24:27], v[92:95], v[156:159], v[24:27]\n" \
    "v_mfma_f32_16x16x32_bf16 v[20:23], v[92:95], v[152:155], v[20:23]\n" \
    "ds_read2_b32 v[92:93], v163 offset0:32 offset1:33\n" \
    "v_mfma_f32_16x16x32_bf16 v[44:47], v[96:99], v[140:143], v[44:47]\n" \
    "ds_read_b128 v[140:143], %[hp] offset:2944\n" \
    "v_mfma_f32_16x16x32_bf16 v[40:43], v[96:99], v[136:139], v[40:43]\n" \
    "ds_read2_b32 v[94:95], v163 offset0:34 offset1:35\n" \
    "v_mfma_f32_16x16x32_bf16 v[36:39], v[96:99], v[132:135], v[36:39]\n" \
    "v_mfma_f32_16x16x32_bf16 v[32:35], v[96:99], v[128:131], v[32:35]\n" \
    "v_mfma_f32_16x16x32_bf16 v[28:31], v[96:99], v[156:159], v[28:31]\n" \
    "v_mfma_f32_16x16x32_bf16 v[24:27], v[96:99], v[152:155], v[24:27]\n" \
    "ds_read2_b32 v[96:97], v162 offset0:176 offset1:177\n" \
    "v_mfma_f32_16x16x32_bf16 v[44:47], v[100:103], v[136:139], v[44:47]\n" \
    "ds_read_b128 v[136:139], %[hp] offset:2432\n" \
    "v_mfma_f32_16x16x32_bf16 v[40:43], v[100:103], v[132:135], v[40:43]\n" \
    "ds_read2_b32 v[98:99], v162 offset0:178 offset1:179\n" \
    "v_mfma_f32_16x16x32_bf16 v[36:39], v[100:103], v[128:131], v[36:39]\n" \
    "v_mfma_f32_16x16x32_bf16 v[32:35], v[100:103], v[156:159], v[32:35]\n" \
    "s_waitcnt lgkmcnt(6)\n" \
    "v_lshlrev_b32 v84, 16, v86\n" \
    "v_mfma_f32_16x16x32_bf16 v[28:31], v[100:103], v[152:155], v[28:31]\n" \
    "ds_read2_b32 v[100:101], v162 offset0:32 offset1:33\n" \
    "v_mfma_f32_16x16x32_bf16 v[44:47], v[104:107], v[132:135], v[44:47]\n" \
    "ds_read_b128 v[132:135], %[hp] offset:1920\n" \
    "v_and_b32 v85, 0xffff0000, v86\n" \
    "v_mfma_f32_16x16x32_bf16 v[40:43], v[104:107], v[128:131], v[40:43]\n" \
    "ds_read2_b32 v[102:103], v162 offset0:34 offset1:35\n" \
    "v_mfma_f32_16x16x32_bf16 v[36:39], v[104:107], v[156:159], v[36:39]\n" \
    "v_lshlrev_b32 v86, 16, v87\n" \
    "v_mfma_f32_16x16x32_bf16 v[32:35], v[104:107], v[152:155], v[32:35]\n" \
    "ds_read2_b32 v[104:105], v161 offset0:176 offset1:177\n" \
    "v_mfma_f32_16x16x32_bf16 v[44:47], v[108:111], v[128:131], v[44:47]\n" \
    "ds_read_b128 v[128:131], %[hp] offset:1408\n" \
    "v_and_b32 v87, 0xffff0000, v87\n" \
    "v_mfma_f32_16x16x32_bf16 v[40:43], v[108:111], v[156:159], v[40:43]\n" \
    "ds_read2_b32 v[106:107], v161 offset0:178 offset1:179\n" \
    "v_mfma_f32_16x16x32_bf16 v[36:39], v[108:111], v[152:155], v[36:39]\n" \
    "ds_read2_b32 v[108:109], v161 offset0:32 offset1:33\n" \
    "v_mfma_f32_16x16x32_bf16 v[44:47], v[112:115], v[156:159], v[44:47]\n" \
    "ds_read2_b32 v[110:111], v161 offset0:34 offset1:35\n" \
    "v_mfma_f32_16x16x32_bf16 v[40:43], v[112:115], v[152:155], v[40:43]\n" \
    "ds_read2_b32 v[112:113], v160 offset0:176 offset1:177\n" \
    "v_mfma_f32_16x16x32_bf16 v[44:47], v[116:119], v[152:155], v[44:47]\n" \
    "ds_read2_b32 v[114:115], v160 offset0:178 offset1:179\n" \
    "v_mfma_f32_16x16x32_bf16 v[44:47], v[88:91], v[148:151], v[44:47]\n" \
    "ds_read2_b32 v[116:117], v160 offset0:32 offset1:33\n" \
    "v_mfma_f32_16x16x32_bf16 v[40:43], v[88:91], v[144:147], v[40:43]\n" \
    "ds_read2_b32 v[118:119], v160 offset0:34 offset1:35\n" \
    "s_waitcnt lgkmcnt(15)\n" \
    "v_mfma_f32_16x16x32_bf16 v[36:39], v[88:91], v[140:143], v[36:39]\n" \
    "ds_read_b128 v[152:155], %[hp] offset:448\n" \
    "s_waitcnt lgkmcnt(14)\n" \
    "v_mfma_f32_16x16x32_bf16 v[32:35], v[88:91], v[136:139], v[32:35]\n" \
    "ds_read_b128 v[156:159], %[hp] offset:960\n" \
    "s_waitcnt lgkmcnt(12)\n" \
    "v_mfma_f32_16x16x32_bf16 v[28:31], v[88:91], v[132:135], v[28:31]\n" \
    "ds_read_b128 v[148:151], %[hp] offset:4032\n" \
    "v_fmac_f32 v40, %[dd], v84\n" \
    "v_fmac_f32 v41, %[dd], v85\n" \
    "s_waitcnt lgkmcnt(10)\n" \
    "v_mfma_f32_16x16x32_bf16 v[24:27], v[88:91], v[128:131], v[24:27]\n" \
    "v_fmac_f32 v42, %[dd], v86\n" \
    "v_fmac_f32 v43, %[dd], v87\n" \
    "v_mfma_f32_16x16x32_bf16 v[20:23], v[88:91], v[124:127], v[20:23]\n" \
    "v_mfma_f32_16x16x32_bf16 v[16:19], v[88:91], v[120:123], v[16:19]\n" \
    "ds_read2_b32 v[88:89], v163 offset0:192 offset1:193\n" \
    "v_mfma_f32_16x16x32_bf16 v[44:47], v[92:95], v[144:147], v[44:47]\n" \
    "ds_read2_b32 v[90:91], v163 offset0:194 offset1:195\n" \
    "v_mfma_f32_16x16x32_bf16 v[40:43], v[92:95], v[140:143], v[40:43]\n" \
    "ds_read_b128 v[144:147], %[hp] offset:3520\n" \
    "v_mfma_f32_16x16x32_bf16 v[36:39], v[92:95], v[136:139], v[36:39]\n" \
    "v_mfma_f32_16x16x32_bf16 v[32:35], v[92:95], v[132:135], v[32:35]\n" \
    "v_mfma_f32_16x16x32_bf16 v[28:31], v[92:95], v[128:131], v[28:31]\n" \
    "ds_read_b64 v[86:87], %[kvr] offset:3584\n" \
    "v_mfma_f32_16x16x32_bf16 v[24:27], v[92:95], v[124:127], v[24:27]\n" \
    "v_mfma_f32_16x16x32_bf16 v[20:23], v[92:95], v[120:123], v[20:23]\n" \
    "ds_read2_b32 v[92:93], v163 offset0:48 offset1:49\n" \
    "v_mfma_f32_16x16x32_bf16 v[44:47], v[96:99], v[140:143], v[44:47]\n" \
    "ds_read2_b32 v[94:95], v163 offset0:50 offset1:51\n" \
    "v_mfma_f32_16x16x32_bf16 v[40:43], v[96:99], v[136:139], v[40:43]\n" \
    "ds_read_b128 v[140:143], %[hp] offset:3008\n" \
    "v_mfma_f32_16x16x32_bf16 v[36:39], v[96:99], v[132:135], v[36:39]\n" \
    "v_mfma_f32_16x16x32_bf16 v[32:35], v[96:99], v[128:131], v[32:35]\n" \
    "v_mfma_f32_16x16x32_bf16 v[28:31], v[96:99], v[124:127], v[28:31]\n" \
    "v_mfma_f32_16x16x32_bf16 v[24:27], v[96:99], v[120:123], v[24:27]\n" \
    "ds_read2_b32 v[96:97], v162 offset0:192 offset1:193\n" \
    "v_mfma_f32_16x16x32_bf16 v[44:47], v[100:103], v[136:139], v[44:47]\n" \
    "ds_read2_b32 v[98:99], v162 offset0:194 offset1:195\n" \
    "v_mfma_f32_16x16x32_bf16 v[40:43], v[100:103], v[132:135], v[40:43]\n" \
    "ds_read_b128 v[136:139], %[hp] offset:2496\n" \
    "v_mfma_f32_16x16x32_bf16 v[36:39], v[100:103], v[128:131], v[36:39]\n" \
    "v_mfma_f32_16x16x32_bf16 v[32:35], v[100:103], v[124:127], v[32:35]\n" \
    "s_waitcnt lgkmcnt(6)\n" \
    "v_lshlrev_b32 v84, 16, v86\n" \
    "v_mfma_f32_16x16x32_bf16 v[28:31], v[100:103], v[120:123], v[28:31]\n" \
    "ds_read2_b32 v[100:101], v162 offset0:48 offset1:49\n" \
    "v_mfma_f32_16x16x32_bf16 v[44:47], v[104:107], v[132:135], v[44:47]\n" \
    "ds_read_b128 v[132:135], %[hp] offset:1984\n" \
    "v_and_b32 v85, 0xffff0000, v86\n" \
    "v_mfma_f32_16x16x32_bf16 v[40:43], v[104:107], v[128:131], v[40:43]\n" \
    "ds_read2_b32 v[102:103], v162 offset0:50 offset1:51\n" \
    "v_mfma_f32_16x16x32_bf16 v[36:39], v[104:107], v[124:127], v[36:39]\n" \
    "v_lshlrev_b32 v86, 16, v87\n" \
    "v_mfma_f32_16x16x32_bf16 v[32:35], v[104:107], v[120:123], v[32:35]\n" \
    "ds_read2_b32 v[104:105], v161 offset0:192 offset1:193\n" \
    "v_mfma_f32_16x16x32_bf16 v[44:47], v[108:111], v[128:131], v[44:47]\n" \
    "ds_read_b128 v[128:131], %[hp] offset:1472\n" \
    "v_and_b32 v87, 0xffff0000, v87\n" \
    "v_mfma_f32_16x16x32_bf16 v[40:43], v[108:111], v[124:127], v[40:43]\n" \
    "ds_read2_b32 v[106:107], v161 offset0:194 offset1:195\n" \
    "v_mfma_f32_16x16x32_bf16 v[36:39], v[108:111], v[120:123], v[36:39]\n" \
    "ds_read2_b32 v[108:109], v161 offset0:48 offset1:49\n" \
    "v_mfma_f32_16x16x32_bf16 v[44:47], v[112:115], v[124:127], v[44:47]\n" \
    "ds_read2_b32 v[110:111], v161 offset0:50 offset1:51\n" \
    "v_mfma_f32_16x16x32_bf16 v[40:43], v[112:115], v[120:123], v[40:43]\n" \
    "ds_read2_b32 v[112:113], v160 offset0:192 offset1:193\n" \
    "v_mfma_f32_16x16x32_bf16 v[44:47], v[116:119], v[120:123], v[44:47]\n" \
    "ds_read2_b32 v[114:115], v160 offset0:194 offset1:195\n" \
    "v_mfma_f32_16x16x32_bf16 v[16:19], v[88:91], v[152:155], v[16:19]\n" \
    "ds_read2_b32 v[116:117], v160 offset0:48 offset1:49\n" \
    "v_mfma_f32_16x16x32_bf16 v[20:23], v[88:91], v[156:159], v[20:23]\n" \
    "ds_read2_b32 v[118:119], v160 offset0:50 offset1:51\n" \
    "s_waitcnt lgkmcnt(15)\n" \
    "v_mfma_f32_16x16x32_bf16 v[20:23], v[92:95], v[152:155], v[20:23]\n" \
    "ds_read_b32 v50, %[ctl] offset:52\n" \
    "s_waitcnt lgkmcnt(8)\n" \
    "v_mfma_f32_16x16x32_bf16 v[24:27], v[88:91], v[128:131], v[24:27]\n" \
    "v_fmac_f32 v44, %[dd], v84\n" \
    "v_fmac_f32 v45, %[dd], v85\n" \
    "v_mfma_f32_16x16x32_bf16 v[24:27], v[92:95], v[156:159], v[24:27]\n" \
    "v_fmac_f32 v46, %[dd], v86\n" \
    "v_fmac_f32 v47, %[dd], v87\n" \
    "v_mfma_f32_16x16x32_bf16 v[24:27], v[96:99], v[152:155], v[24:27]\n" \
    "v_mfma_f32_16x16x32_bf16 v[28:31], v[88:91], v[132:135], v[28:31]\n" \
    "v_cvt_pk_bf16_f32 v52, v16, v17\n" \
    "v_cvt_pk_bf16_f32 v54, v20, v21\n" \
    "v_mfma_f32_16x16x32_bf16 v[28:31], v[92:95], v[128:131], v[28:31]\n" \
    "v_cvt_pk_bf16_f32 v53, v18, v19\n" \
    "v_cvt_pk_bf16_f32 v55, v22, v23\n" \
    "v_mfma_f32_16x16x32_bf16 v[28:31], v[96:99], v[156:159], v[28:31]\n" \
    "v_min_u32 v52, -2, v52\n" \
    "v_min_u32 v54, -2, v54\n" \
    "v_mfma_f32_16x16x32_bf16 v[28:31], v[100:103], v[152:155], v[28:31]\n" \
    "v_min_u32 v53, -2, v53\n" \
    "v_min_u32 v55, -2, v55\n" \
    "v_mfma_f32_16x16x32_bf16 v[32:35], v[88:91], v[136:139], v[32:35]\n" \
    "v_permlane16_swap_b32 v52, v54\n" \
    "v_add_u32 v68, 0, %[yo]\n" \
    "v_mfma_f32_16x16x32_bf16 v[32:35], v[92:95], v[132:135], v[32:35]\n" \
    "v_permlane16_swap_b32 v53, v55\n" \
    "v_mfma_f32_16x16x32_bf16 v[32:35], v[96:99], v[128:131], v[32:35]\n" \
    "s_waitcnt lgkmcnt(0)\n" \
    "v_readfirstlane_b32 %[tmp], v50\n" \
    "s_mov_b32 %[cnt], 0\n" \
    "s_cmp_lg_u32 %[tmp], 0\n" \
    "s_cbranch_scc1 Lok_%=\n" \
    "Ls2_%=:\n" \
    "ds_read_b32 v50, %[ctl] offset:52\n" \
    "s_add_u32 %[cnt], %[cnt], 1\n" \
    "s_waitcnt lgkmcnt(0)\n" \
    "v_readfirstlane_b32 %[tmp], v50\n" \
    "s_cmp_ge_u32 %[cnt], %[cap]\n" \
    "s_cbranch_scc1 Lok_%=\n" \
    "s_cmp_eq_u32 %[tmp], 0\n" \
    "s_cbranch_scc1 Ls2_%=\n" \
    "Lok_%=:\n" \
    "global_store_dwordx4 v68, v[52:55], %[wsp] sc1\n" \
    "v_mfma_f32_16x16x32_bf16 v[32:35], v[100:103], v[156:159], v[32:35]\n" \
    "v_cvt_pk_bf16_f32 v56, v24, v25\n" \
    "v_cvt_pk_bf16_f32 v58, v28, v29\n" \
    "v_mfma_f32_16x16x32_bf16 v[32:35], v[104:107], v[152:155], v[32:35]\n" \
    "v_cvt_pk_bf16_f32 v57, v26, v27\n" \
    "v_cvt_pk_bf16_f32 v59, v30, v31\n" \
    "v_mfma_f32_16x16x32_bf16 v[36:39], v[88:91], v[140:143], v[36:39]\n" \
    "v_min_u32 v56, -2, v56\n" \
    "v_min_u32 v58, -2, v58\n" \
    "v_mfma_f32_16x16x32_bf16 v[36:39], v[92:95], v[136:139], v[36:39]\n" \
    "v_min_u32 v57, -2, v57\n" \
    "v_min_u32 v59, -2, v59\n" \
    "v_mfma_f32_16x16x32_bf16 v[36:39], v[96:99], v[132:135], v[36:39]\n" \
    "v_permlane16_swap_b32 v56, v58\n" \
    "v_add_u32 v69, 655360, %[yo]\n" \
    "v_mfma_f32_16x16x32_bf16 v[36:39], v[100:103], v[128:131], v[36:39]\n" \
    "v_permlane16_swap_b32 v57, v59\n" \
    "v_mfma_f32_16x16x32_bf16 v[36:39], v[104:107], v[156:159], v[36:39]\n" \
    "global_store_dwordx4 v69, v[56:59], %[wsp] sc1\n" \
    "v_mfma_f32_16x16x32_bf16 v[36:39], v[108:111], v[152:155], v[36:39]\n" \
    "v_mfma_f32_16x16x32_bf16 v[40:43], v[88:91], v[144:147], v[40:43]\n" \
    "v_mfma_f32_16x16x32_bf16 v[40:43], v[92:95], v[140:143], v[40:43]\n" \
    "v_mfma_f32_16x16x32_bf16 v[40:43], v[96:99], v[136:139], v[40:43]\n" \
    "v_mfma_f32_16x16x32_bf16 v[40:43], v[100:103], v[132:135], v[40:43]\n" \
    "v_cvt_pk_bf16_f32 v60, v32, v33\n" \
    "v_cvt_pk_bf16_f32 v62, v36, v37\n" \
    "v_mfma_f32_16x16x32_bf16 v[40:43], v[104:107], v[128:131], v[40:43]\n" \
    "v_cvt_pk_bf16_f32 v61, v34, v35\n" \
    "v_cvt_pk_bf16_f32 v63, v38, v39\n" \
    "v_mfma_f32_16x16x32_bf16 v[40:43], v[108:111], v[156:159], v[40:43]\n" \
    "v_min_u32 v60, -2, v60\n" \
    "v_min_u32 v62, -2, v62\n" \
    "v_mfma_f32_16x16x32_bf16 v[40:43], v[112:115], v[152:155], v[40:43]\n" \
    "v_min_u32 v61, -2, v61\n" \
    "v_min_u32 v63, -2, v63\n" \
    "v_mfma_f32_16x16x32_bf16 v[44:47], v[88:91], v[148:151], v[44:47]\n" \
    "v_permlane16_swap_b32 v60, v62\n" \
    "v_add_u32 v70, 1310720, %[yo]\n" \
    "v_mfma_f32_16x16x32_bf16 v[44:47], v[92:95], v[144:147], v[44:47]\n" \
    "v_permlane16_swap_b32 v61, v63\n" \
    "v_mfma_f32_16x16x32_bf16 v[44:47], v[96:99], v[140:143], v[44:47]\n" \
    "global_store_dwordx4 v70, v[60:63], %[wsp] sc1\n" \
    "v_mfma_f32_16x16x32_bf16 v[44:47], v[100:103], v[136:139], v[44:47]\n" \
    "v_mfma_f32_16x16x32_bf16 v[44:47], v[104:107], v[132:135], v[44:47]\n" \
    "v_mfma_f32_16x16x32_bf16 v[44:47], v[108:111], v[128:131], v[44:47]\n" \
    "v_mfma_f32_16x16x32_bf16 v[44:47], v[112:115], v[156:159], v[44:47]\n" \
    "v_mfma_f32_16x16x32_bf16 v[44:47], v[116:119], v[152:155], v[44:47]\n" \
    "s_nop 7\n" \
    "v_cvt_pk_bf16_f32 v64, v40, v41\n" \
    "v_cvt_pk_bf16_f32 v66, v44, v45\n" \
    "v_cvt_pk_bf16_f32 v65, v42, v43\n" \
    "v_cvt_pk_bf16_f32 v67, v46, v47\n" \
    "v_min_u32 v64, -2, v64\n" \
    "v_min_u32 v66, -2, v66\n" \
    "v_min_u32 v65, -2, v65\n" \
    "v_min_u32 v67, -2, v67\n" \
    "v_permlane16_swap_b32 v64, v66\n" \
    "v_add_u32 v71, 1966080, %[yo]\n" \
    "v_permlane16_swap_b32 v65, v67\n" \
    "global_store_dwordx4 v71, v[64:67], %[wsp] sc1\n" \
    "s_nop 1\n"
#define CONV_CLOB "v16", "v17", "v18", "v19", "v20", "v21", "v22", "v23", "v24", "v25", "v26", "v27", "v28", "v29", "v30", "v31", "v32", "v33", "v34", "v35", "v36", "v37", "v38", "v39", "v40", "v41", "v42", "v43", "v44", "v45", "v46", "v47", "v50", "v51", "v52", "v53", "v54", "v55", "v56", "v57", "v58", "v59", "v60", "v61", "v62", "v63", "v64", "v65", "v66", "v67", "v68", "v69", "v70", "v71", "v72", "v73", "v74", "v75", "v76", "v77", "v78", "v79", "v80", "v81", "v82", "v83", "v84", "v85", "v86", "v87", "v88", "v89", "v90", "v91", "v92", "v93", "v94", "v95", "v96", "v97", "v98", "v99", "v100", "v101", "v102", "v103", "v104", "v105", "v106", "v107", "v108", "v109", "v110", "v111", "v112", "v113", "v114", "v115", "v116", "v117", "v118", "v119", "v120", "v121", "v122", "v123", "v124", "v125", "v126", "v127", "v128", "v129", "v130", "v131", "v132", "v133", "v134", "v135", "v136", "v137", "v138", "v139", "v140", "v141", "v142", "v143", "v144", "v145", "v146", "v147", "v148", "v149", "v150", "v151", "v152", "v153", "v154", "v155", "v156", "v157", "v158", "v159", "v160", "v161", "v162", "v163", "scc", "memory"

#define G_ASM \
    "v_add_u32 v154, 67584, %[wp0]\n" \
    "v_add_u32 v155, 67584, %[wp1]\n" \
    "v_add_u32 v156, 67584, %[wp2]\n" \
    "v_add_u32 v157, 67584, %[wp3]\n" \
    "s_mov_b32 s60, 0x11111111\n" \
    "s_mov_b32 s61, 0x11111111\n" \
    "s_mov_b32 s62, 0x22222222\n" \
    "s_mov_b32 s63, 0x22222222\n" \
    "s_mov_b32 s64, 0x44444444\n" \
    "s_mov_b32 s65, 0x44444444\n" \
    "s_mov_b32 s66, 0x88888888\n" \
    "s_mov_b32 s67, 0x88888888\n" \
    "s_mov_b32 s68, -1\n" \
    "s_mov_b32 s69, 0\n" \
    "s_cmp_eq_u32 %[half], 0\n" \
    "s_waitcnt lgkmcnt(0)\n" \
    "s_cbranch_scc0 Llate_%=\n" \
    "s_barrier\n" \
    "ds_read_b64_tr_b16 v[126:127], %[wp1] offset:0\n" \
    "ds_read_b64_tr_b16 v[128:129], %[wp1] offset:2048\n" \
    "ds_read_b128 v[98:101], %[ap0] offset:53248\n" \
    "ds_read_b64_tr_b16 v[130:131], %[wp2] offset:0\n" \
    "ds_read_b64_tr_b16 v[132:133], %[wp2] offset:2048\n" \
    "ds_read_b64_tr_b16 v[134:135], %[wp3] offset:0\n" \
    "ds_read_b64_tr_b16 v[136:137], %[wp3] offset:2048\n" \
    "ds_read_b128 v[102:105], %[ap0] offset:55296\n" \
    "ds_read_b64_tr_b16 v[122:123], %[wp0] offset:0\n" \
    "ds_read_b64_tr_b16 v[124:125], %[wp0] offset:2048\n" \
    "ds_read_b128 v[106:109], %[ap0] offset:57344\n" \
    "ds_read_b128 v[110:113], %[ap0] offset:59392\n" \
    "ds_read_b128 v[114:117], %[ap0] offset:61440\n" \
    "s_waitcnt lgkmcnt(10)\n" \
    "v_mfma_f32_16x16x32_bf16 v[38:41], v[126:129], v[98:101], 0\n" \
    "ds_read_b64_tr_b16 v[142:143], %[wp1] offset:34816\n" \
    "s_waitcnt lgkmcnt(9)\n" \
    "v_mfma_f32_16x16x32_bf16 v[58:61], v[98:101], v[130:133], 0\n" \
    "ds_read_b64_tr_b16 v[144:145], %[wp1] offset:36864\n" \
    "s_waitcnt lgkmcnt(8)\n" \
    "v_mfma_f32_16x16x32_bf16 v[78:81], v[134:137], v[98:101], 0\n" \
    "ds_read_b128 v[98:101], %[ap1] offset:53248\n" \
    "s_waitcnt lgkmcnt(8)\n" \
    "v_mfma_f32_16x16x32_bf16 v[42:45], v[126:129], v[102:105], 0\n" \
    "ds_read_b64_tr_b16 v[146:147], %[wp2] offset:34816\n" \
    "v_mfma_f32_16x16x32_bf16 v[62:65], v[102:105], v[130:133], 0\n" \
    "ds_read_b64_tr_b16 v[148:149], %[wp2] offset:36864\n" \
    "v_mfma_f32_16x16x32_bf16 v[82:85], v[134:137], v[102:105], 0\n" \
    "ds_read_b64_tr_b16 v[150:151], %[wp3] offset:34816\n" \
    "s_waitcnt lgkmcnt(9)\n" \
    "v_mfma_f32_16x16x32_bf16 v[22:25], v[122:125], v[102:105], 0\n" \
    "ds_read_b128 v[102:105], %[ap1] offset:55296\n" \
    "s_waitcnt lgkmcnt(9)\n" \
    "v_mfma_f32_16x16x32_bf16 v[46:49], v[126:129], v[106:109], 0\n" \
    "ds_read_b64_tr_b16 v[152:153], %[wp3] offset:36864\n" \
    "v_mfma_f32_16x16x32_bf16 v[66:69], v[106:109], v[130:133], 0\n" \
    "ds_read_b64_tr_b16 v[138:139], %[wp0] offset:34816\n" \
    "v_mfma_f32_16x16x32_bf16 v[86:89], v[134:137], v[106:109], 0\n" \
    "ds_read_b64_tr_b16 v[140:141], %[wp0] offset:36864\n" \
    "v_mfma_f32_16x16x32_bf16 v[26:29], v[122:125], v[106:109], 0\n" \
    "ds_read_b128 v[106:109], %[ap1] offset:57344\n" \
    "s_waitcnt lgkmcnt(12)\n" \
    "v_mfma_f32_16x16x32_bf16 v[50:53], v[126:129], v[110:113], 0\n" \
    "ds_read_b128 v[118:121], %[ap1] offset:61440\n" \
    "v_mfma_f32_16x16x32_bf16 v[70:73], v[110:113], v[130:133], 0\n" \
    "v_mfma_f32_16x16x32_bf16 v[90:93], v[134:137], v[110:113], 0\n" \
    "v_mfma_f32_16x16x32_bf16 v[30:33], v[122:125], v[110:113], 0\n" \
    "ds_read_b128 v[110:113], %[ap1] offset:59392\n" \
    "s_waitcnt lgkmcnt(13)\n" \
    "v_mfma_f32_16x16x32_bf16 v[54:57], v[126:129], v[114:117], 0\n" \
    "s_barrier\n" \
    "v_mfma_f32_16x16x32_bf16 v[74:77], v[114:117], v[130:133], 0\n" \
    "v_mfma_f32_16x16x32_bf16 v[94:97], v[134:137], v[114:117], 0\n" \
    "v_mfma_f32_16x16x32_bf16 v[34:37], v[122:125], v[114:117], 0\n" \
    "s_waitcnt lgkmcnt(10)\n" \
    "v_mfma_f32_16x16x32_bf16 v[38:41], v[142:145], v[98:101], v[38:41]\n" \
    "ds_read_b64_tr_b16 v[126:127], %[wp1] offset:51200\n" \
    "s_waitcnt lgkmcnt(9)\n" \
    "v_mfma_f32_16x16x32_bf16 v[58:61], v[98:101], v[146:149], v[58:61]\n" \
    "ds_read_b64_tr_b16 v[128:129], %[wp1] offset:53248\n" \
    "s_waitcnt lgkmcnt(7)\n" \
    "v_mfma_f32_16x16x32_bf16 v[78:81], v[150:153], v[98:101], v[78:81]\n" \
    "ds_read_b128 v[98:101], %[ap0] offset:0\n" \
    "v_mfma_f32_16x16x32_bf16 v[42:45], v[142:145], v[102:105], v[42:45]\n" \
    "ds_read_b64_tr_b16 v[130:131], %[wp2] offset:51200\n" \
    "v_mfma_f32_16x16x32_bf16 v[62:65], v[102:105], v[146:149], v[62:65]\n" \
    "ds_read_b64_tr_b16 v[132:133], %[wp2] offset:53248\n" \
    "v_mfma_f32_16x16x32_bf16 v[82:85], v[150:153], v[102:105], v[82:85]\n" \
    "ds_read_b64_tr_b16 v[134:135], %[wp3] offset:51200\n" \
    "s_waitcnt lgkmcnt(9)\n" \
    "v_mfma_f32_16x16x32_bf16 v[22:25], v[138:141], v[102:105], v[22:25]\n" \
    "ds_read_b128 v[102:105], %[ap0] offset:2048\n" \
    "s_waitcnt lgkmcnt(9)\n" \
    "v_mfma_f32_16x16x32_bf16 v[46:49], v[142:145], v[106:109], v[46:49]\n" \
    "ds_read_b64_tr_b16 v[136:137], %[wp3] offset:53248\n" \
    "v_mfma_f32_16x16x32_bf16 v[66:69], v[106:109], v[146:149], v[66:69]\n" \
    "ds_read_b64_tr_b16 v[122:123], %[wp0] offset:51200\n" \
    "v_mfma_f32_16x16x32_bf16 v[86:89], v[150:153], v[106:109], v[86:89]\n" \
    "ds_read_b64_tr_b16 v[124:125], %[wp0] offset:53248\n" \
    "v_mfma_f32_16x16x32_bf16 v[26:29], v[138:141], v[106:109], v[26:29]\n" \
    "ds_read_b128 v[106:109], %[ap0] offset:4096\n" \
    "s_waitcnt lgkmcnt(11)\n" \
    "v_mfma_f32_16x16x32_bf16 v[50:53], v[142:145], v[110:113], v[50:53]\n" \
    "ds_read_b128 v[114:117], %[ap0] offset:8192\n" \
    "v_mfma_f32_16x16x32_bf16 v[70:73], v[110:113], v[146:149], v[70:73]\n" \
    "v_mfma_f32_16x16x32_bf16 v[90:93], v[150:153], v[110:113], v[90:93]\n" \
    "v_mfma_f32_16x16x32_bf16 v[30:33], v[138:141], v[110:113], v[30:33]\n" \
    "ds_read_b128 v[110:113], %[ap0] offset:6144\n" \
    "v_mfma_f32_16x16x32_bf16 v[54:57], v[142:145], v[118:121], v[54:57]\n" \
    "s_barrier\n" \
    "v_mfma_f32_16x16x32_bf16 v[74:77], v[118:121], v[146:149], v[74:77]\n" \
    "v_mfma_f32_16x16x32_bf16 v[94:97], v[150:153], v[118:121], v[94:97]\n" \
    "v_mfma_f32_16x16x32_bf16 v[34:37], v[138:141], v[118:121], v[34:37]\n" \
    "s_waitcnt lgkmcnt(10)\n" \
    "v_mfma_f32_16x16x32_bf16 v[38:41], v[126:129], v[98:101], v[38:41]\n" \
    "ds_read_b64_tr_b16 v[142:143], v155 offset:0\n" \
    "s_waitcnt lgkmcnt(9)\n" \
    "v_mfma_f32_16x16x32_bf16 v[58:61], v[98:101], v[130:133], v[58:61]\n" \
    "ds_read_b64_tr_b16 v[144:145], v155 offset:2048\n" \
    "s_waitcnt lgkmcnt(7)\n" \
    "v_mfma_f32_16x16x32_bf16 v[78:81], v[134:137], v[98:101], v[78:81]\n" \
    "ds_read_b128 v[98:101], %[ap1] offset:0\n" \
    "v_mfma_f32_16x16x32_bf16 v[42:45], v[126:129], v[102:105], v[42:45]\n" \
    "ds_read_b64_tr_b16 v[146:147], v156 offset:0\n" \
    "v_mfma_f32_16x16x32_bf16 v[62:65], v[102:105], v[130:133], v[62:65]\n" \
    "ds_read_b64_tr_b16 v[148:149], v156 offset:2048\n" \
    "v_mfma_f32_16x16x32_bf16 v[82:85], v[134:137], v[102:105], v[82:85]\n" \
    "ds_read_b64_tr_b16 v[150:151], v157 offset:0\n" \
    "s_waitcnt lgkmcnt(9)\n" \
    "v_mfma_f32_16x16x32_bf16 v[22:25], v[122:125], v[102:105], v[22:25]\n" \
    "ds_read_b128 v[102:105], %[ap1] offset:2048\n" \
    "s_waitcnt lgkmcnt(9)\n" \
    "v_mfma_f32_16x16x32_bf16 v[46:49], v[126:129], v[106:109], v[46:49]\n" \
    "ds_read_b64_tr_b16 v[152:153], v157 offset:2048\n" \
    "v_mfma_f32_16x16x32_bf16 v[66:69], v[106:109], v[130:133], v[66:69]\n" \
    "ds_read_b64_tr_b16 v[138:139], v154 offset:0\n" \
    "v_mfma_f32_16x16x32_bf16 v[86:89], v[134:137], v[106:109], v[86:89]\n" \
    "ds_read_b64_tr_b16 v[140:141], v154 offset:2048\n" \
    "v_mfma_f32_16x16x32_bf16 v[26:29], v[122:125], v[106:109], v[26:29]\n" \
    "ds_read_b128 v[106:109], %[ap1] offset:4096\n" \
    "s_waitcnt lgkmcnt(11)\n" \
    "v_mfma_f32_16x16x32_bf16 v[50:53], v[126:129], v[110:113], v[50:53]\n" \
    "ds_read_b128 v[118:121], %[ap1] offset:8192\n" \
    "v_mfma_f32_16x16x32_bf16 v[70:73], v[110:113], v[130:133], v[70:73]\n" \
    "v_mfma_f32_16x16x32_bf16 v[90:93], v[134:137], v[110:113], v[90:93]\n" \
    "v_mfma_f32_16x16x32_bf16 v[30:33], v[122:125], v[110:113], v[30:33]\n" \
    "ds_read_b128 v[110:113], %[ap1] offset:6144\n" \
    "v_mfma_f32_16x16x32_bf16 v[54:57], v[126:129], v[114:117], v[54:57]\n" \
    "s_barrier\n" \
    "v_mfma_f32_16x16x32_bf16 v[74:77], v[114:117], v[130:133], v[74:77]\n" \
    "v_mfma_f32_16x16x32_bf16 v[94:97], v[134:137], v[114:117], v[94:97]\n" \
    "v_mfma_f32_16x16x32_bf16 v[34:37], v[122:125], v[114:117], v[34:37]\n" \
    "s_waitcnt lgkmcnt(10)\n" \
    "v_mfma_f32_16x16x32_bf16 v[38:41], v[142:145], v[98:101], v[38:41]\n" \
    "ds_read_b64_tr_b16 v[126:127], v155 offset:16384\n" \
    "s_waitcnt lgkmcnt(9)\n" \
    "v_mfma_f32_16x16x32_bf16 v[58:61], v[98:101], v[146:149], v[58:61]\n" \
    "ds_read_b64_tr_b16 v[128:129], v155 offset:18432\n" \
    "s_waitcnt lgkmcnt(7)\n" \
    "v_mfma_f32_16x16x32_bf16 v[78:81], v[150:153], v[98:101], v[78:81]\n" \
    "ds_read_b128 v[98:101], %[ap0] offset:18432\n" \
    "v_mfma_f32_16x16x32_bf16 v[42:45], v[142:145], v[102:105], v[42:45]\n" \
    "ds_read_b64_tr_b16 v[130:131], v156 offset:16384\n" \
    "v_mfma_f32_16x16x32_bf16 v[62:65], v[102:105], v[146:149], v[62:65]\n" \
    "ds_read_b64_tr_b16 v[132:133], v156 offset:18432\n" \
    "v_mfma_f32_16x16x32_bf16 v[82:85], v[150:153], v[102:105], v[82:85]\n" \
    "ds_read_b64_tr_b16 v[134:135], v157 offset:16384\n" \
    "s_waitcnt lgkmcnt(9)\n" \
    "v_mfma_f32_16x16x32_bf16 v[22:25], v[138:141], v[102:105], v[22:25]\n" \
    "ds_read_b128 v[102:105], %[ap0] offset:20480\n" \
    "s_waitcnt lgkmcnt(9)\n" \
    "v_mfma_f32_16x16x32_bf16 v[46:49], v[142:145], v[106:109], v[46:49]\n" \
    "ds_read_b64_tr_b16 v[136:137], v157 offset:18432\n" \
    "v_mfma_f32_16x16x32_bf16 v[66:69], v[106:109], v[146:149], v[66:69]\n" \
    "ds_read_b64_tr_b16 v[122:123], v154 offset:16384\n" \
    "v_mfma_f32_16x16x32_bf16 v[86:89], v[150:153], v[106:109], v[86:89]\n" \
    "ds_read_b64_tr_b16 v[124:125], v154 offset:18432\n" \
    "v_mfma_f32_16x16x32_bf16 v[26:29], v[138:141], v[106:109], v[26:29]\n" \
    "ds_read_b128 v[106:109], %[ap0] offset:22528\n" \
    "s_waitcnt lgkmcnt(11)\n" \
    "v_mfma_f32_16x16x32_bf16 v[50:53], v[142:145], v[110:113], v[50:53]\n" \
    "ds_read_b128 v[114:117], %[ap0] offset:26624\n" \
    "v_mfma_f32_16x16x32_bf16 v[70:73], v[110:113], v[146:149], v[70:73]\n" \
    "v_mfma_f32_16x16x32_bf16 v[90:93], v[150:153], v[110:113], v[90:93]\n" \
    "v_mfma_f32_16x16x32_bf16 v[30:33], v[138:141], v[110:113], v[30:33]\n" \
    "ds_read_b128 v[110:113], %[ap0] offset:24576\n" \
    "v_mfma_f32_16x16x32_bf16 v[54:57], v[142:145], v[118:121], v[54:57]\n" \
    "s_barrier\n" \
    "v_mfma_f32_16x16x32_bf16 v[74:77], v[118:121], v[146:149], v[74:77]\n" \
    "v_mfma_f32_16x16x32_bf16 v[94:97], v[150:153], v[118:121], v[94:97]\n" \
    "v_mfma_f32_16x16x32_bf16 v[34:37], v[138:141], v[118:121], v[34:37]\n" \
    "s_waitcnt lgkmcnt(10)\n" \
    "v_mfma_f32_16x16x32_bf16 v[38:41], v[126:129], v[98:101], v[38:41]\n" \
    "ds_read_b64_tr_b16 v[142:143], v155 offset:32768\n" \
    "s_waitcnt lgkmcnt(9)\n" \
    "v_mfma_f32_16x16x32_bf16 v[58:61], v[98:101], v[130:133], v[58:61]\n" \
    "ds_read_b64_tr_b16 v[144:145], v155 offset:34816\n" \
    "s_waitcnt lgkmcnt(7)\n" \
    "v_mfma_f32_16x16x32_bf16 v[78:81], v[134:137], v[98:101], v[78:81]\n" \
    "ds_read_b128 v[98:101], %[ap1] offset:18432\n" \
    "v_mfma_f32_16x16x32_bf16 v[42:45], v[126:129], v[102:105], v[42:45]\n" \
    "ds_read_b64_tr_b16 v[146:147], v156 offset:32768\n" \
    "v_mfma_f32_16x16x32_bf16 v[62:65], v[102:105], v[130:133], v[62:65]\n" \
    "ds_read_b64_tr_b16 v[148:149], v156 offset:34816\n" \
    "v_mfma_f32_16x16x32_bf16 v[82:85], v[134:137], v[102:105], v[82:85]\n" \
    "ds_read_b64_tr_b16 v[150:151], v157 offset:32768\n" \
    "s_waitcnt lgkmcnt(9)\n" \
    "v_mfma_f32_16x16x32_bf16 v[22:25], v[122:125], v[102:105], v[22:25]\n" \
    "ds_read_b128 v[102:105], %[ap1] offset:20480\n" \
    "s_waitcnt lgkmcnt(9)\n" \
    "v_mfma_f32_16x16x32_bf16 v[46:49], v[126:129], v[106:109], v[46:49]\n" \
    "ds_read_b64_tr_b16 v[152:153], v157 offset:34816\n" \
    "v_mfma_f32_16x16x32_bf16 v[66:69], v[106:109], v[130:133], v[66:69]\n" \
    "ds_read_b64_tr_b16 v[138:139], v154 offset:32768\n" \
    "v_mfma_f32_16x16x32_bf16 v[86:89], v[134:137], v[106:109], v[86:89]\n" \
    "ds_read_b64_tr_b16 v[140:141], v154 offset:34816\n" \
    "v_mfma_f32_16x16x32_bf16 v[26:29], v[122:125], v[106:109], v[26:29]\n" \
    "ds_read_b128 v[106:109], %[ap1] offset:22528\n" \
    "s_waitcnt lgkmcnt(11)\n" \
    "v_mfma_f32_16x16x32_bf16 v[50:53], v[126:129], v[110:113], v[50:53]\n" \
    "ds_read_b128 v[118:121], %[ap1] offset:26624\n" \
    "v_mfma_f32_16x16x32_bf16 v[70:73], v[110:113], v[130:133], v[70:73]\n" \
    "v_mfma_f32_16x16x32_bf16 v[90:93], v[134:137], v[110:113], v[90:93]\n" \
    "v_mfma_f32_16x16x32_bf16 v[30:33], v[122:125], v[110:113], v[30:33]\n" \
    "ds_read_b128 v[110:113], %[ap1] offset:24576\n" \
    "v_mfma_f32_16x16x32_bf16 v[54:57], v[126:129], v[114:117], v[54:57]\n" \
    "s_barrier\n" \
    "v_mfma_f32_16x16x32_bf16 v[74:77], v[114:117], v[130:133], v[74:77]\n" \
    "v_mfma_f32_16x16x32_bf16 v[94:97], v[134:137], v[114:117], v[94:97]\n" \
    "v_mfma_f32_16x16x32_bf16 v[34:37], v[122:125], v[114:117], v[34:37]\n" \
    "s_waitcnt lgkmcnt(10)\n" \
    "v_mfma_f32_16x16x32_bf16 v[38:41], v[142:145], v[98:101], v[38:41]\n" \
    "ds_read_b64_tr_b16 v[126:127], %[wp1] offset:0\n" \
    "s_waitcnt lgkmcnt(9)\n" \
    "v_mfma_f32_16x16x32_bf16 v[58:61], v[98:101], v[146:149], v[58:61]\n" \
    "ds_read_b64_tr_b16 v[128:129], %[wp1] offset:2048\n" \
    "s_waitcnt lgkmcnt(7)\n" \
    "v_mfma_f32_16x16x32_bf16 v[78:81], v[150:153], v[98:101], v[78:81]\n" \
    "ds_read_b128 v[98:101], %[ap0] offset:53248\n" \
    "v_mfma_f32_16x16x32_bf16 v[42:45], v[142:145], v[102:105], v[42:45]\n" \
    "ds_read_b64_tr_b16 v[130:131], %[wp2] offset:0\n" \
    "v_mfma_f32_16x16x32_bf16 v[62:65], v[102:105], v[146:149], v[62:65]\n" \
    "ds_read_b64_tr_b16 v[132:133], %[wp2] offset:2048\n" \
    "v_mfma_f32_16x16x32_bf16 v[82:85], v[150:153], v[102:105], v[82:85]\n" \
    "ds_read_b64_tr_b16 v[134:135], %[wp3] offset:0\n" \
    "s_waitcnt lgkmcnt(9)\n" \
    "v_mfma_f32_16x16x32_bf16 v[22:25], v[138:141], v[102:105], v[22:25]\n" \
    "ds_read_b128 v[102:105], %[ap0] offset:55296\n" \
    "s_waitcnt lgkmcnt(9)\n" \
    "v_mfma_f32_16x16x32_bf16 v[46:49], v[142:145], v[106:109], v[46:49]\n" \
    "ds_read_b64_tr_b16 v[136:137], %[wp3] offset:2048\n" \
    "v_mfma_f32_16x16x32_bf16 v[66:69], v[106:109], v[146:149], v[66:69]\n" \
    "ds_read_b64_tr_b16 v[122:123], %[wp0] offset:0\n" \
    "v_mfma_f32_16x16x32_bf16 v[86:89], v[150:153], v[106:109], v[86:89]\n" \
    "ds_read_b64_tr_b16 v[124:125], %[wp0] offset:2048\n" \
    "v_mfma_f32_16x16x32_bf16 v[26:29], v[138:141], v[106:109], v[26:29]\n" \
    "ds_read_b128 v[106:109], %[ap0] offset:57344\n" \
    "s_waitcnt lgkmcnt(11)\n" \
    "v_mfma_f32_16x16x32_bf16 v[50:53], v[142:145], v[110:113], v[50:53]\n" \
    "ds_read_b128 v[114:117], %[ap0] offset:61440\n" \
    "v_mfma_f32_16x16x32_bf16 v[70:73], v[110:113], v[146:149], v[70:73]\n" \
    "v_mfma_f32_16x16x32_bf16 v[90:93], v[150:153], v[110:113], v[90:93]\n" \
    "v_mfma_f32_16x16x32_bf16 v[30:33], v[138:141], v[110:113], v[30:33]\n" \
    "ds_read_b128 v[110:113], %[ap0] offset:59392\n" \
    "v_mfma_f32_16x16x32_bf16 v[54:57], v[142:145], v[118:121], v[54:57]\n" \
    "s_barrier\n" \
    "v_mfma_f32_16x16x32_bf16 v[74:77], v[118:121], v[146:149], v[74:77]\n" \
    "v_mfma_f32_16x16x32_bf16 v[94:97], v[150:153], v[118:121], v[94:97]\n" \
    "v_mfma_f32_16x16x32_bf16 v[34:37], v[138:141], v[118:121], v[34:37]\n" \
    "s_waitcnt lgkmcnt(10)\n" \
    "v_mfma_f32_16x16x32_bf16 v[38:41], v[126:129], v[98:101], v[38:41]\n" \
    "ds_read_b64_tr_b16 v[142:143], %[wp1] offset:34816\n" \
    "s_waitcnt lgkmcnt(9)\n" \
    "v_mfma_f32_16x16x32_bf16 v[58:61], v[98:101], v[130:133], v[58:61]\n" \
    "ds_read_b64_tr_b16 v[144:145], %[wp1] offset:36864\n" \
    "s_waitcnt lgkmcnt(7)\n" \
    "v_mfma_f32_16x16x32_bf16 v[78:81], v[134:137], v[98:101], v[78:81]\n" \
    "ds_read_b128 v[98:101], %[ap1] offset:53248\n" \
    "v_mfma_f32_16x16x32_bf16 v[42:45], v[126:129], v[102:105], v[42:45]\n" \
    "ds_read_b64_tr_b16 v[146:147], %[wp2] offset:34816\n" \
    "v_mfma_f32_16x16x32_bf16 v[62:65], v[102:105], v[130:133], v[62:65]\n" \
    "ds_read_b64_tr_b16 v[148:149], %[wp2] offset:36864\n" \
    "v_mfma_f32_16x16x32_bf16 v[82:85], v[134:137], v[102:105], v[82:85]\n" \
    "ds_read_b64_tr_b16 v[150:151], %[wp3] offset:34816\n" \
    "s_waitcnt lgkmcnt(9)\n" \
    "v_mfma_f32_16x16x32_bf16 v[22:25], v[122:125], v[102:105], v[22:25]\n" \
    "ds_read_b128 v[102:105], %[ap1] offset:55296\n" \
    "s_waitcnt lgkmcnt(9)\n" \
    "v_mfma_f32_16x16x32_bf16 v[46:49], v[126:129], v[106:109], v[46:49]\n" \
    "ds_read_b64_tr_b16 v[152:153], %[wp3] offset:36864\n" \
    "v_mfma_f32_16x16x32_bf16 v[66:69], v[106:109], v[130:133], v[66:69]\n" \
    "ds_read_b64_tr_b16 v[138:139], %[wp0] offset:34816\n" \
    "v_mfma_f32_16x16x32_bf16 v[86:89], v[134:137], v[106:109], v[86:89]\n" \
    "ds_read_b64_tr_b16 v[140:141], %[wp0] offset:36864\n" \
    "v_mfma_f32_16x16x32_bf16 v[26:29], v[122:125], v[106:109], v[26:29]\n" \
    "ds_read_b128 v[106:109], %[ap1] offset:57344\n" \
    "s_waitcnt lgkmcnt(11)\n" \
    "v_mfma_f32_16x16x32_bf16 v[50:53], v[126:129], v[110:113], v[50:53]\n" \
    "ds_read_b128 v[118:121], %[ap1] offset:61440\n" \
    "v_mfma_f32_16x16x32_bf16 v[70:73], v[110:113], v[130:133], v[70:73]\n" \
    "v_mfma_f32_16x16x32_bf16 v[90:93], v[134:137], v[110:113], v[90:93]\n" \
    "v_mfma_f32_16x16x32_bf16 v[30:33], v[122:125], v[110:113], v[30:33]\n" \
    "ds_read_b128 v[110:113], %[ap1] offset:59392\n" \
    "v_mfma_f32_16x16x32_bf16 v[54:57], v[126:129], v[114:117], v[54:57]\n" \
    "s_barrier\n" \
    "v_mfma_f32_16x16x32_bf16 v[74:77], v[114:117], v[130:133], v[74:77]\n" \
    "v_mfma_f32_16x16x32_bf16 v[94:97], v[134:137], v[114:117], v[94:97]\n" \
    "v_mfma_f32_16x16x32_bf16 v[34:37], v[122:125], v[114:117], v[34:37]\n" \
    "s_waitcnt lgkmcnt(10)\n" \
    "v_mfma_f32_16x16x32_bf16 v[38:41], v[142:145], v[98:101], v[38:41]\n" \
    "ds_read_b64_tr_b16 v[126:127], %[wp1] offset:51200\n" \
    "s_waitcnt lgkmcnt(9)\n" \
    "v_mfma_f32_16x16x32_bf16 v[58:61], v[98:101], v[146:149], v[58:61]\n" \
    "ds_read_b64_tr_b16 v[128:129], %[wp1] offset:53248\n" \
    "s_waitcnt lgkmcnt(7)\n" \
    "v_mfma_f32_16x16x32_bf16 v[78:81], v[150:153], v[98:101], v[78:81]\n" \
    "ds_read_b128 v[98:101], %[ap0] offset:0\n" \
    "v_mfma_f32_16x16x32_bf16 v[42:45], v[142:145], v[102:105], v[42:45]\n" \
    "ds_read_b64_tr_b16 v[130:131], %[wp2] offset:51200\n" \
    "v_mfma_f32_16x16x32_bf16 v[62:65], v[102:105], v[146:149], v[62:65]\n" \
    "ds_read_b64_tr_b16 v[132:133], %[wp2] offset:53248\n" \
    "v_mfma_f32_16x16x32_bf16 v[82:85], v[150:153], v[102:105], v[82:85]\n" \
    "ds_read_b64_tr_b16 v[134:135], %[wp3] offset:51200\n" \
    "s_waitcnt lgkmcnt(9)\n" \
    "v_mfma_f32_16x16x32_bf16 v[22:25], v[138:141], v[102:105], v[22:25]\n" \
    "ds_read_b128 v[102:105], %[ap0] offset:2048\n" \
    "s_waitcnt lgkmcnt(9)\n" \
    "v_mfma_f32_16x16x32_bf16 v[46:49], v[142:145], v[106:109], v[46:49]\n" \
    "ds_read_b64_tr_b16 v[136:137], %[wp3] offset:53248\n" \
    "v_mfma_f32_16x16x32_bf16 v[66:69], v[106:109], v[146:149], v[66:69]\n" \
    "ds_read_b64_tr_b16 v[122:123], %[wp0] offset:51200\n" \
    "v_mfma_f32_16x16x32_bf16 v[86:89], v[150:153], v[106:109], v[86:89]\n" \
    "ds_read_b64_tr_b16 v[124:125], %[wp0] offset:53248\n" \
    "v_mfma_f32_16x16x32_bf16 v[26:29], v[138:141], v[106:109], v[26:29]\n" \
    "ds_read_b128 v[106:109], %[ap0] offset:4096\n" \
    "s_waitcnt lgkmcnt(11)\n" \
    "v_mfma_f32_16x16x32_bf16 v[50:53], v[142:145], v[110:113], v[50:53]\n" \
    "ds_read_b128 v[114:117], %[ap0] offset:8192\n" \
    "v_mfma_f32_16x16x32_bf16 v[70:73], v[110:113], v[146:149], v[70:73]\n" \
    "v_mfma_f32_16x16x32_bf16 v[90:93], v[150:153], v[110:113], v[90:93]\n" \
    "v_mfma_f32_16x16x32_bf16 v[30:33], v[138:141], v[110:113], v[30:33]\n" \
    "ds_read_b128 v[110:113], %[ap0] offset:6144\n" \
    "v_mfma_f32_16x16x32_bf16 v[54:57], v[142:145], v[118:121], v[54:57]\n" \
    "s_barrier\n" \
    "v_mfma_f32_16x16x32_bf16 v[74:77], v[118:121], v[146:149], v[74:77]\n" \
    "v_mfma_f32_16x16x32_bf16 v[94:97], v[150:153], v[118:121], v[94:97]\n" \
    "v_mfma_f32_16x16x32_bf16 v[34:37], v[138:141], v[118:121], v[34:37]\n" \
    "s_waitcnt lgkmcnt(10)\n" \
    "v_mfma_f32_16x16x32_bf16 v[38:41], v[126:129], v[98:101], v[38:41]\n" \
    "ds_read_b64_tr_b16 v[142:143], v155 offset:0\n" \
    "s_waitcnt lgkmcnt(9)\n" \
    "v_mfma_f32_16x16x32_bf16 v[58:61], v[98:101], v[130:133], v[58:61]\n" \
    "ds_read_b64_tr_b16 v[144:145], v155 offset:2048\n" \
    "s_waitcnt lgkmcnt(7)\n" \
    "v_mfma_f32_16x16x32_bf16 v[78:81], v[134:137], v[98:101], v[78:81]\n" \
    "ds_read_b128 v[98:101], %[ap1] offset:0\n" \
    "v_mfma_f32_16x16x32_bf16 v[42:45], v[126:129], v[102:105], v[42:45]\n" \
    "ds_read_b64_tr_b16 v[146:147], v156 offset:0\n" \
    "v_mfma_f32_16x16x32_bf16 v[62:65], v[102:105], v[130:133], v[62:65]\n" \
    "ds_read_b64_tr_b16 v[148:149], v156 offset:2048\n" \
    "v_mfma_f32_16x16x32_bf16 v[82:85], v[134:137], v[102:105], v[82:85]\n" \
    "ds_read_b64_tr_b16 v[150:151], v157 offset:0\n" \
    "s_waitcnt lgkmcnt(9)\n" \
    "v_mfma_f32_16x16x32_bf16 v[22:25], v[122:125], v[102:105], v[22:25]\n" \
    "ds_read_b128 v[102:105], %[ap1] offset:2048\n" \
    "s_waitcnt lgkmcnt(9)\n" \
    "v_mfma_f32_16x16x32_bf16 v[46:49], v[126:129], v[106:109], v[46:49]\n" \
    "ds_read_b64_tr_b16 v[152:153], v157 offset:2048\n" \
    "v_mfma_f32_16x16x32_bf16 v[66:69], v[106:109], v[130:133], v[66:69]\n" \
    "ds_read_b64_tr_b16 v[138:139], v154 offset:0\n" \
    "v_mfma_f32_16x16x32_bf16 v[86:89], v[134:137], v[106:109], v[86:89]\n" \
    "ds_read_b64_tr_b16 v[140:141], v154 offset:2048\n" \
    "v_mfma_f32_16x16x32_bf16 v[26:29], v[122:125], v[106:109], v[26:29]\n" \
    "ds_read_b128 v[106:109], %[ap1] offset:4096\n" \
    "s_waitcnt lgkmcnt(11)\n" \
    "v_mfma_f32_16x16x32_bf16 v[50:53], v[126:129], v[110:113], v[50:53]\n" \
    "ds_read_b128 v[118:121], %[ap1] offset:8192\n" \
    "v_mfma_f32_16x16x32_bf16 v[70:73], v[110:113], v[130:133], v[70:73]\n" \
    "v_mfma_f32_16x16x32_bf16 v[90:93], v[134:137], v[110:113], v[90:93]\n" \
    "v_mfma_f32_16x16x32_bf16 v[30:33], v[122:125], v[110:113], v[30:33]\n" \
    "ds_read_b128 v[110:113], %[ap1] offset:6144\n" \
    "v_mfma_f32_16x16x32_bf16 v[54:57], v[126:129], v[114:117], v[54:57]\n" \
    "s_barrier\n" \
    "v_mfma_f32_16x16x32_bf16 v[74:77], v[114:117], v[130:133], v[74:77]\n" \
    "v_mfma_f32_16x16x32_bf16 v[94:97], v[134:137], v[114:117], v[94:97]\n" \
    "v_mfma_f32_16x16x32_bf16 v[34:37], v[122:125], v[114:117], v[34:37]\n" \
    "s_waitcnt lgkmcnt(10)\n" \
    "v_mfma_f32_16x16x32_bf16 v[38:41], v[142:145], v[98:101], v[38:41]\n" \
    "ds_read_b64_tr_b16 v[126:127], v155 offset:16384\n" \
    "s_waitcnt lgkmcnt(9)\n" \
    "v_mfma_f32_16x16x32_bf16 v[58:61], v[98:101], v[146:149], v[58:61]\n" \
    "ds_read_b64_tr_b16 v[128:129], v155 offset:18432\n" \
    "s_waitcnt lgkmcnt(7)\n" \
    "v_mfma_f32_16x16x32_bf16 v[78:81], v[150:153], v[98:101], v[78:81]\n" \
    "ds_read_b128 v[98:101], %[ap0] offset:18432\n" \
    "v_mfma_f32_16x16x32_bf16 v[42:45], v[142:145], v[102:105], v[42:45]\n" \
    "ds_read_b64_tr_b16 v[130:131], v156 offset:16384\n" \
    "v_mfma_f32_16x16x32_bf16 v[62:65], v[102:105], v[146:149], v[62:65]\n" \
    "ds_read_b64_tr_b16 v[132:133], v156 offset:18432\n" \
    "v_mfma_f32_16x16x32_bf16 v[82:85], v[150:153], v[102:105], v[82:85]\n" \
    "ds_read_b64_tr_b16 v[134:135], v157 offset:16384\n" \
    "s_waitcnt lgkmcnt(9)\n" \
    "v_mfma_f32_16x16x32_bf16 v[22:25], v[138:141], v[102:105], v[22:25]\n" \
    "ds_read_b128 v[102:105], %[ap0] offset:20480\n" \
    "s_waitcnt lgkmcnt(9)\n" \
    "v_mfma_f32_16x16x32_bf16 v[46:49], v[142:145], v[106:109], v[46:49]\n" \
    "ds_read_b64_tr_b16 v[136:137], v157 offset:18432\n" \
    "v_mfma_f32_16x16x32_bf16 v[66:69], v[106:109], v[146:149], v[66:69]\n" \
    "ds_read_b64_tr_b16 v[122:123], v154 offset:16384\n" \
    "v_mfma_f32_16x16x32_bf16 v[86:89], v[150:153], v[106:109], v[86:89]\n" \
    "ds_read_b64_tr_b16 v[124:125], v154 offset:18432\n" \
    "v_mfma_f32_16x16x32_bf16 v[26:29], v[138:141], v[106:109], v[26:29]\n" \
    "ds_read_b128 v[106:109], %[ap0] offset:22528\n" \
    "s_waitcnt lgkmcnt(11)\n" \
    "v_mfma_f32_16x16x32_bf16 v[50:53], v[142:145], v[110:113], v[50:53]\n" \
    "ds_read_b128 v[114:117], %[ap0] offset:26624\n" \
    "v_mfma_f32_16x16x32_bf16 v[70:73], v[110:113], v[146:149], v[70:73]\n" \
    "v_mfma_f32_16x16x32_bf16 v[90:93], v[150:153], v[110:113], v[90:93]\n" \
    "v_mfma_f32_16x16x32_bf16 v[30:33], v[138:141], v[110:113], v[30:33]\n" \
    "ds_read_b128 v[110:113], %[ap0] offset:24576\n" \
    "v_mfma_f32_16x16x32_bf16 v[54:57], v[142:145], v[118:121], v[54:57]\n" \
    "s_barrier\n" \
    "v_mfma_f32_16x16x32_bf16 v[74:77], v[118:121], v[146:149], v[74:77]\n" \
    "v_mfma_f32_16x16x32_bf16 v[94:97], v[150:153], v[118:121], v[94:97]\n" \
    "v_mfma_f32_16x16x32_bf16 v[34:37], v[138:141], v[118:121], v[34:37]\n" \
    "s_waitcnt lgkmcnt(10)\n" \
    "v_mfma_f32_16x16x32_bf16 v[38:41], v[126:129], v[98:101], v[38:41]\n" \
    "ds_read_b64_tr_b16 v[142:143], v155 offset:32768\n" \
    "s_waitcnt lgkmcnt(9)\n" \
    "v_mfma_f32_16x16x32_bf16 v[58:61], v[98:101], v[130:133], v[58:61]\n" \
    "ds_read_b64_tr_b16 v[144:145], v155 offset:34816\n" \
    "s_waitcnt lgkmcnt(7)\n" \
    "v_mfma_f32_16x16x32_bf16 v[78:81], v[134:137], v[98:101], v[78:81]\n" \
    "ds_read_b128 v[98:101], %[ap1] offset:18432\n" \
    "v_mfma_f32_16x16x32_bf16 v[42:45], v[126:129], v[102:105], v[42:45]\n" \
    "ds_read_b64_tr_b16 v[146:147], v156 offset:32768\n" \
    "v_mfma_f32_16x16x32_bf16 v[62:65], v[102:105], v[130:133], v[62:65]\n" \
    "ds_read_b64_tr_b16 v[148:149], v156 offset:34816\n" \
    "v_mfma_f32_16x16x32_bf16 v[82:85], v[134:137], v[102:105], v[82:85]\n" \
    "ds_read_b64_tr_b16 v[150:151], v157 offset:32768\n" \
    "s_waitcnt lgkmcnt(9)\n" \
    "v_mfma_f32_16x16x32_bf16 v[22:25], v[122:125], v[102:105], v[22:25]\n" \
    "ds_read_b128 v[102:105], %[ap1] offset:20480\n" \
    "s_waitcnt lgkmcnt(9)\n" \
    "v_mfma_f32_16x16x32_bf16 v[46:49], v[126:129], v[106:109], v[46:49]\n" \
    "ds_read_b64_tr_b16 v[152:153], v157 offset:34816\n" \
    "v_mfma_f32_16x16x32_bf16 v[66:69], v[106:109], v[130:133], v[66:69]\n" \
    "ds_read_b64_tr_b16 v[138:139], v154 offset:32768\n" \
    "v_mfma_f32_16x16x32_bf16 v[86:89], v[134:137], v[106:109], v[86:89]\n" \
    "ds_read_b64_tr_b16 v[140:141], v154 offset:34816\n" \
    "v_mfma_f32_16x16x32_bf16 v[26:29], v[122:125], v[106:109], v[26:29]\n" \
    "ds_read_b128 v[106:109], %[ap1] offset:22528\n" \
    "s_waitcnt lgkmcnt(11)\n" \
    "v_mfma_f32_16x16x32_bf16 v[50:53], v[126:129], v[110:113], v[50:53]\n" \
    "ds_read_b128 v[118:121], %[ap1] offset:26624\n" \
    "v_mfma_f32_16x16x32_bf16 v[70:73], v[110:113], v[130:133], v[70:73]\n" \
    "v_mfma_f32_16x16x32_bf16 v[90:93], v[134:137], v[110:113], v[90:93]\n" \
    "v_mfma_f32_16x16x32_bf16 v[30:33], v[122:125], v[110:113], v[30:33]\n" \
    "ds_read_b128 v[110:113], %[ap1] offset:24576\n" \
    "v_mfma_f32_16x16x32_bf16 v[54:57], v[126:129], v[114:117], v[54:57]\n" \
    "s_barrier\n" \
    "v_mfma_f32_16x16x32_bf16 v[74:77], v[114:117], v[130:133], v[74:77]\n" \
    "v_mfma_f32_16x16x32_bf16 v[94:97], v[134:137], v[114:117], v[94:97]\n" \
    "v_mfma_f32_16x16x32_bf16 v[34:37], v[122:125], v[114:117], v[34:37]\n" \
    "s_waitcnt lgkmcnt(10)\n" \
    "v_mfma_f32_16x16x32_bf16 v[38:41], v[142:145], v[98:101], v[38:41]\n" \
    "ds_read_b64_tr_b16 v[126:127], %[wp1] offset:0\n" \
    "s_waitcnt lgkmcnt(9)\n" \
    "v_mfma_f32_16x16x32_bf16 v[58:61], v[98:101], v[146:149], v[58:61]\n" \
    "ds_read_b64_tr_b16 v[128:129], %[wp1] offset:2048\n" \
    "s_waitcnt lgkmcnt(7)\n" \
    "v_mfma_f32_16x16x32_bf16 v[78:81], v[150:153], v[98:101], v[78:81]\n" \
    "ds_read_b128 v[98:101], %[ap0] offset:53248\n" \
    "v_mfma_f32_16x16x32_bf16 v[42:45], v[142:145], v[102:105], v[42:45]\n" \
    "ds_read_b64_tr_b16 v[130:131], %[wp2] offset:0\n" \
    "v_mfma_f32_16x16x32_bf16 v[62:65], v[102:105], v[146:149], v[62:65]\n" \
    "ds_read_b64_tr_b16 v[132:133], %[wp2] offset:2048\n" \
    "v_mfma_f32_16x16x32_bf16 v[82:85], v[150:153], v[102:105], v[82:85]\n" \
    "ds_read_b64_tr_b16 v[134:135], %[wp3] offset:0\n" \
    "s_waitcnt lgkmcnt(9)\n" \
    "v_mfma_f32_16x16x32_bf16 v[22:25], v[138:141], v[102:105], v[22:25]\n" \
    "ds_read_b128 v[102:105], %[ap0] offset:55296\n" \
    "s_waitcnt lgkmcnt(9)\n" \
    "v_mfma_f32_16x16x32_bf16 v[46:49], v[142:145], v[106:109], v[46:49]\n" \
    "ds_read_b64_tr_b16 v[136:137], %[wp3] offset:2048\n" \
    "v_mfma_f32_16x16x32_bf16 v[66:69], v[106:109], v[146:149], v[66:69]\n" \
    "ds_read_b64_tr_b16 v[122:123], %[wp0] offset:0\n" \
    "v_mfma_f32_16x16x32_bf16 v[86:89], v[150:153], v[106:109], v[86:89]\n" \
    "ds_read_b64_tr_b16 v[124:125], %[wp0] offset:2048\n" \
    "v_mfma_f32_16x16x32_bf16 v[26:29], v[138:141], v[106:109], v[26:29]\n" \
    "ds_read_b128 v[106:109], %[ap0] offset:57344\n" \
    "s_waitcnt lgkmcnt(11)\n" \
    "v_mfma_f32_16x16x32_bf16 v[50:53], v[142:145], v[110:113], v[50:53]\n" \
    "ds_read_b128 v[114:117], %[ap0] offset:61440\n" \
    "v_mfma_f32_16x16x32_bf16 v[70:73], v[110:113], v[146:149], v[70:73]\n" \
    "v_mfma_f32_16x16x32_bf16 v[90:93], v[150:153], v[110:113], v[90:93]\n" \
    "v_mfma_f32_16x16x32_bf16 v[30:33], v[138:141], v[110:113], v[30:33]\n" \
    "ds_read_b128 v[110:113], %[ap0] offset:59392\n" \
    "v_mfma_f32_16x16x32_bf16 v[54:57], v[142:145], v[118:121], v[54:57]\n" \
    "s_barrier\n" \
    "v_mfma_f32_16x16x32_bf16 v[74:77], v[118:121], v[146:149], v[74:77]\n" \
    "v_mfma_f32_16x16x32_bf16 v[94:97], v[150:153], v[118:121], v[94:97]\n" \
    "v_mfma_f32_16x16x32_bf16 v[34:37], v[138:141], v[118:121], v[34:37]\n" \
    "s_waitcnt lgkmcnt(10)\n" \
    "v_mfma_f32_16x16x32_bf16 v[38:41], v[126:129], v[98:101], v[38:41]\n" \
    "ds_read_b64_tr_b16 v[142:143], %[wp1] offset:34816\n" \
    "s_waitcnt lgkmcnt(9)\n" \
    "v_mfma_f32_16x16x32_bf16 v[58:61], v[98:101], v[130:133], v[58:61]\n" \
    "ds_read_b64_tr_b16 v[144:145], %[wp1] offset:36864\n" \
    "s_waitcnt lgkmcnt(7)\n" \
    "v_mfma_f32_16x16x32_bf16 v[78:81], v[134:137], v[98:101], v[78:81]\n" \
    "ds_read_b128 v[98:101], %[ap1] offset:53248\n" \
    "v_mfma_f32_16x16x32_bf16 v[42:45], v[126:129], v[102:105], v[42:45]\n" \
    "ds_read_b64_tr_b16 v[146:147], %[wp2] offset:34816\n" \
    "v_mfma_f32_16x16x32_bf16 v[62:65], v[102:105], v[130:133], v[62:65]\n" \
    "ds_read_b64_tr_b16 v[148:149], %[wp2] offset:36864\n" \
    "v_mfma_f32_16x16x32_bf16 v[82:85], v[134:137], v[102:105], v[82:85]\n" \
    "ds_read_b64_tr_b16 v[150:151], %[wp3] offset:34816\n" \
    "s_waitcnt lgkmcnt(9)\n" \
    "v_mfma_f32_16x16x32_bf16 v[22:25], v[122:125], v[102:105], v[22:25]\n" \
    "ds_read_b128 v[102:105], %[ap1] offset:55296\n" \
    "s_waitcnt lgkmcnt(9)\n" \
    "v_mfma_f32_16x16x32_bf16 v[46:49], v[126:129], v[106:109], v[46:49]\n" \
    "ds_read_b64_tr_b16 v[152:153], %[wp3] offset:36864\n" \
    "v_mfma_f32_16x16x32_bf16 v[66:69], v[106:109], v[130:133], v[66:69]\n" \
    "ds_read_b64_tr_b16 v[138:139], %[wp0] offset:34816\n" \
    "v_mfma_f32_16x16x32_bf16 v[86:89], v[134:137], v[106:109], v[86:89]\n" \
    "ds_read_b64_tr_b16 v[140:141], %[wp0] offset:36864\n" \
    "v_mfma_f32_16x16x32_bf16 v[26:29], v[122:125], v[106:109], v[26:29]\n" \
    "ds_read_b128 v[106:109], %[ap1] offset:57344\n" \
    "s_waitcnt lgkmcnt(11)\n" \
    "v_mfma_f32_16x16x32_bf16 v[50:53], v[126:129], v[110:113], v[50:53]\n" \
    "ds_read_b128 v[118:121], %[ap1] offset:61440\n" \
    "v_mfma_f32_16x16x32_bf16 v[70:73], v[110:113], v[130:133], v[70:73]\n" \
    "v_mfma_f32_16x16x32_bf16 v[90:93], v[134:137], v[110:113], v[90:93]\n" \
    "v_mfma_f32_16x16x32_bf16 v[30:33], v[122:125], v[110:113], v[30:33]\n" \
    "ds_read_b128 v[110:113], %[ap1] offset:59392\n" \
    "v_mfma_f32_16x16x32_bf16 v[54:57], v[126:129], v[114:117], v[54:57]\n" \
    "s_barrier\n" \
    "v_mfma_f32_16x16x32_bf16 v[74:77], v[114:117], v[130:133], v[74:77]\n" \
    "v_mfma_f32_16x16x32_bf16 v[94:97], v[134:137], v[114:117], v[94:97]\n" \
    "v_mfma_f32_16x16x32_bf16 v[34:37], v[122:125], v[114:117], v[34:37]\n" \
    "s_waitcnt lgkmcnt(10)\n" \
    "v_mfma_f32_16x16x32_bf16 v[38:41], v[142:145], v[98:101], v[38:41]\n" \
    "ds_read_b64_tr_b16 v[126:127], %[wp1] offset:51200\n" \
    "s_waitcnt lgkmcnt(9)\n" \
    "v_mfma_f32_16x16x32_bf16 v[58:61], v[98:101], v[146:149], v[58:61]\n" \
    "ds_read_b64_tr_b16 v[128:129], %[wp1] offset:53248\n" \
    "s_waitcnt lgkmcnt(7)\n" \
    "v_mfma_f32_16x16x32_bf16 v[78:81], v[150:153], v[98:101], v[78:81]\n" \
    "ds_read_b128 v[98:101], %[ap0] offset:0\n" \
    "v_mfma_f32_16x16x32_bf16 v[42:45], v[142:145], v[102:105], v[42:45]\n" \
    "ds_read_b64_tr_b16 v[130:131], %[wp2] offset:51200\n" \
    "v_mfma_f32_16x16x32_bf16 v[62:65], v[102:105], v[146:149], v[62:65]\n" \
    "ds_read_b64_tr_b16 v[132:133], %[wp2] offset:53248\n" \
    "v_mfma_f32_16x16x32_bf16 v[82:85], v[150:153], v[102:105], v[82:85]\n" \
    "ds_read_b64_tr_b16 v[134:135], %[wp3] offset:51200\n" \
    "s_waitcnt lgkmcnt(9)\n" \
    "v_mfma_f32_16x16x32_bf16 v[22:25], v[138:141], v[102:105], v[22:25]\n" \
    "ds_read_b128 v[102:105], %[ap0] offset:2048\n" \
    "s_waitcnt lgkmcnt(9)\n" \
    "v_mfma_f32_16x16x32_bf16 v[46:49], v[142:145], v[106:109], v[46:49]\n" \
    "ds_read_b64_tr_b16 v[136:137], %[wp3] offset:53248\n" \
    "v_mfma_f32_16x16x32_bf16 v[66:69], v[106:109], v[146:149], v[66:69]\n" \
    "ds_read_b64_tr_b16 v[122:123], %[wp0] offset:51200\n" \
    "v_mfma_f32_16x16x32_bf16 v[86:89], v[150:153], v[106:109], v[86:89]\n" \
    "ds_read_b64_tr_b16 v[124:125], %[wp0] offset:53248\n" \
    "v_mfma_f32_16x16x32_bf16 v[26:29], v[138:141], v[106:109], v[26:29]\n" \
    "ds_read_b128 v[106:109], %[ap0] offset:4096\n" \
    "s_waitcnt lgkmcnt(11)\n" \
    "v_mfma_f32_16x16x32_bf16 v[50:53], v[142:145], v[110:113], v[50:53]\n" \
    "ds_read_b128 v[114:117], %[ap0] offset:8192\n" \
    "v_mfma_f32_16x16x32_bf16 v[70:73], v[110:113], v[146:149], v[70:73]\n" \
    "v_mfma_f32_16x16x32_bf16 v[90:93], v[150:153], v[110:113], v[90:93]\n" \
    "v_mfma_f32_16x16x32_bf16 v[30:33], v[138:141], v[110:113], v[30:33]\n" \
    "ds_read_b128 v[110:113], %[ap0] offset:6144\n" \
    "v_mfma_f32_16x16x32_bf16 v[54:57], v[142:145], v[118:121], v[54:57]\n" \
    "s_barrier\n" \
    "v_mfma_f32_16x16x32_bf16 v[74:77], v[118:121], v[146:149], v[74:77]\n" \
    "v_mfma_f32_16x16x32_bf16 v[94:97], v[150:153], v[118:121], v[94:97]\n" \
    "v_mfma_f32_16x16x32_bf16 v[34:37], v[138:141], v[118:121], v[34:37]\n" \
    "s_waitcnt lgkmcnt(10)\n" \
    "v_mfma_f32_16x16x32_bf16 v[38:41], v[126:129], v[98:101], v[38:41]\n" \
    "ds_read_b64_tr_b16 v[142:143], v155 offset:0\n" \
    "s_waitcnt lgkmcnt(9)\n" \
    "v_mfma_f32_16x16x32_bf16 v[58:61], v[98:101], v[130:133], v[58:61]\n" \
    "ds_read_b64_tr_b16 v[144:145], v155 offset:2048\n" \
    "s_waitcnt lgkmcnt(7)\n" \
    "v_mfma_f32_16x16x32_bf16 v[78:81], v[134:137], v[98:101], v[78:81]\n" \
    "ds_read_b128 v[98:101], %[ap1] offset:0\n" \
    "v_mfma_f32_16x16x32_bf16 v[42:45], v[126:129], v[102:105], v[42:45]\n" \
    "ds_read_b64_tr_b16 v[146:147], v156 offset:0\n" \
    "v_mfma_f32_16x16x32_bf16 v[62:65], v[102:105], v[130:133], v[62:65]\n" \
    "ds_read_b64_tr_b16 v[148:149], v156 offset:2048\n" \
    "v_mfma_f32_16x16x32_bf16 v[82:85], v[134:137], v[102:105], v[82:85]\n" \
    "ds_read_b64_tr_b16 v[150:151], v157 offset:0\n" \
    "s_waitcnt lgkmcnt(9)\n" \
    "v_mfma_f32_16x16x32_bf16 v[22:25], v[122:125], v[102:105], v[22:25]\n" \
    "ds_read_b128 v[102:105], %[ap1] offset:2048\n" \
    "s_waitcnt lgkmcnt(9)\n" \
    "v_mfma_f32_16x16x32_bf16 v[46:49], v[126:129], v[106:109], v[46:49]\n" \
    "ds_read_b64_tr_b16 v[152:153], v157 offset:2048\n" \
    "v_mfma_f32_16x16x32_bf16 v[66:69], v[106:109], v[130:133], v[66:69]\n" \
    "ds_read_b64_tr_b16 v[138:139], v154 offset:0\n" \
    "v_mfma_f32_16x16x32_bf16 v[86:89], v[134:137], v[106:109], v[86:89]\n" \
    "ds_read_b64_tr_b16 v[140:141], v154 offset:2048\n" \
    "v_mfma_f32_16x16x32_bf16 v[26:29], v[122:125], v[106:109], v[26:29]\n" \
    "ds_read_b128 v[106:109], %[ap1] offset:4096\n" \
    "s_waitcnt lgkmcnt(11)\n" \
    "v_mfma_f32_16x16x32_bf16 v[50:53], v[126:129], v[110:113], v[50:53]\n" \
    "ds_read_b128 v[118:121], %[ap1] offset:8192\n" \
    "v_mfma_f32_16x16x32_bf16 v[70:73], v[110:113], v[130:133], v[70:73]\n" \
    "v_mfma_f32_16x16x32_bf16 v[90:93], v[134:137], v[110:113], v[90:93]\n" \
    "v_mfma_f32_16x16x32_bf16 v[30:33], v[122:125], v[110:113], v[30:33]\n" \
    "ds_read_b128 v[110:113], %[ap1] offset:6144\n" \
    "v_mfma_f32_16x16x32_bf16 v[54:57], v[126:129], v[114:117], v[54:57]\n" \
    "s_barrier\n" \
    "v_mfma_f32_16x16x32_bf16 v[74:77], v[114:117], v[130:133], v[74:77]\n" \
    "v_mfma_f32_16x16x32_bf16 v[94:97], v[134:137], v[114:117], v[94:97]\n" \
    "v_mfma_f32_16x16x32_bf16 v[34:37], v[122:125], v[114:117], v[34:37]\n" \
    "s_waitcnt lgkmcnt(10)\n" \
    "v_mfma_f32_16x16x32_bf16 v[38:41], v[142:145], v[98:101], v[38:41]\n" \
    "ds_read_b64_tr_b16 v[126:127], v155 offset:16384\n" \
    "s_waitcnt lgkmcnt(9)\n" \
    "v_mfma_f32_16x16x32_bf16 v[58:61], v[98:101], v[146:149], v[58:61]\n" \
    "ds_read_b64_tr_b16 v[128:129], v155 offset:18432\n" \
    "s_waitcnt lgkmcnt(7)\n" \
    "v_mfma_f32_16x16x32_bf16 v[78:81], v[150:153], v[98:101], v[78:81]\n" \
    "ds_read_b128 v[98:101], %[ap0] offset:18432\n" \
    "v_mfma_f32_16x16x32_bf16 v[42:45], v[142:145], v[102:105], v[42:45]\n" \
    "ds_read_b64_tr_b16 v[130:131], v156 offset:16384\n" \
    "v_mfma_f32_16x16x32_bf16 v[62:65], v[102:105], v[146:149], v[62:65]\n" \
    "ds_read_b64_tr_b16 v[132:133], v156 offset:18432\n" \
    "v_mfma_f32_16x16x32_bf16 v[82:85], v[150:153], v[102:105], v[82:85]\n" \
    "ds_read_b64_tr_b16 v[134:135], v157 offset:16384\n" \
    "s_waitcnt lgkmcnt(9)\n" \
    "v_mfma_f32_16x16x32_bf16 v[22:25], v[138:141], v[102:105], v[22:25]\n" \
    "ds_read_b128 v[102:105], %[ap0] offset:20480\n" \
    "s_waitcnt lgkmcnt(9)\n" \
    "v_mfma_f32_16x16x32_bf16 v[46:49], v[142:145], v[106:109], v[46:49]\n" \
    "ds_read_b64_tr_b16 v[136:137], v157 offset:18432\n" \
    "v_mfma_f32_16x16x32_bf16 v[66:69], v[106:109], v[146:149], v[66:69]\n" \
    "ds_read_b64_tr_b16 v[122:123], v154 offset:16384\n" \
    "v_mfma_f32_16x16x32_bf16 v[86:89], v[150:153], v[106:109], v[86:89]\n" \
    "ds_read_b64_tr_b16 v[124:125], v154 offset:18432\n" \
    "v_mfma_f32_16x16x32_bf16 v[26:29], v[138:141], v[106:109], v[26:29]\n" \
    "ds_read_b128 v[106:109], %[ap0] offset:22528\n" \
    "s_waitcnt lgkmcnt(11)\n" \
    "v_mfma_f32_16x16x32_bf16 v[50:53], v[142:145], v[110:113], v[50:53]\n" \
    "ds_read_b128 v[114:117], %[ap0] offset:26624\n" \
    "v_mfma_f32_16x16x32_bf16 v[70:73], v[110:113], v[146:149], v[70:73]\n" \
    "v_mfma_f32_16x16x32_bf16 v[90:93], v[150:153], v[110:113], v[90:93]\n" \
    "v_mfma_f32_16x16x32_bf16 v[30:33], v[138:141], v[110:113], v[30:33]\n" \
    "ds_read_b128 v[110:113], %[ap0] offset:24576\n" \
    "v_mfma_f32_16x16x32_bf16 v[54:57], v[142:145], v[118:121], v[54:57]\n" \
    "s_barrier\n" \
    "v_mfma_f32_16x16x32_bf16 v[74:77], v[118:121], v[146:149], v[74:77]\n" \
    "v_mfma_f32_16x16x32_bf16 v[94:97], v[150:153], v[118:121], v[94:97]\n" \
    "v_mfma_f32_16x16x32_bf16 v[34:37], v[138:141], v[118:121], v[34:37]\n" \
    "s_waitcnt lgkmcnt(10)\n" \
    "v_mfma_f32_16x16x32_bf16 v[38:41], v[126:129], v[98:101], v[38:41]\n" \
    "ds_read_b64_tr_b16 v[142:143], v155 offset:32768\n" \
    "s_waitcnt lgkmcnt(9)\n" \
    "v_mfma_f32_16x16x32_bf16 v[58:61], v[98:101], v[130:133], v[58:61]\n" \
    "ds_read_b64_tr_b16 v[144:145], v155 offset:34816\n" \
    "s_waitcnt lgkmcnt(7)\n" \
    "v_mfma_f32_16x16x32_bf16 v[78:81], v[134:137], v[98:101], v[78:81]\n" \
    "ds_read_b128 v[98:101], %[ap1] offset:18432\n" \
    "v_mfma_f32_16x16x32_bf16 v[42:45], v[126:129], v[102:105], v[42:45]\n" \
    "ds_read_b64_tr_b16 v[146:147], v156 offset:32768\n" \
    "v_mfma_f32_16x16x32_bf16 v[62:65], v[102:105], v[130:133], v[62:65]\n" \
    "ds_read_b64_tr_b16 v[148:149], v156 offset:34816\n" \
    "v_mfma_f32_16x16x32_bf16 v[82:85], v[134:137], v[102:105], v[82:85]\n" \
    "ds_read_b64_tr_b16 v[150:151], v157 offset:32768\n" \
    "s_waitcnt lgkmcnt(9)\n" \
    "v_mfma_f32_16x16x32_bf16 v[22:25], v[122:125], v[102:105], v[22:25]\n" \
    "ds_read_b128 v[102:105], %[ap1] offset:20480\n" \
    "s_waitcnt lgkmcnt(9)\n" \
    "v_mfma_f32_16x16x32_bf16 v[46:49], v[126:129], v[106:109], v[46:49]\n" \
    "ds_read_b64_tr_b16 v[152:153], v157 offset:34816\n" \
    "v_mfma_f32_16x16x32_bf16 v[66:69], v[106:109], v[130:133], v[66:69]\n" \
    "ds_read_b64_tr_b16 v[138:139], v154 offset:32768\n" \
    "v_mfma_f32_16x16x32_bf16 v[86:89], v[134:137], v[106:109], v[86:89]\n" \
    "ds_read_b64_tr_b16 v[140:141], v154 offset:34816\n" \
    "v_mfma_f32_16x16x32_bf16 v[26:29], v[122:125], v[106:109], v[26:29]\n" \
    "ds_read_b128 v[106:109], %[ap1] offset:22528\n" \
    "s_waitcnt lgkmcnt(11)\n" \
    "v_mfma_f32_16x16x32_bf16 v[50:53], v[126:129], v[110:113], v[50:53]\n" \
    "ds_read_b128 v[118:121], %[ap1] offset:26624\n" \
    "v_mfma_f32_16x16x32_bf16 v[70:73], v[110:113], v[130:133], v[70:73]\n" \
    "v_mfma_f32_16x16x32_bf16 v[90:93], v[134:137], v[110:113], v[90:93]\n" \
    "v_mfma_f32_16x16x32_bf16 v[30:33], v[122:125], v[110:113], v[30:33]\n" \
    "ds_read_b128 v[110:113], %[ap1] offset:24576\n" \
    "v_mov_b32 v161, 16\n" \
    "ds_write_b32 %[fr], v161\n" \
    "v_mfma_f32_16x16x32_bf16 v[54:57], v[126:129], v[114:117], v[54:57]\n" \
    "s_barrier\n" \
    "v_mfma_f32_16x16x32_bf16 v[74:77], v[114:117], v[130:133], v[74:77]\n" \
    "v_mfma_f32_16x16x32_bf16 v[94:97], v[134:137], v[114:117], v[94:97]\n" \
    "v_mfma_f32_16x16x32_bf16 v[34:37], v[122:125], v[114:117], v[34:37]\n" \
    "s_waitcnt lgkmcnt(11)\n" \
    "v_mfma_f32_16x16x32_bf16 v[38:41], v[142:145], v[98:101], v[38:41]\n" \
    "ds_read_b64_tr_b16 v[126:127], %[wp1] offset:0\n" \
    "s_waitcnt lgkmcnt(10)\n" \
    "v_mfma_f32_16x16x32_bf16 v[58:61], v[98:101], v[146:149], v[58:61]\n" \
    "ds_read_b64_tr_b16 v[128:129], %[wp1] offset:2048\n" \
    "s_waitcnt lgkmcnt(8)\n" \
    "v_mfma_f32_16x16x32_bf16 v[78:81], v[150:153], v[98:101], v[78:81]\n" \
    "ds_read_b128 v[98:101], %[ap0] offset:53248\n" \
    "v_mfma_f32_16x16x32_bf16 v[42:45], v[142:145], v[102:105], v[42:45]\n" \
    "ds_read_b64_tr_b16 v[130:131], %[wp2] offset:0\n" \
    "v_mfma_f32_16x16x32_bf16 v[62:65], v[102:105], v[146:149], v[62:65]\n" \
    "ds_read_b64_tr_b16 v[132:133], %[wp2] offset:2048\n" \
    "v_mfma_f32_16x16x32_bf16 v[82:85], v[150:153], v[102:105], v[82:85]\n" \
    "ds_read_b64_tr_b16 v[134:135], %[wp3] offset:0\n" \
    "s_waitcnt lgkmcnt(10)\n" \
    "v_mfma_f32_16x16x32_bf16 v[22:25], v[138:141], v[102:105], v[22:25]\n" \
    "ds_read_b128 v[102:105], %[ap0] offset:55296\n" \
    "s_waitcnt lgkmcnt(10)\n" \
    "v_mfma_f32_16x16x32_bf16 v[46:49], v[142:145], v[106:109], v[46:49]\n" \
    "ds_read_b64_tr_b16 v[136:137], %[wp3] offset:2048\n" \
    "v_mfma_f32_16x16x32_bf16 v[66:69], v[106:109], v[146:149], v[66:69]\n" \
    "ds_read_b64_tr_b16 v[122:123], %[wp0] offset:0\n" \
    "v_mfma_f32_16x16x32_bf16 v[86:89], v[150:153], v[106:109], v[86:89]\n" \
    "ds_read_b64_tr_b16 v[124:125], %[wp0] offset:2048\n" \
    "v_mfma_f32_16x16x32_bf16 v[26:29], v[138:141], v[106:109], v[26:29]\n" \
    "ds_read_b128 v[106:109], %[ap0] offset:57344\n" \
    "s_waitcnt lgkmcnt(12)\n" \
    "v_mfma_f32_16x16x32_bf16 v[50:53], v[142:145], v[110:113], v[50:53]\n" \
    "ds_read_b128 v[114:117], %[ap0] offset:61440\n" \
    "v_mfma_f32_16x16x32_bf16 v[70:73], v[110:113], v[146:149], v[70:73]\n" \
    "v_mfma_f32_16x16x32_bf16 v[90:93], v[150:153], v[110:113], v[90:93]\n" \
    "v_mfma_f32_16x16x32_bf16 v[30:33], v[138:141], v[110:113], v[30:33]\n" \
    "ds_read_b128 v[110:113], %[ap0] offset:59392\n" \
    "v_mov_b32 v161, 17\n" \
    "ds_write_b32 %[fr], v161\n" \
    "v_mfma_f32_16x16x32_bf16 v[54:57], v[142:145], v[118:121], v[54:57]\n" \
    "s_barrier\n" \
    "ds_read_b64_tr_b16 v[142:143], %[wp1] offset:34816\n" \
    "ds_read_b64_tr_b16 v[144:145], %[wp1] offset:36864\n" \
    "v_mfma_f32_16x16x32_bf16 v[74:77], v[118:121], v[146:149], v[74:77]\n" \
    "ds_read_b64_tr_b16 v[146:147], %[wp2] offset:34816\n" \
    "ds_read_b64_tr_b16 v[148:149], %[wp2] offset:36864\n" \
    "v_mfma_f32_16x16x32_bf16 v[94:97], v[150:153], v[118:121], v[94:97]\n" \
    "ds_read_b64_tr_b16 v[150:151], %[wp3] offset:34816\n" \
    "ds_read_b64_tr_b16 v[152:153], %[wp3] offset:36864\n" \
    "v_mfma_f32_16x16x32_bf16 v[34:37], v[138:141], v[118:121], v[34:37]\n" \
    "ds_read_b128 v[118:121], %[ap1] offset:61440\n" \
    "ds_read_b64_tr_b16 v[138:139], %[wp0] offset:34816\n" \
    "ds_read_b64_tr_b16 v[140:141], %[wp0] offset:36864\n" \
    "s_waitcnt lgkmcnt(11)\n" \
    "v_mfma_f32_16x16x32_bf16 v[54:57], v[126:129], v[114:117], v[54:57]\n" \
    "v_add_u32 %[wp0], %[tabc], %[l16]\n" \
    "v_cndmask_b32_e64 %[wp2], 0, -1, s[68:69]\n" \
    "v_mfma_f32_16x16x32_bf16 v[74:77], v[114:117], v[130:133], v[74:77]\n" \
    "v_add_u32 %[wp1], %[hxc], %[l16]\n" \
    "v_lshrrev_b32 %[wp3], 2, %[l16]\n" \
    "v_mfma_f32_16x16x32_bf16 v[94:97], v[134:137], v[114:117], v[94:97]\n" \
    "v_add_u32 %[wp3], %[hxd], %[wp3]\n" \
    "v_mfma_f32_16x16x32_bf16 v[34:37], v[122:125], v[114:117], v[34:37]\n" \
    "s_waitcnt lgkmcnt(10)\n" \
    "v_mfma_f32_16x16x32_bf16 v[50:53], v[126:129], v[110:113], v[50:53]\n" \
    "v_mfma_f32_16x16x32_bf16 v[70:73], v[110:113], v[130:133], v[70:73]\n" \
    "v_mfma_f32_16x16x32_bf16 v[90:93], v[134:137], v[110:113], v[90:93]\n" \
    "v_mfma_f32_16x16x32_bf16 v[30:33], v[122:125], v[110:113], v[30:33]\n" \
    "ds_read_b128 v[110:113], %[ap1] offset:59392\n" \
    "s_waitcnt lgkmcnt(3)\n" \
    "v_mfma_f32_16x16x32_bf16 v[54:57], v[142:145], v[118:121], v[54:57]\n" \
    "v_cndmask_b32_e64 v154, %[zr], %[vxb], s[60:61]\n" \
    "v_cndmask_b32_e64 v155, %[zr], %[vxb], s[62:63]\n" \
    "v_mfma_f32_16x16x32_bf16 v[74:77], v[118:121], v[146:149], v[74:77]\n" \
    "v_cndmask_b32_e64 v156, %[zr], %[vxb], s[64:65]\n" \
    "v_cndmask_b32_e64 v157, %[zr], %[vxb], s[66:67]\n" \
    "v_mfma_f32_16x16x32_bf16 v[94:97], v[150:153], v[118:121], v[94:97]\n" \
    "s_waitcnt lgkmcnt(1)\n" \
    "v_mfma_f32_16x16x32_bf16 v[34:37], v[138:141], v[118:121], v[34:37]\n" \
    "v_mfma_f32_16x16x32_bf16 v[46:49], v[126:129], v[106:109], v[46:49]\n" \
    "v_mfma_f32_16x16x32_bf16 v[66:69], v[106:109], v[130:133], v[66:69]\n" \
    "v_cvt_pk_bf16_f32 v54, v54, v55\n" \
    "v_cvt_pk_bf16_f32 v55, v56, v57\n" \
    "v_mfma_f32_16x16x32_bf16 v[86:89], v[134:137], v[106:109], v[86:89]\n" \
    "v_cvt_pk_bf16_f32 v120, v74, v75\n" \
    "v_cvt_pk_bf16_f32 v121, v76, v77\n" \
    "v_mfma_f32_16x16x32_bf16 v[26:29], v[122:125], v[106:109], v[26:29]\n" \
    "ds_read_b128 v[106:109], %[ap1] offset:57344\n" \
    "v_cvt_pk_bf16_f32 v94, v94, v95\n" \
    "v_cvt_pk_bf16_f32 v56, v96, v97\n" \
    "ds_write2_b32 %[scw], v94, v56 offset0:128 offset1:129\n" \
    "s_waitcnt lgkmcnt(2)\n" \
    "v_mfma_f32_16x16x32_bf16 v[50:53], v[142:145], v[110:113], v[50:53]\n" \
    "v_permlane16_swap_b32 v94, v56\n" \
    "v_mov_b32 v161, 1\n" \
    "ds_write_b64 %[wp1], v[54:55]\n" \
    "ds_write_b64 %[wp1], v[120:121] offset:8\n" \
    "ds_write_b32 %[wp3], v56\n" \
    "ds_write_b32 %[fr], v161 offset:144\n" \
    "v_mfma_f32_16x16x32_bf16 v[70:73], v[110:113], v[146:149], v[70:73]\n" \
    "v_cvt_pk_bf16_f32 v34, v34, v35\n" \
    "v_cvt_pk_bf16_f32 v35, v36, v37\n" \
    "v_mfma_f32_16x16x32_bf16 v[90:93], v[150:153], v[110:113], v[90:93]\n" \
    "v_and_b32 v36, v94, %[wp2]\n" \
    "v_mov_b32 v37, 0\n" \
    "v_mov_b32 v57, 0\n" \
    "v_mfma_f32_16x16x32_bf16 v[30:33], v[138:141], v[110:113], v[30:33]\n" \
    "v_mfma_f32_16x16x32_bf16 v[42:45], v[126:129], v[102:105], v[42:45]\n" \
    "v_mfma_f32_16x16x32_bf16 v[62:65], v[102:105], v[130:133], v[62:65]\n" \
    "v_cvt_pk_bf16_f32 v50, v50, v51\n" \
    "v_cvt_pk_bf16_f32 v51, v52, v53\n" \
    "v_mfma_f32_16x16x32_bf16 v[82:85], v[134:137], v[102:105], v[82:85]\n" \
    "v_cvt_pk_bf16_f32 v118, v70, v71\n" \
    "v_cvt_pk_bf16_f32 v119, v72, v73\n" \
    "v_mfma_f32_16x16x32_bf16 v[22:25], v[122:125], v[102:105], v[22:25]\n" \
    "ds_read_b128 v[102:105], %[ap1] offset:55296\n" \
    "v_cvt_pk_bf16_f32 v90, v90, v91\n" \
    "v_cvt_pk_bf16_f32 v52, v92, v93\n" \
    "ds_write2_b32 %[scw], v90, v52 offset0:96 offset1:97\n" \
    "s_waitcnt lgkmcnt(7)\n" \
    "v_mfma_f32_16x16x32_bf16 v[46:49], v[142:145], v[106:109], v[46:49]\n" \
    "v_permlane16_swap_b32 v90, v52\n" \
    "v_mfma_f32_16x16x32_bf16 v[66:69], v[106:109], v[146:149], v[66:69]\n" \
    "v_cvt_pk_bf16_f32 v30, v30, v31\n" \
    "v_cvt_pk_bf16_f32 v31, v32, v33\n" \
    "v_mfma_f32_16x16x32_bf16 v[86:89], v[150:153], v[106:109], v[86:89]\n" \
    "v_and_b32 v32, v90, %[wp2]\n" \
    "v_mov_b32 v33, 0\n" \
    "v_mov_b32 v53, 0\n" \
    "v_mfma_f32_16x16x32_bf16 v[26:29], v[138:141], v[106:109], v[26:29]\n" \
    "v_mfma_f32_16x16x32_bf16 v[38:41], v[126:129], v[98:101], v[38:41]\n" \
    "v_mfma_f32_16x16x32_bf16 v[58:61], v[98:101], v[130:133], v[58:61]\n" \
    "v_cvt_pk_bf16_f32 v46, v46, v47\n" \
    "v_cvt_pk_bf16_f32 v47, v48, v49\n" \
    "v_mfma_f32_16x16x32_bf16 v[78:81], v[134:137], v[98:101], v[78:81]\n" \
    "ds_read_b128 v[98:101], %[ap1] offset:53248\n" \
    "v_mov_b32 v161, 18\n" \
    "ds_write_b32 %[fr], v161\n" \
    "ds_read_b128 v[126:129], %[wp0] offset:1024\n" \
    "ds_read_b128 v[122:125], %[wp0]\n" \
    "ds_read_b128 v[130:133], %[wp0] offset:2048\n" \
    "v_cvt_pk_bf16_f32 v116, v66, v67\n" \
    "v_cvt_pk_bf16_f32 v117, v68, v69\n" \
    "s_waitcnt lgkmcnt(6)\n" \
    "v_mfma_f32_16x16x32_bf16 v[42:45], v[142:145], v[102:105], v[42:45]\n" \
    "v_cvt_pk_bf16_f32 v86, v86, v87\n" \
    "v_cvt_pk_bf16_f32 v48, v88, v89\n" \
    "ds_write2_b32 %[scw], v86, v48 offset0:64 offset1:65\n" \
    "v_mfma_f32_16x16x32_bf16 v[62:65], v[102:105], v[146:149], v[62:65]\n" \
    "v_permlane16_swap_b32 v86, v48\n" \
    "v_mfma_f32_16x16x32_bf16 v[82:85], v[150:153], v[102:105], v[82:85]\n" \
    "v_cvt_pk_bf16_f32 v26, v26, v27\n" \
    "v_cvt_pk_bf16_f32 v27, v28, v29\n" \
    "v_mfma_f32_16x16x32_bf16 v[22:25], v[138:141], v[102:105], v[22:25]\n" \
    "v_and_b32 v28, v86, %[wp2]\n" \
    "v_mov_b32 v29, 0\n" \
    "v_mov_b32 v49, 0\n" \
    "s_waitcnt lgkmcnt(5)\n" \
    "v_mfma_f32_16x16x32_bf16 v[38:41], v[142:145], v[98:101], v[38:41]\n" \
    "v_mfma_f32_16x16x32_bf16 v[58:61], v[98:101], v[146:149], v[58:61]\n" \
    "v_cvt_pk_bf16_f32 v42, v42, v43\n" \
    "v_cvt_pk_bf16_f32 v43, v44, v45\n" \
    "v_mfma_f32_16x16x32_bf16 v[78:81], v[150:153], v[98:101], v[78:81]\n" \
    "ds_read_b32 v159, %[zr] offset:1332\n" \
    "s_setprio 0\n" \
    "s_waitcnt lgkmcnt(4)\n" \
    "v_mfma_f32_16x16x32_bf16 v[74:77], v[54:57], v[34:37], v[126:129]\n" \
    "v_cvt_pk_bf16_f32 v82, v82, v83\n" \
    "v_cvt_pk_bf16_f32 v44, v84, v85\n" \
    "ds_write2_b32 %[scw], v82, v44 offset0:32 offset1:33\n" \
    "s_waitcnt lgkmcnt(4)\n" \
    "v_mfma_f32_16x16x32_bf16 v[94:97], v[50:53], v[34:37], v[122:125]\n" \
    "v_cvt_pk_bf16_f32 v114, v62, v63\n" \
    "v_cvt_pk_bf16_f32 v115, v64, v65\n" \
    "v_permlane16_swap_b32 v82, v44\n" \
    "v_mfma_f32_16x16x32_bf16 v[70:73], v[50:53], v[30:33], v[126:129]\n" \
    "v_cvt_pk_bf16_f32 v22, v22, v23\n" \
    "v_cvt_pk_bf16_f32 v23, v24, v25\n" \
    "v_and_b32 v24, v82, %[wp2]\n" \
    "v_mov_b32 v25, 0\n" \
    "v_mov_b32 v45, 0\n" \
    "v_mfma_f32_16x16x32_bf16 v[90:93], v[46:49], v[30:33], v[122:125]\n" \
    "v_cvt_pk_bf16_f32 v38, v38, v39\n" \
    "v_cvt_pk_bf16_f32 v39, v40, v41\n" \
    "v_cvt_pk_bf16_f32 v78, v78, v79\n" \
    "v_cvt_pk_bf16_f32 v40, v80, v81\n" \
    "ds_write2_b32 %[scw], v78, v40 offset0:0 offset1:1\n" \
    "v_mfma_f32_16x16x32_bf16 v[66:69], v[46:49], v[26:29], v[126:129]\n" \
    "v_mov_b32 v41, 0\n" \
    "v_permlane16_swap_b32 v78, v40\n" \
    "v_maximum3_f32 v144, v74, v75, v75\n" \
    "v_maximum3_f32 v145, v76, v77, v77\n" \
    "v_mfma_f32_16x16x32_bf16 v[86:89], v[42:45], v[26:29], v[122:125]\n" \
    "v_cvt_pk_bf16_f32 v112, v58, v59\n" \
    "v_cvt_pk_bf16_f32 v113, v60, v61\n" \
    "ds_read_b64_tr_b16 v[106:107], v157 offset:384\n" \
    "ds_read_b64_tr_b16 v[108:109], v157 offset:512\n" \
    "v_mfma_f32_16x16x32_bf16 v[62:65], v[42:45], v[22:25], v[126:129]\n" \
    "v_maximum3_f32 v144, v94, v95, v144\n" \
    "v_maximum3_f32 v145, v96, v97, v145\n" \
    "ds_read_b64_tr_b16 v[102:103], v156 offset:256\n" \
    "ds_read_b64_tr_b16 v[104:105], v156 offset:384\n" \
    "s_cmp_eq_u32 %[rb2], 0\n" \
    "s_cbranch_scc1 Lz_%=\n" \
    "v_mfma_f32_16x16x32_bf16 v[82:85], v[38:41], v[22:25], v[122:125]\n" \
    "s_branch Ly_%=\n" \
    "Lz_%=:\n" \
    "s_nop 7\n" \
    "v_mov_b32 v82, 0xff61b1e6\n" \
    "v_mov_b32 v83, 0xff61b1e6\n" \
    "v_mov_b32 v84, 0xff61b1e6\n" \
    "v_mov_b32 v85, 0xff61b1e6\n" \
    "Ly_%=:\n" \
    "v_maximum3_f32 v142, v70, v71, v71\n" \
    "v_maximum3_f32 v143, v72, v73, v73\n" \
    "ds_read_b64_tr_b16 v[98:99], v155 offset:128\n" \
    "ds_read_b64_tr_b16 v[100:101], v155 offset:256\n" \
    "v_maximum3_f32 v142, v90, v91, v142\n" \
    "v_maximum3_f32 v143, v92, v93, v143\n" \
    "ds_read_b64_tr_b16 v[134:135], v154 offset:0\n" \
    "ds_read_b64_tr_b16 v[136:137], v154 offset:128\n" \
    "v_maximum3_f32 v149, v144, v145, v145\n" \
    "v_maximum3_f32 v140, v66, v67, v67\n" \
    "v_maximum3_f32 v141, v68, v69, v69\n" \
    "v_maximum3_f32 v148, v142, v143, v143\n" \
    "v_maximum3_f32 v140, v86, v87, v140\n" \
    "v_maximum3_f32 v141, v88, v89, v141\n" \
    "v_maximum3_f32 v138, v62, v63, v63\n" \
    "v_maximum3_f32 v139, v64, v65, v65\n" \
    "v_maximum3_f32 v147, v140, v141, v141\n" \
    "v_maximum3_f32 v138, v82, v83, v138\n" \
    "v_maximum3_f32 v139, v84, v85, v139\n" \
    "v_maximum3_f32 v146, v138, v139, v139\n" \
    "s_waitcnt lgkmcnt(8)\n" \
    "s_branch Lend_%=\n" \
    "Llate_%=:\n" \
    "s_barrier\n" \
    "ds_read_b64_tr_b16 v[126:127], %[wp1] offset:0\n" \
    "ds_read_b64_tr_b16 v[128:129], %[wp1] offset:2048\n" \
    "ds_read_b128 v[102:105], %[ap0] offset:55296\n" \
    "ds_read_b64_tr_b16 v[130:131], %[wp2] offset:0\n" \
    "ds_read_b64_tr_b16 v[132:133], %[wp2] offset:2048\n" \
    "ds_read_b64_tr_b16 v[134:135], %[wp3] offset:0\n" \
    "ds_read_b64_tr_b16 v[136:137], %[wp3] offset:2048\n" \
    "ds_read_b64_tr_b16 v[122:123], %[wp0] offset:0\n" \
    "ds_read_b64_tr_b16 v[124:125], %[wp0] offset:2048\n" \
    "ds_read_b128 v[106:109], %[ap0] offset:57344\n" \
    "ds_read_b128 v[110:113], %[ap0] offset:59392\n" \
    "ds_read_b128 v[114:117], %[ap0] offset:61440\n" \
    "s_waitcnt lgkmcnt(9)\n" \
    "v_mfma_f32_16x16x32_bf16 v[42:45], v[126:129], v[102:105], 0\n" \
    "ds_read_b64_tr_b16 v[142:143], %[wp1] offset:34816\n" \
    "s_waitcnt lgkmcnt(8)\n" \
    "v_mfma_f32_16x16x32_bf16 v[62:65], v[102:105], v[130:133], 0\n" \
    "ds_read_b64_tr_b16 v[144:145], %[wp1] offset:36864\n" \
    "s_waitcnt lgkmcnt(7)\n" \
    "v_mfma_f32_16x16x32_bf16 v[82:85], v[134:137], v[102:105], 0\n" \
    "ds_read_b64_tr_b16 v[146:147], %[wp2] offset:34816\n" \
    "s_waitcnt lgkmcnt(6)\n" \
    "v_mfma_f32_16x16x32_bf16 v[22:25], v[122:125], v[102:105], 0\n" \
    "ds_read_b128 v[102:105], %[ap1] offset:55296\n" \
    "s_waitcnt lgkmcnt(6)\n" \
    "v_mfma_f32_16x16x32_bf16 v[46:49], v[126:129], v[106:109], 0\n" \
    "ds_read_b64_tr_b16 v[148:149], %[wp2] offset:36864\n" \
    "v_mfma_f32_16x16x32_bf16 v[66:69], v[106:109], v[130:133], 0\n" \
    "ds_read_b64_tr_b16 v[150:151], %[wp3] offset:34816\n" \
    "v_mfma_f32_16x16x32_bf16 v[86:89], v[134:137], v[106:109], 0\n" \
    "ds_read_b64_tr_b16 v[152:153], %[wp3] offset:36864\n" \
    "v_mfma_f32_16x16x32_bf16 v[26:29], v[122:125], v[106:109], 0\n" \
    "ds_read_b128 v[106:109], %[ap1] offset:57344\n" \
    "s_waitcnt lgkmcnt(9)\n" \
    "v_mfma_f32_16x16x32_bf16 v[50:53], v[126:129], v[110:113], 0\n" \
    "ds_read_b64_tr_b16 v[138:139], %[wp0] offset:34816\n" \
    "v_mfma_f32_16x16x32_bf16 v[70:73], v[110:113], v[130:133], 0\n" \
    "ds_read_b64_tr_b16 v[140:141], %[wp0] offset:36864\n" \
    "v_mfma_f32_16x16x32_bf16 v[90:93], v[134:137], v[110:113], 0\n" \
    "ds_read_b128 v[118:121], %[ap1] offset:61440\n" \
    "v_mfma_f32_16x16x32_bf16 v[30:33], v[122:125], v[110:113], 0\n" \
    "ds_read_b128 v[110:113], %[ap1] offset:59392\n" \
    "s_waitcnt lgkmcnt(12)\n" \
    "v_mfma_f32_16x16x32_bf16 v[54:57], v[126:129], v[114:117], 0\n" \
    "s_barrier\n" \
    "v_mfma_f32_16x16x32_bf16 v[74:77], v[114:117], v[130:133], 0\n" \
    "v_mfma_f32_16x16x32_bf16 v[94:97], v[134:137], v[114:117], 0\n" \
    "v_mfma_f32_16x16x32_bf16 v[34:37], v[122:125], v[114:117], 0\n" \
    "s_waitcnt lgkmcnt(8)\n" \
    "v_mfma_f32_16x16x32_bf16 v[42:45], v[142:145], v[102:105], v[42:45]\n" \
    "ds_read_b64_tr_b16 v[126:127], %[wp1] offset:51200\n" \
    "s_waitcnt lgkmcnt(8)\n" \
    "v_mfma_f32_16x16x32_bf16 v[62:65], v[102:105], v[146:149], v[62:65]\n" \
    "ds_read_b64_tr_b16 v[128:129], %[wp1] offset:53248\n" \
    "s_waitcnt lgkmcnt(7)\n" \
    "v_mfma_f32_16x16x32_bf16 v[82:85], v[150:153], v[102:105], v[82:85]\n" \
    "ds_read_b64_tr_b16 v[130:131], %[wp2] offset:51200\n" \
    "s_waitcnt lgkmcnt(5)\n" \
    "v_mfma_f32_16x16x32_bf16 v[22:25], v[138:141], v[102:105], v[22:25]\n" \
    "ds_read_b128 v[102:105], %[ap0] offset:2048\n" \
    "v_mfma_f32_16x16x32_bf16 v[46:49], v[142:145], v[106:109], v[46:49]\n" \
    "ds_read_b64_tr_b16 v[132:133], %[wp2] offset:53248\n" \
    "v_mfma_f32_16x16x32_bf16 v[66:69], v[106:109], v[146:149], v[66:69]\n" \
    "ds_read_b64_tr_b16 v[134:135], %[wp3] offset:51200\n" \
    "v_mfma_f32_16x16x32_bf16 v[86:89], v[150:153], v[106:109], v[86:89]\n" \
    "ds_read_b64_tr_b16 v[136:137], %[wp3] offset:53248\n" \
    "v_mfma_f32_16x16x32_bf16 v[26:29], v[138:141], v[106:109], v[26:29]\n" \
    "ds_read_b128 v[106:109], %[ap0] offset:4096\n" \
    "s_waitcnt lgkmcnt(8)\n" \
    "v_mfma_f32_16x16x32_bf16 v[50:53], v[142:145], v[110:113], v[50:53]\n" \
    "ds_read_b64_tr_b16 v[122:123], %[wp0] offset:51200\n" \
    "v_mfma_f32_16x16x32_bf16 v[70:73], v[110:113], v[146:149], v[70:73]\n" \
    "ds_read_b64_tr_b16 v[124:125], %[wp0] offset:53248\n" \
    "v_mfma_f32_16x16x32_bf16 v[90:93], v[150:153], v[110:113], v[90:93]\n" \
    "ds_read_b128 v[114:117], %[ap0] offset:8192\n" \
    "v_mfma_f32_16x16x32_bf16 v[30:33], v[138:141], v[110:113], v[30:33]\n" \
    "ds_read_b128 v[110:113], %[ap0] offset:6144\n" \
    "v_mfma_f32_16x16x32_bf16 v[54:57], v[142:145], v[118:121], v[54:57]\n" \
    "s_barrier\n" \
    "v_mfma_f32_16x16x32_bf16 v[74:77], v[118:121], v[146:149], v[74:77]\n" \
    "v_mfma_f32_16x16x32_bf16 v[94:97], v[150:153], v[118:121], v[94:97]\n" \
    "v_mfma_f32_16x16x32_bf16 v[34:37], v[138:141], v[118:121], v[34:37]\n" \
    "s_waitcnt lgkmcnt(8)\n" \
    "v_mfma_f32_16x16x32_bf16 v[42:45], v[126:129], v[102:105], v[42:45]\n" \
    "ds_read_b64_tr_b16 v[142:143], v155 offset:0\n" \
    "s_waitcnt lgkmcnt(8)\n" \
    "v_mfma_f32_16x16x32_bf16 v[62:65], v[102:105], v[130:133], v[62:65]\n" \
    "ds_read_b64_tr_b16 v[144:145], v155 offset:2048\n" \
    "s_waitcnt lgkmcnt(7)\n" \
    "v_mfma_f32_16x16x32_bf16 v[82:85], v[134:137], v[102:105], v[82:85]\n" \
    "ds_read_b64_tr_b16 v[146:147], v156 offset:0\n" \
    "s_waitcnt lgkmcnt(5)\n" \
    "v_mfma_f32_16x16x32_bf16 v[22:25], v[122:125], v[102:105], v[22:25]\n" \
    "ds_read_b128 v[102:105], %[ap1] offset:2048\n" \
    "v_mfma_f32_16x16x32_bf16 v[46:49], v[126:129], v[106:109], v[46:49]\n" \
    "ds_read_b64_tr_b16 v[148:149], v156 offset:2048\n" \
    "v_mfma_f32_16x16x32_bf16 v[66:69], v[106:109], v[130:133], v[66:69]\n" \
    "ds_read_b64_tr_b16 v[150:151], v157 offset:0\n" \
    "v_mfma_f32_16x16x32_bf16 v[86:89], v[134:137], v[106:109], v[86:89]\n" \
    "ds_read_b64_tr_b16 v[152:153], v157 offset:2048\n" \
    "v_mfma_f32_16x16x32_bf16 v[26:29], v[122:125], v[106:109], v[26:29]\n" \
    "ds_read_b128 v[106:109], %[ap1] offset:4096\n" \
    "s_waitcnt lgkmcnt(8)\n" \
    "v_mfma_f32_16x16x32_bf16 v[50:53], v[126:129], v[110:113], v[50:53]\n" \
    "ds_read_b64_tr_b16 v[138:139], v154 offset:0\n" \
    "v_mfma_f32_16x16x32_bf16 v[70:73], v[110:113], v[130:133], v[70:73]\n" \
    "ds_read_b64_tr_b16 v[140:141], v154 offset:2048\n" \
    "v_mfma_f32_16x16x32_bf16 v[90:93], v[134:137], v[110:113], v[90:93]\n" \
    "ds_read_b128 v[118:121], %[ap1] offset:8192\n" \
    "v_mfma_f32_16x16x32_bf16 v[30:33], v[122:125], v[110:113], v[30:33]\n" \
    "ds_read_b128 v[110:113], %[ap1] offset:6144\n" \
    "v_mfma_f32_16x16x32_bf16 v[54:57], v[126:129], v[114:117], v[54:57]\n" \
    "s_barrier\n" \
    "v_mfma_f32_16x16x32_bf16 v[74:77], v[114:117], v[130:133], v[74:77]\n" \
    "v_mfma_f32_16x16x32_bf16 v[94:97], v[134:137], v[114:117], v[94:97]\n" \
    "v_mfma_f32_16x16x32_bf16 v[34:37], v[122:125], v[114:117], v[34:37]\n" \
    "s_waitcnt lgkmcnt(8)\n" \
    "v_mfma_f32_16x16x32_bf16 v[42:45], v[142:145], v[102:105], v[42:45]\n" \
    "ds_read_b64_tr_b16 v[126:127], v155 offset:16384\n" \
    "s_waitcnt lgkmcnt(8)\n" \
    "v_mfma_f32_16x16x32_bf16 v[62:65], v[102:105], v[146:149], v[62:65]\n" \
    "ds_read_b64_tr_b16 v[128:129], v155 offset:18432\n" \
    "s_waitcnt lgkmcnt(7)\n" \
    "v_mfma_f32_16x16x32_bf16 v[82:85], v[150:153], v[102:105], v[82:85]\n" \
    "ds_read_b64_tr_b16 v[130:131], v156 offset:16384\n" \
    "s_waitcnt lgkmcnt(5)\n" \
    "v_mfma_f32_16x16x32_bf16 v[22:25], v[138:141], v[102:105], v[22:25]\n" \
    "ds_read_b128 v[102:105], %[ap0] offset:20480\n" \
    "v_mfma_f32_16x16x32_bf16 v[46:49], v[142:145], v[106:109], v[46:49]\n" \
    "ds_read_b64_tr_b16 v[132:133], v156 offset:18432\n" \
    "v_mfma_f32_16x16x32_bf16 v[66:69], v[106:109], v[146:149], v[66:69]\n" \
    "ds_read_b64_tr_b16 v[134:135], v157 offset:16384\n" \
    "v_mfma_f32_16x16x32_bf16 v[86:89], v[150:153], v[106:109], v[86:89]\n" \
    "ds_read_b64_tr_b16 v[136:137], v157 offset:18432\n" \
    "v_mfma_f32_16x16x32_bf16 v[26:29], v[138:141], v[106:109], v[26:29]\n" \
    "ds_read_b128 v[106:109], %[ap0] offset:22528\n" \
    "s_waitcnt lgkmcnt(8)\n" \
    "v_mfma_f32_16x16x32_bf16 v[50:53], v[142:145], v[110:113], v[50:53]\n" \
    "ds_read_b64_tr_b16 v[122:123], v154 offset:16384\n" \
    "v_mfma_f32_16x16x32_bf16 v[70:73], v[110:113], v[146:149], v[70:73]\n" \
    "ds_read_b64_tr_b16 v[124:125], v154 offset:18432\n" \
    "v_mfma_f32_16x16x32_bf16 v[90:93], v[150:153], v[110:113], v[90:93]\n" \
    "ds_read_b128 v[114:117], %[ap0] offset:26624\n" \
    "v_mfma_f32_16x16x32_bf16 v[30:33], v[138:141], v[110:113], v[30:33]\n" \
    "ds_read_b128 v[110:113], %[ap0] offset:24576\n" \
    "v_mfma_f32_16x16x32_bf16 v[54:57], v[142:145], v[118:121], v[54:57]\n" \
    "s_barrier\n" \
    "v_mfma_f32_16x16x32_bf16 v[74:77], v[118:121], v[146:149], v[74:77]\n" \
    "v_mfma_f32_16x16x32_bf16 v[94:97], v[150:153], v[118:121], v[94:97]\n" \
    "v_mfma_f32_16x16x32_bf16 v[34:37], v[138:141], v[118:121], v[34:37]\n" \
    "s_waitcnt lgkmcnt(8)\n" \
    "v_mfma_f32_16x16x32_bf16 v[42:45], v[126:129], v[102:105], v[42:45]\n" \
    "ds_read_b64_tr_b16 v[142:143], v155 offset:32768\n" \
    "s_waitcnt lgkmcnt(8)\n" \
    "v_mfma_f32_16x16x32_bf16 v[62:65], v[102:105], v[130:133], v[62:65]\n" \
    "ds_read_b64_tr_b16 v[144:145], v155 offset:34816\n" \
    "s_waitcnt lgkmcnt(7)\n" \
    "v_mfma_f32_16x16x32_bf16 v[82:85], v[134:137], v[102:105], v[82:85]\n" \
    "ds_read_b64_tr_b16 v[146:147], v156 offset:32768\n" \
    "s_waitcnt lgkmcnt(5)\n" \
    "v_mfma_f32_16x16x32_bf16 v[22:25], v[122:125], v[102:105], v[22:25]\n" \
    "ds_read_b128 v[102:105], %[ap1] offset:20480\n" \
    "v_mfma_f32_16x16x32_bf16 v[46:49], v[126:129], v[106:109], v[46:49]\n" \
    "ds_read_b64_tr_b16 v[148:149], v156 offset:34816\n" \
    "v_mfma_f32_16x16x32_bf16 v[66:69], v[106:109], v[130:133], v[66:69]\n" \
    "ds_read_b64_tr_b16 v[150:151], v157 offset:32768\n" \
    "v_mfma_f32_16x16x32_bf16 v[86:89], v[134:137], v[106:109], v[86:89]\n" \
    "ds_read_b64_tr_b16 v[152:153], v157 offset:34816\n" \
    "v_mfma_f32_16x16x32_bf16 v[26:29], v[122:125], v[106:109], v[26:29]\n" \
    "ds_read_b128 v[106:109], %[ap1] offset:22528\n" \
    "s_waitcnt lgkmcnt(8)\n" \
    "v_mfma_f32_16x16x32_bf16 v[50:53], v[126:129], v[110:113], v[50:53]\n" \
    "ds_read_b64_tr_b16 v[138:139], v154 offset:32768\n" \
    "v_mfma_f32_16x16x32_bf16 v[70:73], v[110:113], v[130:133], v[70:73]\n" \
    "ds_read_b64_tr_b16 v[140:141], v154 offset:34816\n" \
    "v_mfma_f32_16x16x32_bf16 v[90:93], v[134:137], v[110:113], v[90:93]\n" \
    "ds_read_b128 v[118:121], %[ap1] offset:26624\n" \
    "v_mfma_f32_16x16x32_bf16 v[30:33], v[122:125], v[110:113], v[30:33]\n" \
    "ds_read_b128 v[110:113], %[ap1] offset:24576\n" \
    "v_mfma_f32_16x16x32_bf16 v[54:57], v[126:129], v[114:117], v[54:57]\n" \
    "s_barrier\n" \
    "v_mfma_f32_16x16x32_bf16 v[74:77], v[114:117], v[130:133], v[74:77]\n" \
    "v_mfma_f32_16x16x32_bf16 v[94:97], v[134:137], v[114:117], v[94:97]\n" \
    "v_mfma_f32_16x16x32_bf16 v[34:37], v[122:125], v[114:117], v[34:37]\n" \
    "s_waitcnt lgkmcnt(8)\n" \
    "v_mfma_f32_16x16x32_bf16 v[42:45], v[142:145], v[102:105], v[42:45]\n" \
    "ds_read_b64_tr_b16 v[126:127], %[wp1] offset:0\n" \
    "s_waitcnt lgkmcnt(8)\n" \
    "v_mfma_f32_16x16x32_bf16 v[62:65], v[102:105], v[146:149], v[62:65]\n" \
    "ds_read_b64_tr_b16 v[128:129], %[wp1] offset:2048\n" \
    "s_waitcnt lgkmcnt(7)\n" \
    "v_mfma_f32_16x16x32_bf16 v[82:85], v[150:153], v[102:105], v[82:85]\n" \
    "ds_read_b64_tr_b16 v[130:131], %[wp2] offset:0\n" \
    "s_waitcnt lgkmcnt(5)\n" \
    "v_mfma_f32_16x16x32_bf16 v[22:25], v[138:141], v[102:105], v[22:25]\n" \
    "ds_read_b128 v[102:105], %[ap0] offset:55296\n" \
    "v_mfma_f32_16x16x32_bf16 v[46:49], v[142:145], v[106:109], v[46:49]\n" \
    "ds_read_b64_tr_b16 v[132:133], %[wp2] offset:2048\n" \
    "v_mfma_f32_16x16x32_bf16 v[66:69], v[106:109], v[146:149], v[66:69]\n" \
    "ds_read_b64_tr_b16 v[134:135], %[wp3] offset:0\n" \
    "v_mfma_f32_16x16x32_bf16 v[86:89], v[150:153], v[106:109], v[86:89]\n" \
    "ds_read_b64_tr_b16 v[136:137], %[wp3] offset:2048\n" \
    "v_mfma_f32_16x16x32_bf16 v[26:29], v[138:141], v[106:109], v[26:29]\n" \
    "ds_read_b128 v[106:109], %[ap0] offset:57344\n" \
    "s_waitcnt lgkmcnt(8)\n" \
    "v_mfma_f32_16x16x32_bf16 v[50:53], v[142:145], v[110:113], v[50:53]\n" \
    "ds_read_b64_tr_b16 v[122:123], %[wp0] offset:0\n" \
    "v_mfma_f32_16x16x32_bf16 v[70:73], v[110:113], v[146:149], v[70:73]\n" \
    "ds_read_b64_tr_b16 v[124:125], %[wp0] offset:2048\n" \
    "v_mfma_f32_16x16x32_bf16 v[90:93], v[150:153], v[110:113], v[90:93]\n" \
    "ds_read_b128 v[114:117], %[ap0] offset:61440\n" \
    "v_mfma_f32_16x16x32_bf16 v[30:33], v[138:141], v[110:113], v[30:33]\n" \
    "ds_read_b128 v[110:113], %[ap0] offset:59392\n" \
    "v_mfma_f32_16x16x32_bf16 v[54:57], v[142:145], v[118:121], v[54:57]\n" \
    "s_barrier\n" \
    "v_mfma_f32_16x16x32_bf16 v[74:77], v[118:121], v[146:149], v[74:77]\n" \
    "v_mfma_f32_16x16x32_bf16 v[94:97], v[150:153], v[118:121], v[94:97]\n" \
    "v_mfma_f32_16x16x32_bf16 v[34:37], v[138:141], v[118:121], v[34:37]\n" \
    "s_waitcnt lgkmcnt(8)\n" \
    "v_mfma_f32_16x16x32_bf16 v[42:45], v[126:129], v[102:105], v[42:45]\n" \
    "ds_read_b64_tr_b16 v[142:143], %[wp1] offset:34816\n" \
    "s_waitcnt lgkmcnt(8)\n" \
    "v_mfma_f32_16x16x32_bf16 v[62:65], v[102:105], v[130:133], v[62:65]\n" \
    "ds_read_b64_tr_b16 v[144:145], %[wp1] offset:36864\n" \
    "s_waitcnt lgkmcnt(7)\n" \
    "v_mfma_f32_16x16x32_bf16 v[82:85], v[134:137], v[102:105], v[82:85]\n" \
    "ds_read_b64_tr_b16 v[146:147], %[wp2] offset:34816\n" \
    "s_waitcnt lgkmcnt(5)\n" \
    "v_mfma_f32_16x16x32_bf16 v[22:25], v[122:125], v[102:105], v[22:25]\n" \
    "ds_read_b128 v[102:105], %[ap1] offset:55296\n" \
    "v_mfma_f32_16x16x32_bf16 v[46:49], v[126:129], v[106:109], v[46:49]\n" \
    "ds_read_b64_tr_b16 v[148:149], %[wp2] offset:36864\n" \
    "v_mfma_f32_16x16x32_bf16 v[66:69], v[106:109], v[130:133], v[66:69]\n" \
    "ds_read_b64_tr_b16 v[150:151], %[wp3] offset:34816\n" \
    "v_mfma_f32_16x16x32_bf16 v[86:89], v[134:137], v[106:109], v[86:89]\n" \
    "ds_read_b64_tr_b16 v[152:153], %[wp3] offset:36864\n" \
    "v_mfma_f32_16x16x32_bf16 v[26:29], v[122:125], v[106:109], v[26:29]\n" \
    "ds_read_b128 v[106:109], %[ap1] offset:57344\n" \
    "s_waitcnt lgkmcnt(8)\n" \
    "v_mfma_f32_16x16x32_bf16 v[50:53], v[126:129], v[110:113], v[50:53]\n" \
    "ds_read_b64_tr_b16 v[138:139], %[wp0] offset:34816\n" \
    "v_mfma_f32_16x16x32_bf16 v[70:73], v[110:113], v[130:133], v[70:73]\n" \
    "ds_read_b64_tr_b16 v[140:141], %[wp0] offset:36864\n" \
    "v_mfma_f32_16x16x32_bf16 v[90:93], v[134:137], v[110:113], v[90:93]\n" \
    "ds_read_b128 v[118:121], %[ap1] offset:61440\n" \
    "v_mfma_f32_16x16x32_bf16 v[30:33], v[122:125], v[110:113], v[30:33]\n" \
    "ds_read_b128 v[110:113], %[ap1] offset:59392\n" \
    "v_mfma_f32_16x16x32_bf16 v[54:57], v[126:129], v[114:117], v[54:57]\n" \
    "s_barrier\n" \
    "v_mfma_f32_16x16x32_bf16 v[74:77], v[114:117], v[130:133], v[74:77]\n" \
    "v_mfma_f32_16x16x32_bf16 v[94:97], v[134:137], v[114:117], v[94:97]\n" \
    "v_mfma_f32_16x16x32_bf16 v[34:37], v[122:125], v[114:117], v[34:37]\n" \
    "s_waitcnt lgkmcnt(8)\n" \
    "v_mfma_f32_16x16x32_bf16 v[42:45], v[142:145], v[102:105], v[42:45]\n" \
    "ds_read_b64_tr_b16 v[126:127], %[wp1] offset:51200\n" \
    "s_waitcnt lgkmcnt(8)\n" \
    "v_mfma_f32_16x16x32_bf16 v[62:65], v[102:105], v[146:149], v[62:65]\n" \
    "ds_read_b64_tr_b16 v[128:129], %[wp1] offset:53248\n" \
    "s_waitcnt lgkmcnt(7)\n" \
    "v_mfma_f32_16x16x32_bf16 v[82:85], v[150:153], v[102:105], v[82:85]\n" \
    "ds_read_b64_tr_b16 v[130:131], %[wp2] offset:51200\n" \
    "s_waitcnt lgkmcnt(5)\n" \
    "v_mfma_f32_16x16x32_bf16 v[22:25], v[138:141], v[102:105], v[22:25]\n" \
    "ds_read_b128 v[102:105], %[ap0] offset:2048\n" \
    "v_mfma_f32_16x16x32_bf16 v[46:49], v[142:145], v[106:109], v[46:49]\n" \
    "ds_read_b64_tr_b16 v[132:133], %[wp2] offset:53248\n" \
    "v_mfma_f32_16x16x32_bf16 v[66:69], v[106:109], v[146:149], v[66:69]\n" \
    "ds_read_b64_tr_b16 v[134:135], %[wp3] offset:51200\n" \
    "v_mfma_f32_16x16x32_bf16 v[86:89], v[150:153], v[106:109], v[86:89]\n" \
    "ds_read_b64_tr_b16 v[136:137], %[wp3] offset:53248\n" \
    "v_mfma_f32_16x16x32_bf16 v[26:29], v[138:141], v[106:109], v[26:29]\n" \
    "ds_read_b128 v[106:109], %[ap0] offset:4096\n" \
    "s_waitcnt lgkmcnt(8)\n" \
    "v_mfma_f32_16x16x32_bf16 v[50:53], v[142:145], v[110:113], v[50:53]\n" \
    "ds_read_b64_tr_b16 v[122:123], %[wp0] offset:51200\n" \
    "v_mfma_f32_16x16x32_bf16 v[70:73], v[110:113], v[146:149], v[70:73]\n" \
    "ds_read_b64_tr_b16 v[124:125], %[wp0] offset:53248\n" \
    "v_mfma_f32_16x16x32_bf16 v[90:93], v[150:153], v[110:113], v[90:93]\n" \
    "ds_read_b128 v[114:117], %[ap0] offset:8192\n" \
    "v_mfma_f32_16x16x32_bf16 v[30:33], v[138:141], v[110:113], v[30:33]\n" \
    "ds_read_b128 v[110:113], %[ap0] offset:6144\n" \
    "v_mfma_f32_16x16x32_bf16 v[54:57], v[142:145], v[118:121], v[54:57]\n" \
    "s_barrier\n" \
    "v_mfma_f32_16x16x32_bf16 v[74:77], v[118:121], v[146:149], v[74:77]\n" \
    "v_mfma_f32_16x16x32_bf16 v[94:97], v[150:153], v[118:121], v[94:97]\n" \
    "v_mfma_f32_16x16x32_bf16 v[34:37], v[138:141], v[118:121], v[34:37]\n" \
    "s_waitcnt lgkmcnt(8)\n" \
    "v_mfma_f32_16x16x32_bf16 v[42:45], v[126:129], v[102:105], v[42:45]\n" \
    "ds_read_b64_tr_b16 v[142:143], v155 offset:0\n" \
    "s_waitcnt lgkmcnt(8)\n" \
    "v_mfma_f32_16x16x32_bf16 v[62:65], v[102:105], v[130:133], v[62:65]\n" \
    "ds_read_b64_tr_b16 v[144:145], v155 offset:2048\n" \
    "s_waitcnt lgkmcnt(7)\n" \
    "v_mfma_f32_16x16x32_bf16 v[82:85], v[134:137], v[102:105], v[82:85]\n" \
    "ds_read_b64_tr_b16 v[146:147], v156 offset:0\n" \
    "s_waitcnt lgkmcnt(5)\n" \
    "v_mfma_f32_16x16x32_bf16 v[22:25], v[122:125], v[102:105], v[22:25]\n" \
    "ds_read_b128 v[102:105], %[ap1] offset:2048\n" \
    "v_mfma_f32_16x16x32_bf16 v[46:49], v[126:129], v[106:109], v[46:49]\n" \
    "ds_read_b64_tr_b16 v[148:149], v156 offset:2048\n" \
    "v_mfma_f32_16x16x32_bf16 v[66:69], v[106:109], v[130:133], v[66:69]\n" \
    "ds_read_b64_tr_b16 v[150:151], v157 offset:0\n" \
    "v_mfma_f32_16x16x32_bf16 v[86:89], v[134:137], v[106:109], v[86:89]\n" \
    "ds_read_b64_tr_b16 v[152:153], v157 offset:2048\n" \
    "v_mfma_f32_16x16x32_bf16 v[26:29], v[122:125], v[106:109], v[26:29]\n" \
    "ds_read_b128 v[106:109], %[ap1] offset:4096\n" \
    "s_waitcnt lgkmcnt(8)\n" \
    "v_mfma_f32_16x16x32_bf16 v[50:53], v[126:129], v[110:113], v[50:53]\n" \
    "ds_read_b64_tr_b16 v[138:139], v154 offset:0\n" \
    "v_mfma_f32_16x16x32_bf16 v[70:73], v[110:113], v[130:133], v[70:73]\n" \
    "ds_read_b64_tr_b16 v[140:141], v154 offset:2048\n" \
    "v_mfma_f32_16x16x32_bf16 v[90:93], v[134:137], v[110:113], v[90:93]\n" \
    "ds_read_b128 v[118:121], %[ap1] offset:8192\n" \
    "v_mfma_f32_16x16x32_bf16 v[30:33], v[122:125], v[110:113], v[30:33]\n" \
    "ds_read_b128 v[110:113], %[ap1] offset:6144\n" \
    "v_mfma_f32_16x16x32_bf16 v[54:57], v[126:129], v[114:117], v[54:57]\n" \
    "s_barrier\n" \
    "v_mfma_f32_16x16x32_bf16 v[74:77], v[114:117], v[130:133], v[74:77]\n" \
    "v_mfma_f32_16x16x32_bf16 v[94:97], v[134:137], v[114:117], v[94:97]\n" \
    "v_mfma_f32_16x16x32_bf16 v[34:37], v[122:125], v[114:117], v[34:37]\n" \
    "s_waitcnt lgkmcnt(8)\n" \
    "v_mfma_f32_16x16x32_bf16 v[42:45], v[142:145], v[102:105], v[42:45]\n" \
    "ds_read_b64_tr_b16 v[126:127], v155 offset:16384\n" \
    "s_waitcnt lgkmcnt(8)\n" \
    "v_mfma_f32_16x16x32_bf16 v[62:65], v[102:105], v[146:149], v[62:65]\n" \
    "ds_read_b64_tr_b16 v[128:129], v155 offset:18432\n" \
    "s_waitcnt lgkmcnt(7)\n" \
    "v_mfma_f32_16x16x32_bf16 v[82:85], v[150:153], v[102:105], v[82:85]\n" \
    "ds_read_b64_tr_b16 v[130:131], v156 offset:16384\n" \
    "s_waitcnt lgkmcnt(5)\n" \
    "v_mfma_f32_16x16x32_bf16 v[22:25], v[138:141], v[102:105], v[22:25]\n" \
    "ds_read_b128 v[102:105], %[ap0] offset:20480\n" \
    "v_mfma_f32_16x16x32_bf16 v[46:49], v[142:145], v[106:109], v[46:49]\n" \
    "ds_read_b64_tr_b16 v[132:133], v156 offset:18432\n" \
    "v_mfma_f32_16x16x32_bf16 v[66:69], v[106:109], v[146:149], v[66:69]\n" \
    "ds_read_b64_tr_b16 v[134:135], v157 offset:16384\n" \
    "v_mfma_f32_16x16x32_bf16 v[86:89], v[150:153], v[106:109], v[86:89]\n" \
    "ds_read_b64_tr_b16 v[136:137], v157 offset:18432\n" \
    "v_mfma_f32_16x16x32_bf16 v[26:29], v[138:141], v[106:109], v[26:29]\n" \
    "ds_read_b128 v[106:109], %[ap0] offset:22528\n" \
    "s_waitcnt lgkmcnt(8)\n" \
    "v_mfma_f32_16x16x32_bf16 v[50:53], v[142:145], v[110:113], v[50:53]\n" \
    "ds_read_b64_tr_b16 v[122:123], v154 offset:16384\n" \
    "v_mfma_f32_16x16x32_bf16 v[70:73], v[110:113], v[146:149], v[70:73]\n" \
    "ds_read_b64_tr_b16 v[124:125], v154 offset:18432\n" \
    "v_mfma_f32_16x16x32_bf16 v[90:93], v[150:153], v[110:113], v[90:93]\n" \
    "ds_read_b128 v[114:117], %[ap0] offset:26624\n" \
    "v_mfma_f32_16x16x32_bf16 v[30:33], v[138:141], v[110:113], v[30:33]\n" \
    "ds_read_b128 v[110:113], %[ap0] offset:24576\n" \
    "v_mfma_f32_16x16x32_bf16 v[54:57], v[142:145], v[118:121], v[54:57]\n" \
    "s_barrier\n" \
    "v_mfma_f32_16x16x32_bf16 v[74:77], v[118:121], v[146:149], v[74:77]\n" \
    "v_mfma_f32_16x16x32_bf16 v[94:97], v[150:153], v[118:121], v[94:97]\n" \
    "v_mfma_f32_16x16x32_bf16 v[34:37], v[138:141], v[118:121], v[34:37]\n" \
    "s_waitcnt lgkmcnt(8)\n" \
    "v_mfma_f32_16x16x32_bf16 v[42:45], v[126:129], v[102:105], v[42:45]\n" \
    "ds_read_b64_tr_b16 v[142:143], v155 offset:32768\n" \
    "s_waitcnt lgkmcnt(8)\n" \
    "v_mfma_f32_16x16x32_bf16 v[62:65], v[102:105], v[130:133], v[62:65]\n" \
    "ds_read_b64_tr_b16 v[144:145], v155 offset:34816\n" \
    "s_waitcnt lgkmcnt(7)\n" \
    "v_mfma_f32_16x16x32_bf16 v[82:85], v[134:137], v[102:105], v[82:85]\n" \
    "ds_read_b64_tr_b16 v[146:147], v156 offset:32768\n" \
    "s_waitcnt lgkmcnt(5)\n" \
    "v_mfma_f32_16x16x32_bf16 v[22:25], v[122:125], v[102:105], v[22:25]\n" \
    "ds_read_b128 v[102:105], %[ap1] offset:20480\n" \
    "v_mfma_f32_16x16x32_bf16 v[46:49], v[126:129], v[106:109], v[46:49]\n" \
    "ds_read_b64_tr_b16 v[148:149], v156 offset:34816\n" \
    "v_mfma_f32_16x16x32_bf16 v[66:69], v[106:109], v[130:133], v[66:69]\n" \
    "ds_read_b64_tr_b16 v[150:151], v157 offset:32768\n" \
    "v_mfma_f32_16x16x32_bf16 v[86:89], v[134:137], v[106:109], v[86:89]\n" \
    "ds_read_b64_tr_b16 v[152:153], v157 offset:34816\n" \
    "v_mfma_f32_16x16x32_bf16 v[26:29], v[122:125], v[106:109], v[26:29]\n" \
    "ds_read_b128 v[106:109], %[ap1] offset:22528\n" \
    "s_waitcnt lgkmcnt(8)\n" \
    "v_mfma_f32_16x16x32_bf16 v[50:53], v[126:129], v[110:113], v[50:53]\n" \
    "ds_read_b64_tr_b16 v[138:139], v154 offset:32768\n" \
    "v_mfma_f32_16x16x32_bf16 v[70:73], v[110:113], v[130:133], v[70:73]\n" \
    "ds_read_b64_tr_b16 v[140:141], v154 offset:34816\n" \
    "v_mfma_f32_16x16x32_bf16 v[90:93], v[134:137], v[110:113], v[90:93]\n" \
    "ds_read_b128 v[118:121], %[ap1] offset:26624\n" \
    "v_mfma_f32_16x16x32_bf16 v[30:33], v[122:125], v[110:113], v[30:33]\n" \
    "ds_read_b128 v[110:113], %[ap1] offset:24576\n" \
    "v_mfma_f32_16x16x32_bf16 v[54:57], v[126:129], v[114:117], v[54:57]\n" \
    "s_barrier\n" \
    "v_mfma_f32_16x16x32_bf16 v[74:77], v[114:117], v[130:133], v[74:77]\n" \
    "v_mfma_f32_16x16x32_bf16 v[94:97], v[134:137], v[114:117], v[94:97]\n" \
    "v_mfma_f32_16x16x32_bf16 v[34:37], v[122:125], v[114:117], v[34:37]\n" \
    "s_waitcnt lgkmcnt(8)\n" \
    "v_mfma_f32_16x16x32_bf16 v[42:45], v[142:145], v[102:105], v[42:45]\n" \
    "ds_read_b64_tr_b16 v[126:127], %[wp1] offset:0\n" \
    "s_waitcnt lgkmcnt(8)\n" \
    "v_mfma_f32_16x16x32_bf16 v[62:65], v[102:105], v[146:149], v[62:65]\n" \
    "ds_read_b64_tr_b16 v[128:129], %[wp1] offset:2048\n" \
    "s_waitcnt lgkmcnt(7)\n" \
    "v_mfma_f32_16x16x32_bf16 v[82:85], v[150:153], v[102:105], v[82:85]\n" \
    "ds_read_b64_tr_b16 v[130:131], %[wp2] offset:0\n" \
    "s_waitcnt lgkmcnt(5)\n" \
    "v_mfma_f32_16x16x32_bf16 v[22:25], v[138:141], v[102:105], v[22:25]\n" \
    "ds_read_b128 v[102:105], %[ap0] offset:55296\n" \
    "v_mfma_f32_16x16x32_bf16 v[46:49], v[142:145], v[106:109], v[46:49]\n" \
    "ds_read_b64_tr_b16 v[132:133], %[wp2] offset:2048\n" \
    "v_mfma_f32_16x16x32_bf16 v[66:69], v[106:109], v[146:149], v[66:69]\n" \
    "ds_read_b64_tr_b16 v[134:135], %[wp3] offset:0\n" \
    "v_mfma_f32_16x16x32_bf16 v[86:89], v[150:153], v[106:109], v[86:89]\n" \
    "ds_read_b64_tr_b16 v[136:137], %[wp3] offset:2048\n" \
    "v_mfma_f32_16x16x32_bf16 v[26:29], v[138:141], v[106:109], v[26:29]\n" \
    "ds_read_b128 v[106:109], %[ap0] offset:57344\n" \
    "s_waitcnt lgkmcnt(8)\n" \
    "v_mfma_f32_16x16x32_bf16 v[50:53], v[142:145], v[110:113], v[50:53]\n" \
    "ds_read_b64_tr_b16 v[122:123], %[wp0] offset:0\n" \
    "v_mfma_f32_16x16x32_bf16 v[70:73], v[110:113], v[146:149], v[70:73]\n" \
    "ds_read_b64_tr_b16 v[124:125], %[wp0] offset:2048\n" \
    "v_mfma_f32_16x16x32_bf16 v[90:93], v[150:153], v[110:113], v[90:93]\n" \
    "ds_read_b128 v[114:117], %[ap0] offset:61440\n" \
    "v_mfma_f32_16x16x32_bf16 v[30:33], v[138:141], v[110:113], v[30:33]\n" \
    "ds_read_b128 v[110:113], %[ap0] offset:59392\n" \
    "v_mfma_f32_16x16x32_bf16 v[54:57], v[142:145], v[118:121], v[54:57]\n" \
    "s_barrier\n" \
    "v_mfma_f32_16x16x32_bf16 v[74:77], v[118:121], v[146:149], v[74:77]\n" \
    "v_mfma_f32_16x16x32_bf16 v[94:97], v[150:153], v[118:121], v[94:97]\n" \
    "v_mfma_f32_16x16x32_bf16 v[34:37], v[138:141], v[118:121], v[34:37]\n" \
    "s_waitcnt lgkmcnt(8)\n" \
    "v_mfma_f32_16x16x32_bf16 v[42:45], v[126:129], v[102:105], v[42:45]\n" \
    "ds_read_b64_tr_b16 v[142:143], %[wp1] offset:34816\n" \
    "s_waitcnt lgkmcnt(8)\n" \
    "v_mfma_f32_16x16x32_bf16 v[62:65], v[102:105], v[130:133], v[62:65]\n" \
    "ds_read_b64_tr_b16 v[144:145], %[wp1] offset:36864\n" \
    "s_waitcnt lgkmcnt(7)\n" \
    "v_mfma_f32_16x16x32_bf16 v[82:85], v[134:137], v[102:105], v[82:85]\n" \
    "ds_read_b64_tr_b16 v[146:147], %[wp2] offset:34816\n" \
    "s_waitcnt lgkmcnt(5)\n" \
    "v_mfma_f32_16x16x32_bf16 v[22:25], v[122:125], v[102:105], v[22:25]\n" \
    "ds_read_b128 v[102:105], %[ap1] offset:55296\n" \
    "v_mfma_f32_16x16x32_bf16 v[46:49], v[126:129], v[106:109], v[46:49]\n" \
    "ds_read_b64_tr_b16 v[148:149], %[wp2] offset:36864\n" \
    "v_mfma_f32_16x16x32_bf16 v[66:69], v[106:109], v[130:133], v[66:69]\n" \
    "ds_read_b64_tr_b16 v[150:151], %[wp3] offset:34816\n" \
    "v_mfma_f32_16x16x32_bf16 v[86:89], v[134:137], v[106:109], v[86:89]\n" \
    "ds_read_b64_tr_b16 v[152:153], %[wp3] offset:36864\n" \
    "v_mfma_f32_16x16x32_bf16 v[26:29], v[122:125], v[106:109], v[26:29]\n" \
    "ds_read_b128 v[106:109], %[ap1] offset:57344\n" \
    "s_waitcnt lgkmcnt(8)\n" \
    "v_mfma_f32_16x16x32_bf16 v[50:53], v[126:129], v[110:113], v[50:53]\n" \
    "ds_read_b64_tr_b16 v[138:139], %[wp0] offset:34816\n" \
    "v_mfma_f32_16x16x32_bf16 v[70:73], v[110:113], v[130:133], v[70:73]\n" \
    "ds_read_b64_tr_b16 v[140:141], %[wp0] offset:36864\n" \
    "v_mfma_f32_16x16x32_bf16 v[90:93], v[134:137], v[110:113], v[90:93]\n" \
    "ds_read_b128 v[118:121], %[ap1] offset:61440\n" \
    "v_mfma_f32_16x16x32_bf16 v[30:33], v[122:125], v[110:113], v[30:33]\n" \
    "ds_read_b128 v[110:113], %[ap1] offset:59392\n" \
    "v_mfma_f32_16x16x32_bf16 v[54:57], v[126:129], v[114:117], v[54:57]\n" \
    "s_barrier\n" \
    "v_mfma_f32_16x16x32_bf16 v[74:77], v[114:117], v[130:133], v[74:77]\n" \
    "v_mfma_f32_16x16x32_bf16 v[94:97], v[134:137], v[114:117], v[94:97]\n" \
    "v_mfma_f32_16x16x32_bf16 v[34:37], v[122:125], v[114:117], v[34:37]\n" \
    "s_waitcnt lgkmcnt(8)\n" \
    "v_mfma_f32_16x16x32_bf16 v[42:45], v[142:145], v[102:105], v[42:45]\n" \
    "ds_read_b64_tr_b16 v[126:127], %[wp1] offset:51200\n" \
    "s_waitcnt lgkmcnt(8)\n" \
    "v_mfma_f32_16x16x32_bf16 v[62:65], v[102:105], v[146:149], v[62:65]\n" \
    "ds_read_b64_tr_b16 v[128:129], %[wp1] offset:53248\n" \
    "s_waitcnt lgkmcnt(7)\n" \
    "v_mfma_f32_16x16x32_bf16 v[82:85], v[150:153], v[102:105], v[82:85]\n" \
    "ds_read_b64_tr_b16 v[130:131], %[wp2] offset:51200\n" \
    "s_waitcnt lgkmcnt(5)\n" \
    "v_mfma_f32_16x16x32_bf16 v[22:25], v[138:141], v[102:105], v[22:25]\n" \
    "ds_read_b128 v[102:105], %[ap0] offset:2048\n" \
    "v_mfma_f32_16x16x32_bf16 v[46:49], v[142:145], v[106:109], v[46:49]\n" \
    "ds_read_b64_tr_b16 v[132:133], %[wp2] offset:53248\n" \
    "v_mfma_f32_16x16x32_bf16 v[66:69], v[106:109], v[146:149], v[66:69]\n" \
    "ds_read_b64_tr_b16 v[134:135], %[wp3] offset:51200\n" \
    "v_mfma_f32_16x16x32_bf16 v[86:89], v[150:153], v[106:109], v[86:89]\n" \
    "ds_read_b64_tr_b16 v[136:137], %[wp3] offset:53248\n" \
    "v_mfma_f32_16x16x32_bf16 v[26:29], v[138:141], v[106:109], v[26:29]\n" \
    "ds_read_b128 v[106:109], %[ap0] offset:4096\n" \
    "s_waitcnt lgkmcnt(8)\n" \
    "v_mfma_f32_16x16x32_bf16 v[50:53], v[142:145], v[110:113], v[50:53]\n" \
    "ds_read_b64_tr_b16 v[122:123], %[wp0] offset:51200\n" \
    "v_mfma_f32_16x16x32_bf16 v[70:73], v[110:113], v[146:149], v[70:73]\n" \
    "ds_read_b64_tr_b16 v[124:125], %[wp0] offset:53248\n" \
    "v_mfma_f32_16x16x32_bf16 v[90:93], v[150:153], v[110:113], v[90:93]\n" \
    "ds_read_b128 v[114:117], %[ap0] offset:8192\n" \
    "v_mfma_f32_16x16x32_bf16 v[30:33], v[138:141], v[110:113], v[30:33]\n" \
    "ds_read_b128 v[110:113], %[ap0] offset:6144\n" \
    "v_mfma_f32_16x16x32_bf16 v[54:57], v[142:145], v[118:121], v[54:57]\n" \
    "s_barrier\n" \
    "v_mfma_f32_16x16x32_bf16 v[74:77], v[118:121], v[146:149], v[74:77]\n" \
    "v_mfma_f32_16x16x32_bf16 v[94:97], v[150:153], v[118:121], v[94:97]\n" \
    "v_mfma_f32_16x16x32_bf16 v[34:37], v[138:141], v[118:121], v[34:37]\n" \
    "s_waitcnt lgkmcnt(8)\n" \
    "v_mfma_f32_16x16x32_bf16 v[42:45], v[126:129], v[102:105], v[42:45]\n" \
    "ds_read_b64_tr_b16 v[142:143], v155 offset:0\n" \
    "s_waitcnt lgkmcnt(8)\n" \
    "v_mfma_f32_16x16x32_bf16 v[62:65], v[102:105], v[130:133], v[62:65]\n" \
    "ds_read_b64_tr_b16 v[144:145], v155 offset:2048\n" \
    "s_waitcnt lgkmcnt(7)\n" \
    "v_mfma_f32_16x16x32_bf16 v[82:85], v[134:137], v[102:105], v[82:85]\n" \
    "ds_read_b64_tr_b16 v[146:147], v156 offset:0\n" \
    "s_waitcnt lgkmcnt(5)\n" \
    "v_mfma_f32_16x16x32_bf16 v[22:25], v[122:125], v[102:105], v[22:25]\n" \
    "ds_read_b128 v[102:105], %[ap1] offset:2048\n" \
    "v_mfma_f32_16x16x32_bf16 v[46:49], v[126:129], v[106:109], v[46:49]\n" \
    "ds_read_b64_tr_b16 v[148:149], v156 offset:2048\n" \
    "v_mfma_f32_16x16x32_bf16 v[66:69], v[106:109], v[130:133], v[66:69]\n" \
    "ds_read_b64_tr_b16 v[150:151], v157 offset:0\n" \
    "v_mfma_f32_16x16x32_bf16 v[86:89], v[134:137], v[106:109], v[86:89]\n" \
    "ds_read_b64_tr_b16 v[152:153], v157 offset:2048\n" \
    "v_mfma_f32_16x16x32_bf16 v[26:29], v[122:125], v[106:109], v[26:29]\n" \
    "ds_read_b128 v[106:109], %[ap1] offset:4096\n" \
    "s_waitcnt lgkmcnt(8)\n" \
    "v_mfma_f32_16x16x32_bf16 v[50:53], v[126:129], v[110:113], v[50:53]\n" \
    "ds_read_b64_tr_b16 v[138:139], v154 offset:0\n" \
    "v_mfma_f32_16x16x32_bf16 v[70:73], v[110:113], v[130:133], v[70:73]\n" \
    "ds_read_b64_tr_b16 v[140:141], v154 offset:2048\n" \
    "v_mfma_f32_16x16x32_bf16 v[90:93], v[134:137], v[110:113], v[90:93]\n" \
    "ds_read_b128 v[118:121], %[ap1] offset:8192\n" \
    "v_mfma_f32_16x16x32_bf16 v[30:33], v[122:125], v[110:113], v[30:33]\n" \
    "ds_read_b128 v[110:113], %[ap1] offset:6144\n" \
    "v_mfma_f32_16x16x32_bf16 v[54:57], v[126:129], v[114:117], v[54:57]\n" \
    "s_barrier\n" \
    "v_mfma_f32_16x16x32_bf16 v[74:77], v[114:117], v[130:133], v[74:77]\n" \
    "v_mfma_f32_16x16x32_bf16 v[94:97], v[134:137], v[114:117], v[94:97]\n" \
    "v_mfma_f32_16x16x32_bf16 v[34:37], v[122:125], v[114:117], v[34:37]\n" \
    "s_waitcnt lgkmcnt(8)\n" \
    "v_mfma_f32_16x16x32_bf16 v[42:45], v[142:145], v[102:105], v[42:45]\n" \
    "ds_read_b64_tr_b16 v[126:127], v155 offset:16384\n" \
    "s_waitcnt lgkmcnt(8)\n" \
    "v_mfma_f32_16x16x32_bf16 v[62:65], v[102:105], v[146:149], v[62:65]\n" \
    "ds_read_b64_tr_b16 v[128:129], v155 offset:18432\n" \
    "s_waitcnt lgkmcnt(7)\n" \
    "v_mfma_f32_16x16x32_bf16 v[82:85], v[150:153], v[102:105], v[82:85]\n" \
    "ds_read_b64_tr_b16 v[130:131], v156 offset:16384\n" \
    "s_waitcnt lgkmcnt(5)\n" \
    "v_mfma_f32_16x16x32_bf16 v[22:25], v[138:141], v[102:105], v[22:25]\n" \
    "ds_read_b128 v[102:105], %[ap0] offset:20480\n" \
    "v_mfma_f32_16x16x32_bf16 v[46:49], v[142:145], v[106:109], v[46:49]\n" \
    "ds_read_b64_tr_b16 v[132:133], v156 offset:18432\n" \
    "v_mfma_f32_16x16x32_bf16 v[66:69], v[106:109], v[146:149], v[66:69]\n" \
    "ds_read_b64_tr_b16 v[134:135], v157 offset:16384\n" \
    "v_mfma_f32_16x16x32_bf16 v[86:89], v[150:153], v[106:109], v[86:89]\n" \
    "ds_read_b64_tr_b16 v[136:137], v157 offset:18432\n" \
    "v_mfma_f32_16x16x32_bf16 v[26:29], v[138:141], v[106:109], v[26:29]\n" \
    "ds_read_b128 v[106:109], %[ap0] offset:22528\n" \
    "s_waitcnt lgkmcnt(8)\n" \
    "v_mfma_f32_16x16x32_bf16 v[50:53], v[142:145], v[110:113], v[50:53]\n" \
    "ds_read_b64_tr_b16 v[122:123], v154 offset:16384\n" \
    "v_mfma_f32_16x16x32_bf16 v[70:73], v[110:113], v[146:149], v[70:73]\n" \
    "ds_read_b64_tr_b16 v[124:125], v154 offset:18432\n" \
    "v_mfma_f32_16x16x32_bf16 v[90:93], v[150:153], v[110:113], v[90:93]\n" \
    "ds_read_b128 v[114:117], %[ap0] offset:26624\n" \
    "v_mfma_f32_16x16x32_bf16 v[30:33], v[138:141], v[110:113], v[30:33]\n" \
    "ds_read_b128 v[110:113], %[ap0] offset:24576\n" \
    "v_mfma_f32_16x16x32_bf16 v[54:57], v[142:145], v[118:121], v[54:57]\n" \
    "s_barrier\n" \
    "v_mfma_f32_16x16x32_bf16 v[74:77], v[118:121], v[146:149], v[74:77]\n" \
    "v_mfma_f32_16x16x32_bf16 v[94:97], v[150:153], v[118:121], v[94:97]\n" \
    "v_mfma_f32_16x16x32_bf16 v[34:37], v[138:141], v[118:121], v[34:37]\n" \
    "s_waitcnt lgkmcnt(8)\n" \
    "v_mfma_f32_16x16x32_bf16 v[42:45], v[126:129], v[102:105], v[42:45]\n" \
    "ds_read_b64_tr_b16 v[142:143], v155 offset:32768\n" \
    "s_waitcnt lgkmcnt(8)\n" \
    "v_mfma_f32_16x16x32_bf16 v[62:65], v[102:105], v[130:133], v[62:65]\n" \
    "ds_read_b64_tr_b16 v[144:145], v155 offset:34816\n" \
    "s_waitcnt lgkmcnt(7)\n" \
    "v_mfma_f32_16x16x32_bf16 v[82:85], v[134:137], v[102:105], v[82:85]\n" \
    "ds_read_b64_tr_b16 v[146:147], v156 offset:32768\n" \
    "s_waitcnt lgkmcnt(5)\n" \
    "v_mfma_f32_16x16x32_bf16 v[22:25], v[122:125], v[102:105], v[22:25]\n" \
    "ds_read_b128 v[102:105], %[ap1] offset:20480\n" \
    "v_mfma_f32_16x16x32_bf16 v[46:49], v[126:129], v[106:109], v[46:49]\n" \
    "ds_read_b64_tr_b16 v[148:149], v156 offset:34816\n" \
    "v_mfma_f32_16x16x32_bf16 v[66:69], v[106:109], v[130:133], v[66:69]\n" \
    "ds_read_b64_tr_b16 v[150:151], v157 offset:32768\n" \
    "v_mfma_f32_16x16x32_bf16 v[86:89], v[134:137], v[106:109], v[86:89]\n" \
    "ds_read_b64_tr_b16 v[152:153], v157 offset:34816\n" \
    "v_mfma_f32_16x16x32_bf16 v[26:29], v[122:125], v[106:109], v[26:29]\n" \
    "ds_read_b128 v[106:109], %[ap1] offset:22528\n" \
    "s_waitcnt lgkmcnt(8)\n" \
    "v_mfma_f32_16x16x32_bf16 v[50:53], v[126:129], v[110:113], v[50:53]\n" \
    "ds_read_b64_tr_b16 v[138:139], v154 offset:32768\n" \
    "v_mfma_f32_16x16x32_bf16 v[70:73], v[110:113], v[130:133], v[70:73]\n" \
    "ds_read_b64_tr_b16 v[140:141], v154 offset:34816\n" \
    "v_mfma_f32_16x16x32_bf16 v[90:93], v[134:137], v[110:113], v[90:93]\n" \
    "ds_read_b128 v[118:121], %[ap1] offset:26624\n" \
    "v_mfma_f32_16x16x32_bf16 v[30:33], v[122:125], v[110:113], v[30:33]\n" \
    "ds_read_b128 v[110:113], %[ap1] offset:24576\n" \
    "v_mov_b32 v161, 16\n" \
    "ds_write_b32 %[fr], v161\n" \
    "v_mfma_f32_16x16x32_bf16 v[54:57], v[126:129], v[114:117], v[54:57]\n" \
    "s_barrier\n" \
    "v_mfma_f32_16x16x32_bf16 v[74:77], v[114:117], v[130:133], v[74:77]\n" \
    "v_mfma_f32_16x16x32_bf16 v[94:97], v[134:137], v[114:117], v[94:97]\n" \
    "v_mfma_f32_16x16x32_bf16 v[34:37], v[122:125], v[114:117], v[34:37]\n" \
    "s_waitcnt lgkmcnt(9)\n" \
    "v_mfma_f32_16x16x32_bf16 v[42:45], v[142:145], v[102:105], v[42:45]\n" \
    "ds_read_b64_tr_b16 v[126:127], %[wp1] offset:0\n" \
    "s_waitcnt lgkmcnt(9)\n" \
    "v_mfma_f32_16x16x32_bf16 v[62:65], v[102:105], v[146:149], v[62:65]\n" \
    "ds_read_b64_tr_b16 v[128:129], %[wp1] offset:2048\n" \
    "s_waitcnt lgkmcnt(8)\n" \
    "v_mfma_f32_16x16x32_bf16 v[82:85], v[150:153], v[102:105], v[82:85]\n" \
    "ds_read_b64_tr_b16 v[130:131], %[wp2] offset:0\n" \
    "s_waitcnt lgkmcnt(6)\n" \
    "v_mfma_f32_16x16x32_bf16 v[22:25], v[138:141], v[102:105], v[22:25]\n" \
    "ds_read_b128 v[102:105], %[ap0] offset:55296\n" \
    "v_mfma_f32_16x16x32_bf16 v[46:49], v[142:145], v[106:109], v[46:49]\n" \
    "ds_read_b64_tr_b16 v[132:133], %[wp2] offset:2048\n" \
    "v_mfma_f32_16x16x32_bf16 v[66:69], v[106:109], v[146:149], v[66:69]\n" \
    "ds_read_b64_tr_b16 v[134:135], %[wp3] offset:0\n" \
    "v_mfma_f32_16x16x32_bf16 v[86:89], v[150:153], v[106:109], v[86:89]\n" \
    "ds_read_b64_tr_b16 v[136:137], %[wp3] offset:2048\n" \
    "v_mfma_f32_16x16x32_bf16 v[26:29], v[138:141], v[106:109], v[26:29]\n" \
    "ds_read_b128 v[106:109], %[ap0] offset:57344\n" \
    "s_waitcnt lgkmcnt(9)\n" \
    "v_mfma_f32_16x16x32_bf16 v[50:53], v[142:145], v[110:113], v[50:53]\n" \
    "ds_read_b64_tr_b16 v[122:123], %[wp0] offset:0\n" \
    "v_mfma_f32_16x16x32_bf16 v[70:73], v[110:113], v[146:149], v[70:73]\n" \
    "ds_read_b64_tr_b16 v[124:125], %[wp0] offset:2048\n" \
    "v_mfma_f32_16x16x32_bf16 v[90:93], v[150:153], v[110:113], v[90:93]\n" \
    "ds_read_b128 v[114:117], %[ap0] offset:61440\n" \
    "v_mfma_f32_16x16x32_bf16 v[30:33], v[138:141], v[110:113], v[30:33]\n" \
    "ds_read_b128 v[110:113], %[ap0] offset:59392\n" \
    "v_mov_b32 v161, 17\n" \
    "ds_write_b32 %[fr], v161\n" \
    "v_mfma_f32_16x16x32_bf16 v[54:57], v[142:145], v[118:121], v[54:57]\n" \
    "s_barrier\n" \
    "ds_read_b64_tr_b16 v[142:143], %[wp1] offset:34816\n" \
    "ds_read_b64_tr_b16 v[144:145], %[wp1] offset:36864\n" \
    "v_mfma_f32_16x16x32_bf16 v[74:77], v[118:121], v[146:149], v[74:77]\n" \
    "ds_read_b64_tr_b16 v[146:147], %[wp2] offset:34816\n" \
    "ds_read_b64_tr_b16 v[148:149], %[wp2] offset:36864\n" \
    "v_mfma_f32_16x16x32_bf16 v[94:97], v[150:153], v[118:121], v[94:97]\n" \
    "ds_read_b64_tr_b16 v[150:151], %[wp3] offset:34816\n" \
    "ds_read_b64_tr_b16 v[152:153], %[wp3] offset:36864\n" \
    "v_mfma_f32_16x16x32_bf16 v[34:37], v[138:141], v[118:121], v[34:37]\n" \
    "ds_read_b128 v[118:121], %[ap1] offset:61440\n" \
    "ds_read_b64_tr_b16 v[138:139], %[wp0] offset:34816\n" \
    "ds_read_b64_tr_b16 v[140:141], %[wp0] offset:36864\n" \
    "s_waitcnt lgkmcnt(11)\n" \
    "v_mfma_f32_16x16x32_bf16 v[54:57], v[126:129], v[114:117], v[54:57]\n" \
    "v_add_u32 %[wp0], %[tabc], %[l16]\n" \
    "v_cndmask_b32_e64 %[wp2], 0, -1, s[68:69]\n" \
    "v_mfma_f32_16x16x32_bf16 v[74:77], v[114:117], v[130:133], v[74:77]\n" \
    "v_add_u32 %[wp1], %[hxc], %[l16]\n" \
    "v_lshrrev_b32 %[wp3], 2, %[l16]\n" \
    "v_mfma_f32_16x16x32_bf16 v[94:97], v[134:137], v[114:117], v[94:97]\n" \
    "v_add_u32 %[wp3], %[hxd], %[wp3]\n" \
    "v_mfma_f32_16x16x32_bf16 v[34:37], v[122:125], v[114:117], v[34:37]\n" \
    "s_waitcnt lgkmcnt(10)\n" \
    "v_mfma_f32_16x16x32_bf16 v[50:53], v[126:129], v[110:113], v[50:53]\n" \
    "v_mfma_f32_16x16x32_bf16 v[70:73], v[110:113], v[130:133], v[70:73]\n" \
    "v_mfma_f32_16x16x32_bf16 v[90:93], v[134:137], v[110:113], v[90:93]\n" \
    "v_mfma_f32_16x16x32_bf16 v[30:33], v[122:125], v[110:113], v[30:33]\n" \
    "ds_read_b128 v[110:113], %[ap1] offset:59392\n" \
    "s_waitcnt lgkmcnt(3)\n" \
    "v_mfma_f32_16x16x32_bf16 v[54:57], v[142:145], v[118:121], v[54:57]\n" \
    "v_cndmask_b32_e64 v154, %[zr], %[vxb], s[60:61]\n" \
    "v_cndmask_b32_e64 v155, %[zr], %[vxb], s[62:63]\n" \
    "v_mfma_f32_16x16x32_bf16 v[74:77], v[118:121], v[146:149], v[74:77]\n" \
    "v_cndmask_b32_e64 v156, %[zr], %[vxb], s[64:65]\n" \
    "v_cndmask_b32_e64 v157, %[zr], %[vxb], s[66:67]\n" \
    "v_mfma_f32_16x16x32_bf16 v[94:97], v[150:153], v[118:121], v[94:97]\n" \
    "v_mov_b32 v41, 0\n" \
    "s_waitcnt lgkmcnt(1)\n" \
    "v_mfma_f32_16x16x32_bf16 v[34:37], v[138:141], v[118:121], v[34:37]\n" \
    "v_mfma_f32_16x16x32_bf16 v[46:49], v[126:129], v[106:109], v[46:49]\n" \
    "v_mfma_f32_16x16x32_bf16 v[66:69], v[106:109], v[130:133], v[66:69]\n" \
    "v_cvt_pk_bf16_f32 v54, v54, v55\n" \
    "v_cvt_pk_bf16_f32 v55, v56, v57\n" \
    "v_mfma_f32_16x16x32_bf16 v[86:89], v[134:137], v[106:109], v[86:89]\n" \
    "v_cvt_pk_bf16_f32 v120, v74, v75\n" \
    "v_cvt_pk_bf16_f32 v121, v76, v77\n" \
    "v_mfma_f32_16x16x32_bf16 v[26:29], v[122:125], v[106:109], v[26:29]\n" \
    "ds_read_b128 v[106:109], %[ap1] offset:57344\n" \
    "v_cvt_pk_bf16_f32 v94, v94, v95\n" \
    "v_cvt_pk_bf16_f32 v56, v96, v97\n" \
    "ds_write2_b32 %[scw], v94, v56 offset0:128 offset1:129\n" \
    "s_waitcnt lgkmcnt(2)\n" \
    "v_mfma_f32_16x16x32_bf16 v[50:53], v[142:145], v[110:113], v[50:53]\n" \
    "v_permlane16_swap_b32 v94, v56\n" \
    "v_mfma_f32_16x16x32_bf16 v[70:73], v[110:113], v[146:149], v[70:73]\n" \
    "v_cvt_pk_bf16_f32 v34, v34, v35\n" \
    "v_cvt_pk_bf16_f32 v35, v36, v37\n" \
    "v_mfma_f32_16x16x32_bf16 v[90:93], v[150:153], v[110:113], v[90:93]\n" \
    "v_and_b32 v36, v94, %[wp2]\n" \
    "v_mov_b32 v37, 0\n" \
    "v_mov_b32 v57, 0\n" \
    "v_mfma_f32_16x16x32_bf16 v[30:33], v[138:141], v[110:113], v[30:33]\n" \
    "v_mfma_f32_16x16x32_bf16 v[42:45], v[126:129], v[102:105], v[42:45]\n" \
    "v_mfma_f32_16x16x32_bf16 v[62:65], v[102:105], v[130:133], v[62:65]\n" \
    "v_cvt_pk_bf16_f32 v50, v50, v51\n" \
    "v_cvt_pk_bf16_f32 v51, v52, v53\n" \
    "v_mfma_f32_16x16x32_bf16 v[82:85], v[134:137], v[102:105], v[82:85]\n" \
    "v_cvt_pk_bf16_f32 v118, v70, v71\n" \
    "v_cvt_pk_bf16_f32 v119, v72, v73\n" \
    "v_mfma_f32_16x16x32_bf16 v[22:25], v[122:125], v[102:105], v[22:25]\n" \
    "ds_read_b128 v[102:105], %[ap1] offset:55296\n" \
    "v_mov_b32 v161, 18\n" \
    "ds_write_b32 %[fr], v161\n" \
    "ds_read_b128 v[126:129], %[wp0] offset:1024\n" \
    "ds_read_b128 v[122:125], %[wp0]\n" \
    "ds_read_b128 v[130:133], %[wp0] offset:2048\n" \
    "v_cvt_pk_bf16_f32 v90, v90, v91\n" \
    "v_cvt_pk_bf16_f32 v52, v92, v93\n" \
    "ds_write2_b32 %[scw], v90, v52 offset0:96 offset1:97\n" \
    "s_waitcnt lgkmcnt(7)\n" \
    "v_mfma_f32_16x16x32_bf16 v[46:49], v[142:145], v[106:109], v[46:49]\n" \
    "v_permlane16_swap_b32 v90, v52\n" \
    "ds_read_b32 v160, %[fr] offset:128\n" \
    "v_mfma_f32_16x16x32_bf16 v[66:69], v[106:109], v[146:149], v[66:69]\n" \
    "v_cvt_pk_bf16_f32 v30, v30, v31\n" \
    "v_cvt_pk_bf16_f32 v31, v32, v33\n" \
    "v_mfma_f32_16x16x32_bf16 v[86:89], v[150:153], v[106:109], v[86:89]\n" \
    "v_and_b32 v32, v90, %[wp2]\n" \
    "v_mov_b32 v33, 0\n" \
    "v_mov_b32 v53, 0\n" \
    "v_mfma_f32_16x16x32_bf16 v[26:29], v[138:141], v[106:109], v[26:29]\n" \
    "s_waitcnt lgkmcnt(6)\n" \
    "v_mfma_f32_16x16x32_bf16 v[42:45], v[142:145], v[102:105], v[42:45]\n" \
    "v_mfma_f32_16x16x32_bf16 v[62:65], v[102:105], v[146:149], v[62:65]\n" \
    "v_cvt_pk_bf16_f32 v46, v46, v47\n" \
    "v_cvt_pk_bf16_f32 v47, v48, v49\n" \
    "v_mfma_f32_16x16x32_bf16 v[82:85], v[150:153], v[102:105], v[82:85]\n" \
    "v_cvt_pk_bf16_f32 v116, v66, v67\n" \
    "v_cvt_pk_bf16_f32 v117, v68, v69\n" \
    "v_mfma_f32_16x16x32_bf16 v[22:25], v[138:141], v[102:105], v[22:25]\n" \
    "ds_read_b32 v159, %[zr] offset:1332\n" \
    "s_setprio 0\n" \
    "s_waitcnt lgkmcnt(1)\n" \
    "v_readfirstlane_b32 %[tmp], v160\n" \
    "s_cmp_lg_u32 %[tmp], 0\n" \
    "s_cbranch_scc1 Lhx_%=\n" \
    "s_mov_b32 %[cnt], 0\n" \
    "Lhxs_%=:\n" \
    "ds_read_b32 v160, %[fr] offset:128\n" \
    "s_add_u32 %[cnt], %[cnt], 1\n" \
    "s_waitcnt lgkmcnt(0)\n" \
    "v_readfirstlane_b32 %[tmp], v160\n" \
    "s_cmp_ge_u32 %[cnt], %[cap]\n" \
    "s_cbranch_scc1 Lhx_%=\n" \
    "s_cmp_eq_u32 %[tmp], 0\n" \
    "s_cbranch_scc1 Lhxs_%=\n" \
    "Lhx_%=:\n" \
    "ds_read_b64 v[38:39], %[wp1]\n" \
    "ds_read_b32 v40, %[wp3]\n" \
    "ds_read_b64 v[112:113], %[wp1] offset:8\n" \
    "v_mfma_f32_16x16x32_bf16 v[74:77], v[54:57], v[34:37], v[126:129]\n" \
    "v_cvt_pk_bf16_f32 v86, v86, v87\n" \
    "v_cvt_pk_bf16_f32 v48, v88, v89\n" \
    "ds_write2_b32 %[scw], v86, v48 offset0:64 offset1:65\n" \
    "v_mfma_f32_16x16x32_bf16 v[94:97], v[50:53], v[34:37], v[122:125]\n" \
    "v_cvt_pk_bf16_f32 v42, v42, v43\n" \
    "v_cvt_pk_bf16_f32 v43, v44, v45\n" \
    "v_permlane16_swap_b32 v86, v48\n" \
    "v_mfma_f32_16x16x32_bf16 v[70:73], v[50:53], v[30:33], v[126:129]\n" \
    "v_cvt_pk_bf16_f32 v26, v26, v27\n" \
    "v_cvt_pk_bf16_f32 v27, v28, v29\n" \
    "v_and_b32 v28, v86, %[wp2]\n" \
    "v_mov_b32 v29, 0\n" \
    "v_mov_b32 v49, 0\n" \
    "s_nop 1\n" \
    "v_mfma_f32_16x16x32_bf16 v[90:93], v[46:49], v[30:33], v[122:125]\n" \
    "v_cvt_pk_bf16_f32 v82, v82, v83\n" \
    "v_cvt_pk_bf16_f32 v44, v84, v85\n" \
    "ds_write2_b32 %[scw], v82, v44 offset0:32 offset1:33\n" \
    "v_cvt_pk_bf16_f32 v114, v62, v63\n" \
    "v_cvt_pk_bf16_f32 v115, v64, v65\n" \
    "v_mfma_f32_16x16x32_bf16 v[66:69], v[46:49], v[26:29], v[126:129]\n" \
    "v_mov_b32 v45, 0\n" \
    "v_permlane16_swap_b32 v82, v44\n" \
    "v_maximum3_f32 v144, v74, v75, v75\n" \
    "s_nop 0\n" \
    "v_mfma_f32_16x16x32_bf16 v[86:89], v[42:45], v[26:29], v[122:125]\n" \
    "v_cvt_pk_bf16_f32 v22, v22, v23\n" \
    "v_cvt_pk_bf16_f32 v23, v24, v25\n" \
    "v_and_b32 v24, v82, %[wp2]\n" \
    "v_mov_b32 v25, 0\n" \
    "v_maximum3_f32 v145, v76, v77, v77\n" \
    "ds_read_b64_tr_b16 v[106:107], v157 offset:384\n" \
    "ds_read_b64_tr_b16 v[108:109], v157 offset:512\n" \
    "v_mfma_f32_16x16x32_bf16 v[62:65], v[42:45], v[22:25], v[126:129]\n" \
    "v_maximum3_f32 v144, v94, v95, v144\n" \
    "v_maximum3_f32 v145, v96, v97, v145\n" \
    "s_waitcnt lgkmcnt(5)\n" \
    "v_mfma_f32_16x16x32_bf16 v[82:85], v[38:41], v[22:25], v[122:125]\n" \
    "ds_read_b64_tr_b16 v[102:103], v156 offset:256\n" \
    "ds_read_b64_tr_b16 v[104:105], v156 offset:384\n" \
    "v_maximum3_f32 v142, v70, v71, v71\n" \
    "v_maximum3_f32 v143, v72, v73, v73\n" \
    "ds_read_b64_tr_b16 v[98:99], v155 offset:128\n" \
    "ds_read_b64_tr_b16 v[100:101], v155 offset:256\n" \
    "v_maximum3_f32 v142, v90, v91, v142\n" \
    "v_maximum3_f32 v143, v92, v93, v143\n" \
    "ds_read_b64_tr_b16 v[134:135], v154 offset:0\n" \
    "ds_read_b64_tr_b16 v[136:137], v154 offset:128\n" \
    "v_maximum3_f32 v149, v144, v145, v145\n" \
    "v_maximum3_f32 v140, v66, v67, v67\n" \
    "v_maximum3_f32 v141, v68, v69, v69\n" \
    "v_maximum3_f32 v148, v142, v143, v143\n" \
    "v_maximum3_f32 v140, v86, v87, v140\n" \
    "v_maximum3_f32 v141, v88, v89, v141\n" \
    "v_maximum3_f32 v138, v62, v63, v63\n" \
    "v_maximum3_f32 v139, v64, v65, v65\n" \
    "v_maximum3_f32 v147, v140, v141, v141\n" \
    "v_maximum3_f32 v138, v82, v83, v138\n" \
    "v_maximum3_f32 v139, v84, v85, v139\n" \
    "v_maximum3_f32 v146, v138, v139, v139\n" \
    "s_waitcnt lgkmcnt(8)\n" \
    "Lend_%=:\n" \
    "s_nop 0\n" \
    "v_permlane16_swap_b32 v146, v147\n" \
    "v_permlane16_swap_b32 v148, v149\n" \
    "v_maximum3_f32 v146, v146, v147, v147\n" \
    "v_maximum3_f32 v148, v148, v149, v149\n" \
    "s_nop 1\n" \
    "v_permlane32_swap_b32 v146, v148\n" \
    "v_maximum3_f32 v146, v146, v148, v148\n" \
    "v_mul_f32 v146, 0x3ea52b5b, v146\n" \
    "v_mov_b32 v148, v146\n" \
    "s_nop 1\n" \
    "v_permlane32_swap_b32 v146, v148\n" \
    "v_mov_b32 v147, v146\n" \
    "v_mov_b32 v149, v148\n" \
    "s_nop 0\n" \
    "v_permlane16_swap_b32 v146, v147\n" \
    "v_permlane16_swap_b32 v148, v149\n" \
    "v_fma_f32 v138, v138, %[sc], -v146\n" \
    "v_fma_f32 v139, v139, %[sc], -v146\n" \
    "v_fma_f32 v140, v140, %[sc], -v147\n" \
    "v_fma_f32 v141, v141, %[sc], -v147\n" \
    "v_exp_f32 v138, v138\n" \
    "v_exp_f32 v139, v139\n" \
    "v_fma_f32 v142, v142, %[sc], -v148\n" \
    "v_fma_f32 v143, v143, %[sc], -v148\n" \
    "v_exp_f32 v140, v140\n" \
    "v_exp_f32 v141, v141\n" \
    "v_fma_f32 v144, v144, %[sc], -v149\n" \
    "v_fma_f32 v145, v145, %[sc], -v149\n" \
    "v_exp_f32 v142, v142\n" \
    "v_exp_f32 v143, v143\n" \
    "v_add_f32 v146, v138, v139\n" \
    "v_exp_f32 v144, v144\n" \
    "v_exp_f32 v145, v145\n" \
    "v_add_f32 v147, v140, v141\n" \
    "v_add_f32 v148, v142, v143\n" \
    "v_add_f32 v149, v144, v145\n" \
    "v_permlane16_swap_b32 v146, v147\n" \
    "s_nop 0\n" \
    "v_permlane16_swap_b32 v148, v149\n" \
    "v_add_f32 v146, v146, v147\n" \
    "v_add_f32 v148, v148, v149\n" \
    "s_nop 1\n" \
    "v_permlane32_swap_b32 v146, v148\n" \
    "v_add_f32 v146, v146, v148\n" \
    "v_rcp_f32 v146, v146\n" \
    "v_add_u32 v150, 40960, %[oo]\n" \
    "v_mov_b32 v148, v146\n" \
    "s_nop 1\n" \
    "v_permlane32_swap_b32 v146, v148\n" \
    "v_mov_b32 v147, v146\n" \
    "v_mov_b32 v149, v148\n" \
    "s_nop 0\n" \
    "v_permlane16_swap_b32 v146, v147\n" \
    "v_permlane16_swap_b32 v148, v149\n" \
    "v_mul_f32 v144, v144, v149\n" \
    "v_mul_f32 v145, v145, v149\n" \
    "v_cvt_pk_bf16_f32 v144, v144, v145\n" \
    "v_mul_f32 v142, v142, v148\n" \
    "v_mul_f32 v143, v143, v148\n" \
    "v_cvt_pk_bf16_f32 v142, v142, v143\n" \
    "v_perm_b32 v34, v144, v144, v130\n" \
    "v_perm_b32 v35, v144, v144, v131\n" \
    "v_perm_b32 v36, v144, v144, v132\n" \
    "v_perm_b32 v37, v144, v144, v133\n" \
    "v_mul_f32 v140, v140, v147\n" \
    "v_mul_f32 v141, v141, v147\n" \
    "v_cvt_pk_bf16_f32 v140, v140, v141\n" \
    "v_mfma_f32_16x16x32_bf16 v[74:77], v[118:121], v[34:37], 0\n" \
    "s_waitcnt lgkmcnt(6)\n" \
    "v_mfma_f32_16x16x32_bf16 v[94:97], v[106:109], v[34:37], 0\n" \
    "v_perm_b32 v30, v142, v142, v130\n" \
    "v_perm_b32 v31, v142, v142, v131\n" \
    "v_perm_b32 v32, v142, v142, v132\n" \
    "v_perm_b32 v33, v142, v142, v133\n" \
    "v_mul_f32 v138, v138, v146\n" \
    "v_mul_f32 v139, v139, v146\n" \
    "v_cvt_pk_bf16_f32 v138, v138, v139\n" \
    "v_mfma_f32_16x16x32_bf16 v[70:73], v[116:119], v[30:33], 0\n" \
    "s_waitcnt lgkmcnt(4)\n" \
    "v_mfma_f32_16x16x32_bf16 v[94:97], v[102:105], v[30:33], v[94:97]\n" \
    "v_perm_b32 v26, v140, v140, v130\n" \
    "v_perm_b32 v27, v140, v140, v131\n" \
    "v_perm_b32 v28, v140, v140, v132\n" \
    "v_perm_b32 v29, v140, v140, v133\n" \
    "s_nop 1\n" \
    "v_mfma_f32_16x16x32_bf16 v[66:69], v[114:117], v[26:29], 0\n" \
    "s_waitcnt lgkmcnt(2)\n" \
    "v_mfma_f32_16x16x32_bf16 v[94:97], v[98:101], v[26:29], v[94:97]\n" \
    "v_perm_b32 v22, v138, v138, v130\n" \
    "v_perm_b32 v23, v138, v138, v131\n" \
    "v_perm_b32 v24, v138, v138, v132\n" \
    "v_perm_b32 v25, v138, v138, v133\n" \
    "s_nop 1\n" \
    "v_mfma_f32_16x16x32_bf16 v[62:65], v[112:115], v[22:25], 0\n" \
    "s_waitcnt lgkmcnt(0)\n" \
    "v_mfma_f32_16x16x32_bf16 v[94:97], v[134:137], v[22:25], v[94:97]\n" \
    "v_readfirstlane_b32 %[tmp], v159\n" \
    "s_cmp_lg_u32 %[tmp], 0\n" \
    "s_cbranch_scc1 Lse_%=\n" \
    "s_mov_b32 %[cnt], 0\n" \
    "Lses_%=:\n" \
    "ds_read_b32 v159, %[zr] offset:1332\n" \
    "s_add_u32 %[cnt], %[cnt], 1\n" \
    "s_waitcnt lgkmcnt(0)\n" \
    "v_readfirstlane_b32 %[tmp], v159\n" \
    "s_cmp_ge_u32 %[cnt], %[cap]\n" \
    "s_cbranch_scc1 Lse_%=\n" \
    "s_cmp_eq_u32 %[tmp], 0\n" \
    "s_cbranch_scc1 Lses_%=\n" \
    "Lse_%=:\n" \
    "v_cvt_pk_bf16_f32 v52, v74, v75\n" \
    "v_cvt_pk_bf16_f32 v53, v76, v77\n" \
    "v_cvt_pk_bf16_f32 v50, v70, v71\n" \
    "v_cvt_pk_bf16_f32 v51, v72, v73\n" \
    "v_cvt_pk_bf16_f32 v44, v66, v67\n" \
    "v_cvt_pk_bf16_f32 v45, v68, v69\n" \
    "v_permlane16_swap_b32 v50, v52\n" \
    "v_permlane16_swap_b32 v51, v53\n" \
    "v_cvt_pk_bf16_f32 v42, v62, v63\n" \
    "v_cvt_pk_bf16_f32 v43, v64, v65\n" \
    "global_store_dwordx4 v150, v[50:53], %[wsp] sc1\n" \
    "v_cvt_pk_bf16_f32 v94, v94, v95\n" \
    "v_cvt_pk_bf16_f32 v95, v96, v97\n" \
    "v_permlane16_swap_b32 v42, v44\n" \
    "v_permlane16_swap_b32 v43, v45\n" \
    "global_store_dwordx4 %[oo], v[42:45], %[wsp] sc1\n" \
    "global_store_dwordx2 %[ox], v[94:95], %[wsp] sc1\n" \
    "s_nop 1\n"
#define G_CLOB "v22", "v23", "v24", "v25", "v26", "v27", "v28", "v29", "v30", "v31", "v32", "v33", "v34", "v35", "v36", "v37", "v38", "v39", "v40", "v41", "v42", "v43", "v44", "v45", "v46", "v47", "v48", "v49", "v50", "v51", "v52", "v53", "v54", "v55", "v56", "v57", "v58", "v59", "v60", "v61", "v62", "v63", "v64", "v65", "v66", "v67", "v68", "v69", "v70", "v71", "v72", "v73", "v74", "v75", "v76", "v77", "v78", "v79", "v80", "v81", "v82", "v83", "v84", "v85", "v86", "v87", "v88", "v89", "v90", "v91", "v92", "v93", "v94", "v95", "v96", "v97", "v98", "v99", "v100", "v101", "v102", "v103", "v104", "v105", "v106", "v107", "v108", "v109", "v110", "v111", "v112", "v113", "v114", "v115", "v116", "v117", "v118", "v119", "v120", "v121", "v122", "v123", "v124", "v125", "v126", "v127", "v128", "v129", "v130", "v131", "v132", "v133", "v134", "v135", "v136", "v137", "v138", "v139", "v140", "v141", "v142", "v143", "v144", "v145", "v146", "v147", "v148", "v149", "v150", "v151", "v152", "v153", "v154", "v155", "v156", "v157", "v158", "v159", "v160", "v161", "s60", "s61", "s62", "s63", "s64", "s65", "s66", "s67", "s68", "s69", "s70", "s71", "vcc", "scc", "memory"
#define P_ASM \
    "s_mov_b32 %[cnt], 0\n" \
    "La_%=:\n" \
    "ds_read_b32 v157, %[ctl] offset:56\n" \
    "s_add_u32 %[cnt], %[cnt], 1\n" \
    "s_waitcnt lgkmcnt(0)\n" \
    "v_readfirstlane_b32 %[tmp], v157\n" \
    "s_cmp_ge_u32 %[tmp], %[won]\n" \
    "s_cbranch_scc1 Lax_%=\n" \
    "s_sleep 3\n" \
    "s_cmp_lt_u32 %[cnt], %[cap]\n" \
    "s_cbranch_scc1 La_%=\n" \
    "Lax_%=:\n" \
    "ds_read_b64_tr_b16 v[16:17], %[pc] offset:0\n" \
    "ds_read_b64_tr_b16 v[18:19], %[pd] offset:0\n" \
    "ds_read_b64_tr_b16 v[20:21], %[pc] offset:32\n" \
    "ds_read_b64_tr_b16 v[22:23], %[pd] offset:32\n" \
    "ds_read_b64_tr_b16 v[24:25], %[pc] offset:64\n" \
    "ds_read_b64_tr_b16 v[26:27], %[pd] offset:64\n" \
    "ds_read_b64_tr_b16 v[28:29], %[pc] offset:96\n" \
    "ds_read_b64_tr_b16 v[30:31], %[pd] offset:96\n" \
    "ds_read_b64_tr_b16 v[32:33], %[pc] offset:128\n" \
    "ds_read_b64_tr_b16 v[34:35], %[pd] offset:128\n" \
    "ds_read_b64_tr_b16 v[36:37], %[pc] offset:5120\n" \
    "ds_read_b64_tr_b16 v[38:39], %[pd] offset:5120\n" \
    "ds_read_b64_tr_b16 v[40:41], %[pc] offset:5152\n" \
    "ds_read_b64_tr_b16 v[42:43], %[pd] offset:5152\n" \
    "ds_read_b64_tr_b16 v[44:45], %[pc] offset:5184\n" \
    "ds_read_b64_tr_b16 v[46:47], %[pd] offset:5184\n" \
    "ds_read_b64_tr_b16 v[48:49], %[pc] offset:5216\n" \
    "ds_read_b64_tr_b16 v[50:51], %[pd] offset:5216\n" \
    "ds_read_b64_tr_b16 v[52:53], %[pc] offset:5248\n" \
    "ds_read_b64_tr_b16 v[54:55], %[pd] offset:5248\n" \
    "ds_read_b64_tr_b16 v[56:57], %[pc] offset:10240\n" \
    "ds_read_b64_tr_b16 v[58:59], %[pd] offset:10240\n" \
    "ds_read_b64_tr_b16 v[60:61], %[pc] offset:10272\n" \
    "ds_read_b64_tr_b16 v[62:63], %[pd] offset:10272\n" \
    "ds_read_b64_tr_b16 v[64:65], %[pc] offset:10304\n" \
    "ds_read_b64_tr_b16 v[66:67], %[pd] offset:10304\n" \
    "ds_read_b64_tr_b16 v[68:69], %[pc] offset:10336\n" \
    "ds_read_b64_tr_b16 v[70:71], %[pd] offset:10336\n" \
    "ds_read_b64_tr_b16 v[72:73], %[pc] offset:10368\n" \
    "ds_read_b64_tr_b16 v[74:75], %[pd] offset:10368\n" \
    "ds_read_b64_tr_b16 v[76:77], %[pc] offset:15360\n" \
    "ds_read_b64_tr_b16 v[78:79], %[pd] offset:15360\n" \
    "ds_read_b64_tr_b16 v[80:81], %[pc] offset:15392\n" \
    "ds_read_b64_tr_b16 v[82:83], %[pd] offset:15392\n" \
    "ds_read_b64_tr_b16 v[84:85], %[pc] offset:15424\n" \
    "ds_read_b64_tr_b16 v[86:87], %[pd] offset:15424\n" \
    "ds_read_b64_tr_b16 v[88:89], %[pc] offset:15456\n" \
    "ds_read_b64_tr_b16 v[90:91], %[pd] offset:15456\n" \
    "ds_read_b64_tr_b16 v[92:93], %[pc] offset:15488\n" \
    "ds_read_b64_tr_b16 v[94:95], %[pd] offset:15488\n" \
    "ds_read_b64_tr_b16 v[96:97], %[pc] offset:20480\n" \
    "ds_read_b64_tr_b16 v[98:99], %[pd] offset:20480\n" \
    "ds_read_b64_tr_b16 v[100:101], %[pc] offset:20512\n" \
    "ds_read_b64_tr_b16 v[102:103], %[pd] offset:20512\n" \
    "ds_read_b64_tr_b16 v[104:105], %[pc] offset:20544\n" \
    "ds_read_b64_tr_b16 v[106:107], %[pd] offset:20544\n" \
    "ds_read_b64_tr_b16 v[108:109], %[pc] offset:20576\n" \
    "ds_read_b64_tr_b16 v[110:111], %[pd] offset:20576\n" \
    "ds_read_b64_tr_b16 v[112:113], %[pc] offset:20608\n" \
    "ds_read_b64_tr_b16 v[114:115], %[pd] offset:20608\n" \
    "v_mov_b32 v156, 1\n" \
    "s_mov_b64 exec, 1\n" \
    "ds_add_u32 %[ctl], v156 offset:96\n" \
    "s_mov_b64 exec, -1\n" \
    "s_mov_b32 %[cnt], 0\n" \
    "Lb_%=:\n" \
    "ds_read_b32 v157, %[ctl] offset:52\n" \
    "s_add_u32 %[cnt], %[cnt], 1\n" \
    "s_waitcnt lgkmcnt(0)\n" \
    "v_readfirstlane_b32 %[tmp], v157\n" \
    "s_cmp_ge_u32 %[tmp], 1\n" \
    "s_cbranch_scc1 Lbx_%=\n" \
    "s_sleep 1\n" \
    "s_cmp_lt_u32 %[cnt], %[cap]\n" \
    "s_cbranch_scc1 Lb_%=\n" \
    "Lbx_%=:\n" \
    "s_mov_b32 %[cnt], 0\n" \
    "Lc_%=:\n" \
    "ds_read_b32 v157, %[ctl] offset:48\n" \
    "s_add_u32 %[cnt], %[cnt], 1\n" \
    "s_waitcnt lgkmcnt(0)\n" \
    "v_readfirstlane_b32 %[tmp], v157\n" \
    "s_cmp_ge_u32 %[tmp], %[ost]\n" \
    "s_cbranch_scc1 Lcx_%=\n" \
    "s_sleep 1\n" \
    "s_cmp_lt_u32 %[cnt], %[cap]\n" \
    "s_cbranch_scc1 Lc_%=\n" \
    "Lcx_%=:\n" \
    "s_mov_b32 %[cnt], 0\n" \
    "Ld_%=:\n" \
    "global_load_dword v156, %[wat], %[wsp] sc1\n" \
    "s_add_u32 %[cnt], %[cnt], 1\n" \
    "s_waitcnt vmcnt(0)\n" \
    "v_cmp_eq_u32 vcc, %[so], v156\n" \
    "s_mov_b32 %[wm], vcc_lo\n" \
    "s_and_b32 %[tmp], %[wm], 3\n" \
    "s_cbranch_scc0 Ldx_%=\n" \
    "s_sleep 4\n" \
    "s_cmp_lt_u32 %[cnt], %[cap]\n" \
    "s_cbranch_scc1 Ld_%=\n" \
    "Ldx_%=:\n" \
    "s_setprio 1\n" \
    "global_load_dwordx4 v[116:119], %[ofo], %[wsp] offset:0 sc1\n" \
    "global_load_dwordx4 v[120:123], %[ofo], %[wsp] offset:1024 sc1\n" \
    "global_load_dwordx4 v[124:127], %[ofo], %[wsp] offset:2048 sc1\n" \
    "global_load_dwordx4 v[128:131], %[ofo], %[wsp] offset:3072 sc1\n" \
    "global_load_dwordx2 v[132:133], %[of4], %[wsp] sc1\n" \
    "global_load_dwordx2 v[134:135], %[of4], %[wsp] offset:128 sc1\n" \
    "ds_read_b32 v157, %[ctl] offset:96\n" \
    "s_mov_b32 %[ah], 1\n" \
    "s_and_b32 %[tmp], %[wm], 12\n" \
    "s_cbranch_scc1 Ln_%=\n" \
    "s_waitcnt vmcnt(5)\n" \
    "v_mfma_f32_16x16x32_bf16 v[136:139], v[16:19], v[116:119], 0\n" \
    "v_mfma_f32_16x16x32_bf16 v[140:143], v[20:23], v[116:119], 0\n" \
    "v_mfma_f32_16x16x32_bf16 v[144:147], v[24:27], v[116:119], 0\n" \
    "v_mfma_f32_16x16x32_bf16 v[148:151], v[28:31], v[116:119], 0\n" \
    "v_mfma_f32_16x16x32_bf16 v[152:155], v[32:35], v[116:119], 0\n" \
    "global_load_dwordx4 v[116:119], %[ofo], %[ws2] offset:0 sc1\n" \
    "s_waitcnt vmcnt(5)\n" \
    "v_mfma_f32_16x16x32_bf16 v[136:139], v[36:39], v[120:123], v[136:139]\n" \
    "v_mfma_f32_16x16x32_bf16 v[140:143], v[40:43], v[120:123], v[140:143]\n" \
    "v_mfma_f32_16x16x32_bf16 v[144:147], v[44:47], v[120:123], v[144:147]\n" \
    "v_mfma_f32_16x16x32_bf16 v[148:151], v[48:51], v[120:123], v[148:151]\n" \
    "v_mfma_f32_16x16x32_bf16 v[152:155], v[52:55], v[120:123], v[152:155]\n" \
    "global_load_dwordx4 v[120:123], %[ofo], %[ws2] offset:1024 sc1\n" \
    "s_waitcnt vmcnt(5)\n" \
    "v_mfma_f32_16x16x32_bf16 v[136:139], v[56:59], v[124:127], v[136:139]\n" \
    "v_mfma_f32_16x16x32_bf16 v[140:143], v[60:63], v[124:127], v[140:143]\n" \
    "v_mfma_f32_16x16x32_bf16 v[144:147], v[64:67], v[124:127], v[144:147]\n" \
    "v_mfma_f32_16x16x32_bf16 v[148:151], v[68:71], v[124:127], v[148:151]\n" \
    "v_mfma_f32_16x16x32_bf16 v[152:155], v[72:75], v[124:127], v[152:155]\n" \
    "global_load_dwordx4 v[124:127], %[ofo], %[ws2] offset:2048 sc1\n" \
    "s_waitcnt vmcnt(5)\n" \
    "v_mfma_f32_16x16x32_bf16 v[136:139], v[76:79], v[128:131], v[136:139]\n" \
    "v_mfma_f32_16x16x32_bf16 v[140:143], v[80:83], v[128:131], v[140:143]\n" \
    "v_mfma_f32_16x16x32_bf16 v[144:147], v[84:87], v[128:131], v[144:147]\n" \
    "v_mfma_f32_16x16x32_bf16 v[148:151], v[88:91], v[128:131], v[148:151]\n" \
    "v_mfma_f32_16x16x32_bf16 v[152:155], v[92:95], v[128:131], v[152:155]\n" \
    "global_load_dwordx4 v[128:131], %[ofo], %[ws2] offset:3072 sc1\n" \
    "s_waitcnt vmcnt(4)\n" \
    "v_mfma_f32_16x16x32_bf16 v[136:139], v[96:99], v[132:135], v[136:139]\n" \
    "v_mfma_f32_16x16x32_bf16 v[140:143], v[100:103], v[132:135], v[140:143]\n" \
    "v_mfma_f32_16x16x32_bf16 v[144:147], v[104:107], v[132:135], v[144:147]\n" \
    "v_mfma_f32_16x16x32_bf16 v[148:151], v[108:111], v[132:135], v[148:151]\n" \
    "v_mfma_f32_16x16x32_bf16 v[152:155], v[112:115], v[132:135], v[152:155]\n" \
    "global_load_dwordx2 v[132:133], %[of4], %[ws2] sc1\n" \
    "global_load_dwordx2 v[134:135], %[of4], %[ws2] offset:128 sc1\n" \
    "s_branch Lj_%=\n" \
    "Ln_%=:\n" \
    "s_waitcnt vmcnt(5)\n" \
    "global_load_dword v156, %[wat], %[wsp] sc1\n" \
    "s_waitcnt vmcnt(6)\n" \
    "v_mfma_f32_16x16x32_bf16 v[136:139], v[16:19], v[116:119], 0\n" \
    "v_mfma_f32_16x16x32_bf16 v[140:143], v[20:23], v[116:119], 0\n" \
    "v_mfma_f32_16x16x32_bf16 v[144:147], v[24:27], v[116:119], 0\n" \
    "v_mfma_f32_16x16x32_bf16 v[148:151], v[28:31], v[116:119], 0\n" \
    "v_mfma_f32_16x16x32_bf16 v[152:155], v[32:35], v[116:119], 0\n" \
    "s_waitcnt vmcnt(5)\n" \
    "v_mfma_f32_16x16x32_bf16 v[136:139], v[36:39], v[120:123], v[136:139]\n" \
    "v_mfma_f32_16x16x32_bf16 v[140:143], v[40:43], v[120:123], v[140:143]\n" \
    "v_mfma_f32_16x16x32_bf16 v[144:147], v[44:47], v[120:123], v[144:147]\n" \
    "v_mfma_f32_16x16x32_bf16 v[148:151], v[48:51], v[120:123], v[148:151]\n" \
    "v_mfma_f32_16x16x32_bf16 v[152:155], v[52:55], v[120:123], v[152:155]\n" \
    "s_waitcnt vmcnt(4)\n" \
    "v_mfma_f32_16x16x32_bf16 v[136:139], v[56:59], v[124:127], v[136:139]\n" \
    "v_mfma_f32_16x16x32_bf16 v[140:143], v[60:63], v[124:127], v[140:143]\n" \
    "v_mfma_f32_16x16x32_bf16 v[144:147], v[64:67], v[124:127], v[144:147]\n" \
    "v_mfma_f32_16x16x32_bf16 v[148:151], v[68:71], v[124:127], v[148:151]\n" \
    "v_mfma_f32_16x16x32_bf16 v[152:155], v[72:75], v[124:127], v[152:155]\n" \
    "s_waitcnt vmcnt(3)\n" \
    "v_mfma_f32_16x16x32_bf16 v[136:139], v[76:79], v[128:131], v[136:139]\n" \
    "v_mfma_f32_16x16x32_bf16 v[140:143], v[80:83], v[128:131], v[140:143]\n" \
    "v_mfma_f32_16x16x32_bf16 v[144:147], v[84:87], v[128:131], v[144:147]\n" \
    "v_mfma_f32_16x16x32_bf16 v[148:151], v[88:91], v[128:131], v[148:151]\n" \
    "v_mfma_f32_16x16x32_bf16 v[152:155], v[92:95], v[128:131], v[152:155]\n" \
    "s_waitcnt vmcnt(1)\n" \
    "v_mfma_f32_16x16x32_bf16 v[136:139], v[96:99], v[132:135], v[136:139]\n" \
    "v_mfma_f32_16x16x32_bf16 v[140:143], v[100:103], v[132:135], v[140:143]\n" \
    "v_mfma_f32_16x16x32_bf16 v[144:147], v[104:107], v[132:135], v[144:147]\n" \
    "v_mfma_f32_16x16x32_bf16 v[148:151], v[108:111], v[132:135], v[148:151]\n" \
    "v_mfma_f32_16x16x32_bf16 v[152:155], v[112:115], v[132:135], v[152:155]\n" \
    "s_waitcnt vmcnt(0)\n" \
    "v_cmp_eq_u32 vcc, %[so], v156\n" \
    "s_mov_b32 %[wm], vcc_lo\n" \
    "s_mov_b32 %[ah], 0\n" \
    "s_and_b32 %[tmp], %[wm], 12\n" \
    "s_cbranch_scc1 Lj_%=\n" \
    "s_mov_b32 %[ah], 1\n" \
    "global_load_dwordx4 v[116:119], %[ofo], %[ws2] offset:0 sc1\n" \
    "global_load_dwordx4 v[120:123], %[ofo], %[ws2] offset:1024 sc1\n" \
    "global_load_dwordx4 v[124:127], %[ofo], %[ws2] offset:2048 sc1\n" \
    "global_load_dwordx4 v[128:131], %[ofo], %[ws2] offset:3072 sc1\n" \
    "global_load_dwordx2 v[132:133], %[of4], %[ws2] sc1\n" \
    "global_load_dwordx2 v[134:135], %[of4], %[ws2] offset:128 sc1\n" \
    "Lj_%=:\n" \
    "s_waitcnt lgkmcnt(0)\n" \
    "v_readfirstlane_b32 %[tmp], v157\n" \
    "s_cmp_ge_u32 %[tmp], 8\n" \
    "s_cbranch_scc1 Lex_%=\n" \
    "s_mov_b32 %[cnt], 0\n" \
    "Le_%=:\n" \
    "ds_read_b32 v157, %[ctl] offset:96\n" \
    "s_add_u32 %[cnt], %[cnt], 1\n" \
    "s_waitcnt lgkmcnt(0)\n" \
    "v_readfirstlane_b32 %[tmp], v157\n" \
    "s_cmp_ge_u32 %[tmp], 8\n" \
    "s_cbranch_scc1 Lex_%=\n" \
    "s_sleep 1\n" \
    "s_cmp_lt_u32 %[cnt], %[cap]\n" \
    "s_cbranch_scc1 Le_%=\n" \
    "Lex_%=:\n" \
    "s_nop 3\n" \
    "v_cmp_u_f32 vcc, v136, v136\n" \
    "s_cbranch_vccz Lfx_%=\n" \
    "s_waitcnt vmcnt(0)\n" \
    "s_mov_b32 %[ah], 0\n" \
    "s_mov_b32 %[cnt], 0\n" \
    "Lf_%=:\n" \
    "global_load_dwordx4 v[116:119], %[ofo], %[wsp] offset:0 sc1\n" \
    "global_load_dwordx4 v[120:123], %[ofo], %[wsp] offset:1024 sc1\n" \
    "global_load_dwordx4 v[124:127], %[ofo], %[wsp] offset:2048 sc1\n" \
    "global_load_dwordx4 v[128:131], %[ofo], %[wsp] offset:3072 sc1\n" \
    "global_load_dwordx2 v[132:133], %[of4], %[wsp] sc1\n" \
    "global_load_dwordx2 v[134:135], %[of4], %[wsp] offset:128 sc1\n" \
    "s_add_u32 %[cnt], %[cnt], 1\n" \
    "s_waitcnt vmcnt(0)\n" \
    "v_xor_b32 v157, %[so], v116\n" \
    "v_xor_b32 v156, %[so], v117\n" \
    "v_min_u32 v157, v157, v156\n" \
    "v_xor_b32 v156, %[so], v118\n" \
    "v_min_u32 v157, v157, v156\n" \
    "v_xor_b32 v156, %[so], v119\n" \
    "v_min_u32 v157, v157, v156\n" \
    "v_xor_b32 v156, %[so], v120\n" \
    "v_min_u32 v157, v157, v156\n" \
    "v_xor_b32 v156, %[so], v121\n" \
    "v_min_u32 v157, v157, v156\n" \
    "v_xor_b32 v156, %[so], v122\n" \
    "v_min_u32 v157, v157, v156\n" \
    "v_xor_b32 v156, %[so], v123\n" \
    "v_min_u32 v157, v157, v156\n" \
    "v_xor_b32 v156, %[so], v124\n" \
    "v_min_u32 v157, v157, v156\n" \
    "v_xor_b32 v156, %[so], v125\n" \
    "v_min_u32 v157, v157, v156\n" \
    "v_xor_b32 v156, %[so], v126\n" \
    "v_min_u32 v157, v157, v156\n" \
    "v_xor_b32 v156, %[so], v127\n" \
    "v_min_u32 v157, v157, v156\n" \
    "v_xor_b32 v156, %[so], v128\n" \
    "v_min_u32 v157, v157, v156\n" \
    "v_xor_b32 v156, %[so], v129\n" \
    "v_min_u32 v157, v157, v156\n" \
    "v_xor_b32 v156, %[so], v130\n" \
    "v_min_u32 v157, v157, v156\n" \
    "v_xor_b32 v156, %[so], v131\n" \
    "v_min_u32 v157, v157, v156\n" \
    "v_xor_b32 v156, %[so], v132\n" \
    "v_min_u32 v157, v157, v156\n" \
    "v_xor_b32 v156, %[so], v133\n" \
    "v_min_u32 v157, v157, v156\n" \
    "v_xor_b32 v156, %[so], v134\n" \
    "v_min_u32 v157, v157, v156\n" \
    "v_xor_b32 v156, %[so], v135\n" \
    "v_min_u32 v157, v157, v156\n" \
    "v_cmp_eq_u32 vcc, 0, v157\n" \
    "s_cmp_lt_u32 %[cnt], %[cap]\n" \
    "s_cbranch_vccz Lfy_%=\n" \
    "s_cbranch_scc1 Lf_%=\n" \
    "Lfy_%=:\n" \
    "s_waitcnt vmcnt(0)\n" \
    "v_mfma_f32_16x16x32_bf16 v[136:139], v[16:19], v[116:119], 0\n" \
    "v_mfma_f32_16x16x32_bf16 v[140:143], v[20:23], v[116:119], 0\n" \
    "v_mfma_f32_16x16x32_bf16 v[144:147], v[24:27], v[116:119], 0\n" \
    "v_mfma_f32_16x16x32_bf16 v[148:151], v[28:31], v[116:119], 0\n" \
    "v_mfma_f32_16x16x32_bf16 v[152:155], v[32:35], v[116:119], 0\n" \
    "s_waitcnt vmcnt(0)\n" \
    "v_mfma_f32_16x16x32_bf16 v[136:139], v[36:39], v[120:123], v[136:139]\n" \
    "v_mfma_f32_16x16x32_bf16 v[140:143], v[40:43], v[120:123], v[140:143]\n" \
    "v_mfma_f32_16x16x32_bf16 v[144:147], v[44:47], v[120:123], v[144:147]\n" \
    "v_mfma_f32_16x16x32_bf16 v[148:151], v[48:51], v[120:123], v[148:151]\n" \
    "v_mfma_f32_16x16x32_bf16 v[152:155], v[52:55], v[120:123], v[152:155]\n" \
    "s_waitcnt vmcnt(0)\n" \
    "v_mfma_f32_16x16x32_bf16 v[136:139], v[56:59], v[124:127], v[136:139]\n" \
    "v_mfma_f32_16x16x32_bf16 v[140:143], v[60:63], v[124:127], v[140:143]\n" \
    "v_mfma_f32_16x16x32_bf16 v[144:147], v[64:67], v[124:127], v[144:147]\n" \
    "v_mfma_f32_16x16x32_bf16 v[148:151], v[68:71], v[124:127], v[148:151]\n" \
    "v_mfma_f32_16x16x32_bf16 v[152:155], v[72:75], v[124:127], v[152:155]\n" \
    "s_waitcnt vmcnt(0)\n" \
    "v_mfma_f32_16x16x32_bf16 v[136:139], v[76:79], v[128:131], v[136:139]\n" \
    "v_mfma_f32_16x16x32_bf16 v[140:143], v[80:83], v[128:131], v[140:143]\n" \
    "v_mfma_f32_16x16x32_bf16 v[144:147], v[84:87], v[128:131], v[144:147]\n" \
    "v_mfma_f32_16x16x32_bf16 v[148:151], v[88:91], v[128:131], v[148:151]\n" \
    "v_mfma_f32_16x16x32_bf16 v[152:155], v[92:95], v[128:131], v[152:155]\n" \
    "s_waitcnt vmcnt(0)\n" \
    "v_mfma_f32_16x16x32_bf16 v[136:139], v[96:99], v[132:135], v[136:139]\n" \
    "v_mfma_f32_16x16x32_bf16 v[140:143], v[100:103], v[132:135], v[140:143]\n" \
    "v_mfma_f32_16x16x32_bf16 v[144:147], v[104:107], v[132:135], v[144:147]\n" \
    "v_mfma_f32_16x16x32_bf16 v[148:151], v[108:111], v[132:135], v[148:151]\n" \
    "v_mfma_f32_16x16x32_bf16 v[152:155], v[112:115], v[132:135], v[152:155]\n" \
    "s_nop 3\n" \
    "Lfx_%=:\n" \
    "v_mov_b32 v157, 1\n" \
    "ds_write_b128 %[xa0], v[136:139] offset:0\n" \
    "ds_write_b128 %[xa0], v[140:143] offset:1024\n" \
    "ds_write_b128 %[xa0], v[144:147] offset:2048\n" \
    "ds_write_b128 %[xa0], v[148:151] offset:3072\n" \
    "ds_write_b128 %[xa0], v[152:155] offset:4096\n" \
    "ds_write_b32 %[pf], v157 offset:0\n" \
    "s_cmp_lg_u32 %[ah], 0\n" \
    "s_cbranch_scc1 Lk_%=\n" \
    "s_and_b32 %[tmp], %[wm], 12\n" \
    "s_cbranch_scc0 Lhx_%=\n" \
    "s_mov_b32 %[cnt], 0\n" \
    "Lh_%=:\n" \
    "global_load_dword v156, %[wat], %[wsp] sc1\n" \
    "s_add_u32 %[cnt], %[cnt], 1\n" \
    "s_waitcnt vmcnt(0)\n" \
    "v_cmp_eq_u32 vcc, %[so], v156\n" \
    "s_mov_b32 %[wm], vcc_lo\n" \
    "s_and_b32 %[tmp], %[wm], 12\n" \
    "s_cbranch_scc0 Lhx_%=\n" \
    "s_sleep 4\n" \
    "s_cmp_lt_u32 %[cnt], %[cap]\n" \
    "s_cbranch_scc1 Lh_%=\n" \
    "Lhx_%=:\n" \
    "global_load_dwordx4 v[116:119], %[ofo], %[ws2] offset:0 sc1\n" \
    "global_load_dwordx4 v[120:123], %[ofo], %[ws2] offset:1024 sc1\n" \
    "global_load_dwordx4 v[124:127], %[ofo], %[ws2] offset:2048 sc1\n" \
    "global_load_dwordx4 v[128:131], %[ofo], %[ws2] offset:3072 sc1\n" \
    "global_load_dwordx2 v[132:133], %[of4], %[ws2] sc1\n" \
    "global_load_dwordx2 v[134:135], %[of4], %[ws2] offset:128 sc1\n" \
    "Lk_%=:\n" \
    "s_waitcnt vmcnt(5)\n" \
    "v_mfma_f32_16x16x32_bf16 v[136:139], v[16:19], v[116:119], 0\n" \
    "v_mfma_f32_16x16x32_bf16 v[140:143], v[20:23], v[116:119], 0\n" \
    "v_mfma_f32_16x16x32_bf16 v[144:147], v[24:27], v[116:119], 0\n" \
    "v_mfma_f32_16x16x32_bf16 v[148:151], v[28:31], v[116:119], 0\n" \
    "v_mfma_f32_16x16x32_bf16 v[152:155], v[32:35], v[116:119], 0\n" \
    "s_waitcnt vmcnt(4)\n" \
    "v_mfma_f32_16x16x32_bf16 v[136:139], v[36:39], v[120:123], v[136:139]\n" \
    "v_mfma_f32_16x16x32_bf16 v[140:143], v[40:43], v[120:123], v[140:143]\n" \
    "v_mfma_f32_16x16x32_bf16 v[144:147], v[44:47], v[120:123], v[144:147]\n" \
    "v_mfma_f32_16x16x32_bf16 v[148:151], v[48:51], v[120:123], v[148:151]\n" \
    "v_mfma_f32_16x16x32_bf16 v[152:155], v[52:55], v[120:123], v[152:155]\n" \
    "s_waitcnt vmcnt(3)\n" \
    "v_mfma_f32_16x16x32_bf16 v[136:139], v[56:59], v[124:127], v[136:139]\n" \
    "v_mfma_f32_16x16x32_bf16 v[140:143], v[60:63], v[124:127], v[140:143]\n" \
    "v_mfma_f32_16x16x32_bf16 v[144:147], v[64:67], v[124:127], v[144:147]\n" \
    "v_mfma_f32_16x16x32_bf16 v[148:151], v[68:71], v[124:127], v[148:151]\n" \
    "v_mfma_f32_16x16x32_bf16 v[152:155], v[72:75], v[124:127], v[152:155]\n" \
    "s_waitcnt vmcnt(2)\n" \
    "v_mfma_f32_16x16x32_bf16 v[136:139], v[76:79], v[128:131], v[136:139]\n" \
    "v_mfma_f32_16x16x32_bf16 v[140:143], v[80:83], v[128:131], v[140:143]\n" \
    "v_mfma_f32_16x16x32_bf16 v[144:147], v[84:87], v[128:131], v[144:147]\n" \
    "v_mfma_f32_16x16x32_bf16 v[148:151], v[88:91], v[128:131], v[148:151]\n" \
    "v_mfma_f32_16x16x32_bf16 v[152:155], v[92:95], v[128:131], v[152:155]\n" \
    "s_waitcnt vmcnt(0)\n" \
    "v_mfma_f32_16x16x32_bf16 v[136:139], v[96:99], v[132:135], v[136:139]\n" \
    "v_mfma_f32_16x16x32_bf16 v[140:143], v[100:103], v[132:135], v[140:143]\n" \
    "v_mfma_f32_16x16x32_bf16 v[144:147], v[104:107], v[132:135], v[144:147]\n" \
    "v_mfma_f32_16x16x32_bf16 v[148:151], v[108:111], v[132:135], v[148:151]\n" \
    "v_mfma_f32_16x16x32_bf16 v[152:155], v[112:115], v[132:135], v[152:155]\n" \
    "s_nop 3\n" \
    "v_cmp_u_f32 vcc, v136, v136\n" \
    "s_cbranch_vccz Lix_%=\n" \
    "s_waitcnt vmcnt(0)\n" \
    "s_mov_b32 %[ah], 0\n" \
    "s_mov_b32 %[cnt], 0\n" \
    "Li_%=:\n" \
    "global_load_dwordx4 v[116:119], %[ofo], %[ws2] offset:0 sc1\n" \
    "global_load_dwordx4 v[120:123], %[ofo], %[ws2] offset:1024 sc1\n" \
    "global_load_dwordx4 v[124:127], %[ofo], %[ws2] offset:2048 sc1\n" \
    "global_load_dwordx4 v[128:131], %[ofo], %[ws2] offset:3072 sc1\n" \
    "global_load_dwordx2 v[132:133], %[of4], %[ws2] sc1\n" \
    "global_load_dwordx2 v[134:135], %[of4], %[ws2] offset:128 sc1\n" \
    "s_add_u32 %[cnt], %[cnt], 1\n" \
    "s_waitcnt vmcnt(0)\n" \
    "v_xor_b32 v157, %[so], v116\n" \
    "v_xor_b32 v156, %[so], v117\n" \
    "v_min_u32 v157, v157, v156\n" \
    "v_xor_b32 v156, %[so], v118\n" \
    "v_min_u32 v157, v157, v156\n" \
    "v_xor_b32 v156, %[so], v119\n" \
    "v_min_u32 v157, v157, v156\n" \
    "v_xor_b32 v156, %[so], v120\n" \
    "v_min_u32 v157, v157, v156\n" \
    "v_xor_b32 v156, %[so], v121\n" \
    "v_min_u32 v157, v157, v156\n" \
    "v_xor_b32 v156, %[so], v122\n" \
    "v_min_u32 v157, v157, v156\n" \
    "v_xor_b32 v156, %[so], v123\n" \
    "v_min_u32 v157, v157, v156\n" \
    "v_xor_b32 v156, %[so], v124\n" \
    "v_min_u32 v157, v157, v156\n" \
    "v_xor_b32 v156, %[so], v125\n" \
    "v_min_u32 v157, v157, v156\n" \
    "v_xor_b32 v156, %[so], v126\n" \
    "v_min_u32 v157, v157, v156\n" \
    "v_xor_b32 v156, %[so], v127\n" \
    "v_min_u32 v157, v157, v156\n" \
    "v_xor_b32 v156, %[so], v128\n" \
    "v_min_u32 v157, v157, v156\n" \
    "v_xor_b32 v156, %[so], v129\n" \
    "v_min_u32 v157, v157, v156\n" \
    "v_xor_b32 v156, %[so], v130\n" \
    "v_min_u32 v157, v157, v156\n" \
    "v_xor_b32 v156, %[so], v131\n" \
    "v_min_u32 v157, v157, v156\n" \
    "v_xor_b32 v156, %[so], v132\n" \
    "v_min_u32 v157, v157, v156\n" \
    "v_xor_b32 v156, %[so], v133\n" \
    "v_min_u32 v157, v157, v156\n" \
    "v_xor_b32 v156, %[so], v134\n" \
    "v_min_u32 v157, v157, v156\n" \
    "v_xor_b32 v156, %[so], v135\n" \
    "v_min_u32 v157, v157, v156\n" \
    "v_cmp_eq_u32 vcc, 0, v157\n" \
    "s_cmp_lt_u32 %[cnt], %[cap]\n" \
    "s_cbranch_vccz Liy_%=\n" \
    "s_cbranch_scc1 Li_%=\n" \
    "Liy_%=:\n" \
    "s_waitcnt vmcnt(0)\n" \
    "v_mfma_f32_16x16x32_bf16 v[136:139], v[16:19], v[116:119], 0\n" \
    "v_mfma_f32_16x16x32_bf16 v[140:143], v[20:23], v[116:119], 0\n" \
    "v_mfma_f32_16x16x32_bf16 v[144:147], v[24:27], v[116:119], 0\n" \
    "v_mfma_f32_16x16x32_bf16 v[148:151], v[28:31], v[116:119], 0\n" \
    "v_mfma_f32_16x16x32_bf16 v[152:155], v[32:35], v[116:119], 0\n" \
    "s_waitcnt vmcnt(0)\n" \
    "v_mfma_f32_16x16x32_bf16 v[136:139], v[36:39], v[120:123], v[136:139]\n" \
    "v_mfma_f32_16x16x32_bf16 v[140:143], v[40:43], v[120:123], v[140:143]\n" \
    "v_mfma_f32_16x16x32_bf16 v[144:147], v[44:47], v[120:123], v[144:147]\n" \
    "v_mfma_f32_16x16x32_bf16 v[148:151], v[48:51], v[120:123], v[148:151]\n" \
    "v_mfma_f32_16x16x32_bf16 v[152:155], v[52:55], v[120:123], v[152:155]\n" \
    "s_waitcnt vmcnt(0)\n" \
    "v_mfma_f32_16x16x32_bf16 v[136:139], v[56:59], v[124:127], v[136:139]\n" \
    "v_mfma_f32_16x16x32_bf16 v[140:143], v[60:63], v[124:127], v[140:143]\n" \
    "v_mfma_f32_16x16x32_bf16 v[144:147], v[64:67], v[124:127], v[144:147]\n" \
    "v_mfma_f32_16x16x32_bf16 v[148:151], v[68:71], v[124:127], v[148:151]\n" \
    "v_mfma_f32_16x16x32_bf16 v[152:155], v[72:75], v[124:127], v[152:155]\n" \
    "s_waitcnt vmcnt(0)\n" \
    "v_mfma_f32_16x16x32_bf16 v[136:139], v[76:79], v[128:131], v[136:139]\n" \
    "v_mfma_f32_16x16x32_bf16 v[140:143], v[80:83], v[128:131], v[140:143]\n" \
    "v_mfma_f32_16x16x32_bf16 v[144:147], v[84:87], v[128:131], v[144:147]\n" \
    "v_mfma_f32_16x16x32_bf16 v[148:151], v[88:91], v[128:131], v[148:151]\n" \
    "v_mfma_f32_16x16x32_bf16 v[152:155], v[92:95], v[128:131], v[152:155]\n" \
    "s_waitcnt vmcnt(0)\n" \
    "v_mfma_f32_16x16x32_bf16 v[136:139], v[96:99], v[132:135], v[136:139]\n" \
    "v_mfma_f32_16x16x32_bf16 v[140:143], v[100:103], v[132:135], v[140:143]\n" \
    "v_mfma_f32_16x16x32_bf16 v[144:147], v[104:107], v[132:135], v[144:147]\n" \
    "v_mfma_f32_16x16x32_bf16 v[148:151], v[108:111], v[132:135], v[148:151]\n" \
    "v_mfma_f32_16x16x32_bf16 v[152:155], v[112:115], v[132:135], v[152:155]\n" \
    "s_nop 3\n" \
    "Lix_%=:\n" \
    "v_mov_b32 v157, 1\n" \
    "s_nop 1\n" \
    "ds_write_b128 %[xa1], v[136:139] offset:0\n" \
    "ds_write_b128 %[xa1], v[140:143] offset:1024\n" \
    "ds_write_b128 %[xa1], v[144:147] offset:2048\n" \
    "ds_write_b128 %[xa1], v[148:151] offset:3072\n" \
    "ds_write_b128 %[xa1], v[152:155] offset:4096\n" \
    "ds_write_b32 %[pf], v157 offset:32\n" \
    "s_nop 0\n"
#define P_CLOB "v16", "v17", "v18", "v19", "v20", "v21", "v22", "v23", "v24", "v25", "v26", "v27", "v28", "v29", "v30", "v31", "v32", "v33", "v34", "v35", "v36", "v37", "v38", "v39", "v40", "v41", "v42", "v43", "v44", "v45", "v46", "v47", "v48", "v49", "v50", "v51", "v52", "v53", "v54", "v55", "v56", "v57", "v58", "v59", "v60", "v61", "v62", "v63", "v64", "v65", "v66", "v67", "v68", "v69", "v70", "v71", "v72", "v73", "v74", "v75", "v76", "v77", "v78", "v79", "v80", "v81", "v82", "v83", "v84", "v85", "v86", "v87", "v88", "v89", "v90", "v91", "v92", "v93", "v94", "v95", "v96", "v97", "v98", "v99", "v100", "v101", "v102", "v103", "v104", "v105", "v106", "v107", "v108", "v109", "v110", "v111", "v112", "v113", "v114", "v115", "v116", "v117", "v118", "v119", "v120", "v121", "v122", "v123", "v124", "v125", "v126", "v127", "v128", "v129", "v130", "v131", "v132", "v133", "v134", "v135", "v136", "v137", "v138", "v139", "v140", "v141", "v142", "v143", "v144", "v145", "v146", "v147", "v148", "v149", "v150", "v151", "v152", "v153", "v154", "v155", "v156", "v157", "v158", "v159", "vcc", "scc", "memory"
static_assert(C_WO - L_CTRL == 56 && C_OST - L_CTRL == 48 && C_BP - L_CTRL == 96, "offsets in the output stream");
static_assert(L_CTRL - G_ZR == 1280 && C_HX - C_FREE == 144 && C_SENT - L_CTRL == 52, "offsets in the projection stream");
static_assert(C_HR - L_CTRL == 144 && C_RED - L_CTRL == 84 && C_XD - L_CTRL == 80 && C_SENT - L_CTRL == 52, "offsets in the conv stream");
typedef __attribute__((ext_vector_type(16))) unsigned u32x16;
typedef __attribute__((address_space(3))) const unsigned lds_cu32;
typedef __attribute__((address_space(3))) u32x2 lds_u32x2;
typedef __attribute__((address_space(3))) u32x4 lds_u32x4;
typedef __attribute__((address_space(3))) f32x4 lds_f32x4;
#define GPTR(T, lo, hi) ((T*)(__attribute__((address_space(1))) T*)(((u64)(hi) << 32) | (u64)(lo)))
#if defined(__HIP_DEVICE_COMPILE__)
#define NOPK __attribute__((target("no-packed-fp32-ops")))
#else
#define NOPK
#endif
__global__ NOPK __launch_bounds__(768, 3) void fused_kernel(const u16* x_, const u16* kin_, const u16* vin_, const u16* shift_k_,
                                                       const u16* ssm_k_, const u16* ssm_D_, const u16* wqkv_, const u16* wo_,
                                                       const u16* gw_, char* ws_, u16* out_) {
    u32x16 ka0;
    u32x4 ka1;
    u32x2 ka2;
    asm volatile("s_load_dwordx16 %0, %3, 0\n\ts_load_dwordx4 %1, %3, 64\n\ts_load_dwordx2 %2, %3, 80"
                 : "=&s"(ka0), "=&s"(ka1), "=&s"(ka2) : "s"(__builtin_amdgcn_kernarg_segment_ptr()));
#define x GPTR(const u16, ka0[0], ka0[1])
#define kin GPTR(const u16, ka0[2], ka0[3])
#define vin GPTR(const u16, ka0[4], ka0[5])
#define shift_k GPTR(const u16, ka0[6], ka0[7])
#define ssm_k GPTR(const u16, ka0[8], ka0[9])
#define ssm_D GPTR(const u16, ka0[10], ka0[11])
#define wqkv GPTR(const u16, ka0[12], ka0[13])
#define wo GPTR(const u16, ka0[14], ka0[15])
#define gw GPTR(const u16, ka1[0], ka1[1])
#define ws GPTR(char, ka1[2], ka1[3])
#define out GPTR(u16, ka2[0], ka2[1])
#define wsr __builtin_amdgcn_make_buffer_rsrc(ws, 0, 0x4000000, 0x00020000)
    __shared__ __attribute__((aligned(16))) char smem[LDS_TOTAL];
    const int bid = __builtin_amdgcn_workgroup_id_x();
    const int tid = __builtin_amdgcn_workitem_id_x();
    int lane = tid & 63;
    const int wave = __builtin_amdgcn_readfirstlane(tid >> 6);
    const int xcd = bid & 7, r5 = bid >> 3, r = r5 & 15;
    const bool isG = r5 >= 16;
    const int hg = (xcd & 1) * 4 + (r & 3);
    const int rb2 = (xcd >> 1) * 4 + (r >> 2);
    const int cq = (xcd & 1) * 2 + (r & 1), rbv = (xcd >> 1) * 8 + (r >> 1);
    const int vid = cq * 32 + rbv;
    const int sid = rbv * 4 + cq;
    const int rbo = xcd * 4 + (r >> 3) * 2 + ((r5 >> 4) ^ GH ^ 1), oc = r5 & 7;
    const int w = wave & 3;
    const int wr = wave < 4 ? w ^ WR_X : w;
    const int rto = (rbo >> 1) * 8 + (wr >> 1) * 4 + (rbo & 1) * 2 + (wr & 1);
    const int rtq = (rbo >> 1) * 8 + (w >> 1) * 4 + (rbo & 1) * 2 + (w & 1);
    const int g = lane >> 4, n16 = lane & 15, q4 = n16 >> 2, p4 = lane & 3;
    const unsigned lb = (unsigned)(size_t)(lds_char*)smem;
    u64 tag;
    {
        u64 id = llvm_dispatch_id();
        u64 dp = (u64)(size_t)__builtin_amdgcn_dispatch_ptr();
        tag = ((u64)((unsigned)id ^ 0x5bd1e995u) << 32) | (u64)((unsigned)(dp >> 6) ^ (unsigned)(id >> 32) ^ 0x9e3779b9u);
    }
    const unsigned tlo = (unsigned)tag, thi = (unsigned)(tag >> 32);
    if (wave == 0) {
        u64 pc;
        asm volatile("s_getpc_b64 %0" : "=s"(pc));
        pc &= ~255ull;
        const unsigned cl = lane * 32 + r5;
        if (cl < CODE_LINES) GLT(cl * 128, pc);
    }
#define S_O 0xff81ff81u

#define WO_PATS() unsigned wpm, wpo, wpm4, wpo4; { \
        const int c0_ = w * 64 + lane, r0_ = (c0_ * 6554) >> 16, k0_ = r0_ ^ ((r0_ & 8) >> 1), b0_ = oc * 160 + (c0_ - r0_ * 10) * 16; \
        const int c4_ = 256 + lane, r4_ = (c4_ * 6554) >> 16, k4_ = r4_ ^ ((r4_ & 8) >> 1), b4_ = oc * 160 + (c4_ - r4_ * 10) * 16; \
        wpm = opaque((k0_ + ((k0_ >> 4) << 3)) * (D_DIM * 2) + b0_); wpo = opaque((20 * (k0_ >> 2) + (k0_ & 3) + ((~k0_ & 4) << 2)) * (D_DIM * 2) + b0_); \
        wpm4 = opaque((k4_ + 8) * (D_DIM * 2) + b4_); wpo4 = opaque((20 * (k4_ >> 2) + (k4_ & 3) + ((~k4_ & 4) << 2)) * (D_DIM * 2) + b4_); }
#define WO_ROW(ks) (((ks) / 5) * 160 + ((ks) % 5 < 4 ? 40 * ((ks) % 5) : 0))
#define WO_K(ks) GLS((ks) % 5 == 4 ? wpo : wpm, (const char*)wo + WO_ROW(ks) * (D_DIM * 2), lb + w * 1024, WO_POS(ks));
#define SEL4(a, b, c, d) (w == 0 ? (a) : w == 1 ? (b) : w == 2 ? (c) : (d))
#define WO_X(a, b, c, d) GLS(SEL4((a) % 5 == 4, (b) % 5 == 4, (c) % 5 == 4, (d) % 5 == 4) ? wpo4 : wpm4, \
        (const char*)wo + SEL4(WO_ROW(a), WO_ROW(b), WO_ROW(c), WO_ROW(d)) * (D_DIM * 2), lb + SEL4(WO_POS(a), WO_POS(b), WO_POS(c), WO_POS(d)), 4096);
#define WO_4(a, b, c, d) { WO_K(a) WO_K(b) WO_K(c) WO_K(d) WO_X(a, b, c, d) }
    const int kr = KROT;
#define T_C(i) __builtin_amdgcn_readfirstlane((kr + (i)) >= 10 ? kr + (i) - 10 : kr + (i))
#define T_WV() ({ const int j_ = min(n16, 14); (unsigned)((r * 4 + g) * (NQKV * 2) + j_ * 128 + ((j_ >= 5) + (j_ >= 10) + (xcd & 1)) * 640); })
#define T_XV() ((unsigned)(((xcd >> 1) * 512 + r * 32 + (lane & 31)) * (D_DIM * 2)))
#define T_W(wv, i) GLT(wv, (const char*)wqkv + (size_t)T_C(i) * (64 * NQKV * 2))
#define T_X(xv, i) { const int c0_ = T_C(i), c1_ = T_C((i) + 1); GLT((xv) + (lane < 32 ? c0_ : c1_) * 128, x); }
#define T_PAIR(i) { T_W(wv, i); T_X(xv_, i); T_W(wv, (i) + 1); }

    unsigned e0;
    if (isG) {
        const unsigned t0_ = T_WV() + (kr + (w >> 1)) * (64 * NQKV * 2);
        const unsigned t1_ = ((xcd >> 1) * 512 + (w == 1 ? r * 32 + (lane & 31) : r - ((xcd >> 1) ? 16 : 0))) * (D_DIM * 2) + (w == 1 ? kr + (lane >> 5) : min(lane, 9)) * 128;
        e0 = (w & 1) ? t1_ : t0_;
    } else {
        const unsigned c = (wave >= 5 ? wave - 5 : wave) * 64 + lane, row = (c * 3277) >> 16;
        e0 = rbv * (64 * D_DIM * 2) + cq * 320 + row * (D_DIM * 2 - 320) + c * 16;
    }
    asm volatile("" : "+v"(e0));
    asm volatile("s_waitcnt lgkmcnt(0)" : "+s"(ka0), "+s"(ka1), "+s"(ka2) :: "memory");
    if (isG) {
        if ((wave >> 2) == 1) GLT(e0, (w & 1) ? x : wqkv);
        BARRIER();
    }
    u32x2 skw;
    if (!isG && wave < 10) {
        const int tens = wave >= 5, i5 = wave - 5 * tens;
        const unsigned lw = lb + (tens ? V_VST : V_KST) + i5 * 1024;
        const char* kb = (const char*)(tens ? vin : kin);
        __builtin_amdgcn_s_sleep(V_DELAY);
        GLSP(e0, kb, lw, 0);
        GLSP(e0, kb + 16 * D_DIM * 2, lw, 5152);
        GLSP(e0, kb + 32 * D_DIM * 2, lw, 2 * 5152);
        GLSP(e0, kb + 48 * D_DIM * 2, lw, 3 * 5152);
        asm volatile("global_load_dwordx2 %0, %1, %2" : "=v"(skw) : "v"((cq * 160 + wave * 16 + n16) * 8), "s"(shift_k) : "memory");
        if (wave == 0) { if (lane < 60) GLS(e0, kb - (rbv ? 3 * D_DIM * 2 : 0), lb, V_KST - 32 - 960); }
    }
    if (wave == 11) {
        if (lane < 16) *(u32x4*)(smem + L_CTRL + lane * 16) = (u32x4){0u, 0u, 0u, 0u};
        asm volatile("s_waitcnt lgkmcnt(0)" ::: "memory");
    }
#define Y_OFF (WS_Y + rbo * (D_DIM * 128) + oc * 10240 + w * 1024)
#define Y_FIRST() u32x4 yv[3]; float g1; { yv[0] = __builtin_amdgcn_raw_buffer_load_b128(wsr, lane * 16, Y_OFF, 16); yv[1] = __builtin_amdgcn_raw_buffer_load_b128(wsr, lane * 16, Y_OFF + 4096, 16); \
        yv[2] = __builtin_amdgcn_raw_buffer_load_b128(wsr, lane * 16, Y_OFF + (w < 2 ? 8192 : 0), 16); \
        g1 = __uint_as_float(__builtin_amdgcn_raw_buffer_load_b32(wsr, (lane & 15) * 4, WS_G + rtq * 64, 16)); }
#define RELANE() const int lane_ = opaque(lane); const int g = lane_ >> 4, n16 = lane_ & 15, q4 = n16 >> 2, p4 = lane_ & 3; \
        const unsigned qa = lb + (isG ? G_Q : V_Q) + w * 2560 + n16 * 160 + 8 * g
#define SENT4(v) __any(max(max((v)[0], (v)[1]), max((v)[2], (v)[3])) == 0xffffffffu)
#define Q_READ(base) f32x4 gq[5]; { const int l_ = opaque(lane); _Pragma("unroll") for (int ct = 0; ct < 5; ++ct) { \
            const u32x2 q2 = *(const lds_u32x2*)(size_t)(lb + (base) + w * 2560 + (l_ & 15) * 160 + 8 * (l_ >> 4) + ct * 32); \
            gq[ct] = (f32x4){lo_f(q2[0]), hi_f(q2[0]), lo_f(q2[1]), hi_f(q2[1])}; } \
        asm volatile("" : "+v"(gq[0]), "+v"(gq[1]), "+v"(gq[2]), "+v"(gq[3]), "+v"(gq[4]) :: "memory"); }
#define P_STAGE() { \
        RELANE(); \
        const unsigned l_y = lb + (isG ? G_Y : V_Y); \
        const unsigned yw = l_y + (w * 8 + (lane >> 3)) * Y_PITCH + (lane & 7) * 16; \
        const unsigned yr = l_y + (4 * g + q4) * Y_PITCH + w * 32 + p4 * 8; \
_Pragma("nounroll") \
        for (int it = 0; it < SPIN_CAP; ++it) { \
            const bool b0 = SENT4(yv[0]), b1 = SENT4(yv[1]), b2 = SENT4(yv[2]); \
            if (!(b0 | b1 | b2)) break; \
            if (it || Y_SL0) __builtin_amdgcn_s_sleep(Y_SLEEP); \
            asm volatile("" ::: "memory"); \
            if (b0) yv[0] = __builtin_amdgcn_raw_buffer_load_b128(wsr, lane * 16, Y_OFF, 16); \
            if (b1) yv[1] = __builtin_amdgcn_raw_buffer_load_b128(wsr, lane * 16, Y_OFF + 4096, 16); \
            if (b2) yv[2] = __builtin_amdgcn_raw_buffer_load_b128(wsr, lane * 16, Y_OFF + (w < 2 ? 8192 : 0), 16); \
        } \
        STAMP(8); \
        *(lds_u32x4*)(size_t)yw = yv[0]; \
        *(lds_u32x4*)(size_t)(yw + 32 * Y_PITCH) = yv[1]; \
        if (w < 2) *(lds_u32x4*)(size_t)(yw + 64 * Y_PITCH) = yv[2]; \
        lds_add(lb + C_Y, lane); \
_Pragma("unroll") \
        for (int ct = 0; ct < 5; ++ct) gq[ct] *= g1; \
        asm volatile("" : "+v"(gq[0]), "+v"(gq[1]), "+v"(gq[2]), "+v"(gq[3]), "+v"(gq[4])); \
        spin_ge(lb + C_Y, 4); \
        u32x2 y2[5]; \
_Pragma("unroll") \
        for (int ct = 0; ct < 5; ++ct) y2[ct] = __builtin_bit_cast(u32x2, TRRD((lds_char*)(size_t)(yr + ct * 16 * Y_PITCH))); \
_Pragma("unroll") \
        for (int ct = 0; ct < 5; ++ct) gq[ct] *= (f32x4){lo_f(y2[ct][0]), hi_f(y2[ct][0]), lo_f(y2[ct][1]), hi_f(y2[ct][1])}; \
        const float g0 = 1.f - g1; \
        const unsigned pa = lb + PART(w) + lane_ * 16; \
        const auto outr = __builtin_amdgcn_make_buffer_rsrc(out, 0, S_LEN * D_DIM * 2, 0x00020000); \
        unsigned ob = ((rtq * 16 + n16) * D_DIM + oc * 80) * 2, ob2 = ob + (64 + 4 * g) * 2; \
        ob += ((g & 1) * 16 + (g >> 1) * 8) * 2; \
        asm volatile("" : "+v"(gq[0]), "+v"(gq[1]), "+v"(gq[2]), "+v"(gq[3]), "+v"(gq[4]), "+v"(ob), "+v"(ob2)); \
        STAMP(9); \
          \
        u32x4 pf; \
_Pragma("nounroll") \
        for (int it = 0; it < SPIN_CAP; ++it) { \
            pf = lds_v128(lb + C_PF + w * 16); \
            if (__builtin_amdgcn_readfirstlane((pf[0] & pf[1]) | (pf[2] & pf[3]))) break; \
            __builtin_amdgcn_s_sleep(1); \
        } \
        const unsigned h1 = __builtin_amdgcn_readfirstlane(pf[0] & pf[1]) ? 0 : 10240; \
        f32x4 sa[5]; \
_Pragma("unroll") \
        for (int ct = 0; ct < 5; ++ct) sa[ct] = *(const lds_f32x4*)(size_t)(pa + h1 + ct * 1024) + *(const lds_f32x4*)(size_t)(pa + h1 + 5120 + ct * 1024); \
        asm volatile("" : "+v"(sa[0]), "+v"(sa[1]), "+v"(sa[2]), "+v"(sa[3]), "+v"(sa[4])); \
_Pragma("nounroll") \
        for (int it = 0; it < SPIN_CAP; ++it) { \
            if (__builtin_amdgcn_readfirstlane((pf[0] & pf[1]) & (pf[2] & pf[3]))) break; \
            pf = lds_v128(lb + C_PF + w * 16); \
        } \
        asm volatile("" ::: "memory"); \
        STAMP(13); \
        unsigned pk[5][2]; \
_Pragma("unroll") \
        for (int ct = 0; ct < 5; ++ct) { \
            const f32x4 sb = *(const lds_f32x4*)(size_t)(pa + (10240 - h1) + ct * 1024) + *(const lds_f32x4*)(size_t)(pa + (10240 - h1) + 5120 + ct * 1024); \
            const f32x4 fin = gq[ct] + g0 * (sa[ct] + sb); \
            pk[ct][0] = pack2(fin[0], fin[1]); \
            pk[ct][1] = pack2(fin[2], fin[3]); \
        } \
_Pragma("unroll") \
        for (int p = 0; p < 2; ++p) { \
            const auto r0 = __builtin_amdgcn_permlane16_swap(pk[2 * p][0], pk[2 * p + 1][0], false, false); \
            const auto r1 = __builtin_amdgcn_permlane16_swap(pk[2 * p][1], pk[2 * p + 1][1], false, false); \
            __builtin_amdgcn_raw_buffer_store_b128((u32x4){r0[0], r1[0], r0[1], r1[1]}, outr, ob + p * 64, 0, OUT_AUX); \
        } \
        __builtin_amdgcn_raw_buffer_store_b64((u32x2){pk[4][0], pk[4][1]}, outr, ob2, 0, OUT_AUX); \
        FLUSH(); \
        return; }

    if (isG) {
      if (wave >= 4) {
        const int half = (wave >> 2) - 1;
        if (half) __builtin_amdgcn_s_setprio(C1_PRIO); else __builtin_amdgcn_s_setprio(C0_PRIO);
        const int rb = rb2 * 2 + half;
        unsigned ap[2], wp[4];
#pragma unroll
        for (int sub = 0; sub < 2; ++sub) ap[sub] = lb + half * 8192 + n16 * 128 + (((sub * 4 + g) ^ ((n16 >> 1) & 7)) * 16);
#pragma unroll
        for (int i = 0; i < 4; ++i) {
            const int part = i < 3 ? i : (p4 < 3 ? p4 : 2);
            const int Q = i < 3 ? 5 * w + (w & 1) + p4 : 5 * w + ((w & 1) ? 0 : 4);
            const int c2 = (Q >> 1) - 2 * (hg & 1);
            const int cc = ((unsigned)c2 < 8u ? part * 8 + c2 : 24 + part * 2 + (c2 & 1)) ^ (2 * ((g & 1) * 4 + q4));
            wp[i] = lb + W_OFF(0) + (8 * g + q4) * W_PITCH + cc * 16 + (Q & 1) * 8;
        }
        const int qi = n16, par = lane & 1;
        if (wave == 11) {
            f32x4 cm0, cm1;
            u32x4 psel;
#pragma unroll
            for (int e = 0; e < 4; ++e) {
                const bool own = 4 * g + e <= qi, pm = (e & 1) == par;
                cm0[e] = pm && !own ? 0.f : -3.0e38f;
                cm1[e] = pm && own ? 0.f : -3.0e38f;
                const bool ow = 4 * g + par + 2 * (e & 1) <= qi;
                const unsigned by = (e & 1) ? 0x0302u : 0x0100u;
                psel[e] = (ow == (e >= 2)) ? (par ? (by << 16) | 0x0c0cu : 0x0c0c0000u | by) : 0x0c0c0c0cu;
            }
            *(lds_f32x4*)(size_t)(lb + G_TAB + lane * 16) = cm0;
            *(lds_f32x4*)(size_t)(lb + G_TAB + 1024 + lane * 16) = cm1;
            *(lds_u32x4*)(size_t)(lb + G_TAB + 2048 + lane * 16) = psel;
            if (lane < 5) *(lds_u32x2*)(size_t)(lb + G_ZR + lane * 128) = (u32x2){0u, 0u};
        }
        const unsigned vxw = lb + G_VX + w * 1152;
        const unsigned scw = g == 2 ? vxw + half * 512 + qi * 8 : lb + G_VXD;
        const int hj = (hg & 1) * 4 + w;
        const unsigned oo = WS_O + ((rb * 4 + (g & 1)) * 20 + (hg >> 1) * 5 + (hj >> 1)) * 1024 + (((w & 1) * 2 + (g >> 1)) * 16 + qi) * 16;
        const unsigned ox = WS_O + ((rb * 4 + g) * 20 + (hg >> 1) * 5 + 4) * 1024 + hj * 128 + qi * 8;
        unsigned c0_, c1_, l16 = lane * 16;
        STAMP(6);
        CSTAMP(0);
        asm volatile(G_ASM
                     : [cnt] "=&s"(c0_), [tmp] "=&s"(c1_), [wp0] "+v"(wp[0]), [wp1] "+v"(wp[1]), [wp2] "+v"(wp[2]), [wp3] "+v"(wp[3]), [l16] "+v"(l16)
                     : [ap0] "v"(ap[0]), [ap1] "v"(ap[1]), [scw] "v"(scw), [oo] "v"(oo), [ox] "v"(ox),
                       [vxb] "v"(vxw + half * 512 + (4 * g + q4) * 8), [zr] "v"(lb + G_ZR), [fr] "v"(lb + C_FREE + (wave - 4) * 4),
                       [tabc] "s"(lb + G_TAB), [hxc] "s"(lb + G_HX + w * 1024), [hxd] "s"(lb + G_HXO + w * 256),
                       [wsp] "s"(ws), [half] "s"(half), [rb2] "s"(rb2), [cap] "s"(SPIN_CAP), [sc] "s"(0x3ea52b5bu)
                     : G_CLOB);
        lane = l16 >> 4;
        CSTAMP(1);
        __builtin_amdgcn_s_setprio(T_PRIO);
        if (half) {
            if (GY_DELAY > 0) { lds_add(lb + C_OST1, lane); __builtin_amdgcn_s_sleep(GY_DELAY); }
            Y_FIRST();
            if (GY_DELAY == 0) lds_add(lb + C_OST1, lane);
            STAMP(4);
            spin_ge(lb + C_WO, 4);
            spin_all(lb + C_FREE + (lane & 7) * 4, 18);
            Q_READ(G_Q);
            P_STAGE();
        }
        lds_add(lb + C_OST, lane);
        STAMP(4);
      } else {
        __builtin_amdgcn_s_setprio(L_PRIO);
        unsigned ev[9];
        {
            const int hi = lane >> 5, pos = lane & 31;
#pragma unroll
            for (int v = 0; v < 2; ++v) {
                const int row = 2 * w + hi + 8 * v;
                int L = pos ^ (2 * (v * 4 + (row & 3)));
                L = L > 29 ? L - 2 : L;
                const int part = L < 24 ? L >> 3 : (L - 24) >> 1;
                const int chunk = L < 24 ? (L & 7) + 2 * (hg & 1) : (L & 1) + ((hg & 1) ? 0 : 8);
                ev[v] = row * (NQKV * 2) + chunk * 16 + part * 1280 + hg * 160;
                ev[v + 2] = ev[v] + 16 * NQKV * 2;
            }
            const int ch = (lane & 7) ^ ((4 * (w & 1) + g) & 7);
#pragma unroll
            for (int j = 0; j < 5; ++j) {
                int t = rb2 * 128 - 16 + 8 * w + (lane >> 3) + 32 * j;
                t = t < 0 ? 0 : t;
                t = t > S_LEN - 1 ? S_LEN - 1 : t;
                ev[4 + j] = t * (D_DIM * 2) + ch * 16;
            }
        }
        SB();
        if (L_DELAY > 0) __builtin_amdgcn_s_sleep(L_DELAY);
        STAMP(5);
        if (w < 2) ring_issue<5, 0, ring_need(-1, 5)>(ev, x, wqkv, kr, lb + w * 1024); else ring_issue<4, 0, ring_need(-1, 4)>(ev, x, wqkv, kr, lb + w * 1024);
        SB();
        unsigned qf[3];
        WO_PATS();
#pragma unroll
        for (int j = 0; j < 3; ++j) { const int c = min(j * 64 + lane, 159); const int row = c / 10, ch = c - row * 10; qf[j] = opaque(row * (D_DIM * 2) + ch * 16); }
        const unsigned sfo = opaque(WS_SF + (lane < 8 ? rb2 * 8 + lane : (rbo >> 1) * 8 + (lane & 7)) * 128);
        const unsigned fra = opaque(lb + C_FREE + (lane & 7) * 4);
        SB();
        if (w < 2) {
            ring_issue<5, ring_need(-1, 5), ring_cum(-1, 5)>(ev, x, wqkv, kr, lb + w * 1024);
            waitvm<ring_cum(-1, 5) - ring_need(-1, 5)>();
            STAMP(6);
            BARRIER();
            ring_run<5, 0>(ev, x, wqkv, kr, lb + w * 1024);
        } else {
            ring_issue<4, ring_need(-1, 4), ring_cum(-1, 4)>(ev, x, wqkv, kr, lb + w * 1024);
            waitvm<ring_cum(-1, 4) - ring_need(-1, 4)>();
            BARRIER();
            ring_run<4, 0>(ev, x, wqkv, kr, lb + w * 1024);
        }
        STAMP(2);
#define WO_POS(ks) ((ks) < 10 ? G_R0 + (ks) * 5120 : G_R1 + ((ks) - 10) * 5120)
        static_assert(JL == 17, "last barrier");
        WO_4(0, 1, 2, 3)
        WAIT_VM(9);
        BARRIER();
        WO_4(4, 5, 10, 11)
        WAIT_VM(10);
        BARRIER();
        __builtin_amdgcn_s_setprio(WO_PRIO);
        u32x2 fv;
        if (w == 3) asm volatile("global_load_dwordx2 %0, %1, %2 sc1" : "=v"(fv) : "v"(sfo), "s"(ws) : "memory");
        WO_4(12, 6, 7, 8)
        if (w == 3) {
            asm volatile("s_waitcnt vmcnt(5)" : "+v"(fv) :: "memory");
            if (__all(fv[0] == tlo && fv[1] == thi)) lds_st32(lb + C_SENT, 1);
        }
        spin_all(fra, 16);
        WO_4(9, 13, 14, 15)
        WO_K(16)
        spin_all(fra, 17);
        WO_K(17) WO_K(18) WO_K(19) WO_X(16, 17, 18, 19)
#undef WO_POS
#pragma unroll
        for (int j = 0; j < 3; ++j)
            if (j < 2 || lane < 32) GLS(qf[j], (const char*)x + (size_t)(rtq * 16) * (D_DIM * 2) + oc * 160, lb + w * 2560, G_Q + j * 1024);
        STAMP(3);
        if (w == 3 && !__all(fv[0] == tlo && fv[1] == thi)) {
            poll_flags(wsr, sfo, tlo, thi);
            lds_st32(lb + C_SENT, 1);
        }
        WAIT_VM(0);
        lds_add(lb + C_WO, lane);
        STAMP(4);
      }
    } else {
        __builtin_amdgcn_s_setprio(3);
        const int c5 = wave < 4 ? wave : 4;
        const int d = vid * 5 + c5;
        unsigned Dw = *(const __attribute__((address_space(4))) unsigned*)(size_t)(ssm_D + (d & ~1));
        unsigned hp0 = *(const __attribute__((address_space(4))) unsigned*)(size_t)(ssm_k + (vid * 5 + 4) * S_LEN + w * 512 - 2);
        BARRIER();
        if (wave <= 4) hp0 = 0;
        asm volatile("" : "+s"(Dw), "+s"(hp0));
#define SF_DONE() do { WAIT_VM(0); if (lds_add_rtn(lb + C_SFC, lane) == 5) { \
            if (lane == 0) __hip_atomic_store((u64*)(ws + WS_SF + sid * 128), tag, __ATOMIC_RELAXED, __HIP_MEMORY_SCOPE_AGENT); lds_add(lb + C_SFC, lane); } } while (0)
#define WO_POS(ks) (V_WO + (ks) * 5120)
#define KV_DONE() do { if (lds_add_rtn(lb + C_KVD, lane) == 9) { \
            if (lane == 0) __hip_atomic_store((u64*)(ws + WS_XF + vid * 128), tag, __ATOMIC_RELAXED, __HIP_MEMORY_SCOPE_AGENT); lds_add(lb + C_KVD, lane); } } while (0)
        if (wave == 10) {
            const unsigned wv = T_WV(), xv_ = T_XV();
            T_PAIR(2);
            T_PAIR(4);
            if (WO_MALL) GLT((unsigned)((vid * 50 + min(lane, 49)) * 128), wo);
        }
        if (wave < 10) {
            const int m0 = (g & 1) * 2 + (g >> 1);
            const unsigned kp = opaque(lb + V_KST + (8 * m0 + q4) * 320 + (m0 >> 1) * 32 + wave * 32 + p4 * 8);
            const unsigned km = opaque(kp - 1280 - ((m0 & 1) ? 0 : 32));
            const unsigned sw_ = opaque(lb + V_STG + wave * 2048 + n16 * 128 + ((m0 + n16) & 7) * 16);
            const unsigned sr_ = opaque(lb + V_STG + wave * 2048 + (lane >> 3) * 128 + ((lane + (lane >> 3)) & 7) * 16);
            const unsigned e1 = opaque(WS_KV + (cq * 160 + wave * 16 + (lane >> 3)) * KV_PITCH + rbv * 128 + (lane & 7) * 16), e2 = opaque(e1 + 8 * KV_PITCH);
            asm volatile("s_waitcnt vmcnt(0)" : "+v"(skw) :: "memory");
            lds_add(lb + C_KV, lane);
            const f32x2 s0 = {lo_f(skw[0]), lo_f(skw[0])}, s1 = {hi_f(skw[0]), hi_f(skw[0])}, s2 = {lo_f(skw[1]), lo_f(skw[1])}, s3 = {hi_f(skw[1]), hi_f(skw[1])};
            spin_ge(lb + C_KV, 10);
            STAMP(2);
            u32x2 kq[2][3], vq[2][2];
#pragma unroll
            for (int i = 0; i < 2; ++i) {
                kq[i][0] = __builtin_bit_cast(u32x2, TRRD((lds_char*)(size_t)(km + i * 10304)));
#pragma unroll
                for (int e = 0; e < 2; ++e) kq[i][e + 1] = __builtin_bit_cast(u32x2, TRRD((lds_char*)(size_t)(kp + i * 10304 + e * 1280)));
#pragma unroll
                for (int e = 0; e < 2; ++e) vq[i][e] = __builtin_bit_cast(u32x2, TRRD((lds_char*)(size_t)(kp + i * 10304 + (V_VST - V_KST) + e * 1280)));
            }
            if (rbv == 0 && m0 == 0) kq[0][0] = (u32x2){0u, 0u};
            f32x2 kf[12], res[8];
#pragma unroll
            for (int e = 1; e < 12; ++e) {
                const unsigned u0 = kq[0][e >> 2][(e >> 1) & 1], u1 = kq[1][e >> 2][(e >> 1) & 1];
                kf[e] = (e & 1) ? (f32x2){hi_f(u0), hi_f(u1)} : (f32x2){lo_f(u0), lo_f(u1)};
            }
#pragma unroll
            for (int e = 0; e < 8; ++e) {
                const unsigned u0 = vq[0][e >> 2][(e >> 1) & 1], u1 = vq[1][e >> 2][(e >> 1) & 1];
                f32x2 a = kf[e + 1] * s3;
                a = kf[e + 2] * s2 + a;
                a = kf[e + 3] * s1 + a;
                a = kf[e + 4] * s0 + a;
                res[e] = a * ((e & 1) ? (f32x2){hi_f(u0), hi_f(u1)} : (f32x2){lo_f(u0), lo_f(u1)});
            }
#pragma unroll
            for (int i = 0; i < 2; ++i) {
                u32x4 pk;
#pragma unroll
                for (int e = 0; e < 4; ++e) pk[e] = pack2(res[2 * e][i], res[2 * e + 1][i]);
                *(lds_vu32x4*)(size_t)(sw_ ^ (i * 64)) = pk;
            }
            const u32x4 pk0 = lds_v128(sr_), pk1 = lds_v128(sr_ + 1024);
            __builtin_amdgcn_raw_buffer_store_b128(pk0, wsr, e1, 0, 16);
            __builtin_amdgcn_raw_buffer_store_b128(pk1, wsr, e2, 0, 16);
            lds_add(lb + C_KVST, lane);
            STAMP(3);
        }
        if (wave >= 8) {
            const unsigned wv = T_WV(), xv_ = T_XV();
            WO_PATS();
            unsigned qf[3];
#pragma unroll
            for (int j = 0; j < 3; ++j) { const int c = min(j * 64 + lane, 159); const int row = c / 10, ch = c - row * 10; qf[j] = opaque(row * (D_DIM * 2) + ch * 16); }
            if (wave == 10) {
            } else if (wave == 11) {
                if (lane < 56) {
#pragma unroll
                    for (int c5 = 0; c5 < 5; ++c5)
                        *(lds_u32x4*)(size_t)(lb + (lane < 16 ? V_KV(c5) + lane * 16 : V_F + c5 * XC_BYTES + 15 * XC_ROW + 256 + (lane < 36 ? XC_COPY + (lane - 16) * 16 : XC_B + 4 + (lane - 36) * 16))) = (u32x4){0u, 0u, 0u, 0u};
                }
                const unsigned x4o = opaque(WS_KV + d * KV_PITCH + lane * 16);
                spin_ge(lb + C_KVD, 11);
                if (POLL_DELAY > 0) __builtin_amdgcn_s_sleep(POLL_DELAY);
                const unsigned xfo = WS_XF + (cq * 32 + (lane & 31)) * 128;
                const u32x2 fv = __builtin_amdgcn_raw_buffer_load_b64(wsr, xfo, 0, 16);
                if (!__all(fv[0] == tlo && fv[1] == thi)) {
                    lds_st32(lb + C_GO, 1);
                    poll_flags(wsr, xfo, tlo, thi);
                }
                asm volatile("s_mov_b32 m0, %2\n\ts_nop 0\n\tglobal_load_lds_dwordx4 %0, %1 sc1\n\tglobal_load_lds_dwordx4 %0, %1 offset:1024 sc1\n\t"
                             "global_load_lds_dwordx4 %0, %1 offset:2048 sc1\n\tglobal_load_lds_dwordx4 %0, %1 offset:3072 sc1" ::"v"(x4o), "s"(ws), "s"(lb + V_KV4 + 256) : "memory");
                lds_add(lb + C_KVR, lane);
                STAMP(2);
                WAIT_VM(0);
                lds_st32(lb + C_HR, 4);
                STAMP(3);
                spin_ge(lb + C_XD, 4);
                lds_st32(lb + C_GO, 1);
                if (SF_EARLY == 1) {
                    spin_mid(lb + C_SFC, 7);
                    if (SF_DELAY > 0) __builtin_amdgcn_s_sleep(SF_DELAY);
                    poll_flags(wsr, WS_SF + (lane < 32 ? lane * 4 + cq : (rbo >> 1) * 8 + (lane & 7)) * 128, tlo, thi);
                    lds_st32(lb + C_SENT, 1);
                    STAMP(10);
                }
            } else {
                WAIT_VM(0);
                KV_DONE();
                STAMP(11);
                spin_ge(lb + SENT_W, SENT_N);
                const unsigned sv = wave == 8 ? S_O : 0xffffffffu;
                const u32x4 ones = {sv, sv, sv, sv};
#pragma unroll
                for (int i = 0; i < NSENT; ++i) {
                    if (i >= SENT_PACE) waitvm<SENT_PACE - 1>();
                    __builtin_amdgcn_raw_buffer_store_b128(ones, wsr, (wave == 8 ? WS_O : WS_Y) + sid * 20480 + lane * 16 + i * 1024, 0, 16);
                }
                STAMP(4);
                SF_DONE();
                STAMP(12);
            }
            if (wave == 10) {
                spin_ge(lb + GO_W, GO_N);
                T_PAIR(6);
                if (T4_EARLY) WAIT_VM(1); else spin_ge(lb + GO2_W, GO2_N);
                T_PAIR(8);
            }
            spin_mid(lb + C_XD, 4);
#pragma unroll
            for (int j = 0; j < 3; ++j)
                if (j < 2 || lane < 32) GLS(qf[j], (const char*)x + (size_t)(rtq * 16) * (D_DIM * 2) + oc * 160, lb + w * 2560, V_QS + j * 1024);
            if (wave == 10) {
#pragma unroll
                for (int i = 0; i < 7; ++i) {
                    waitvm<WOT_PACE>();
                    if (i < 6 || lane < 16) GLT((unsigned)(lane * 128), (const char*)wo + (r * 400 + i * 64) * 128);
                }
                WAIT_VM(0);
                lds_st32(lb + C_WT, 1);
                STAMP(4);
            }
            WAIT_VM(0);
            Q_READ(V_QS);
            lds_add(lb + C_QR, lane);
            spin_mid(lb + C_WT, 1);
            STAMP(5);
            u32x2 fv = {0u, 0u};
            const unsigned sfo = WS_SF + (lane < 32 ? lane * 4 + cq : (rbo >> 1) * 8 + (lane & 7)) * 128;
            if (wave == 11 && SF_EARLY == 2) asm volatile("global_load_dwordx2 %0, %1, %2 sc1" : "=v"(fv) : "v"(sfo), "s"(ws) : "memory");
            WO_4(4, 5, 6, 7)
            if (wave == 11 && SF_EARLY == 2) {
                asm volatile("s_waitcnt vmcnt(5)" : "+v"(fv) :: "memory");
                if (__all(fv[0] == tlo && fv[1] == thi)) { lds_st32(lb + C_SENT, 1); STAMP(10); }
            }
            WO_4(8, 9, 10, 11)
            if (wave == 11 && SF_EARLY != 1 && !__all(fv[0] == tlo && fv[1] == thi)) {
                spin_mid(lb + C_SFC, 7);
                poll_flags(wsr, WS_SF + (lane < 32 ? lane * 4 + cq : (rbo >> 1) * 8 + (lane & 7)) * 128, tlo, thi);
                lds_st32(lb + C_SENT, 1);
                STAMP(10);
            }
            WAIT_VM(0);
            lds_add(lb + C_WO, lane);
            STAMP(6);
            spin_mid(lb + C_CONVN, 8);
            if (VY_DELAY > 0) { lds_add(lb + C_OST, lane); __builtin_amdgcn_s_sleep(VY_DELAY); }
            Y_FIRST();
            if (VY_DELAY == 0) lds_add(lb + C_OST, lane);
            STAMP(7);
            P_STAGE();
        } else {
            const unsigned xb = lb + V_F + c5 * XC_BYTES;
            const unsigned hb = lb + V_KV(c5) + 256;
            const float Dd = (d & 1) ? hi_f(Dw) : lo_f(Dw);
            u32x4 hv[4];
            const unsigned l16 = lane * 16;
            {
                const char* hsrc = (const char*)ssm_k + (size_t)d * (S_LEN * 2) + (wave < 4 ? 0 : w * 1024);
                spin_ge(lb + C_KVST, 10);
#define H_LD(i) asm volatile("global_load_dwordx4 %0, %1, %2 offset:%3" H_POL : "=v"(hv[i]) : "v"(l16), "s"(hsrc), "n"((i) * 1024) : "memory")
                H_LD(0);
                if (wave < 4) { H_LD(1); H_LD(2); H_LD(3); WAIT_VM(4); } else WAIT_VM(1);
                KV_DONE();
                STAMP(4);
            }
            {
                const unsigned da = xb + (3 - g) * XC_ROW + (15 - n16) * 16 + (wave < 4 ? 0 : -w * (4 * XC_ROW));
                u32x4 ra[4], rb[4];
                asm volatile("s_waitcnt vmcnt(0)" : "+v"(hv[0]), "+v"(hv[1]), "+v"(hv[2]), "+v"(hv[3]) :: "memory");
#define H_PREV(i) __builtin_amdgcn_update_dpp((i) == 0 ? hp0 : __builtin_amdgcn_readlane(hv[(i) == 0 ? 0 : (i) - 1][3], 63), hv[i][3], 0x138, 0xf, 0xf, false)
#define BFI16(lo, hi) (((lo) & 0xffffu) | ((hi) & 0xffff0000u))
#define H_PREP(i) { ra[i] = (u32x4){__builtin_amdgcn_alignbit(hv[i][3], hv[i][3], 16), __builtin_amdgcn_alignbit(hv[i][2], hv[i][2], 16), __builtin_amdgcn_alignbit(hv[i][1], hv[i][1], 16), __builtin_amdgcn_alignbit(hv[i][0], hv[i][0], 16)}; \
                    rb[i] = (u32x4){BFI16(hv[i][3], hv[i][2]), BFI16(hv[i][2], hv[i][1]), BFI16(hv[i][1], hv[i][0]), BFI16(hv[i][0], H_PREV(i))}; \
                    *(lds_u32x4*)(size_t)(da + (3 - (i)) * (4 * XC_ROW) + XC_COPY) = ra[i]; *(lds_u32x4*)(size_t)(da + (3 - (i)) * (4 * XC_ROW) + XC_B + 4) = rb[i]; }
#define H_DUP(i) { *(lds_u32x4*)(size_t)(da + (3 - (i)) * (4 * XC_ROW) + XC_COPY - 32) = ra[i]; *(lds_u32x4*)(size_t)(da + (3 - (i)) * (4 * XC_ROW) + XC_B + 4 - 32) = rb[i]; }
                H_PREP(0)
                if (wave < 4) {
                    H_PREP(1) H_PREP(2) H_PREP(3)
                    if (n16 == 15) { H_DUP(0) H_DUP(1) H_DUP(2) if (g < 3) H_DUP(3) }
                } else {
                    if (n16 == 15 && (w < 3 || g < 3)) H_DUP(0)
                }
            }
            STAMP(9);
            const int j0 = wave >= 4 ? 2 * w : 0;
            const int mm = (n16 & 3) | ((n16 & 4) << 1) | ((n16 & 8) >> 1);
            const unsigned kp = hb + 2 * (256 * j0 + 128 * (g >> 1) + 8 * mm + 4 * (g & 1));
            const int ts = 256 * (j0 + (g & 1)) + 128 * (g >> 1) + 8 * mm;
            const unsigned yo = WS_Y + d * 128 + ((ts >> 7) * 2 + ((ts >> 5) & 1)) * (D_DIM * 128) + (((ts >> 6) & 1) * 32 + (ts & 31)) * 2;
            if (wave < 4) {
                const int b1 = n16 >> 3, phi = n16 & 7;
                const unsigned xp = xb + ((phi & 1) ? XC_COPY - 4 : XC_B) + (1 - b1) * XC_ROW + 2 * (8 - 2 * (phi >> 1) + 8 * g);
                const unsigned hp = hb + 2 * (8 * (mm + g) - 120);
                const int sx4 = (4 - w) * XC_BYTES + (w >> 1) * XC_ROW + (w & 1) * 128, sh4 = V_KV4 - w * KV_BYTES + w * 128;
                unsigned c0_, c1_;
                __builtin_amdgcn_s_setprio(CONV_PRIO);
                spin_ge(lb + C_KVR, 5);
                STAMP(6);
                CSTAMP(0);
                asm volatile(CONV_ASM
                             : [cnt] "=&s"(c0_), [tmp] "=&s"(c1_)
                             : [xp] "v"(xp), [hp] "v"(hp), [sx4] "s"(sx4), [sh4] "s"(sh4), [kvl] "s"(hb), [kvr] "v"(kp), [scr] "v"(lb + V_SCR + w * 8192 + l16),
                               [ctl] "v"(lb + L_CTRL), [dd] "s"(Dd), [cap] "s"(SPIN_CAP), [xo] "v"(WS_KV + d * KV_PITCH + l16), [wsp] "s"(ws), [yo] "v"(yo)
                             : CONV_CLOB);
                __builtin_amdgcn_s_setprio(3);
                CSTAMP(1);
            } else {
                lds_add(lb + C_KVR, lane);
                WO_PATS();
                {
                    const int grow = (xcd >> 1) * 512 + r * 32 + (xcd & 1) * 16 + w * 4 + g;
                    const unsigned go = opaque(grow * (D_DIM * 2) + n16 * 16);
                    if (GATE_LATE) spin_mid(lb + C_XD, 4);
                    u32x4 gx[5], gv[5];
#pragma unroll
                    for (int i = 0; i < 5; ++i) {
                        gx[i] = *(const u32x4*)((const char*)x + go + i * 256);
                        gv[i] = *(const u32x4*)((const char*)gw + n16 * 16 + i * 256);
                    }
                    float sa[4] = {0.f, 0.f, 0.f, 0.f};
#pragma unroll
                    for (int i = 0; i < 5; ++i)
#pragma unroll
                        for (int e = 0; e < 4; ++e) sa[e] = __builtin_fmaf(hi_f(gx[i][e]), hi_f(gv[i][e]), __builtin_fmaf(lo_f(gx[i][e]), lo_f(gv[i][e]), sa[e]));
                    float sacc = (sa[0] + sa[1]) + (sa[2] + sa[3]);
                    sacc += __builtin_amdgcn_update_dpp(0.f, sacc, 0x128, 0xf, 0xf, true);
                    sacc += __builtin_amdgcn_update_dpp(0.f, sacc, 0x124, 0xf, 0xf, true);
                    sacc += __builtin_amdgcn_update_dpp(0.f, sacc, 0x122, 0xf, 0xf, true);
                    sacc += __builtin_amdgcn_update_dpp(0.f, sacc, 0x121, 0xf, 0xf, true);
                    const float gres = rbf(__builtin_amdgcn_rcpf(1.f + __expf(-rbf(sacc))));
                    if (n16 == 0) __builtin_amdgcn_raw_buffer_store_b32(__float_as_uint(gres), wsr, WS_G + grow * 4, 0, 16);
                    SF_DONE();
                    STAMP(6);
                }
                spin_mid(lb + C_RED, 4);
                STAMP(5);
                f32x4 acc[2];
#pragma unroll
                for (int e = 0; e < 2; ++e) {
                    const u32x2 kk = *(const lds_u32x2*)(size_t)(kp + e * 512);
                    acc[e] = (f32x4){lo_f(kk[0]) * Dd, hi_f(kk[0]) * Dd, lo_f(kk[1]) * Dd, hi_f(kk[1]) * Dd};
#pragma unroll
                    for (int o = 0; o < 4; ++o) acc[e] += *(const lds_f32x4*)(size_t)(lb + V_SCR + o * 8192 + (2 * w + e) * 1024 + l16);
                }
                lds_add(lb + C_RED2, lane);
                const auto r0 = __builtin_amdgcn_permlane16_swap(min(pack2(acc[0][0], acc[0][1]), 0xfffffffeu), min(pack2(acc[1][0], acc[1][1]), 0xfffffffeu), false, false);
                const auto r1 = __builtin_amdgcn_permlane16_swap(min(pack2(acc[0][2], acc[0][3]), 0xfffffffeu), min(pack2(acc[1][2], acc[1][3]), 0xfffffffeu), false, false);
                STAMP(7);
                spin_ge(lb + C_RED2, 4);
                spin_mid(lb + C_WT, 1);
                WO_4(12, 13, 14, 15) WO_4(16, 17, 18, 19)
                spin_mid(lb + C_QR, 4);
                WO_4(0, 1, 2, 3)
                spin_mid(lb + C_SENT, 1);
                __builtin_amdgcn_raw_buffer_store_b128((u32x4){r0[0], r1[0], r0[1], r1[1]}, wsr, yo, 0, 16);
                STAMP(11);
                WAIT_VM(1);
                lds_add(lb + C_WO, lane);
            }
            lds_add(lb + C_CONVN, lane);
            STAMP(12);
        }
    }

    __builtin_amdgcn_s_setprio(T_PRIO);
    {
        RELANE();
        const int kh = wave < 4, kq = kh * 2 + (w & 1), pp = w >> 1;
        const int rte = (rbo >> 1) * 8 + (rbo & 1) * 2 + pp;
        const unsigned ofo = WS_O + (rte * 20 + kq * 5) * 1024 + lane_ * 16;
        const int hsw = (g & 1) * 4;
        const unsigned lofs = lb + (8 * g + hsw + q4) * 160 + p4 * 8 + (w & 1) * 25600;
        const unsigned pc = lofs + (isG ? (kh ? G_R1 : G_R0) : V_WO + kh * 51200);
        const unsigned pd = pc + (4 - 2 * hsw) * 160;
        const unsigned ob4 = WS_O + (rte * 20 + kq * 5 + 4) * 1024;
        const unsigned wo_ = ob4 + (lane_ & 2) * 40960 + (lane_ & 1) * 512 + WATCH_J * 128;
        const unsigned xa = lb + kq * 5120 + lane_ * 16;
        unsigned c0_, c1_, c2_, c3_;
        asm volatile(P_ASM
                     : [cnt] "=&s"(c0_), [tmp] "=&s"(c1_), [wm] "=&s"(c2_), [ah] "=&s"(c3_)
                     : [pc] "v"(pc), [pd] "v"(pd), [ofo] "v"(ofo), [of4] "v"(ob4 + g * 256 + n16 * 8), [wat] "v"(wo_), [xa0] "v"(xa + PART(pp)), [xa1] "v"(xa + PART(2 + pp)),
                       [pf] "v"(lb + C_PF + pp * 16 + kq * 4), [ctl] "v"(lb + L_CTRL), [wsp] "s"(ws), [ws2] "s"(ws + 4 * 20480), [won] "s"(isG ? 4 : 8),
                       [ost] "s"(isG ? G_WST : V_WST0), [cap] "s"(SPIN_CAP), [so] "s"(S_O)
                     : P_CLOB);
        FLUSH();
    }
}

#undef x
#undef kin
#undef vin
#undef shift_k
#undef ssm_k
#undef ssm_D
#undef wqkv
#undef wo
#undef gw
#undef ws
#undef out
#undef wsr
extern "C" void kernel_launch(void* const* d_in, const int* in_sizes, int n_in, void* d_out, int out_size,
                              void* d_ws, size_t ws_size, hipStream_t stream) {
    const u16* q = (const u16*)d_in[0];
    const u16* k = (const u16*)d_in[1];
    const u16* v = (const u16*)d_in[2];
    const u16* shift_k = (const u16*)d_in[3];
    const u16* ssm_k = (const u16*)d_in[4];
    const u16* ssm_D = (const u16*)d_in[5];
    const u16* wqkv = (const u16*)d_in[7];
    const u16* wo = (const u16*)d_in[8];
    const u16* gw = (const u16*)d_in[9];
    fused_kernel<<<256, 768, 0, stream>>>(q, k, v, shift_k, ssm_k, ssm_D, wqkv, wo, gw, (char*)d_ws, (u16*)d_out);
}
```

```cpp
#define V_DELAY 6
#define MAP1 1
#define HINT_AT 9
#define G_OWN 0
#define OUT_AUX 2
#define L_DELAY 0
#define O_ROT 0
#define CONV_PRIO 3
#define GY_DELAY 18
#define VY_DELAY 28
#define Y_SLEEP 3
#define POLL_DELAY 16
#define CODE_LINES 404
#include <hip/hip_runtime.h>
#include <hip/hip_bf16.h>
#include <stdint.h>

#define S_LEN 2048
#define D_DIM 640
#define NQKV 1920

typedef unsigned short u16;
typedef unsigned long long u64;
typedef __attribute__((ext_vector_type(8))) short bf16x8;
typedef __attribute__((ext_vector_type(4))) short s16x4;
typedef __attribute__((ext_vector_type(4))) float f32x4;
typedef __attribute__((ext_vector_type(2))) float f32x2;
typedef __attribute__((ext_vector_type(4))) unsigned u32x4;
typedef __attribute__((ext_vector_type(2))) unsigned u32x2;
typedef __attribute__((ext_vector_type(2))) __bf16 bf16x2_t;
typedef __attribute__((address_space(3))) s16x4 lds_s16x4;
typedef __attribute__((address_space(3))) const s16x4 lds_cs16x4;
typedef __attribute__((address_space(3))) const bf16x8 lds_cbf16x8;
typedef __attribute__((address_space(3))) char lds_char;

#define __float_as_uint(f) __builtin_bit_cast(unsigned, (float)(f))
#define __uint_as_float(u) __builtin_bit_cast(float, (unsigned)(u))
#define __any(c) (__builtin_amdgcn_ballot_w64(c) != 0)
#define __all(c) (__builtin_amdgcn_ballot_w64(!(c)) == 0)
extern "C" __device__ unsigned long long llvm_dispatch_id() __asm("llvm.amdgcn.dispatch.id");

__device__ __forceinline__ float bf2f(u16 x) { return __uint_as_float(((unsigned)x) << 16); }
__device__ __forceinline__ u16 f2bf(float f) {
    __hip_bfloat16 b = __float2bfloat16(f);
    return *reinterpret_cast<u16*>(&b);
}
__device__ __forceinline__ float rbf(float f) { return bf2f(f2bf(f)); }
__device__ __forceinline__ float lo_f(unsigned u) { return __uint_as_float(u << 16); }
__device__ __forceinline__ float hi_f(unsigned u) { return __uint_as_float(u & 0xffff0000u); }
__device__ __forceinline__ unsigned pack2(float a, float b) {
    return __builtin_bit_cast(unsigned, __builtin_convertvector((f32x2){a, b}, bf16x2_t));
}
__device__ __forceinline__ float dot2(unsigned a, unsigned b, float c) {
    return __builtin_amdgcn_fdot2_f32_bf16(__builtin_bit_cast(bf16x2_t, a), __builtin_bit_cast(bf16x2_t, b), c, false);
}

#define SB() __builtin_amdgcn_sched_barrier(0)
#define WAIT_VM(n) asm volatile("s_waitcnt vmcnt(" #n ")" ::: "memory")
#define RAW_BARRIER() do { asm volatile("s_waitcnt lgkmcnt(0)" ::: "memory"); __builtin_amdgcn_s_barrier(); } while (0)

#define KV_PITCH 4224
#define WS_KV 4096
#define WS_O (WS_KV + D_DIM * KV_PITCH)
#define WS_Y (WS_O + D_DIM * S_LEN * 2)
#define WS_G (WS_Y + D_DIM * S_LEN * 2)
#define WS_XF (WS_G + S_LEN * 4)
#define WS_SF (WS_XF + 128 * 128)
#define WS_HF (WS_SF + 128 * 128)
#define WS_H (WS_HF + 128 * 128)
#define WS_END (WS_H + 256 * 128)
#define WS_DBG (WS_END + 4096)

#define A_SLOT 18432
#define W_SLOT 16384
#define W_PITCH 512
#define NW 6
#define A_OFF(s) ((s) == 0 ? 53248 : ((s) - 1) * A_SLOT)
#define W_OFF(s) ((s) == 0 ? 36864 : 71680 + ((s) - 1) * W_SLOT)
#define G_R1 0
#define G_R0 88064
#define G_XCH 53248
#define G_Y (G_XCH + 20480)
#define G_Q (G_R0 + 51200)
#define G_HX (G_Q + 10240)
#define G_VX 153600
#define G_TAB (G_VX + 4608)
#define G_HXO (G_TAB + 3072)
#define G_ZR (G_HXO + 1024)
#define G_VXD (G_ZR + 64)
#define KV_BYTES 4352
#define V_F (4 * KV_BYTES)
#define XC_ROW 288
#define XC_B 12
#define XC_COPY 4944
#define XC_BYTES (XC_COPY + 17 * XC_ROW)
#define V_KV4 (V_F + 5 * XC_BYTES)
#define V_KV(c) ((c) < 4 ? (c) * KV_BYTES : V_KV4)
#define V_WO (V_KV4 + KV_BYTES - 10240)
#define V_KST (V_WO + 10240 + 1312)
#define V_VST (V_KST + 20608)
#define V_STG 113536
#define V_QS (V_WO + 10240)
#define V_SCR 130560
#define V_XCH 0
#define V_Q 20480
#define V_Y (V_Q + 10240)
#define Y_PITCH 160
#define L_TOUCH 163328
#define L_CTRL 163584
#define LDS_TOTAL 163840
static_assert(V_VST + 20608 <= V_STG && V_STG % 128 == 0 && V_STG + 20480 <= L_TOUCH && V_WO >= V_F + 4 * XC_BYTES && V_WO + 10 * 10240 <= L_TOUCH &&
              V_WO + 6 * 10240 <= V_SCR && V_SCR + 32768 <= L_TOUCH && V_Y + 80 * Y_PITCH <= V_WO && V_WO % 16 == 0, "lds");
static_assert(W_OFF(5) + W_SLOT == G_VX && G_HX + 4096 == G_VX && G_ZR + 656 <= L_TOUCH && G_Y + 80 * Y_PITCH <= G_R0 && G_TAB % 16 == 0, "lds");
#define C_FREE (L_CTRL + 16)
#define C_OST (L_CTRL + 48)
#define C_SENT (L_CTRL + 52)
#define C_WO (L_CTRL + 56)
#define C_KV (L_CTRL + 60)
#define C_KVST (L_CTRL + 64)
#define C_CONVN (L_CTRL + 68)
#define C_Y (L_CTRL + 72)
#define C_KVR (L_CTRL + 76)
#define C_XD (L_CTRL + 80)
#define C_RED (L_CTRL + 84)
#define C_ORDY (L_CTRL + 88)
#define C_QR (L_CTRL + 92)
#define C_XCH (L_CTRL + 96)
#define C_PRE (L_CTRL + 112)
#define C_SFC (L_CTRL + 136)
#define C_GO (L_CTRL + 140)
#define C_HR (L_CTRL + 144)
#define C_RED2 (L_CTRL + 148)
#define C_KVD (L_CTRL + 152)
#define C_WT (L_CTRL + 156)
#define C_HX (L_CTRL + 160)
#define C_OST1 (L_CTRL + 176)
#define C_HS (L_CTRL + 180)
#define C_WO0 (L_CTRL + 184)
#define C_QR2 (L_CTRL + 188)
#define V_Q2 (V_WO + 81920)
#define C_SENT2 (L_CTRL + 100)
#define C_YS (L_CTRL + 104)
#define C_BP C_XCH
#define C_PF (L_CTRL + 192)
#define PART(slot) (isG ? ((slot) < 2 ? G_R1 + (slot) * 20480 : G_R0 + ((slot) - 2) * 20480) : V_WO + (slot) * 20480)

#define KNOB(n, v)
#define SPIN_CAP 4000000
#define GH 0
#define WO_PRIO 0
#define T_PRIO 3
#define C0_PRIO 2
#define C1_PRIO 1
#define L_PRIO 3
#define O_SLEEP 4
#define O_DELAY 0
#define POLL_SLEEP 2
#define W_SLEEP 4
#define X_SLEEP 2
#define X_DELAY 0
#define NBUF 4
#define WATCH_RT 2
#define JL 17
#define RING_E0 70
#define RING_F 105
#define WOT_SPLIT 0
#define GO_W C_KVD
#define GO_N 11
#define SENT_PACE 8
#define GATE_LATE 0
#define WOT_PACE 3
#define SENT_W C_XD
#define SENT_N 4
#define NSENT 20
#define T0_PRIO T_PRIO
#define G_WST 4
#define V_WST 4
#define V_WST0 0
#define WR_X 2
#define HALO_AT 0
#define H_SLEEP 2
#define HS_WAIT 1
#define H_DELAY 0
#define LAST_PRIO 3
#define WATCH_C 16
#define GO2_W C_XD
#define GO2_N 4
#define H_POL ""
#define KV_POL ""
#define WATCH_J 0
#define SF_EARLY 2
#define HINT_W 3
#define WT_WAVE 11
#define WO_PACE 1
#define SF_WAVE 11
#define YS_LATE 1
#define SF_W C_RED2
#define SF_N 4
#define SO_AT 17
#define OS_W C_KVD
#define OS_N 11
#define SF1_AT 0
#define GW_DELAY 0
#define O_AHEAD 4
#define PASS_PRIO 1
#define G_OST 8
#define V_ODELAY 0
#define W_LATE 1
#define Y_SL0 0
#define SF_DELAY 8
#define WO_MALL 1
#define T4_EARLY 0
#define LATE_AT C_LOOP
#define LATE_N 4
#define KROT ((0x27057250u >> (xcd * 4)) & 15)

#define STAMP(id)
#define FLUSH()
#define CSTAMP(i)

__device__ __forceinline__ unsigned lds_ld32(unsigned a) {
    unsigned v;
    asm volatile("ds_read_b32 %0, %1\n\ts_waitcnt lgkmcnt(0)" : "=v"(v) : "v"(a) : "memory");
    return v;
}
__device__ __forceinline__ unsigned lds_min4(unsigned a) {
    u32x4 v;
    asm volatile("ds_read_b128 %0, %1\n\ts_waitcnt lgkmcnt(0)" : "=v"(v) : "v"(a) : "memory");
    return min(min(v[0], v[1]), min(v[2], v[3]));
}
__device__ __forceinline__ unsigned lds_min8(unsigned a) {
    u32x4 v, u;
    asm volatile("ds_read_b128 %0, %2\n\tds_read_b128 %1, %2 offset:16\n\ts_waitcnt lgkmcnt(0)" : "=&v"(v), "=&v"(u) : "v"(a) : "memory");
    return min(min(min(v[0], v[1]), min(v[2], v[3])), min(min(u[0], u[1]), min(u[2], u[3])));
}
__device__ __forceinline__ void lds_st32(unsigned a, unsigned v) {
    asm volatile("ds_write_b32 %0, %1" ::"v"(a), "v"(v) : "memory");
}
__device__ __forceinline__ unsigned lds_add_rtn(unsigned a, int lane) {
    unsigned old = 0;
    if (lane == 0) {
        unsigned one = 1;
        asm volatile("ds_add_rtn_u32 %0, %1, %2\n\ts_waitcnt lgkmcnt(0)" : "=v"(old) : "v"(a), "v"(one) : "memory");
    }
    return __builtin_amdgcn_readfirstlane(old);
}
__device__ __forceinline__ void lds_add(unsigned a, int lane, unsigned n = 1) {
    if (lane == 0) asm volatile("ds_add_u32 %0, %1" ::"v"(a), "v"(n) : "memory");
}
__device__ __forceinline__ void spin_min4(unsigned a, unsigned target) {
#pragma nounroll
    for (int i = 0; i < SPIN_CAP; ++i) {
        if (__builtin_amdgcn_readfirstlane(lds_min4(a)) >= target) break;
        __builtin_amdgcn_s_sleep(1);
    }
}
__device__ __forceinline__ void spin_min8(unsigned a, unsigned target) {
#pragma nounroll
    for (int i = 0; i < SPIN_CAP; ++i) {
        if (__builtin_amdgcn_readfirstlane(lds_min8(a)) >= target) break;
        __builtin_amdgcn_s_sleep(1);
    }
}
__device__ __forceinline__ void spin_ge(unsigned a, unsigned target) {
#pragma nounroll
    for (int i = 0; i < SPIN_CAP; ++i) {
        if (__builtin_amdgcn_readfirstlane(lds_ld32(a)) >= target) break;
        __builtin_amdgcn_s_sleep(1);
    }
}
typedef __attribute__((address_space(3))) volatile u32x4 lds_vu32x4;
typedef __attribute__((address_space(3))) volatile unsigned lds_vu32_t;
__device__ __forceinline__ u32x4 lds_v128(unsigned a) { return *(lds_vu32x4*)(size_t)a; }
__device__ __forceinline__ unsigned lds_v32(unsigned a) { return *(lds_vu32_t*)(size_t)a; }
__device__ __forceinline__ float fmax_raw(float a, float b) { return __builtin_elementwise_maximum(a, b); }
__device__ __forceinline__ float fmax3_raw(float a, float b, float c) { return __builtin_elementwise_maximum(__builtin_elementwise_maximum(a, b), c); }
__device__ __forceinline__ unsigned opaque(unsigned v) { asm volatile("" : "+v"(v)); return v; }
__device__ __forceinline__ bf16x8 cat8(s16x4 a, s16x4 b) { return __builtin_shufflevector(a, b, 0, 1, 2, 3, 4, 5, 6, 7); }

__device__ __forceinline__ void poll_flags(__amdgpu_buffer_rsrc_t rs, unsigned off, unsigned tlo, unsigned thi) {
#pragma nounroll
    for (int i = 0; i < SPIN_CAP; ++i) {
        asm volatile("" ::: "memory");
        const u32x2 v = __builtin_amdgcn_raw_buffer_load_b64(rs, off, 0, 16);
        if (__all(v[0] == tlo && v[1] == thi)) break;
        __builtin_amdgcn_s_sleep(POLL_SLEEP);
    }
    __builtin_amdgcn_fence(__ATOMIC_ACQUIRE, "wavefront");
}

__device__ __forceinline__ void spin_slow(unsigned a, unsigned target) {
#pragma nounroll
    for (int i = 0; i < SPIN_CAP; ++i) {
        if (__builtin_amdgcn_readfirstlane(lds_ld32(a)) >= target) break;
        __builtin_amdgcn_s_sleep(6);
    }
}
__device__ __forceinline__ void spin_mid(unsigned a, unsigned target) {
#pragma nounroll
    for (int i = 0; i < SPIN_CAP; ++i) {
        if (__builtin_amdgcn_readfirstlane(lds_ld32(a)) >= target) break;
        __builtin_amdgcn_s_sleep(3);
    }
}
__device__ __forceinline__ void spin_all(unsigned a, unsigned target) {
#pragma nounroll
    for (int i = 0; i < SPIN_CAP; ++i) {
        if (__all(lds_ld32(a) >= target)) break;
        __builtin_amdgcn_s_sleep(1);
    }
}

template <int N> __device__ __forceinline__ void waitvm() { asm volatile("s_waitcnt vmcnt(%0)" ::"n"(N) : "memory"); }
#define BARRIER() asm volatile("s_barrier" ::: "memory")
#define GLS(voff, sbase, lw, ldsoff) asm volatile("s_add_u32 m0, %2, %3\n\ts_nop 0\n\tglobal_load_lds_dwordx4 %0, %1" ::"v"(voff), "s"(sbase), "s"(lw), "n"(ldsoff) : "memory", "scc")
#define GLSP(voff, sbase, lw, ldsoff) asm volatile("s_add_u32 m0, %2, %3\n\ts_nop 0\n\tglobal_load_lds_dwordx4 %0, %1" KV_POL ::"v"(voff), "s"(sbase), "s"(lw), "n"(ldsoff) : "memory", "scc")
#define GLT(voff, sbase) asm volatile("s_mov_b32 m0, %2\n\ts_nop 0\n\tglobal_load_lds_dword %0, %1" ::"v"(voff), "s"(sbase), "s"(lb + L_TOUCH) : "memory")
#define TRRD(p) __builtin_amdgcn_ds_read_tr16_b64_v4i16((lds_s16x4*)(p))

constexpr int ring_total(int NA) { return 10 * (8 + NA); }
constexpr int ring_need(int J, int NA) { return J < 0 ? NA + 8 : J >= JL ? 10 * (8 + NA) : (J & 1) ? (NA + 8) * ((J + 3) / 2) : (NA + 8) * ((J + 2) / 2) + 4 + NA; }
constexpr int ring_isA(int p, int NA) { return p < NA ? 1 : p < NA + 8 ? 0 : (p % (8 + NA) >= 4 && p % (8 + NA) < 4 + NA); }
constexpr int ring_tile(int p, int NA) { return p < NA ? 0 : p < NA + 8 ? (p - NA) / 4 : ring_isA(p, NA) ? p / (8 + NA) : 2 * (p / (8 + NA)) + (p % (8 + NA) >= 4); }
constexpr int ring_sub(int p, int NA) { return p < NA ? p : p < NA + 8 ? (p - NA) % 4 : p % (8 + NA) < 4 ? p % (8 + NA) : p % (8 + NA) < 4 + NA ? p % (8 + NA) - 4 : p % (8 + NA) - 4 - NA; }
constexpr int ring_cum(int J, int NA) {
    int c = J >= (JL < 16 ? JL : 16) ? ring_total(NA) : NA + 8 + (8 + NA) * (2 * RING_E0 + RING_F * (J + 1)) / 200;
    if (c > ring_total(NA)) c = ring_total(NA);
    if (c < ring_need(J, NA)) c = ring_need(J, NA);
    while (ring_isA(c - 1, NA) ? 2 * (ring_tile(c - 1, NA) - 3) + 1 > J - 1 : ring_tile(c - 1, NA) - NW > J - 1) --c;
    return c;
}
static_assert(ring_cum(16, 5) == ring_total(5) && ring_cum(16, 4) == ring_total(4), "ring");
template <int NA, int P0, int P1>
__device__ __forceinline__ void ring_issue(const unsigned (&ev)[9], const void* xp, const void* wp, int kr, unsigned lw) {
    if constexpr (P0 < P1) {
        constexpr int t = ring_tile(P0, NA), sub = ring_sub(P0, NA);
        if constexpr (ring_isA(P0, NA)) {
            const int c = __builtin_amdgcn_readfirstlane(kr + t >= 10 ? kr + t - 10 : kr + t);
            GLS(ev[4 + sub], (const char*)xp + c * 128, lw, A_OFF(t % 3) + sub * 4096);
        } else {
            const int c = __builtin_amdgcn_readfirstlane(2 * kr + t >= 20 ? 2 * kr + t - 20 : 2 * kr + t);
            GLS(ev[sub], (const char*)wp + (size_t)c * (32 * NQKV * 2), lw, W_OFF(t % NW) + sub * 4096);
        }
        ring_issue<NA, P0 + 1, P1>(ev, xp, wp, kr, lw);
    }
}
#define S_O 0xff81ff81u
template <int NA, int J>
__device__ __forceinline__ void ring_run(const unsigned (&ev)[9], const void* xp, const void* wp, int kr, unsigned lw) {
    ring_issue<NA, ring_cum(J - 1, NA), ring_cum(J, NA)>(ev, xp, wp, kr, lw);
    if constexpr (J < 16) {
        waitvm<ring_cum(J, NA) - ring_need(J, NA)>();
        BARRIER();
        ring_run<NA, J + 1>(ev, xp, wp, kr, lw);
    }
}

#define CONV_ASM \
    "s_mov_b32 m0, %[kvl]\n" \
    "v_mov_b32 v50, 1\n" \
    "global_load_lds_dwordx4 %[xo], %[wsp] offset:0 sc1\n" \
    "global_load_lds_dwordx4 %[xo], %[wsp] offset:1024 sc1\n" \
    "global_load_lds_dwordx4 %[xo], %[wsp] offset:2048 sc1\n" \
    "global_load_lds_dwordx4 %[xo], %[wsp] offset:3072 sc1\n" \
    "s_mov_b64 exec, 1\n" \
    "ds_add_u32 %[ctl], v50 offset:80\n" \
    "s_mov_b64 exec, -1\n" \
    "v_add_u32 v84, %[sx4], %[xp]\n" \
    "v_add_u32 v51, %[sh4], %[hp]\n" \
    "v_add_u32 v160, 0, %[xp]\n" \
    "v_add_u32 v161, 1152, %[xp]\n" \
    "v_add_u32 v85, 1152, v84\n" \
    "v_add_u32 v162, 2304, %[xp]\n" \
    "v_add_u32 v86, 2304, v84\n" \
    "v_add_u32 v163, 3456, %[xp]\n" \
    "v_add_u32 v87, 3456, v84\n" \
    "ds_read2_b32 v[88:89], v87 offset0:144 offset1:145\n" \
    "ds_read2_b32 v[90:91], v87 offset0:146 offset1:147\n" \
    "ds_read2_b32 v[92:93], v87 offset0:0 offset1:1\n" \
    "ds_read2_b32 v[94:95], v87 offset0:2 offset1:3\n" \
    "ds_read2_b32 v[96:97], v86 offset0:144 offset1:145\n" \
    "ds_read2_b32 v[98:99], v86 offset0:146 offset1:147\n" \
    "ds_read2_b32 v[100:101], v86 offset0:0 offset1:1\n" \
    "ds_read2_b32 v[102:103], v86 offset0:2 offset1:3\n" \
    "ds_read2_b32 v[104:105], v85 offset0:144 offset1:145\n" \
    "ds_read2_b32 v[106:107], v85 offset0:146 offset1:147\n" \
    "ds_read2_b32 v[108:109], v85 offset0:0 offset1:1\n" \
    "ds_read2_b32 v[110:111], v85 offset0:2 offset1:3\n" \
    "ds_read2_b32 v[112:113], v84 offset0:144 offset1:145\n" \
    "ds_read2_b32 v[114:115], v84 offset0:146 offset1:147\n" \
    "ds_read2_b32 v[116:117], v84 offset0:0 offset1:1\n" \
    "ds_read2_b32 v[118:119], v84 offset0:2 offset1:3\n" \
    "s_mov_b32 %[cnt], 0\n" \
    "Lsp_%=:\n" \
    "ds_read_b32 v50, %[ctl] offset:144\n" \
    "s_add_u32 %[cnt], %[cnt], 1\n" \
    "s_waitcnt lgkmcnt(0)\n" \
    "v_readfirstlane_b32 %[tmp], v50\n" \
    "s_cmp_ge_u32 %[cnt], %[cap]\n" \
    "s_cbranch_scc1 Lgo_%=\n" \
    "s_cmp_lt_u32 %[tmp], 4\n" \
    "s_cbranch_scc1 Lsp_%=\n" \
    "Lgo_%=:\n" \
    "ds_read_b128 v[148:151], v51 offset:3584\n" \
    "ds_read_b128 v[144:147], v51 offset:3072\n" \
    "ds_read_b128 v[140:143], v51 offset:2560\n" \
    "ds_read_b128 v[136:139], v51 offset:2048\n" \
    "ds_read_b128 v[132:135], v51 offset:1536\n" \
    "ds_read_b128 v[128:131], v51 offset:1024\n" \
    "ds_read_b128 v[124:127], v51 offset:512\n" \
    "ds_read_b128 v[120:123], v51 offset:0\n" \
    "s_waitcnt lgkmcnt(7)\n" \
    "v_mfma_f32_16x16x32_bf16 v[80:83], v[88:91], v[148:151], 0\n" \
    "ds_read_b128 v[148:151], v51 offset:3648\n" \
    "s_waitcnt lgkmcnt(7)\n" \
    "v_mfma_f32_16x16x32_bf16 v[76:79], v[88:91], v[144:147], 0\n" \
    "ds_read_b128 v[156:159], v51 offset:576\n" \
    "s_waitcnt lgkmcnt(7)\n" \
    "v_mfma_f32_16x16x32_bf16 v[72:75], v[88:91], v[140:143], 0\n" \
    "ds_read_b128 v[152:155], v51 offset:64\n" \
    "s_waitcnt lgkmcnt(7)\n" \
    "v_mfma_f32_16x16x32_bf16 v[68:71], v[88:91], v[136:139], 0\n" \
    "s_waitcnt lgkmcnt(6)\n" \
    "v_mfma_f32_16x16x32_bf16 v[64:67], v[88:91], v[132:135], 0\n" \
    "s_waitcnt lgkmcnt(5)\n" \
    "v_mfma_f32_16x16x32_bf16 v[60:63], v[88:91], v[128:131], 0\n" \
    "s_waitcnt lgkmcnt(4)\n" \
    "v_mfma_f32_16x16x32_bf16 v[56:59], v[88:91], v[124:127], 0\n" \
    "s_waitcnt lgkmcnt(3)\n" \
    "v_mfma_f32_16x16x32_bf16 v[52:55], v[88:91], v[120:123], 0\n" \
    "ds_read2_b32 v[88:89], v87 offset0:160 offset1:161\n" \
    "v_mfma_f32_16x16x32_bf16 v[80:83], v[92:95], v[144:147], v[80:83]\n" \
    "ds_read2_b32 v[90:91], v87 offset0:162 offset1:163\n" \
    "v_mfma_f32_16x16x32_bf16 v[76:79], v[92:95], v[140:143], v[76:79]\n" \
    "ds_read_b128 v[144:147], v51 offset:3136\n" \
    "v_mfma_f32_16x16x32_bf16 v[72:75], v[92:95], v[136:139], v[72:75]\n" \
    "v_mfma_f32_16x16x32_bf16 v[68:71], v[92:95], v[132:135], v[68:71]\n" \
    "v_mfma_f32_16x16x32_bf16 v[64:67], v[92:95], v[128:131], v[64:67]\n" \
    "v_mfma_f32_16x16x32_bf16 v[60:63], v[92:95], v[124:127], v[60:63]\n" \
    "v_mfma_f32_16x16x32_bf16 v[56:59], v[92:95], v[120:123], v[56:59]\n" \
    "ds_read2_b32 v[92:93], v87 offset0:16 offset1:17\n" \
    "v_mfma_f32_16x16x32_bf16 v[80:83], v[96:99], v[140:143], v[80:83]\n" \
    "ds_read_b128 v[140:143], v51 offset:2624\n" \
    "v_mfma_f32_16x16x32_bf16 v[76:79], v[96:99], v[136:139], v[76:79]\n" \
    "ds_read2_b32 v[94:95], v87 offset0:18 offset1:19\n" \
    "v_mfma_f32_16x16x32_bf16 v[72:75], v[96:99], v[132:135], v[72:75]\n" \
    "v_mfma_f32_16x16x32_bf16 v[68:71], v[96:99], v[128:131], v[68:71]\n" \
    "v_mfma_f32_16x16x32_bf16 v[64:67], v[96:99], v[124:127], v[64:67]\n" \
    "v_mfma_f32_16x16x32_bf16 v[60:63], v[96:99], v[120:123], v[60:63]\n" \
    "ds_read2_b32 v[96:97], v86 offset0:160 offset1:161\n" \
    "v_mfma_f32_16x16x32_bf16 v[80:83], v[100:103], v[136:139], v[80:83]\n" \
    "ds_read_b128 v[136:139], v51 offset:2112\n" \
    "v_mfma_f32_16x16x32_bf16 v[76:79], v[100:103], v[132:135], v[76:79]\n" \
    "ds_read2_b32 v[98:99], v86 offset0:162 offset1:163\n" \
    "v_mfma_f32_16x16x32_bf16 v[72:75], v[100:103], v[128:131], v[72:75]\n" \
    "v_mfma_f32_16x16x32_bf16 v[68:71], v[100:103], v[124:127], v[68:71]\n" \
    "v_mfma_f32_16x16x32_bf16 v[64:67], v[100:103], v[120:123], v[64:67]\n" \
    "ds_read2_b32 v[100:101], v86 offset0:16 offset1:17\n" \
    "v_mfma_f32_16x16x32_bf16 v[80:83], v[104:107], v[132:135], v[80:83]\n" \
    "ds_read_b128 v[132:135], v51 offset:1600\n" \
    "v_mfma_f32_16x16x32_bf16 v[76:79], v[104:107], v[128:131], v[76:79]\n" \
    "ds_read2_b32 v[102:103], v86 offset0:18 offset1:19\n" \
    "v_mfma_f32_16x16x32_bf16 v[72:75], v[104:107], v[124:127], v[72:75]\n" \
    "v_mfma_f32_16x16x32_bf16 v[68:71], v[104:107], v[120:123], v[68:71]\n" \
    "ds_read2_b32 v[104:105], v85 offset0:160 offset1:161\n" \
    "v_mfma_f32_16x16x32_bf16 v[80:83], v[108:111], v[128:131], v[80:83]\n" \
    "ds_read_b128 v[128:131], v51 offset:1088\n" \
    "v_mfma_f32_16x16x32_bf16 v[76:79], v[108:111], v[124:127], v[76:79]\n" \
    "ds_read2_b32 v[106:107], v85 offset0:162 offset1:163\n" \
    "v_mfma_f32_16x16x32_bf16 v[72:75], v[108:111], v[120:123], v[72:75]\n" \
    "ds_read2_b32 v[108:109], v85 offset0:16 offset1:17\n" \
    "v_mfma_f32_16x16x32_bf16 v[80:83], v[112:115], v[124:127], v[80:83]\n" \
    "ds_read2_b32 v[110:111], v85 offset0:18 offset1:19\n" \
    "v_mfma_f32_16x16x32_bf16 v[76:79], v[112:115], v[120:123], v[76:79]\n" \
    "ds_read2_b32 v[112:113], v84 offset0:160 offset1:161\n" \
    "v_mfma_f32_16x16x32_bf16 v[80:83], v[116:119], v[120:123], v[80:83]\n" \
    "ds_read2_b32 v[114:115], v84 offset0:162 offset1:163\n" \
    "s_waitcnt lgkmcnt(15)\n" \
    "v_mfma_f32_16x16x32_bf16 v[80:83], v[88:91], v[148:151], v[80:83]\n" \
    "ds_read2_b32 v[116:117], v84 offset0:16 offset1:17\n" \
    "v_mfma_f32_16x16x32_bf16 v[76:79], v[88:91], v[144:147], v[76:79]\n" \
    "ds_read2_b32 v[118:119], v84 offset0:18 offset1:19\n" \
    "s_waitcnt lgkmcnt(15)\n" \
    "v_mfma_f32_16x16x32_bf16 v[72:75], v[88:91], v[140:143], v[72:75]\n" \
    "s_waitcnt vmcnt(0)\n" \
    "ds_read_b128 v[148:151], %[hp] offset:3584\n" \
    "s_waitcnt lgkmcnt(14)\n" \
    "v_mfma_f32_16x16x32_bf16 v[68:71], v[88:91], v[136:139], v[68:71]\n" \
    "ds_read_b128 v[124:127], %[hp] offset:512\n" \
    "s_waitcnt lgkmcnt(12)\n" \
    "v_mfma_f32_16x16x32_bf16 v[64:67], v[88:91], v[132:135], v[64:67]\n" \
    "ds_read_b128 v[120:123], %[hp] offset:0\n" \
    "s_waitcnt lgkmcnt(10)\n" \
    "v_mfma_f32_16x16x32_bf16 v[60:63], v[88:91], v[128:131], v[60:63]\n" \
    "v_mfma_f32_16x16x32_bf16 v[56:59], v[88:91], v[156:159], v[56:59]\n" \
    "v_mfma_f32_16x16x32_bf16 v[52:55], v[88:91], v[152:155], v[52:55]\n" \
    "ds_read2_b32 v[88:89], v163 offset0:144 offset1:145\n" \
    "v_mfma_f32_16x16x32_bf16 v[80:83], v[92:95], v[144:147], v[80:83]\n" \
    "ds_read2_b32 v[90:91], v163 offset0:146 offset1:147\n" \
    "v_mfma_f32_16x16x32_bf16 v[76:79], v[92:95], v[140:143], v[76:79]\n" \
    "ds_read_b128 v[144:147], %[hp] offset:3072\n" \
    "v_mfma_f32_16x16x32_bf16 v[72:75], v[92:95], v[136:139], v[72:75]\n" \
    "v_mfma_f32_16x16x32_bf16 v[68:71], v[92:95], v[132:135], v[68:71]\n" \
    "ds_write_b128 %[scr], v[52:55] offset:0\n" \
    "v_mfma_f32_16x16x32_bf16 v[64:67], v[92:95], v[128:131], v[64:67]\n" \
    "ds_read_b64 v[86:87], %[kvr] offset:0\n" \
    "v_mfma_f32_16x16x32_bf16 v[60:63], v[92:95], v[156:159], v[60:63]\n" \
    "v_mfma_f32_16x16x32_bf16 v[56:59], v[92:95], v[152:155], v[56:59]\n" \
    "ds_read2_b32 v[92:93], v163 offset0:0 offset1:1\n" \
    "v_mfma_f32_16x16x32_bf16 v[80:83], v[96:99], v[140:143], v[80:83]\n" \
    "ds_read_b128 v[140:143], %[hp] offset:2560\n" \
    "v_mfma_f32_16x16x32_bf16 v[76:79], v[96:99], v[136:139], v[76:79]\n" \
    "ds_read2_b32 v[94:95], v163 offset0:2 offset1:3\n" \
    "v_mfma_f32_16x16x32_bf16 v[72:75], v[96:99], v[132:135], v[72:75]\n" \
    "v_mfma_f32_16x16x32_bf16 v[68:71], v[96:99], v[128:131], v[68:71]\n" \
    "ds_write_b128 %[scr], v[56:59] offset:1024\n" \
    "v_mfma_f32_16x16x32_bf16 v[64:67], v[96:99], v[156:159], v[64:67]\n" \
    "v_mfma_f32_16x16x32_bf16 v[60:63], v[96:99], v[152:155], v[60:63]\n" \
    "ds_read2_b32 v[96:97], v162 offset0:144 offset1:145\n" \
    "v_mfma_f32_16x16x32_bf16 v[80:83], v[100:103], v[136:139], v[80:83]\n" \
    "ds_read_b128 v[136:139], %[hp] offset:2048\n" \
    "v_mfma_f32_16x16x32_bf16 v[76:79], v[100:103], v[132:135], v[76:79]\n" \
    "ds_read2_b32 v[98:99], v162 offset0:146 offset1:147\n" \
    "v_mfma_f32_16x16x32_bf16 v[72:75], v[100:103], v[128:131], v[72:75]\n" \
    "v_mfma_f32_16x16x32_bf16 v[68:71], v[100:103], v[156:159], v[68:71]\n" \
    "s_waitcnt lgkmcnt(7)\n" \
    "v_lshlrev_b32 v84, 16, v86\n" \
    "ds_write_b128 %[scr], v[60:63] offset:2048\n" \
    "v_mfma_f32_16x16x32_bf16 v[64:67], v[100:103], v[152:155], v[64:67]\n" \
    "ds_read2_b32 v[100:101], v162 offset0:0 offset1:1\n" \
    "v_mfma_f32_16x16x32_bf16 v[80:83], v[104:107], v[132:135], v[80:83]\n" \
    "ds_read_b128 v[132:135], %[hp] offset:1536\n" \
    "v_and_b32 v85, 0xffff0000, v86\n" \
    "v_mfma_f32_16x16x32_bf16 v[76:79], v[104:107], v[128:131], v[76:79]\n" \
    "ds_read2_b32 v[102:103], v162 offset0:2 offset1:3\n" \
    "v_mfma_f32_16x16x32_bf16 v[72:75], v[104:107], v[156:159], v[72:75]\n" \
    "v_lshlrev_b32 v86, 16, v87\n" \
    "v_mfma_f32_16x16x32_bf16 v[68:71], v[104:107], v[152:155], v[68:71]\n" \
    "ds_read2_b32 v[104:105], v161 offset0:144 offset1:145\n" \
    "ds_write_b128 %[scr], v[64:67] offset:3072\n" \
    "v_mfma_f32_16x16x32_bf16 v[80:83], v[108:111], v[128:131], v[80:83]\n" \
    "ds_read_b128 v[128:131], %[hp] offset:1024\n" \
    "v_and_b32 v87, 0xffff0000, v87\n" \
    "v_mfma_f32_16x16x32_bf16 v[76:79], v[108:111], v[156:159], v[76:79]\n" \
    "ds_read2_b32 v[106:107], v161 offset0:146 offset1:147\n" \
    "v_mfma_f32_16x16x32_bf16 v[72:75], v[108:111], v[152:155], v[72:75]\n" \
    "ds_read2_b32 v[108:109], v161 offset0:0 offset1:1\n" \
    "v_mfma_f32_16x16x32_bf16 v[80:83], v[112:115], v[156:159], v[80:83]\n" \
    "ds_read2_b32 v[110:111], v161 offset0:2 offset1:3\n" \
    "ds_write_b128 %[scr], v[68:71] offset:4096\n" \
    "v_mfma_f32_16x16x32_bf16 v[76:79], v[112:115], v[152:155], v[76:79]\n" \
    "ds_read2_b32 v[112:113], v160 offset0:144 offset1:145\n" \
    "v_mfma_f32_16x16x32_bf16 v[80:83], v[116:119], v[152:155], v[80:83]\n" \
    "ds_read2_b32 v[114:115], v160 offset0:146 offset1:147\n" \
    "v_mfma_f32_16x16x32_bf16 v[44:47], v[88:91], v[148:151], 0\n" \
    "ds_read2_b32 v[116:117], v160 offset0:0 offset1:1\n" \
    "ds_write_b128 %[scr], v[72:75] offset:5120\n" \
    "v_mfma_f32_16x16x32_bf16 v[40:43], v[88:91], v[144:147], 0\n" \
    "ds_read2_b32 v[118:119], v160 offset0:2 offset1:3\n" \
    "s_waitcnt lgkmcnt(15)\n" \
    "v_mfma_f32_16x16x32_bf16 v[36:39], v[88:91], v[140:143], 0\n" \
    "ds_read_b128 v[148:151], %[hp] offset:3648\n" \
    "ds_write_b128 %[scr], v[76:79] offset:6144\n" \
    "v_mfma_f32_16x16x32_bf16 v[32:35], v[88:91], v[136:139], 0\n" \
    "ds_read_b128 v[156:159], %[hp] offset:576\n" \
    "s_waitcnt lgkmcnt(15)\n" \
    "v_mfma_f32_16x16x32_bf16 v[28:31], v[88:91], v[132:135], 0\n" \
    "ds_read_b128 v[152:155], %[hp] offset:64\n" \
    "ds_write_b128 %[scr], v[80:83] offset:7168\n" \
    "v_mov_b32 v50, 1\n" \
    "s_mov_b64 exec, 1\n" \
    "ds_add_u32 %[ctl], v50 offset:84\n" \
    "s_mov_b64 exec, -1\n" \
    "s_setprio 0\n" \
    "s_waitcnt lgkmcnt(15)\n" \
    "v_mfma_f32_16x16x32_bf16 v[24:27], v[88:91], v[128:131], 0\n" \
    "v_mfma_f32_16x16x32_bf16 v[20:23], v[88:91], v[124:127], 0\n" \
    "v_mfma_f32_16x16x32_bf16 v[16:19], v[88:91], v[120:123], 0\n" \
    "ds_read2_b32 v[88:89], v163 offset0:160 offset1:161\n" \
    "v_mfma_f32_16x16x32_bf16 v[44:47], v[92:95], v[144:147], v[44:47]\n" \
    "ds_read2_b32 v[90:91], v163 offset0:162 offset1:163\n" \
    "v_mfma_f32_16x16x32_bf16 v[40:43], v[92:95], v[140:143], v[40:43]\n" \
    "ds_read_b128 v[144:147], %[hp] offset:3136\n" \
    "v_mfma_f32_16x16x32_bf16 v[36:39], v[92:95], v[136:139], v[36:39]\n" \
    "v_fmac_f32 v16, %[dd], v84\n" \
    "v_fmac_f32 v17, %[dd], v85\n" \
    "v_mfma_f32_16x16x32_bf16 v[32:35], v[92:95], v[132:135], v[32:35]\n" \
    "v_fmac_f32 v18, %[dd], v86\n" \
    "v_fmac_f32 v19, %[dd], v87\n" \
    "v_mfma_f32_16x16x32_bf16 v[28:31], v[92:95], v[128:131], v[28:31]\n" \
    "ds_read_b64 v[86:87], %[kvr] offset:512\n" \
    "v_mfma_f32_16x16x32_bf16 v[24:27], v[92:95], v[124:127], v[24:27]\n" \
    "v_mfma_f32_16x16x32_bf16 v[20:23], v[92:95], v[120:123], v[20:23]\n" \
    "ds_read2_b32 v[92:93], v163 offset0:16 offset1:17\n" \
    "v_mfma_f32_16x16x32_bf16 v[44:47], v[96:99], v[140:143], v[44:47]\n" \
    "ds_read_b128 v[140:143], %[hp] offset:2624\n" \
    "v_mfma_f32_16x16x32_bf16 v[40:43], v[96:99], v[136:139], v[40:43]\n" \
    "ds_read2_b32 v[94:95], v163 offset0:18 offset1:19\n" \
    "v_mfma_f32_16x16x32_bf16 v[36:39], v[96:99], v[132:135], v[36:39]\n" \
    "v_mfma_f32_16x16x32_bf16 v[32:35], v[96:99], v[128:131], v[32:35]\n" \
    "v_mfma_f32_16x16x32_bf16 v[28:31], v[96:99], v[124:127], v[28:31]\n" \
    "v_mfma_f32_16x16x32_bf16 v[24:27], v[96:99], v[120:123], v[24:27]\n" \
    "ds_read2_b32 v[96:97], v162 offset0:160 offset1:161\n" \
    "v_mfma_f32_16x16x32_bf16 v[44:47], v[100:103], v[136:139], v[44:47]\n" \
    "ds_read_b128 v[136:139], %[hp] offset:2112\n" \
    "v_mfma_f32_16x16x32_bf16 v[40:43], v[100:103], v[132:135], v[40:43]\n" \
    "ds_read2_b32 v[98:99], v162 offset0:162 offset1:163\n" \
    "v_mfma_f32_16x16x32_bf16 v[36:39], v[100:103], v[128:131], v[36:39]\n" \
    "v_mfma_f32_16x16x32_bf16 v[32:35], v[100:103], v[124:127], v[32:35]\n" \
    "s_waitcnt lgkmcnt(6)\n" \
    "v_lshlrev_b32 v84, 16, v86\n" \
    "s_setprio 3\n" \
    "v_mfma_f32_16x16x32_bf16 v[28:31], v[100:103], v[120:123], v[28:31]\n" \
    "ds_read2_b32 v[100:101], v162 offset0:16 offset1:17\n" \
    "v_mfma_f32_16x16x32_bf16 v[44:47], v[104:107], v[132:135], v[44:47]\n" \
    "ds_read_b128 v[132:135], %[hp] offset:1600\n" \
    "v_and_b32 v85, 0xffff0000, v86\n" \
    "v_mfma_f32_16x16x32_bf16 v[40:43], v[104:107], v[128:131], v[40:43]\n" \
    "ds_read2_b32 v[102:103], v162 offset0:18 offset1:19\n" \
    "v_mfma_f32_16x16x32_bf16 v[36:39], v[104:107], v[124:127], v[36:39]\n" \
    "v_lshlrev_b32 v86, 16, v87\n" \
    "v_mfma_f32_16x16x32_bf16 v[32:35], v[104:107], v[120:123], v[32:35]\n" \
    "ds_read2_b32 v[104:105], v161 offset0:160 offset1:161\n" \
    "v_mfma_f32_16x16x32_bf16 v[44:47], v[108:111], v[128:131], v[44:47]\n" \
    "ds_read_b128 v[128:131], %[hp] offset:1088\n" \
    "v_and_b32 v87, 0xffff0000, v87\n" \
    "v_mfma_f32_16x16x32_bf16 v[40:43], v[108:111], v[124:127], v[40:43]\n" \
    "ds_read2_b32 v[106:107], v161 offset0:162 offset1:163\n" \
    "v_mfma_f32_16x16x32_bf16 v[36:39], v[108:111], v[120:123], v[36:39]\n" \
    "ds_read2_b32 v[108:109], v161 offset0:16 offset1:17\n" \
    "v_mfma_f32_16x16x32_bf16 v[44:47], v[112:115], v[124:127], v[44:47]\n" \
    "ds_read2_b32 v[110:111], v161 offset0:18 offset1:19\n" \
    "v_mfma_f32_16x16x32_bf16 v[40:43], v[112:115], v[120:123], v[40:43]\n" \
    "ds_read2_b32 v[112:113], v160 offset0:160 offset1:161\n" \
    "v_mfma_f32_16x16x32_bf16 v[44:47], v[116:119], v[120:123], v[44:47]\n" \
    "ds_read2_b32 v[114:115], v160 offset0:162 offset1:163\n" \
    "v_mfma_f32_16x16x32_bf16 v[44:47], v[88:91], v[148:151], v[44:47]\n" \
    "ds_read2_b32 v[116:117], v160 offset0:16 offset1:17\n" \
    "v_mfma_f32_16x16x32_bf16 v[40:43], v[88:91], v[144:147], v[40:43]\n" \
    "ds_read2_b32 v[118:119], v160 offset0:18 offset1:19\n" \
    "s_waitcnt lgkmcnt(15)\n" \
    "v_mfma_f32_16x16x32_bf16 v[36:39], v[88:91], v[140:143], v[36:39]\n" \
    "ds_read_b128 v[148:151], %[hp] offset:3712\n" \
    "s_waitcnt lgkmcnt(14)\n" \
    "v_mfma_f32_16x16x32_bf16 v[32:35], v[88:91], v[136:139], v[32:35]\n" \
    "ds_read_b128 v[124:127], %[hp] offset:640\n" \
    "s_waitcnt lgkmcnt(12)\n" \
    "v_mfma_f32_16x16x32_bf16 v[28:31], v[88:91], v[132:135], v[28:31]\n" \
    "ds_read_b128 v[120:123], %[hp] offset:128\n" \
    "s_waitcnt lgkmcnt(10)\n" \
    "v_mfma_f32_16x16x32_bf16 v[24:27], v[88:91], v[128:131], v[24:27]\n" \
    "v_mfma_f32_16x16x32_bf16 v[20:23], v[88:91], v[156:159], v[20:23]\n" \
    "v_mfma_f32_16x16x32_bf16 v[16:19], v[88:91], v[152:155], v[16:19]\n" \
    "ds_read2_b32 v[88:89], v163 offset0:176 offset1:177\n" \
    "v_mfma_f32_16x16x32_bf16 v[44:47], v[92:95], v[144:147], v[44:47]\n" \
    "ds_read2_b32 v[90:91], v163 offset0:178 offset1:179\n" \
    "v_mfma_f32_16x16x32_bf16 v[40:43], v[92:95], v[140:143], v[40:43]\n" \
    "ds_read_b128 v[144:147], %[hp] offset:3200\n" \
    "v_fmac_f32 v20, %[dd], v84\n" \
    "v_fmac_f32 v21, %[dd], v85\n" \
    "v_mfma_f32_16x16x32_bf16 v[36:39], v[92:95], v[136:139], v[36:39]\n" \
    "v_fmac_f32 v22, %[dd], v86\n" \
    "v_fmac_f32 v23, %[dd], v87\n" \
    "v_mfma_f32_16x16x32_bf16 v[32:35], v[92:95], v[132:135], v[32:35]\n" \
    "v_mfma_f32_16x16x32_bf16 v[28:31], v[92:95], v[128:131], v[28:31]\n" \
    "ds_read_b64 v[86:87], %[kvr] offset:1024\n" \
    "v_mfma_f32_16x16x32_bf16 v[24:27], v[92:95], v[156:159], v[24:27]\n" \
    "v_mfma_f32_16x16x32_bf16 v[20:23], v[92:95], v[152:155], v[20:23]\n" \
    "ds_read2_b32 v[92:93], v163 offset0:32 offset1:33\n" \
    "v_mfma_f32_16x16x32_bf16 v[44:47], v[96:99], v[140:143], v[44:47]\n" \
    "ds_read_b128 v[140:143], %[hp] offset:2688\n" \
    "v_mfma_f32_16x16x32_bf16 v[40:43], v[96:99], v[136:139], v[40:43]\n" \
    "ds_read2_b32 v[94:95], v163 offset0:34 offset1:35\n" \
    "v_mfma_f32_16x16x32_bf16 v[36:39], v[96:99], v[132:135], v[36:39]\n" \
    "v_mfma_f32_16x16x32_bf16 v[32:35], v[96:99], v[128:131], v[32:35]\n" \
    "v_mfma_f32_16x16x32_bf16 v[28:31], v[96:99], v[156:159], v[28:31]\n" \
    "v_mfma_f32_16x16x32_bf16 v[24:27], v[96:99], v[152:155], v[24:27]\n" \
    "ds_read2_b32 v[96:97], v162 offset0:176 offset1:177\n" \
    "v_mfma_f32_16x16x32_bf16 v[44:47], v[100:103], v[136:139], v[44:47]\n" \
    "ds_read_b128 v[136:139], %[hp] offset:2176\n" \
    "v_mfma_f32_16x16x32_bf16 v[40:43], v[100:103], v[132:135], v[40:43]\n" \
    "ds_read2_b32 v[98:99], v162 offset0:178 offset1:179\n" \
    "v_mfma_f32_16x16x32_bf16 v[36:39], v[100:103], v[128:131], v[36:39]\n" \
    "v_mfma_f32_16x16x32_bf16 v[32:35], v[100:103], v[156:159], v[32:35]\n" \
    "s_waitcnt lgkmcnt(6)\n" \
    "v_lshlrev_b32 v84, 16, v86\n" \
    "v_mfma_f32_16x16x32_bf16 v[28:31], v[100:103], v[152:155], v[28:31]\n" \
    "ds_read2_b32 v[100:101], v162 offset0:32 offset1:33\n" \
    "v_mfma_f32_16x16x32_bf16 v[44:47], v[104:107], v[132:135], v[44:47]\n" \
    "ds_read_b128 v[132:135], %[hp] offset:1664\n" \
    "v_and_b32 v85, 0xffff0000, v86\n" \
    "v_mfma_f32_16x16x32_bf16 v[40:43], v[104:107], v[128:131], v[40:43]\n" \
    "ds_read2_b32 v[102:103], v162 offset0:34 offset1:35\n" \
    "v_mfma_f32_16x16x32_bf16 v[36:39], v[104:107], v[156:159], v[36:39]\n" \
    "v_lshlrev_b32 v86, 16, v87\n" \
    "v_mfma_f32_16x16x32_bf16 v[32:35], v[104:107], v[152:155], v[32:35]\n" \
    "ds_read2_b32 v[104:105], v161 offset0:176 offset1:177\n" \
    "v_mfma_f32_16x16x32_bf16 v[44:47], v[108:111], v[128:131], v[44:47]\n" \
    "ds_read_b128 v[128:131], %[hp] offset:1152\n" \
    "v_and_b32 v87, 0xffff0000, v87\n" \
    "v_mfma_f32_16x16x32_bf16 v[40:43], v[108:111], v[156:159], v[40:43]\n" \
    "ds_read2_b32 v[106:107], v161 offset0:178 offset1:179\n" \
    "v_mfma_f32_16x16x32_bf16 v[36:39], v[108:111], v[152:155], v[36:39]\n" \
    "ds_read2_b32 v[108:109], v161 offset0:32 offset1:33\n" \
    "v_mfma_f32_16x16x32_bf16 v[44:47], v[112:115], v[156:159], v[44:47]\n" \
    "ds_read2_b32 v[110:111], v161 offset0:34 offset1:35\n" \
    "v_mfma_f32_16x16x32_bf16 v[40:43], v[112:115], v[152:155], v[40:43]\n" \
    "ds_read2_b32 v[112:113], v160 offset0:176 offset1:177\n" \
    "v_mfma_f32_16x16x32_bf16 v[44:47], v[116:119], v[152:155], v[44:47]\n" \
    "ds_read2_b32 v[114:115], v160 offset0:178 offset1:179\n" \
    "v_mfma_f32_16x16x32_bf16 v[44:47], v[88:91], v[148:151], v[44:47]\n" \
    "ds_read2_b32 v[116:117], v160 offset0:32 offset1:33\n" \
    "v_mfma_f32_16x16x32_bf16 v[40:43], v[88:91], v[144:147], v[40:43]\n" \
    "ds_read2_b32 v[118:119], v160 offset0:34 offset1:35\n" \
    "s_waitcnt lgkmcnt(15)\n" \
    "v_mfma_f32_16x16x32_bf16 v[36:39], v[88:91], v[140:143], v[36:39]\n" \
    "ds_read_b128 v[148:151], %[hp] offset:3776\n" \
    "s_waitcnt lgkmcnt(14)\n" \
    "v_mfma_f32_16x16x32_bf16 v[32:35], v[88:91], v[136:139], v[32:35]\n" \
    "ds_read_b128 v[156:159], %[hp] offset:704\n" \
    "s_waitcnt lgkmcnt(12)\n" \
    "v_mfma_f32_16x16x32_bf16 v[28:31], v[88:91], v[132:135], v[28:31]\n" \
    "ds_read_b128 v[152:155], %[hp] offset:192\n" \
    "s_waitcnt lgkmcnt(10)\n" \
    "v_mfma_f32_16x16x32_bf16 v[24:27], v[88:91], v[128:131], v[24:27]\n" \
    "v_mfma_f32_16x16x32_bf16 v[20:23], v[88:91], v[124:127], v[20:23]\n" \
    "v_mfma_f32_16x16x32_bf16 v[16:19], v[88:91], v[120:123], v[16:19]\n" \
    "ds_read2_b32 v[88:89], v163 offset0:192 offset1:193\n" \
    "v_mfma_f32_16x16x32_bf16 v[44:47], v[92:95], v[144:147], v[44:47]\n" \
    "ds_read2_b32 v[90:91], v163 offset0:194 offset1:195\n" \
    "v_fmac_f32 v24, %[dd], v84\n" \
    "v_fmac_f32 v25, %[dd], v85\n" \
    "v_mfma_f32_16x16x32_bf16 v[40:43], v[92:95], v[140:143], v[40:43]\n" \
    "ds_read_b128 v[144:147], %[hp] offset:3264\n" \
    "v_fmac_f32 v26, %[dd], v86\n" \
    "v_fmac_f32 v27, %[dd], v87\n" \
    "v_mfma_f32_16x16x32_bf16 v[36:39], v[92:95], v[136:139], v[36:39]\n" \
    "v_mfma_f32_16x16x32_bf16 v[32:35], v[92:95], v[132:135], v[32:35]\n" \
    "v_mfma_f32_16x16x32_bf16 v[28:31], v[92:95], v[128:131], v[28:31]\n" \
    "ds_read_b64 v[86:87], %[kvr] offset:1536\n" \
    "v_mfma_f32_16x16x32_bf16 v[24:27], v[92:95], v[124:127], v[24:27]\n" \
    "v_mfma_f32_16x16x32_bf16 v[20:23], v[92:95], v[120:123], v[20:23]\n" \
    "ds_read2_b32 v[92:93], v163 offset0:48 offset1:49\n" \
    "v_mfma_f32_16x16x32_bf16 v[44:47], v[96:99], v[140:143], v[44:47]\n" \
    "ds_read_b128 v[140:143], %[hp] offset:2752\n" \
    "v_mfma_f32_16x16x32_bf16 v[40:43], v[96:99], v[136:139], v[40:43]\n" \
    "ds_read2_b32 v[94:95], v163 offset0:50 offset1:51\n" \
    "v_mfma_f32_16x16x32_bf16 v[36:39], v[96:99], v[132:135], v[36:39]\n" \
    "v_mfma_f32_16x16x32_bf16 v[32:35], v[96:99], v[128:131], v[32:35]\n" \
    "v_mfma_f32_16x16x32_bf16 v[28:31], v[96:99], v[124:127], v[28:31]\n" \
    "v_mfma_f32_16x16x32_bf16 v[24:27], v[96:99], v[120:123], v[24:27]\n" \
    "ds_read2_b32 v[96:97], v162 offset0:192 offset1:193\n" \
    "v_mfma_f32_16x16x32_bf16 v[44:47], v[100:103], v[136:139], v[44:47]\n" \
    "ds_read_b128 v[136:139], %[hp] offset:2240\n" \
    "v_mfma_f32_16x16x32_bf16 v[40:43], v[100:103], v[132:135], v[40:43]\n" \
    "ds_read2_b32 v[98:99], v162 offset0:194 offset1:195\n" \
    "v_mfma_f32_16x16x32_bf16 v[36:39], v[100:103], v[128:131], v[36:39]\n" \
    "v_mfma_f32_16x16x32_bf16 v[32:35], v[100:103], v[124:127], v[32:35]\n" \
    "s_waitcnt lgkmcnt(6)\n" \
    "v_lshlrev_b32 v84, 16, v86\n" \
    "v_mfma_f32_16x16x32_bf16 v[28:31], v[100:103], v[120:123], v[28:31]\n" \
    "ds_read2_b32 v[100:101], v162 offset0:48 offset1:49\n" \
    "v_mfma_f32_16x16x32_bf16 v[44:47], v[104:107], v[132:135], v[44:47]\n" \
    "ds_read_b128 v[132:135], %[hp] offset:1728\n" \
    "v_and_b32 v85, 0xffff0000, v86\n" \
    "v_mfma_f32_16x16x32_bf16 v[40:43], v[104:107], v[128:131], v[40:43]\n" \
    "ds_read2_b32 v[102:103], v162 offset0:50 offset1:51\n" \
    "v_mfma_f32_16x16x32_bf16 v[36:39], v[104:107], v[124:127], v[36:39]\n" \
    "v_lshlrev_b32 v86, 16, v87\n" \
    "v_mfma_f32_16x16x32_bf16 v[32:35], v[104:107], v[120:123], v[32:35]\n" \
    "ds_read2_b32 v[104:105], v161 offset0:192 offset1:193\n" \
    "v_mfma_f32_16x16x32_bf16 v[44:47], v[108:111], v[128:131], v[44:47]\n" \
    "ds_read_b128 v[128:131], %[hp] offset:1216\n" \
    "v_and_b32 v87, 0xffff0000, v87\n" \
    "v_mfma_f32_16x16x32_bf16 v[40:43], v[108:111], v[124:127], v[40:43]\n" \
    "ds_read2_b32 v[106:107], v161 offset0:194 offset1:195\n" \
    "v_mfma_f32_16x16x32_bf16 v[36:39], v[108:111], v[120:123], v[36:39]\n" \
    "ds_read2_b32 v[108:109], v161 offset0:48 offset1:49\n" \
    "v_mfma_f32_16x16x32_bf16 v[44:47], v[112:115], v[124:127], v[44:47]\n" \
    "ds_read2_b32 v[110:111], v161 offset0:50 offset1:51\n" \
    "v_mfma_f32_16x16x32_bf16 v[40:43], v[112:115], v[120:123], v[40:43]\n" \
    "ds_read2_b32 v[112:113], v160 offset0:192 offset1:193\n" \
    "v_mfma_f32_16x16x32_bf16 v[44:47], v[116:119], v[120:123], v[44:47]\n" \
    "ds_read2_b32 v[114:115], v160 offset0:194 offset1:195\n" \
    "v_mfma_f32_16x16x32_bf16 v[44:47], v[88:91], v[148:151], v[44:47]\n" \
    "ds_read2_b32 v[116:117], v160 offset0:48 offset1:49\n" \
    "v_mfma_f32_16x16x32_bf16 v[40:43], v[88:91], v[144:147], v[40:43]\n" \
    "ds_read2_b32 v[118:119], v160 offset0:50 offset1:51\n" \
    "s_waitcnt lgkmcnt(15)\n" \
    "v_mfma_f32_16x16x32_bf16 v[36:39], v[88:91], v[140:143], v[36:39]\n" \
    "ds_read_b128 v[148:151], %[hp] offset:3840\n" \
    "s_waitcnt lgkmcnt(14)\n" \
    "v_mfma_f32_16x16x32_bf16 v[32:35], v[88:91], v[136:139], v[32:35]\n" \
    "ds_read_b128 v[124:127], %[hp] offset:768\n" \
    "s_waitcnt lgkmcnt(12)\n" \
    "v_mfma_f32_16x16x32_bf16 v[28:31], v[88:91], v[132:135], v[28:31]\n" \
    "ds_read_b128 v[120:123], %[hp] offset:256\n" \
    "s_waitcnt lgkmcnt(10)\n" \
    "v_mfma_f32_16x16x32_bf16 v[24:27], v[88:91], v[128:131], v[24:27]\n" \
    "v_mfma_f32_16x16x32_bf16 v[20:23], v[88:91], v[156:159], v[20:23]\n" \
    "v_mfma_f32_16x16x32_bf16 v[16:19], v[88:91], v[152:155], v[16:19]\n" \
    "ds_read2_b32 v[88:89], v163 offset0:216 offset1:217\n" \
    "v_fmac_f32 v28, %[dd], v84\n" \
    "v_fmac_f32 v29, %[dd], v85\n" \
    "v_mfma_f32_16x16x32_bf16 v[44:47], v[92:95], v[144:147], v[44:47]\n" \
    "ds_read2_b32 v[90:91], v163 offset0:218 offset1:219\n" \
    "v_fmac_f32 v30, %[dd], v86\n" \
    "v_fmac_f32 v31, %[dd], v87\n" \
    "v_mfma_f32_16x16x32_bf16 v[40:43], v[92:95], v[140:143], v[40:43]\n" \
    "ds_read_b128 v[144:147], %[hp] offset:3328\n" \
    "v_mfma_f32_16x16x32_bf16 v[36:39], v[92:95], v[136:139], v[36:39]\n" \
    "v_mfma_f32_16x16x32_bf16 v[32:35], v[92:95], v[132:135], v[32:35]\n" \
    "v_mfma_f32_16x16x32_bf16 v[28:31], v[92:95], v[128:131], v[28:31]\n" \
    "ds_read_b64 v[86:87], %[kvr] offset:2048\n" \
    "v_mfma_f32_16x16x32_bf16 v[24:27], v[92:95], v[156:159], v[24:27]\n" \
    "v_mfma_f32_16x16x32_bf16 v[20:23], v[92:95], v[152:155], v[20:23]\n" \
    "ds_read2_b32 v[92:93], v163 offset0:72 offset1:73\n" \
    "v_mfma_f32_16x16x32_bf16 v[44:47], v[96:99], v[140:143], v[44:47]\n" \
    "ds_read_b128 v[140:143], %[hp] offset:2816\n" \
    "v_mfma_f32_16x16x32_bf16 v[40:43], v[96:99], v[136:139], v[40:43]\n" \
    "ds_read2_b32 v[94:95], v163 offset0:74 offset1:75\n" \
    "v_mfma_f32_16x16x32_bf16 v[36:39], v[96:99], v[132:135], v[36:39]\n" \
    "v_mfma_f32_16x16x32_bf16 v[32:35], v[96:99], v[128:131], v[32:35]\n" \
    "v_mfma_f32_16x16x32_bf16 v[28:31], v[96:99], v[156:159], v[28:31]\n" \
    "v_mfma_f32_16x16x32_bf16 v[24:27], v[96:99], v[152:155], v[24:27]\n" \
    "ds_read2_b32 v[96:97], v162 offset0:216 offset1:217\n" \
    "v_mfma_f32_16x16x32_bf16 v[44:47], v[100:103], v[136:139], v[44:47]\n" \
    "ds_read_b128 v[136:139], %[hp] offset:2304\n" \
    "v_mfma_f32_16x16x32_bf16 v[40:43], v[100:103], v[132:135], v[40:43]\n" \
    "ds_read2_b32 v[98:99], v162 offset0:218 offset1:219\n" \
    "v_mfma_f32_16x16x32_bf16 v[36:39], v[100:103], v[128:131], v[36:39]\n" \
    "v_mfma_f32_16x16x32_bf16 v[32:35], v[100:103], v[156:159], v[32:35]\n" \
    "s_waitcnt lgkmcnt(6)\n" \
    "v_lshlrev_b32 v84, 16, v86\n" \
    "v_mfma_f32_16x16x32_bf16 v[28:31], v[100:103], v[152:155], v[28:31]\n" \
    "ds_read2_b32 v[100:101], v162 offset0:72 offset1:73\n" \
    "v_mfma_f32_16x16x32_bf16 v[44:47], v[104:107], v[132:135], v[44:47]\n" \
    "ds_read_b128 v[132:135], %[hp] offset:1792\n" \
    "v_and_b32 v85, 0xffff0000, v86\n" \
    "v_mfma_f32_16x16x32_bf16 v[40:43], v[104:107], v[128:131], v[40:43]\n" \
    "ds_read2_b32 v[102:103], v162 offset0:74 offset1:75\n" \
    "v_mfma_f32_16x16x32_bf16 v[36:39], v[104:107], v[156:159], v[36:39]\n" \
    "v_lshlrev_b32 v86, 16, v87\n" \
    "v_mfma_f32_16x16x32_bf16 v[32:35], v[104:107], v[152:155], v[32:35]\n" \
    "ds_read2_b32 v[104:105], v161 offset0:216 offset1:217\n" \
    "v_mfma_f32_16x16x32_bf16 v[44:47], v[108:111], v[128:131], v[44:47]\n" \
    "ds_read_b128 v[128:131], %[hp] offset:1280\n" \
    "v_and_b32 v87, 0xffff0000, v87\n" \
    "v_mfma_f32_16x16x32_bf16 v[40:43], v[108:111], v[156:159], v[40:43]\n" \
    "ds_read2_b32 v[106:107], v161 offset0:218 offset1:219\n" \
    "v_mfma_f32_16x16x32_bf16 v[36:39], v[108:111], v[152:155], v[36:39]\n" \
    "ds_read2_b32 v[108:109], v161 offset0:72 offset1:73\n" \
    "v_mfma_f32_16x16x32_bf16 v[44:47], v[112:115], v[156:159], v[44:47]\n" \
    "ds_read2_b32 v[110:111], v161 offset0:74 offset1:75\n" \
    "v_mfma_f32_16x16x32_bf16 v[40:43], v[112:115], v[152:155], v[40:43]\n" \
    "ds_read2_b32 v[112:113], v160 offset0:216 offset1:217\n" \
    "v_mfma_f32_16x16x32_bf16 v[44:47], v[116:119], v[152:155], v[44:47]\n" \
    "ds_read2_b32 v[114:115], v160 offset0:218 offset1:219\n" \
    "v_add_u32 v160, 288, v160\n" \
    "v_add_u32 v161, 288, v161\n" \
    "v_add_u32 v162, 288, v162\n" \
    "v_add_u32 v163, 288, v163\n" \
    "v_mfma_f32_16x16x32_bf16 v[44:47], v[88:91], v[148:151], v[44:47]\n" \
    "ds_read2_b32 v[116:117], v160 offset0:0 offset1:1\n" \
    "v_mfma_f32_16x16x32_bf16 v[40:43], v[88:91], v[144:147], v[40:43]\n" \
    "ds_read2_b32 v[118:119], v160 offset0:2 offset1:3\n" \
    "s_waitcnt lgkmcnt(15)\n" \
    "v_mfma_f32_16x16x32_bf16 v[36:39], v[88:91], v[140:143], v[36:39]\n" \
    "ds_read_b128 v[148:151], %[hp] offset:3904\n" \
    "s_waitcnt lgkmcnt(14)\n" \
    "v_mfma_f32_16x16x32_bf16 v[32:35], v[88:91], v[136:139], v[32:35]\n" \
    "ds_read_b128 v[156:159], %[hp] offset:832\n" \
    "s_waitcnt lgkmcnt(12)\n" \
    "v_mfma_f32_16x16x32_bf16 v[28:31], v[88:91], v[132:135], v[28:31]\n" \
    "ds_read_b128 v[152:155], %[hp] offset:320\n" \
    "s_waitcnt lgkmcnt(10)\n" \
    "v_mfma_f32_16x16x32_bf16 v[24:27], v[88:91], v[128:131], v[24:27]\n" \
    "v_mfma_f32_16x16x32_bf16 v[20:23], v[88:91], v[124:127], v[20:23]\n" \
    "v_fmac_f32 v32, %[dd], v84\n" \
    "v_fmac_f32 v33, %[dd], v85\n" \
    "v_mfma_f32_16x16x32_bf16 v[16:19], v[88:91], v[120:123], v[16:19]\n" \
    "ds_read2_b32 v[88:89], v163 offset0:160 offset1:161\n" \
    "v_fmac_f32 v34, %[dd], v86\n" \
    "v_fmac_f32 v35, %[dd], v87\n" \
    "v_mfma_f32_16x16x32_bf16 v[44:47], v[92:95], v[144:147], v[44:47]\n" \
    "ds_read2_b32 v[90:91], v163 offset0:162 offset1:163\n" \
    "v_mfma_f32_16x16x32_bf16 v[40:43], v[92:95], v[140:143], v[40:43]\n" \
    "ds_read_b128 v[144:147], %[hp] offset:3392\n" \
    "v_mfma_f32_16x16x32_bf16 v[36:39], v[92:95], v[136:139], v[36:39]\n" \
    "v_mfma_f32_16x16x32_bf16 v[32:35], v[92:95], v[132:135], v[32:35]\n" \
    "v_mfma_f32_16x16x32_bf16 v[28:31], v[92:95], v[128:131], v[28:31]\n" \
    "ds_read_b64 v[86:87], %[kvr] offset:2560\n" \
    "v_mfma_f32_16x16x32_bf16 v[24:27], v[92:95], v[124:127], v[24:27]\n" \
    "v_mfma_f32_16x16x32_bf16 v[20:23], v[92:95], v[120:123], v[20:23]\n" \
    "ds_read2_b32 v[92:93], v163 offset0:16 offset1:17\n" \
    "v_mfma_f32_16x16x32_bf16 v[44:47], v[96:99], v[140:143], v[44:47]\n" \
    "ds_read_b128 v[140:143], %[hp] offset:2880\n" \
    "v_mfma_f32_16x16x32_bf16 v[40:43], v[96:99], v[136:139], v[40:43]\n" \
    "ds_read2_b32 v[94:95], v163 offset0:18 offset1:19\n" \
    "v_mfma_f32_16x16x32_bf16 v[36:39], v[96:99], v[132:135], v[36:39]\n" \
    "v_mfma_f32_16x16x32_bf16 v[32:35], v[96:99], v[128:131], v[32:35]\n" \
    "v_mfma_f32_16x16x32_bf16 v[28:31], v[96:99], v[124:127], v[28:31]\n" \
    "v_mfma_f32_16x16x32_bf16 v[24:27], v[96:99], v[120:123], v[24:27]\n" \
    "ds_read2_b32 v[96:97], v162 offset0:160 offset1:161\n" \
    "v_mfma_f32_16x16x32_bf16 v[44:47], v[100:103], v[136:139], v[44:47]\n" \
    "ds_read_b128 v[136:139], %[hp] offset:2368\n" \
    "v_mfma_f32_16x16x32_bf16 v[40:43], v[100:103], v[132:135], v[40:43]\n" \
    "ds_read2_b32 v[98:99], v162 offset0:162 offset1:163\n" \
    "v_mfma_f32_16x16x32_bf16 v[36:39], v[100:103], v[128:131], v[36:39]\n" \
    "v_mfma_f32_16x16x32_bf16 v[32:35], v[100:103], v[124:127], v[32:35]\n" \
    "s_waitcnt lgkmcnt(6)\n" \
    "v_lshlrev_b32 v84, 16, v86\n" \
    "v_mfma_f32_16x16x32_bf16 v[28:31], v[100:103], v[120:123], v[28:31]\n" \
    "ds_read2_b32 v[100:101], v162 offset0:16 offset1:17\n" \
    "v_mfma_f32_16x16x32_bf16 v[44:47], v[104:107], v[132:135], v[44:47]\n" \
    "ds_read_b128 v[132:135], %[hp] offset:1856\n" \
    "v_and_b32 v85, 0xffff0000, v86\n" \
    "v_mfma_f32_16x16x32_bf16 v[40:43], v[104:107], v[128:131], v[40:43]\n" \
    "ds_read2_b32 v[102:103], v162 offset0:18 offset1:19\n" \
    "v_mfma_f32_16x16x32_bf16 v[36:39], v[104:107], v[124:127], v[36:39]\n" \
    "v_lshlrev_b32 v86, 16, v87\n" \
    "v_mfma_f32_16x16x32_bf16 v[32:35], v[104:107], v[120:123], v[32:35]\n" \
    "ds_read2_b32 v[104:105], v161 offset0:160 offset1:161\n" \
    "v_mfma_f32_16x16x32_bf16 v[44:47], v[108:111], v[128:131], v[44:47]\n" \
    "ds_read_b128 v[128:131], %[hp] offset:1344\n" \
    "v_and_b32 v87, 0xffff0000, v87\n" \
    "v_mfma_f32_16x16x32_bf16 v[40:43], v[108:111], v[124:127], v[40:43]\n" \
    "ds_read2_b32 v[106:107], v161 offset0:162 offset1:163\n" \
    "v_mfma_f32_16x16x32_bf16 v[36:39], v[108:111], v[120:123], v[36:39]\n" \
    "ds_read2_b32 v[108:109], v161 offset0:16 offset1:17\n" \
    "v_mfma_f32_16x16x32_bf16 v[44:47], v[112:115], v[124:127], v[44:47]\n" \
    "ds_read2_b32 v[110:111], v161 offset0:18 offset1:19\n" \
    "v_mfma_f32_16x16x32_bf16 v[40:43], v[112:115], v[120:123], v[40:43]\n" \
    "ds_read2_b32 v[112:113], v160 offset0:160 offset1:161\n" \
    "v_mfma_f32_16x16x32_bf16 v[44:47], v[116:119], v[120:123], v[44:47]\n" \
    "ds_read2_b32 v[114:115], v160 offset0:162 offset1:163\n" \
    "v_mfma_f32_16x16x32_bf16 v[44:47], v[88:91], v[148:151], v[44:47]\n" \
    "ds_read2_b32 v[116:117], v160 offset0:16 offset1:17\n" \
    "v_mfma_f32_16x16x32_bf16 v[40:43], v[88:91], v[144:147], v[40:43]\n" \
    "ds_read2_b32 v[118:119], v160 offset0:18 offset1:19\n" \
    "s_waitcnt lgkmcnt(15)\n" \
    "v_mfma_f32_16x16x32_bf16 v[36:39], v[88:91], v[140:143], v[36:39]\n" \
    "ds_read_b128 v[148:151], %[hp] offset:3968\n" \
    "s_waitcnt lgkmcnt(14)\n" \
    "v_mfma_f32_16x16x32_bf16 v[32:35], v[88:91], v[136:139], v[32:35]\n" \
    "ds_read_b128 v[124:127], %[hp] offset:896\n" \
    "s_waitcnt lgkmcnt(12)\n" \
    "v_mfma_f32_16x16x32_bf16 v[28:31], v[88:91], v[132:135], v[28:31]\n" \
    "ds_read_b128 v[120:123], %[hp] offset:384\n" \
    "s_waitcnt lgkmcnt(10)\n" \
    "v_mfma_f32_16x16x32_bf16 v[24:27], v[88:91], v[128:131], v[24:27]\n" \
    "v_fmac_f32 v36, %[dd], v84\n" \
    "v_fmac_f32 v37, %[dd], v85\n" \
    "v_mfma_f32_16x16x32_bf16 v[20:23], v[88:91], v[156:159], v[20:23]\n" \
    "v_fmac_f32 v38, %[dd], v86\n" \
    "v_fmac_f32 v39, %[dd], v87\n" \
    "v_mfma_f32_16x16x32_bf16 v[16:19], v[88:91], v[152:155], v[16:19]\n" \
    "ds_read2_b32 v[88:89], v163 offset0:176 offset1:177\n" \
    "v_mfma_f32_16x16x32_bf16 v[44:47], v[92:95], v[144:147], v[44:47]\n" \
    "ds_read2_b32 v[90:91], v163 offset0:178 offset1:179\n" \
    "v_mfma_f32_16x16x32_bf16 v[40:43], v[92:95], v[140:143], v[40:43]\n" \
    "ds_read_b128 v[144:147], %[hp] offset:3456\n" \
    "v_mfma_f32_16x16x32_bf16 v[36:39], v[92:95], v[136:139], v[36:39]\n" \
    "v_mfma_f32_16x16x32_bf16 v[32:35], v[92:95], v[132:135], v[32:35]\n" \
    "v_mfma_f32_16x16x32_bf16 v[28:31], v[92:95], v[128:131], v[28:31]\n" \
    "ds_read_b64 v[86:87], %[kvr] offset:3072\n" \
    "v_mfma_f32_16x16x32_bf16 v[24:27], v[92:95], v[156:159], v[24:27]\n" \
    "v_mfma_f32_16x16x32_bf16 v[20:23], v[92:95], v[152:155], v[20:23]\n" \
    "ds_read2_b32 v[92:93], v163 offset0:32 offset1:33\n" \
    "v_mfma_f32_16x16x32_bf16 v[44:47], v[96:99], v[140:143], v[44:47]\n" \
    "ds_read_b128 v[140:143], %[hp] offset:2944\n" \
    "v_mfma_f32_16x16x32_bf16 v[40:43], v[96:99], v[136:139], v[40:43]\n" \
    "ds_read2_b32 v[94:95], v163 offset0:34 offset1:35\n" \
    "v_mfma_f32_16x16x32_bf16 v[36:39], v[96:99], v[132:135], v[36:39]\n" \
    "v_mfma_f32_16x16x32_bf16 v[32:35], v[96:99], v[128:131], v[32:35]\n" \
    "v_mfma_f32_16x16x32_bf16 v[28:31], v[96:99], v[156:159], v[28:31]\n" \
    "v_mfma_f32_16x16x32_bf16 v[24:27], v[96:99], v[152:155], v[24:27]\n" \
    "ds_read2_b32 v[96:97], v162 offset0:176 offset1:177\n" \
    "v_mfma_f32_16x16x32_bf16 v[44:47], v[100:103], v[136:139], v[44:47]\n" \
    "ds_read_b128 v[136:139], %[hp] offset:2432\n" \
    "v_mfma_f32_16x16x32_bf16 v[40:43], v[100:103], v[132:135], v[40:43]\n" \
    "ds_read2_b32 v[98:99], v162 offset0:178 offset1:179\n" \
    "v_mfma_f32_16x16x32_bf16 v[36:39], v[100:103], v[128:131], v[36:39]\n" \
    "v_mfma_f32_16x16x32_bf16 v[32:35], v[100:103], v[156:159], v[32:35]\n" \
    "s_waitcnt lgkmcnt(6)\n" \
    "v_lshlrev_b32 v84, 16, v86\n" \
    "v_mfma_f32_16x16x32_bf16 v[28:31], v[100:103], v[152:155], v[28:31]\n" \
    "ds_read2_b32 v[100:101], v162 offset0:32 offset1:33\n" \
    "v_mfma_f32_16x16x32_bf16 v[44:47], v[104:107], v[132:135], v[44:47]\n" \
    "ds_read_b128 v[132:135], %[hp] offset:1920\n" \
    "v_and_b32 v85, 0xffff0000, v86\n" \
    "v_mfma_f32_16x16x32_bf16 v[40:43], v[104:107], v[128:131], v[40:43]\n" \
    "ds_read2_b32 v[102:103], v162 offset0:34 offset1:35\n" \
    "v_mfma_f32_16x16x32_bf16 v[36:39], v[104:107], v[156:159], v[36:39]\n" \
    "v_lshlrev_b32 v86, 16, v87\n" \
    "v_mfma_f32_16x16x32_bf16 v[32:35], v[104:107], v[152:155], v[32:35]\n" \
    "ds_read2_b32 v[104:105], v161 offset0:176 offset1:177\n" \
    "v_mfma_f32_16x16x32_bf16 v[44:47], v[108:111], v[128:131], v[44:47]\n" \
    "ds_read_b128 v[128:131], %[hp] offset:1408\n" \
    "v_and_b32 v87, 0xffff0000, v87\n" \
    "v_mfma_f32_16x16x32_bf16 v[40:43], v[108:111], v[156:159], v[40:43]\n" \
    "ds_read2_b32 v[106:107], v161 offset0:178 offset1:179\n" \
    "v_mfma_f32_16x16x32_bf16 v[36:39], v[108:111], v[152:155], v[36:39]\n" \
    "ds_read2_b32 v[108:109], v161 offset0:32 offset1:33\n" \
    "v_mfma_f32_16x16x32_bf16 v[44:47], v[112:115], v[156:159], v[44:47]\n" \
    "ds_read2_b32 v[110:111], v161 offset0:34 offset1:35\n" \
    "v_mfma_f32_16x16x32_bf16 v[40:43], v[112:115], v[152:155], v[40:43]\n" \
    "ds_read2_b32 v[112:113], v160 offset0:176 offset1:177\n" \
    "v_mfma_f32_16x16x32_bf16 v[44:47], v[116:119], v[152:155], v[44:47]\n" \
    "ds_read2_b32 v[114:115], v160 offset0:178 offset1:179\n" \
    "v_mfma_f32_16x16x32_bf16 v[44:47], v[88:91], v[148:151], v[44:47]\n" \
    "ds_read2_b32 v[116:117], v160 offset0:32 offset1:33\n" \
    "v_mfma_f32_16x16x32_bf16 v[40:43], v[88:91], v[144:147], v[40:43]\n" \
    "ds_read2_b32 v[118:119], v160 offset0:34 offset1:35\n" \
    "s_waitcnt lgkmcnt(15)\n" \
    "v_mfma_f32_16x16x32_bf16 v[36:39], v[88:91], v[140:143], v[36:39]\n" \
    "ds_read_b128 v[152:155], %[hp] offset:448\n" \
    "s_waitcnt lgkmcnt(14)\n" \
    "v_mfma_f32_16x16x32_bf16 v[32:35], v[88:91], v[136:139], v[32:35]\n" \
    "ds_read_b128 v[156:159], %[hp] offset:960\n" \
    "s_waitcnt lgkmcnt(12)\n" \
    "v_mfma_f32_16x16x32_bf16 v[28:31], v[88:91], v[132:135], v[28:31]\n" \
    "ds_read_b128 v[148:151], %[hp] offset:4032\n" \
    "v_fmac_f32 v40, %[dd], v84\n" \
    "v_fmac_f32 v41, %[dd], v85\n" \
    "s_waitcnt lgkmcnt(10)\n" \
    "v_mfma_f32_16x16x32_bf16 v[24:27], v[88:91], v[128:131], v[24:27]\n" \
    "v_fmac_f32 v42, %[dd], v86\n" \
    "v_fmac_f32 v43, %[dd], v87\n" \
    "v_mfma_f32_16x16x32_bf16 v[20:23], v[88:91], v[124:127], v[20:23]\n" \
    "v_mfma_f32_16x16x32_bf16 v[16:19], v[88:91], v[120:123], v[16:19]\n" \
    "ds_read2_b32 v[88:89], v163 offset0:192 offset1:193\n" \
    "v_mfma_f32_16x16x32_bf16 v[44:47], v[92:95], v[144:147], v[44:47]\n" \
    "ds_read2_b32 v[90:91], v163 offset0:194 offset1:195\n" \
    "v_mfma_f32_16x16x32_bf16 v[40:43], v[92:95], v[140:143], v[40:43]\n" \
    "ds_read_b128 v[144:147], %[hp] offset:3520\n" \
    "v_mfma_f32_16x16x32_bf16 v[36:39], v[92:95], v[136:139], v[36:39]\n" \
    "v_mfma_f32_16x16x32_bf16 v[32:35], v[92:95], v[132:135], v[32:35]\n" \
    "v_mfma_f32_16x16x32_bf16 v[28:31], v[92:95], v[128:131], v[28:31]\n" \
    "ds_read_b64 v[86:87], %[kvr] offset:3584\n" \
    "v_mfma_f32_16x16x32_bf16 v[24:27], v[92:95], v[124:127], v[24:27]\n" \
    "v_mfma_f32_16x16x32_bf16 v[20:23], v[92:95], v[120:123], v[20:23]\n" \
    "ds_read2_b32 v[92:93], v163 offset0:48 offset1:49\n" \
    "v_mfma_f32_16x16x32_bf16 v[44:47], v[96:99], v[140:143], v[44:47]\n" \
    "ds_read2_b32 v[94:95], v163 offset0:50 offset1:51\n" \
    "v_mfma_f32_16x16x32_bf16 v[40:43], v[96:99], v[136:139], v[40:43]\n" \
    "ds_read_b128 v[140:143], %[hp] offset:3008\n" \
    "v_mfma_f32_16x16x32_bf16 v[36:39], v[96:99], v[132:135], v[36:39]\n" \
    "v_mfma_f32_16x16x32_bf16 v[32:35], v[96:99], v[128:131], v[32:35]\n" \
    "v_mfma_f32_16x16x32_bf16 v[28:31], v[96:99], v[124:127], v[28:31]\n" \
    "v_mfma_f32_16x16x32_bf16 v[24:27], v[96:99], v[120:123], v[24:27]\n" \
    "ds_read2_b32 v[96:97], v162 offset0:192 offset1:193\n" \
    "v_mfma_f32_16x16x32_bf16 v[44:47], v[100:103], v[136:139], v[44:47]\n" \
    "ds_read2_b32 v[98:99], v162 offset0:194 offset1:195\n" \
    "v_mfma_f32_16x16x32_bf16 v[40:43], v[100:103], v[132:135], v[40:43]\n" \
    "ds_read_b128 v[136:139], %[hp] offset:2496\n" \
    "v_mfma_f32_16x16x32_bf16 v[36:39], v[100:103], v[128:131], v[36:39]\n" \
    "v_mfma_f32_16x16x32_bf16 v[32:35], v[100:103], v[124:127], v[32:35]\n" \
    "s_waitcnt lgkmcnt(6)\n" \
    "v_lshlrev_b32 v84, 16, v86\n" \
    "v_mfma_f32_16x16x32_bf16 v[28:31], v[100:103], v[120:123], v[28:31]\n" \
    "ds_read2_b32 v[100:101], v162 offset0:48 offset1:49\n" \
    "v_mfma_f32_16x16x32_bf16 v[44:47], v[104:107], v[132:135], v[44:47]\n" \
    "ds_read_b128 v[132:135], %[hp] offset:1984\n" \
    "v_and_b32 v85, 0xffff0000, v86\n" \
    "v_mfma_f32_16x16x32_bf16 v[40:43], v[104:107], v[128:131], v[40:43]\n" \
    "ds_read2_b32 v[102:103], v162 offset0:50 offset1:51\n" \
    "v_mfma_f32_16x16x32_bf16 v[36:39], v[104:107], v[124:127], v[36:39]\n" \
    "v_lshlrev_b32 v86, 16, v87\n" \
    "v_mfma_f32_16x16x32_bf16 v[32:35], v[104:107], v[120:123], v[32:35]\n" \
    "ds_read2_b32 v[104:105], v161 offset0:192 offset1:193\n" \
    "v_mfma_f32_16x16x32_bf16 v[44:47], v[108:111], v[128:131], v[44:47]\n" \
    "ds_read_b128 v[128:131], %[hp] offset:1472\n" \
    "v_and_b32 v87, 0xffff0000, v87\n" \
    "v_mfma_f32_16x16x32_bf16 v[40:43], v[108:111], v[124:127], v[40:43]\n" \
    "ds_read2_b32 v[106:107], v161 offset0:194 offset1:195\n" \
    "v_mfma_f32_16x16x32_bf16 v[36:39], v[108:111], v[120:123], v[36:39]\n" \
    "ds_read2_b32 v[108:109], v161 offset0:48 offset1:49\n" \
    "v_mfma_f32_16x16x32_bf16 v[44:47], v[112:115], v[124:127], v[44:47]\n" \
    "ds_read2_b32 v[110:111], v161 offset0:50 offset1:51\n" \
    "v_mfma_f32_16x16x32_bf16 v[40:43], v[112:115], v[120:123], v[40:43]\n" \
    "ds_read2_b32 v[112:113], v160 offset0:192 offset1:193\n" \
    "v_mfma_f32_16x16x32_bf16 v[44:47], v[116:119], v[120:123], v[44:47]\n" \
    "ds_read2_b32 v[114:115], v160 offset0:194 offset1:195\n" \
    "v_mfma_f32_16x16x32_bf16 v[16:19], v[88:91], v[152:155], v[16:19]\n" \
    "ds_read2_b32 v[116:117], v160 offset0:48 offset1:49\n" \
    "v_mfma_f32_16x16x32_bf16 v[20:23], v[88:91], v[156:159], v[20:23]\n" \
    "ds_read2_b32 v[118:119], v160 offset0:50 offset1:51\n" \
    "s_waitcnt lgkmcnt(15)\n" \
    "v_mfma_f32_16x16x32_bf16 v[20:23], v[92:95], v[152:155], v[20:23]\n" \
    "ds_read_b32 v50, %[ctl] offset:52\n" \
    "s_waitcnt lgkmcnt(8)\n" \
    "v_mfma_f32_16x16x32_bf16 v[24:27], v[88:91], v[128:131], v[24:27]\n" \
    "v_fmac_f32 v44, %[dd], v84\n" \
    "v_fmac_f32 v45, %[dd], v85\n" \
    "v_mfma_f32_16x16x32_bf16 v[24:27], v[92:95], v[156:159], v[24:27]\n" \
    "v_fmac_f32 v46, %[dd], v86\n" \
    "v_fmac_f32 v47, %[dd], v87\n" \
    "v_mfma_f32_16x16x32_bf16 v[24:27], v[96:99], v[152:155], v[24:27]\n" \
    "v_mfma_f32_16x16x32_bf16 v[28:31], v[88:91], v[132:135], v[28:31]\n" \
    "v_cvt_pk_bf16_f32 v52, v16, v17\n" \
    "v_cvt_pk_bf16_f32 v54, v20, v21\n" \
    "v_mfma_f32_16x16x32_bf16 v[28:31], v[92:95], v[128:131], v[28:31]\n" \
    "v_cvt_pk_bf16_f32 v53, v18, v19\n" \
    "v_cvt_pk_bf16_f32 v55, v22, v23\n" \
    "v_mfma_f32_16x16x32_bf16 v[28:31], v[96:99], v[156:159], v[28:31]\n" \
    "v_min_u32 v52, -2, v52\n" \
    "v_min_u32 v54, -2, v54\n" \
    "v_mfma_f32_16x16x32_bf16 v[28:31], v[100:103], v[152:155], v[28:31]\n" \
    "v_min_u32 v53, -2, v53\n" \
    "v_min_u32 v55, -2, v55\n" \
    "v_mfma_f32_16x16x32_bf16 v[32:35], v[88:91], v[136:139], v[32:35]\n" \
    "v_permlane16_swap_b32 v52, v54\n" \
    "v_add_u32 v68, 0, %[yo]\n" \
    "v_mfma_f32_16x16x32_bf16 v[32:35], v[92:95], v[132:135], v[32:35]\n" \
    "v_permlane16_swap_b32 v53, v55\n" \
    "v_mfma_f32_16x16x32_bf16 v[32:35], v[96:99], v[128:131], v[32:35]\n" \
    "s_waitcnt lgkmcnt(0)\n" \
    "v_readfirstlane_b32 %[tmp], v50\n" \
    "s_mov_b32 %[cnt], 0\n" \
    "s_cmp_lg_u32 %[tmp], 0\n" \
    "s_cbranch_scc1 Lok_%=\n" \
    "Ls2_%=:\n" \
    "ds_read_b32 v50, %[ctl] offset:52\n" \
    "s_add_u32 %[cnt], %[cnt], 1\n" \
    "s_waitcnt lgkmcnt(0)\n" \
    "v_readfirstlane_b32 %[tmp], v50\n" \
    "s_cmp_ge_u32 %[cnt], %[cap]\n" \
    "s_cbranch_scc1 Lok_%=\n" \
    "s_cmp_eq_u32 %[tmp], 0\n" \
    "s_cbranch_scc1 Ls2_%=\n" \
    "Lok_%=:\n" \
    "global_store_dwordx4 v68, v[52:55], %[wsp] sc1\n" \
    "v_mfma_f32_16x16x32_bf16 v[32:35], v[100:103], v[156:159], v[32:35]\n" \
    "v_cvt_pk_bf16_f32 v56, v24, v25\n" \
    "v_cvt_pk_bf16_f32 v58, v28, v29\n" \
    "v_mfma_f32_16x16x32_bf16 v[32:35], v[104:107], v[152:155], v[32:35]\n" \
    "v_cvt_pk_bf16_f32 v57, v26, v27\n" \
    "v_cvt_pk_bf16_f32 v59, v30, v31\n" \
    "v_mfma_f32_16x16x32_bf16 v[36:39], v[88:91], v[140:143], v[36:39]\n" \
    "v_min_u32 v56, -2, v56\n" \
    "v_min_u32 v58, -2, v58\n" \
    "v_mfma_f32_16x16x32_bf16 v[36:39], v[92:95], v[136:139], v[36:39]\n" \
    "v_min_u32 v57, -2, v57\n" \
    "v_min_u32 v59, -2, v59\n" \
    "v_mfma_f32_16x16x32_bf16 v[36:39], v[96:99], v[132:135], v[36:39]\n" \
    "v_permlane16_swap_b32 v56, v58\n" \
    "v_add_u32 v69, 655360, %[yo]\n" \
    "v_mfma_f32_16x16x32_bf16 v[36:39], v[100:103], v[128:131], v[36:39]\n" \
    "v_permlane16_swap_b32 v57, v59\n" \
    "v_mfma_f32_16x16x32_bf16 v[36:39], v[104:107], v[156:159], v[36:39]\n" \
    "global_store_dwordx4 v69, v[56:59], %[wsp] sc1\n" \
    "v_mfma_f32_16x16x32_bf16 v[36:39], v[108:111], v[152:155], v[36:39]\n" \
    "v_mfma_f32_16x16x32_bf16 v[40:43], v[88:91], v[144:147], v[40:43]\n" \
    "v_mfma_f32_16x16x32_bf16 v[40:43], v[92:95], v[140:143], v[40:43]\n" \
    "v_mfma_f32_16x16x32_bf16 v[40:43], v[96:99], v[136:139], v[40:43]\n" \
    "v_mfma_f32_16x16x32_bf16 v[40:43], v[100:103], v[132:135], v[40:43]\n" \
    "v_cvt_pk_bf16_f32 v60, v32, v33\n" \
    "v_cvt_pk_bf16_f32 v62, v36, v37\n" \
    "v_mfma_f32_16x16x32_bf16 v[40:43], v[104:107], v[128:131], v[40:43]\n" \
    "v_cvt_pk_bf16_f32 v61, v34, v35\n" \
    "v_cvt_pk_bf16_f32 v63, v38, v39\n" \
    "v_mfma_f32_16x16x32_bf16 v[40:43], v[108:111], v[156:159], v[40:43]\n" \
    "v_min_u32 v60, -2, v60\n" \
    "v_min_u32 v62, -2, v62\n" \
    "v_mfma_f32_16x16x32_bf16 v[40:43], v[112:115], v[152:155], v[40:43]\n" \
    "v_min_u32 v61, -2, v61\n" \
    "v_min_u32 v63, -2, v63\n" \
    "v_mfma_f32_16x16x32_bf16 v[44:47], v[88:91], v[148:151], v[44:47]\n" \
    "v_permlane16_swap_b32 v60, v62\n" \
    "v_add_u32 v70, 1310720, %[yo]\n" \
    "v_mfma_f32_16x16x32_bf16 v[44:47], v[92:95], v[144:147], v[44:47]\n" \
    "v_permlane16_swap_b32 v61, v63\n" \
    "v_mfma_f32_16x16x32_bf16 v[44:47], v[96:99], v[140:143], v[44:47]\n" \
    "global_store_dwordx4 v70, v[60:63], %[wsp] sc1\n" \
    "v_mfma_f32_16x16x32_bf16 v[44:47], v[100:103], v[136:139], v[44:47]\n" \
    "v_mfma_f32_16x16x32_bf16 v[44:47], v[104:107], v[132:135], v[44:47]\n" \
    "v_mfma_f32_16x16x32_bf16 v[44:47], v[108:111], v[128:131], v[44:47]\n" \
    "v_mfma_f32_16x16x32_bf16 v[44:47], v[112:115], v[156:159], v[44:47]\n" \
    "v_mfma_f32_16x16x32_bf16 v[44:47], v[116:119], v[152:155], v[44:47]\n" \
    "s_nop 7\n" \
    "v_cvt_pk_bf16_f32 v64, v40, v41\n" \
    "v_cvt_pk_bf16_f32 v66, v44, v45\n" \
    "v_cvt_pk_bf16_f32 v65, v42, v43\n" \
    "v_cvt_pk_bf16_f32 v67, v46, v47\n" \
    "v_min_u32 v64, -2, v64\n" \
    "v_min_u32 v66, -2, v66\n" \
    "v_min_u32 v65, -2, v65\n" \
    "v_min_u32 v67, -2, v67\n" \
    "v_permlane16_swap_b32 v64, v66\n" \
    "v_add_u32 v71, 1966080, %[yo]\n" \
    "v_permlane16_swap_b32 v65, v67\n" \
    "global_store_dwordx4 v71, v[64:67], %[wsp] sc1\n" \
    "s_nop 1\n"
#define CONV_CLOB "v16", "v17", "v18", "v19", "v20", "v21", "v22", "v23", "v24", "v25", "v26", "v27", "v28", "v29", "v30", "v31", "v32", "v33", "v34", "v35", "v36", "v37", "v38", "v39", "v40", "v41", "v42", "v43", "v44", "v45", "v46", "v47", "v50", "v51", "v52", "v53", "v54", "v55", "v56", "v57", "v58", "v59", "v60", "v61", "v62", "v63", "v64", "v65", "v66", "v67", "v68", "v69", "v70", "v71", "v72", "v73", "v74", "v75", "v76", "v77", "v78", "v79", "v80", "v81", "v82", "v83", "v84", "v85", "v86", "v87", "v88", "v89", "v90", "v91", "v92", "v93", "v94", "v95", "v96", "v97", "v98", "v99", "v100", "v101", "v102", "v103", "v104", "v105", "v106", "v107", "v108", "v109", "v110", "v111", "v112", "v113", "v114", "v115", "v116", "v117", "v118", "v119", "v120", "v121", "v122", "v123", "v124", "v125", "v126", "v127", "v128", "v129", "v130", "v131", "v132", "v133", "v134", "v135", "v136", "v137", "v138", "v139", "v140", "v141", "v142", "v143", "v144", "v145", "v146", "v147", "v148", "v149", "v150", "v151", "v152", "v153", "v154", "v155", "v156", "v157", "v158", "v159", "v160", "v161", "v162", "v163", "scc", "memory"
#define G_ASM \
    "v_add_u32 v154, 67584, %[wp0]\n" \
    "v_add_u32 v155, 67584, %[wp1]\n" \
    "v_add_u32 v156, 67584, %[wp2]\n" \
    "v_add_u32 v157, 67584, %[wp3]\n" \
    "s_mov_b32 s60, 0x11111111\n" \
    "s_mov_b32 s61, 0x11111111\n" \
    "s_mov_b32 s62, 0x22222222\n" \
    "s_mov_b32 s63, 0x22222222\n" \
    "s_mov_b32 s64, 0x44444444\n" \
    "s_mov_b32 s65, 0x44444444\n" \
    "s_mov_b32 s66, 0x88888888\n" \
    "s_mov_b32 s67, 0x88888888\n" \
    "s_mov_b32 s68, -1\n" \
    "s_mov_b32 s69, 0\n" \
    "s_cmp_eq_u32 %[half], 0\n" \
    "s_waitcnt lgkmcnt(0)\n" \
    "s_cbranch_scc0 Llate_%=\n" \
    "s_barrier\n" \
    "ds_read_b64_tr_b16 v[126:127], %[wp1] offset:0\n" \
    "ds_read_b64_tr_b16 v[128:129], %[wp1] offset:2048\n" \
    "ds_read_b128 v[98:101], %[ap0] offset:53248\n" \
    "ds_read_b64_tr_b16 v[130:131], %[wp2] offset:0\n" \
    "ds_read_b64_tr_b16 v[132:133], %[wp2] offset:2048\n" \
    "ds_read_b64_tr_b16 v[134:135], %[wp3] offset:0\n" \
    "ds_read_b64_tr_b16 v[136:137], %[wp3] offset:2048\n" \
    "ds_read_b128 v[102:105], %[ap0] offset:55296\n" \
    "ds_read_b64_tr_b16 v[122:123], %[wp0] offset:0\n" \
    "ds_read_b64_tr_b16 v[124:125], %[wp0] offset:2048\n" \
    "ds_read_b128 v[106:109], %[ap0] offset:57344\n" \
    "ds_read_b128 v[110:113], %[ap0] offset:59392\n" \
    "ds_read_b128 v[114:117], %[ap0] offset:61440\n" \
    "s_waitcnt lgkmcnt(10)\n" \
    "v_mfma_f32_16x16x32_bf16 v[38:41], v[126:129], v[98:101], 0\n" \
    "ds_read_b64_tr_b16 v[142:143], %[wp1] offset:34816\n" \
    "s_waitcnt lgkmcnt(9)\n" \
    "v_mfma_f32_16x16x32_bf16 v[58:61], v[98:101], v[130:133], 0\n" \
    "ds_read_b64_tr_b16 v[144:145], %[wp1] offset:36864\n" \
    "s_waitcnt lgkmcnt(8)\n" \
    "v_mfma_f32_16x16x32_bf16 v[78:81], v[134:137], v[98:101], 0\n" \
    "ds_read_b128 v[98:101], %[ap1] offset:53248\n" \
    "s_waitcnt lgkmcnt(8)\n" \
    "v_mfma_f32_16x16x32_bf16 v[42:45], v[126:129], v[102:105], 0\n" \
    "ds_read_b64_tr_b16 v[146:147], %[wp2] offset:34816\n" \
    "v_mfma_f32_16x16x32_bf16 v[62:65], v[102:105], v[130:133], 0\n" \
    "ds_read_b64_tr_b16 v[148:149], %[wp2] offset:36864\n" \
    "v_mfma_f32_16x16x32_bf16 v[82:85], v[134:137], v[102:105], 0\n" \
    "ds_read_b64_tr_b16 v[150:151], %[wp3] offset:34816\n" \
    "s_waitcnt lgkmcnt(9)\n" \
    "v_mfma_f32_16x16x32_bf16 v[22:25], v[122:125], v[102:105], 0\n" \
    "ds_read_b128 v[102:105], %[ap1] offset:55296\n" \
    "s_waitcnt lgkmcnt(9)\n" \
    "v_mfma_f32_16x16x32_bf16 v[46:49], v[126:129], v[106:109], 0\n" \
    "ds_read_b64_tr_b16 v[152:153], %[wp3] offset:36864\n" \
    "v_mfma_f32_16x16x32_bf16 v[66:69], v[106:109], v[130:133], 0\n" \
    "ds_read_b64_tr_b16 v[138:139], %[wp0] offset:34816\n" \
    "v_mfma_f32_16x16x32_bf16 v[86:89], v[134:137], v[106:109], 0\n" \
    "ds_read_b64_tr_b16 v[140:141], %[wp0] offset:36864\n" \
    "v_mfma_f32_16x16x32_bf16 v[26:29], v[122:125], v[106:109], 0\n" \
    "ds_read_b128 v[106:109], %[ap1] offset:57344\n" \
    "s_waitcnt lgkmcnt(12)\n" \
    "v_mfma_f32_16x16x32_bf16 v[50:53], v[126:129], v[110:113], 0\n" \
    "ds_read_b128 v[118:121], %[ap1] offset:61440\n" \
    "v_mfma_f32_16x16x32_bf16 v[70:73], v[110:113], v[130:133], 0\n" \
    "v_mfma_f32_16x16x32_bf16 v[90:93], v[134:137], v[110:113], 0\n" \
    "v_mfma_f32_16x16x32_bf16 v[30:33], v[122:125], v[110:113], 0\n" \
    "ds_read_b128 v[110:113], %[ap1] offset:59392\n" \
    "s_waitcnt lgkmcnt(13)\n" \
    "v_mfma_f32_16x16x32_bf16 v[54:57], v[126:129], v[114:117], 0\n" \
    "s_barrier\n" \
    "v_mfma_f32_16x16x32_bf16 v[74:77], v[114:117], v[130:133], 0\n" \
    "v_mfma_f32_16x16x32_bf16 v[94:97], v[134:137], v[114:117], 0\n" \
    "v_mfma_f32_16x16x32_bf16 v[34:37], v[122:125], v[114:117], 0\n" \
    "s_waitcnt lgkmcnt(10)\n" \
    "v_mfma_f32_16x16x32_bf16 v[38:41], v[142:145], v[98:101], v[38:41]\n" \
    "ds_read_b64_tr_b16 v[126:127], %[wp1] offset:51200\n" \
    "s_waitcnt lgkmcnt(9)\n" \
    "v_mfma_f32_16x16x32_bf16 v[58:61], v[98:101], v[146:149], v[58:61]\n" \
    "ds_read_b64_tr_b16 v[128:129], %[wp1] offset:53248\n" \
    "s_waitcnt lgkmcnt(7)\n" \
    "v_mfma_f32_16x16x32_bf16 v[78:81], v[150:153], v[98:101], v[78:81]\n" \
    "ds_read_b128 v[98:101], %[ap0] offset:0\n" \
    "v_mfma_f32_16x16x32_bf16 v[42:45], v[142:145], v[102:105], v[42:45]\n" \
    "ds_read_b64_tr_b16 v[130:131], %[wp2] offset:51200\n" \
    "v_mfma_f32_16x16x32_bf16 v[62:65], v[102:105], v[146:149], v[62:65]\n" \
    "ds_read_b64_tr_b16 v[132:133], %[wp2] offset:53248\n" \
    "v_mfma_f32_16x16x32_bf16 v[82:85], v[150:153], v[102:105], v[82:85]\n" \
    "ds_read_b64_tr_b16 v[134:135], %[wp3] offset:51200\n" \
    "s_waitcnt lgkmcnt(9)\n" \
    "v_mfma_f32_16x16x32_bf16 v[22:25], v[138:141], v[102:105], v[22:25]\n" \
    "ds_read_b128 v[102:105], %[ap0] offset:2048\n" \
    "s_waitcnt lgkmcnt(9)\n" \
    "v_mfma_f32_16x16x32_bf16 v[46:49], v[142:145], v[106:109], v[46:49]\n" \
    "ds_read_b64_tr_b16 v[136:137], %[wp3] offset:53248\n" \
    "v_mfma_f32_16x16x32_bf16 v[66:69], v[106:109], v[146:149], v[66:69]\n" \
    "ds_read_b64_tr_b16 v[122:123], %[wp0] offset:51200\n" \
    "v_mfma_f32_16x16x32_bf16 v[86:89], v[150:153], v[106:109], v[86:89]\n" \
    "ds_read_b64_tr_b16 v[124:125], %[wp0] offset:53248\n" \
    "v_mfma_f32_16x16x32_bf16 v[26:29], v[138:141], v[106:109], v[26:29]\n" \
    "ds_read_b128 v[106:109], %[ap0] offset:4096\n" \
    "s_waitcnt lgkmcnt(11)\n" \
    "v_mfma_f32_16x16x32_bf16 v[50:53], v[142:145], v[110:113], v[50:53]\n" \
    "ds_read_b128 v[114:117], %[ap0] offset:8192\n" \
    "v_mfma_f32_16x16x32_bf16 v[70:73], v[110:113], v[146:149], v[70:73]\n" \
    "v_mfma_f32_16x16x32_bf16 v[90:93], v[150:153], v[110:113], v[90:93]\n" \
    "v_mfma_f32_16x16x32_bf16 v[30:33], v[138:141], v[110:113], v[30:33]\n" \
    "ds_read_b128 v[110:113], %[ap0] offset:6144\n" \
    "v_mfma_f32_16x16x32_bf16 v[54:57], v[142:145], v[118:121], v[54:57]\n" \
    "s_barrier\n" \
    "v_mfma_f32_16x16x32_bf16 v[74:77], v[118:121], v[146:149], v[74:77]\n" \
    "v_mfma_f32_16x16x32_bf16 v[94:97], v[150:153], v[118:121], v[94:97]\n" \
    "v_mfma_f32_16x16x32_bf16 v[34:37], v[138:141], v[118:121], v[34:37]\n" \
    "s_waitcnt lgkmcnt(10)\n" \
    "v_mfma_f32_16x16x32_bf16 v[38:41], v[126:129], v[98:101], v[38:41]\n" \
    "ds_read_b64_tr_b16 v[142:143], v155 offset:0\n" \
    "s_waitcnt lgkmcnt(9)\n" \
    "v_mfma_f32_16x16x32_bf16 v[58:61], v[98:101], v[130:133], v[58:61]\n" \
    "ds_read_b64_tr_b16 v[144:145], v155 offset:2048\n" \
    "s_waitcnt lgkmcnt(7)\n" \
    "v_mfma_f32_16x16x32_bf16 v[78:81], v[134:137], v[98:101], v[78:81]\n" \
    "ds_read_b128 v[98:101], %[ap1] offset:0\n" \
    "v_mfma_f32_16x16x32_bf16 v[42:45], v[126:129], v[102:105], v[42:45]\n" \
    "ds_read_b64_tr_b16 v[146:147], v156 offset:0\n" \
    "v_mfma_f32_16x16x32_bf16 v[62:65], v[102:105], v[130:133], v[62:65]\n" \
    "ds_read_b64_tr_b16 v[148:149], v156 offset:2048\n" \
    "v_mfma_f32_16x16x32_bf16 v[82:85], v[134:137], v[102:105], v[82:85]\n" \
    "ds_read_b64_tr_b16 v[150:151], v157 offset:0\n" \
    "s_waitcnt lgkmcnt(9)\n" \
    "v_mfma_f32_16x16x32_bf16 v[22:25], v[122:125], v[102:105], v[22:25]\n" \
    "ds_read_b128 v[102:105], %[ap1] offset:2048\n" \
    "s_waitcnt lgkmcnt(9)\n" \
    "v_mfma_f32_16x16x32_bf16 v[46:49], v[126:129], v[106:109], v[46:49]\n" \
    "ds_read_b64_tr_b16 v[152:153], v157 offset:2048\n" \
    "v_mfma_f32_16x16x32_bf16 v[66:69], v[106:109], v[130:133], v[66:69]\n" \
    "ds_read_b64_tr_b16 v[138:139], v154 offset:0\n" \
    "v_mfma_f32_16x16x32_bf16 v[86:89], v[134:137], v[106:109], v[86:89]\n" \
    "ds_read_b64_tr_b16 v[140:141], v154 offset:2048\n" \
    "v_mfma_f32_16x16x32_bf16 v[26:29], v[122:125], v[106:109], v[26:29]\n" \
    "ds_read_b128 v[106:109], %[ap1] offset:4096\n" \
    "s_waitcnt lgkmcnt(11)\n" \
    "v_mfma_f32_16x16x32_bf16 v[50:53], v[126:129], v[110:113], v[50:53]\n" \
    "ds_read_b128 v[118:121], %[ap1] offset:8192\n" \
    "v_mfma_f32_16x16x32_bf16 v[70:73], v[110:113], v[130:133], v[70:73]\n" \
    "v_mfma_f32_16x16x32_bf16 v[90:93], v[134:137], v[110:113], v[90:93]\n" \
    "v_mfma_f32_16x16x32_bf16 v[30:33], v[122:125], v[110:113], v[30:33]\n" \
    "ds_read_b128 v[110:113], %[ap1] offset:6144\n" \
    "v_mfma_f32_16x16x32_bf16 v[54:57], v[126:129], v[114:117], v[54:57]\n" \
    "s_barrier\n" \
    "v_mfma_f32_16x16x32_bf16 v[74:77], v[114:117], v[130:133], v[74:77]\n" \
    "v_mfma_f32_16x16x32_bf16 v[94:97], v[134:137], v[114:117], v[94:97]\n" \
    "v_mfma_f32_16x16x32_bf16 v[34:37], v[122:125], v[114:117], v[34:37]\n" \
    "s_waitcnt lgkmcnt(10)\n" \
    "v_mfma_f32_16x16x32_bf16 v[38:41], v[142:145], v[98:101], v[38:41]\n" \
    "ds_read_b64_tr_b16 v[126:127], v155 offset:16384\n" \
    "s_waitcnt lgkmcnt(9)\n" \
    "v_mfma_f32_16x16x32_bf16 v[58:61], v[98:101], v[146:149], v[58:61]\n" \
    "ds_read_b64_tr_b16 v[128:129], v155 offset:18432\n" \
    "s_waitcnt lgkmcnt(7)\n" \
    "v_mfma_f32_16x16x32_bf16 v[78:81], v[150:153], v[98:101], v[78:81]\n" \
    "ds_read_b128 v[98:101], %[ap0] offset:18432\n" \
    "v_mfma_f32_16x16x32_bf16 v[42:45], v[142:145], v[102:105], v[42:45]\n" \
    "ds_read_b64_tr_b16 v[130:131], v156 offset:16384\n" \
    "v_mfma_f32_16x16x32_bf16 v[62:65], v[102:105], v[146:149], v[62:65]\n" \
    "ds_read_b64_tr_b16 v[132:133], v156 offset:18432\n" \
    "v_mfma_f32_16x16x32_bf16 v[82:85], v[150:153], v[102:105], v[82:85]\n" \
    "ds_read_b64_tr_b16 v[134:135], v157 offset:16384\n" \
    "s_waitcnt lgkmcnt(9)\n" \
    "v_mfma_f32_16x16x32_bf16 v[22:25], v[138:141], v[102:105], v[22:25]\n" \
    "ds_read_b128 v[102:105], %[ap0] offset:20480\n" \
    "s_waitcnt lgkmcnt(9)\n" \
    "v_mfma_f32_16x16x32_bf16 v[46:49], v[142:145], v[106:109], v[46:49]\n" \
    "ds_read_b64_tr_b16 v[136:137], v157 offset:18432\n" \
    "v_mfma_f32_16x16x32_bf16 v[66:69], v[106:109], v[146:149], v[66:69]\n" \
    "ds_read_b64_tr_b16 v[122:123], v154 offset:16384\n" \
    "v_mfma_f32_16x16x32_bf16 v[86:89], v[150:153], v[106:109], v[86:89]\n" \
    "ds_read_b64_tr_b16 v[124:125], v154 offset:18432\n" \
    "v_mfma_f32_16x16x32_bf16 v[26:29], v[138:141], v[106:109], v[26:29]\n" \
    "ds_read_b128 v[106:109], %[ap0] offset:22528\n" \
    "s_waitcnt lgkmcnt(11)\n" \
    "v_mfma_f32_16x16x32_bf16 v[50:53], v[142:145], v[110:113], v[50:53]\n" \
    "ds_read_b128 v[114:117], %[ap0] offset:26624\n" \
    "v_mfma_f32_16x16x32_bf16 v[70:73], v[110:113], v[146:149], v[70:73]\n" \
    "v_mfma_f32_16x16x32_bf16 v[90:93], v[150:153], v[110:113], v[90:93]\n" \
    "v_mfma_f32_16x16x32_bf16 v[30:33], v[138:141], v[110:113], v[30:33]\n" \
    "ds_read_b128 v[110:113], %[ap0] offset:24576\n" \
    "v_mfma_f32_16x16x32_bf16 v[54:57], v[142:145], v[118:121], v[54:57]\n" \
    "s_barrier\n" \
    "v_mfma_f32_16x16x32_bf16 v[74:77], v[118:121], v[146:149], v[74:77]\n" \
    "v_mfma_f32_16x16x32_bf16 v[94:97], v[150:153], v[118:121], v[94:97]\n" \
    "v_mfma_f32_16x16x32_bf16 v[34:37], v[138:141], v[118:121], v[34:37]\n" \
    "s_waitcnt lgkmcnt(10)\n" \
    "v_mfma_f32_16x16x32_bf16 v[38:41], v[126:129], v[98:101], v[38:41]\n" \
    "ds_read_b64_tr_b16 v[142:143], v155 offset:32768\n" \
    "s_waitcnt lgkmcnt(9)\n" \
    "v_mfma_f32_16x16x32_bf16 v[58:61], v[98:101], v[130:133], v[58:61]\n" \
    "ds_read_b64_tr_b16 v[144:145], v155 offset:34816\n" \
    "s_waitcnt lgkmcnt(7)\n" \
    "v_mfma_f32_16x16x32_bf16 v[78:81], v[134:137], v[98:101], v[78:81]\n" \
    "ds_read_b128 v[98:101], %[ap1] offset:18432\n" \
    "v_mfma_f32_16x16x32_bf16 v[42:45], v[126:129], v[102:105], v[42:45]\n" \
    "ds_read_b64_tr_b16 v[146:147], v156 offset:32768\n" \
    "v_mfma_f32_16x16x32_bf16 v[62:65], v[102:105], v[130:133], v[62:65]\n" \
    "ds_read_b64_tr_b16 v[148:149], v156 offset:34816\n" \
    "v_mfma_f32_16x16x32_bf16 v[82:85], v[134:137], v[102:105], v[82:85]\n" \
    "ds_read_b64_tr_b16 v[150:151], v157 offset:32768\n" \
    "s_waitcnt lgkmcnt(9)\n" \
    "v_mfma_f32_16x16x32_bf16 v[22:25], v[122:125], v[102:105], v[22:25]\n" \
    "ds_read_b128 v[102:105], %[ap1] offset:20480\n" \
    "s_waitcnt lgkmcnt(9)\n" \
    "v_mfma_f32_16x16x32_bf16 v[46:49], v[126:129], v[106:109], v[46:49]\n" \
    "ds_read_b64_tr_b16 v[152:153], v157 offset:34816\n" \
    "v_mfma_f32_16x16x32_bf16 v[66:69], v[106:109], v[130:133], v[66:69]\n" \
    "ds_read_b64_tr_b16 v[138:139], v154 offset:32768\n" \
    "v_mfma_f32_16x16x32_bf16 v[86:89], v[134:137], v[106:109], v[86:89]\n" \
    "ds_read_b64_tr_b16 v[140:141], v154 offset:34816\n" \
    "v_mfma_f32_16x16x32_bf16 v[26:29], v[122:125], v[106:109], v[26:29]\n" \
    "ds_read_b128 v[106:109], %[ap1] offset:22528\n" \
    "s_waitcnt lgkmcnt(11)\n" \
    "v_mfma_f32_16x16x32_bf16 v[50:53], v[126:129], v[110:113], v[50:53]\n" \
    "ds_read_b128 v[118:121], %[ap1] offset:26624\n" \
    "v_mfma_f32_16x16x32_bf16 v[70:73], v[110:113], v[130:133], v[70:73]\n" \
    "v_mfma_f32_16x16x32_bf16 v[90:93], v[134:137], v[110:113], v[90:93]\n" \
    "v_mfma_f32_16x16x32_bf16 v[30:33], v[122:125], v[110:113], v[30:33]\n" \
    "ds_read_b128 v[110:113], %[ap1] offset:24576\n" \
    "v_mfma_f32_16x16x32_bf16 v[54:57], v[126:129], v[114:117], v[54:57]\n" \
    "s_barrier\n" \
    "v_mfma_f32_16x16x32_bf16 v[74:77], v[114:117], v[130:133], v[74:77]\n" \
    "v_mfma_f32_16x16x32_bf16 v[94:97], v[134:137], v[114:117], v[94:97]\n" \
    "v_mfma_f32_16x16x32_bf16 v[34:37], v[122:125], v[114:117], v[34:37]\n" \
    "s_waitcnt lgkmcnt(10)\n" \
    "v_mfma_f32_16x16x32_bf16 v[38:41], v[142:145], v[98:101], v[38:41]\n" \
    "ds_read_b64_tr_b16 v[126:127], %[wp1] offset:0\n" \
    "s_waitcnt lgkmcnt(9)\n" \
    "v_mfma_f32_16x16x32_bf16 v[58:61], v[98:101], v[146:149], v[58:61]\n" \
    "ds_read_b64_tr_b16 v[128:129], %[wp1] offset:2048\n" \
    "s_waitcnt lgkmcnt(7)\n" \
    "v_mfma_f32_16x16x32_bf16 v[78:81], v[150:153], v[98:101], v[78:81]\n" \
    "ds_read_b128 v[98:101], %[ap0] offset:53248\n" \
    "v_mfma_f32_16x16x32_bf16 v[42:45], v[142:145], v[102:105], v[42:45]\n" \
    "ds_read_b64_tr_b16 v[130:131], %[wp2] offset:0\n" \
    "v_mfma_f32_16x16x32_bf16 v[62:65], v[102:105], v[146:149], v[62:65]\n" \
    "ds_read_b64_tr_b16 v[132:133], %[wp2] offset:2048\n" \
    "v_mfma_f32_16x16x32_bf16 v[82:85], v[150:153], v[102:105], v[82:85]\n" \
    "ds_read_b64_tr_b16 v[134:135], %[wp3] offset:0\n" \
    "s_waitcnt lgkmcnt(9)\n" \
    "v_mfma_f32_16x16x32_bf16 v[22:25], v[138:141], v[102:105], v[22:25]\n" \
    "ds_read_b128 v[102:105], %[ap0] offset:55296\n" \
    "s_waitcnt lgkmcnt(9)\n" \
    "v_mfma_f32_16x16x32_bf16 v[46:49], v[142:145], v[106:109], v[46:49]\n" \
    "ds_read_b64_tr_b16 v[136:137], %[wp3] offset:2048\n" \
    "v_mfma_f32_16x16x32_bf16 v[66:69], v[106:109], v[146:149], v[66:69]\n" \
    "ds_read_b64_tr_b16 v[122:123], %[wp0] offset:0\n" \
    "v_mfma_f32_16x16x32_bf16 v[86:89], v[150:153], v[106:109], v[86:89]\n" \
    "ds_read_b64_tr_b16 v[124:125], %[wp0] offset:2048\n" \
    "v_mfma_f32_16x16x32_bf16 v[26:29], v[138:141], v[106:109], v[26:29]\n" \
    "ds_read_b128 v[106:109], %[ap0] offset:57344\n" \
    "s_waitcnt lgkmcnt(11)\n" \
    "v_mfma_f32_16x16x32_bf16 v[50:53], v[142:145], v[110:113], v[50:53]\n" \
    "ds_read_b128 v[114:117], %[ap0] offset:61440\n" \
    "v_mfma_f32_16x16x32_bf16 v[70:73], v[110:113], v[146:149], v[70:73]\n" \
    "v_mfma_f32_16x16x32_bf16 v[90:93], v[150:153], v[110:113], v[90:93]\n" \
    "v_mfma_f32_16x16x32_bf16 v[30:33], v[138:141], v[110:113], v[30:33]\n" \
    "ds_read_b128 v[110:113], %[ap0] offset:59392\n" \
    "v_mfma_f32_16x16x32_bf16 v[54:57], v[142:145], v[118:121], v[54:57]\n" \
    "s_barrier\n" \
    "v_mfma_f32_16x16x32_bf16 v[74:77], v[118:121], v[146:149], v[74:77]\n" \
    "v_mfma_f32_16x16x32_bf16 v[94:97], v[150:153], v[118:121], v[94:97]\n" \
    "v_mfma_f32_16x16x32_bf16 v[34:37], v[138:141], v[118:121], v[34:37]\n" \
    "s_waitcnt lgkmcnt(10)\n" \
    "v_mfma_f32_16x16x32_bf16 v[38:41], v[126:129], v[98:101], v[38:41]\n" \
    "ds_read_b64_tr_b16 v[142:143], %[wp1] offset:34816\n" \
    "s_waitcnt lgkmcnt(9)\n" \
    "v_mfma_f32_16x16x32_bf16 v[58:61], v[98:101], v[130:133], v[58:61]\n" \
    "ds_read_b64_tr_b16 v[144:145], %[wp1] offset:36864\n" \
    "s_waitcnt lgkmcnt(7)\n" \
    "v_mfma_f32_16x16x32_bf16 v[78:81], v[134:137], v[98:101], v[78:81]\n" \
    "ds_read_b128 v[98:101], %[ap1] offset:53248\n" \
    "v_mfma_f32_16x16x32_bf16 v[42:45], v[126:129], v[102:105], v[42:45]\n" \
    "ds_read_b64_tr_b16 v[146:147], %[wp2] offset:34816\n" \
    "v_mfma_f32_16x16x32_bf16 v[62:65], v[102:105], v[130:133], v[62:65]\n" \
    "ds_read_b64_tr_b16 v[148:149], %[wp2] offset:36864\n" \
    "v_mfma_f32_16x16x32_bf16 v[82:85], v[134:137], v[102:105], v[82:85]\n" \
    "ds_read_b64_tr_b16 v[150:151], %[wp3] offset:34816\n" \
    "s_waitcnt lgkmcnt(9)\n" \
    "v_mfma_f32_16x16x32_bf16 v[22:25], v[122:125], v[102:105], v[22:25]\n" \
    "ds_read_b128 v[102:105], %[ap1] offset:55296\n" \
    "s_waitcnt lgkmcnt(9)\n" \
    "v_mfma_f32_16x16x32_bf16 v[46:49], v[126:129], v[106:109], v[46:49]\n" \
    "ds_read_b64_tr_b16 v[152:153], %[wp3] offset:36864\n" \
    "v_mfma_f32_16x16x32_bf16 v[66:69], v[106:109], v[130:133], v[66:69]\n" \
    "ds_read_b64_tr_b16 v[138:139], %[wp0] offset:34816\n" \
    "v_mfma_f32_16x16x32_bf16 v[86:89], v[134:137], v[106:109], v[86:89]\n" \
    "ds_read_b64_tr_b16 v[140:141], %[wp0] offset:36864\n" \
    "v_mfma_f32_16x16x32_bf16 v[26:29], v[122:125], v[106:109], v[26:29]\n" \
    "ds_read_b128 v[106:109], %[ap1] offset:57344\n" \
    "s_waitcnt lgkmcnt(11)\n" \
    "v_mfma_f32_16x16x32_bf16 v[50:53], v[126:129], v[110:113], v[50:53]\n" \
    "ds_read_b128 v[118:121], %[ap1] offset:61440\n" \
    "v_mfma_f32_16x16x32_bf16 v[70:73], v[110:113], v[130:133], v[70:73]\n" \
    "v_mfma_f32_16x16x32_bf16 v[90:93], v[134:137], v[110:113], v[90:93]\n" \
    "v_mfma_f32_16x16x32_bf16 v[30:33], v[122:125], v[110:113], v[30:33]\n" \
    "ds_read_b128 v[110:113], %[ap1] offset:59392\n" \
    "v_mfma_f32_16x16x32_bf16 v[54:57], v[126:129], v[114:117], v[54:57]\n" \
    "s_barrier\n" \
    "v_mfma_f32_16x16x32_bf16 v[74:77], v[114:117], v[130:133], v[74:77]\n" \
    "v_mfma_f32_16x16x32_bf16 v[94:97], v[134:137], v[114:117], v[94:97]\n" \
    "v_mfma_f32_16x16x32_bf16 v[34:37], v[122:125], v[114:117], v[34:37]\n" \
    "s_waitcnt lgkmcnt(10)\n" \
    "v_mfma_f32_16x16x32_bf16 v[38:41], v[142:145], v[98:101], v[38:41]\n" \
    "ds_read_b64_tr_b16 v[126:127], %[wp1] offset:51200\n" \
    "s_waitcnt lgkmcnt(9)\n" \
    "v_mfma_f32_16x16x32_bf16 v[58:61], v[98:101], v[146:149], v[58:61]\n" \
    "ds_read_b64_tr_b16 v[128:129], %[wp1] offset:53248\n" \
    "s_waitcnt lgkmcnt(7)\n" \
    "v_mfma_f32_16x16x32_bf16 v[78:81], v[150:153], v[98:101], v[78:81]\n" \
    "ds_read_b128 v[98:101], %[ap0] offset:0\n" \
    "v_mfma_f32_16x16x32_bf16 v[42:45], v[142:145], v[102:105], v[42:45]\n" \
    "ds_read_b64_tr_b16 v[130:131], %[wp2] offset:51200\n" \
    "v_mfma_f32_16x16x32_bf16 v[62:65], v[102:105], v[146:149], v[62:65]\n" \
    "ds_read_b64_tr_b16 v[132:133], %[wp2] offset:53248\n" \
    "v_mfma_f32_16x16x32_bf16 v[82:85], v[150:153], v[102:105], v[82:85]\n" \
    "ds_read_b64_tr_b16 v[134:135], %[wp3] offset:51200\n" \
    "s_waitcnt lgkmcnt(9)\n" \
    "v_mfma_f32_16x16x32_bf16 v[22:25], v[138:141], v[102:105], v[22:25]\n" \
    "ds_read_b128 v[102:105], %[ap0] offset:2048\n" \
    "s_waitcnt lgkmcnt(9)\n" \
    "v_mfma_f32_16x16x32_bf16 v[46:49], v[142:145], v[106:109], v[46:49]\n" \
    "ds_read_b64_tr_b16 v[136:137], %[wp3] offset:53248\n" \
    "v_mfma_f32_16x16x32_bf16 v[66:69], v[106:109], v[146:149], v[66:69]\n" \
    "ds_read_b64_tr_b16 v[122:123], %[wp0] offset:51200\n" \
    "v_mfma_f32_16x16x32_bf16 v[86:89], v[150:153], v[106:109], v[86:89]\n" \
    "ds_read_b64_tr_b16 v[124:125], %[wp0] offset:53248\n" \
    "v_mfma_f32_16x16x32_bf16 v[26:29], v[138:141], v[106:109], v[26:29]\n" \
    "ds_read_b128 v[106:109], %[ap0] offset:4096\n" \
    "s_waitcnt lgkmcnt(11)\n" \
    "v_mfma_f32_16x16x32_bf16 v[50:53], v[142:145], v[110:113], v[50:53]\n" \
    "ds_read_b128 v[114:117], %[ap0] offset:8192\n" \
    "v_mfma_f32_16x16x32_bf16 v[70:73], v[110:113], v[146:149], v[70:73]\n" \
    "v_mfma_f32_16x16x32_bf16 v[90:93], v[150:153], v[110:113], v[90:93]\n" \
    "v_mfma_f32_16x16x32_bf16 v[30:33], v[138:141], v[110:113], v[30:33]\n" \
    "ds_read_b128 v[110:113], %[ap0] offset:6144\n" \
    "v_mfma_f32_16x16x32_bf16 v[54:57], v[142:145], v[118:121], v[54:57]\n" \
    "s_barrier\n" \
    "v_mfma_f32_16x16x32_bf16 v[74:77], v[118:121], v[146:149], v[74:77]\n" \
    "v_mfma_f32_16x16x32_bf16 v[94:97], v[150:153], v[118:121], v[94:97]\n" \
    "v_mfma_f32_16x16x32_bf16 v[34:37], v[138:141], v[118:121], v[34:37]\n" \
    "s_waitcnt lgkmcnt(10)\n" \
    "v_mfma_f32_16x16x32_bf16 v[38:41], v[126:129], v[98:101], v[38:41]\n" \
    "ds_read_b64_tr_b16 v[142:143], v155 offset:0\n" \
    "s_waitcnt lgkmcnt(9)\n" \
    "v_mfma_f32_16x16x32_bf16 v[58:61], v[98:101], v[130:133], v[58:61]\n" \
    "ds_read_b64_tr_b16 v[144:145], v155 offset:2048\n" \
    "s_waitcnt lgkmcnt(7)\n" \
    "v_mfma_f32_16x16x32_bf16 v[78:81], v[134:137], v[98:101], v[78:81]\n" \
    "ds_read_b128 v[98:101], %[ap1] offset:0\n" \
    "v_mfma_f32_16x16x32_bf16 v[42:45], v[126:129], v[102:105], v[42:45]\n" \
    "ds_read_b64_tr_b16 v[146:147], v156 offset:0\n" \
    "v_mfma_f32_16x16x32_bf16 v[62:65], v[102:105], v[130:133], v[62:65]\n" \
    "ds_read_b64_tr_b16 v[148:149], v156 offset:2048\n" \
    "v_mfma_f32_16x16x32_bf16 v[82:85], v[134:137], v[102:105], v[82:85]\n" \
    "ds_read_b64_tr_b16 v[150:151], v157 offset:0\n" \
    "s_waitcnt lgkmcnt(9)\n" \
    "v_mfma_f32_16x16x32_bf16 v[22:25], v[122:125], v[102:105], v[22:25]\n" \
    "ds_read_b128 v[102:105], %[ap1] offset:2048\n" \
    "s_waitcnt lgkmcnt(9)\n" \
    "v_mfma_f32_16x16x32_bf16 v[46:49], v[126:129], v[106:109], v[46:49]\n" \
    "ds_read_b64_tr_b16 v[152:153], v157 offset:2048\n" \
    "v_mfma_f32_16x16x32_bf16 v[66:69], v[106:109], v[130:133], v[66:69]\n" \
    "ds_read_b64_tr_b16 v[138:139], v154 offset:0\n" \
    "v_mfma_f32_16x16x32_bf16 v[86:89], v[134:137], v[106:109], v[86:89]\n" \
    "ds_read_b64_tr_b16 v[140:141], v154 offset:2048\n" \
    "v_mfma_f32_16x16x32_bf16 v[26:29], v[122:125], v[106:109], v[26:29]\n" \
    "ds_read_b128 v[106:109], %[ap1] offset:4096\n" \
    "s_waitcnt lgkmcnt(11)\n" \
    "v_mfma_f32_16x16x32_bf16 v[50:53], v[126:129], v[110:113], v[50:53]\n" \
    "ds_read_b128 v[118:121], %[ap1] offset:8192\n" \
    "v_mfma_f32_16x16x32_bf16 v[70:73], v[110:113], v[130:133], v[70:73]\n" \
    "v_mfma_f32_16x16x32_bf16 v[90:93], v[134:137], v[110:113], v[90:93]\n" \
    "v_mfma_f32_16x16x32_bf16 v[30:33], v[122:125], v[110:113], v[30:33]\n" \
    "ds_read_b128 v[110:113], %[ap1] offset:6144\n" \
    "v_mfma_f32_16x16x32_bf16 v[54:57], v[126:129], v[114:117], v[54:57]\n" \
    "s_barrier\n" \
    "v_mfma_f32_16x16x32_bf16 v[74:77], v[114:117], v[130:133], v[74:77]\n" \
    "v_mfma_f32_16x16x32_bf16 v[94:97], v[134:137], v[114:117], v[94:97]\n" \
    "v_mfma_f32_16x16x32_bf16 v[34:37], v[122:125], v[114:117], v[34:37]\n" \
    "s_waitcnt lgkmcnt(10)\n" \
    "v_mfma_f32_16x16x32_bf16 v[38:41], v[142:145], v[98:101], v[38:41]\n" \
    "ds_read_b64_tr_b16 v[126:127], v155 offset:16384\n" \
    "s_waitcnt lgkmcnt(9)\n" \
    "v_mfma_f32_16x16x32_bf16 v[58:61], v[98:101], v[146:149], v[58:61]\n" \
    "ds_read_b64_tr_b16 v[128:129], v155 offset:18432\n" \
    "s_waitcnt lgkmcnt(7)\n" \
    "v_mfma_f32_16x16x32_bf16 v[78:81], v[150:153], v[98:101], v[78:81]\n" \
    "ds_read_b128 v[98:101], %[ap0] offset:18432\n" \
    "v_mfma_f32_16x16x32_bf16 v[42:45], v[142:145], v[102:105], v[42:45]\n" \
    "ds_read_b64_tr_b16 v[130:131], v156 offset:16384\n" \
    "v_mfma_f32_16x16x32_bf16 v[62:65], v[102:105], v[146:149], v[62:65]\n" \
    "ds_read_b64_tr_b16 v[132:133], v156 offset:18432\n" \
    "v_mfma_f32_16x16x32_bf16 v[82:85], v[150:153], v[102:105], v[82:85]\n" \
    "ds_read_b64_tr_b16 v[134:135], v157 offset:16384\n" \
    "s_waitcnt lgkmcnt(9)\n" \
    "v_mfma_f32_16x16x32_bf16 v[22:25], v[138:141], v[102:105], v[22:25]\n" \
    "ds_read_b128 v[102:105], %[ap0] offset:20480\n" \
    "s_waitcnt lgkmcnt(9)\n" \
    "v_mfma_f32_16x16x32_bf16 v[46:49], v[142:145], v[106:109], v[46:49]\n" \
    "ds_read_b64_tr_b16 v[136:137], v157 offset:18432\n" \
    "v_mfma_f32_16x16x32_bf16 v[66:69], v[106:109], v[146:149], v[66:69]\n" \
    "ds_read_b64_tr_b16 v[122:123], v154 offset:16384\n" \
    "v_mfma_f32_16x16x32_bf16 v[86:89], v[150:153], v[106:109], v[86:89]\n" \
    "ds_read_b64_tr_b16 v[124:125], v154 offset:18432\n" \
    "v_mfma_f32_16x16x32_bf16 v[26:29], v[138:141], v[106:109], v[26:29]\n" \
    "ds_read_b128 v[106:109], %[ap0] offset:22528\n" \
    "s_waitcnt lgkmcnt(11)\n" \
    "v_mfma_f32_16x16x32_bf16 v[50:53], v[142:145], v[110:113], v[50:53]\n" \
    "ds_read_b128 v[114:117], %[ap0] offset:26624\n" \
    "v_mfma_f32_16x16x32_bf16 v[70:73], v[110:113], v[146:149], v[70:73]\n" \
    "v_mfma_f32_16x16x32_bf16 v[90:93], v[150:153], v[110:113], v[90:93]\n" \
    "v_mfma_f32_16x16x32_bf16 v[30:33], v[138:141], v[110:113], v[30:33]\n" \
    "ds_read_b128 v[110:113], %[ap0] offset:24576\n" \
    "v_mfma_f32_16x16x32_bf16 v[54:57], v[142:145], v[118:121], v[54:57]\n" \
    "s_barrier\n" \
    "v_mfma_f32_16x16x32_bf16 v[74:77], v[118:121], v[146:149], v[74:77]\n" \
    "v_mfma_f32_16x16x32_bf16 v[94:97], v[150:153], v[118:121], v[94:97]\n" \
    "v_mfma_f32_16x16x32_bf16 v[34:37], v[138:141], v[118:121], v[34:37]\n" \
    "s_waitcnt lgkmcnt(10)\n" \
    "v_mfma_f32_16x16x32_bf16 v[38:41], v[126:129], v[98:101], v[38:41]\n" \
    "ds_read_b64_tr_b16 v[142:143], v155 offset:32768\n" \
    "s_waitcnt lgkmcnt(9)\n" \
    "v_mfma_f32_16x16x32_bf16 v[58:61], v[98:101], v[130:133], v[58:61]\n" \
    "ds_read_b64_tr_b16 v[144:145], v155 offset:34816\n" \
    "s_waitcnt lgkmcnt(7)\n" \
    "v_mfma_f32_16x16x32_bf16 v[78:81], v[134:137], v[98:101], v[78:81]\n" \
    "ds_read_b128 v[98:101], %[ap1] offset:18432\n" \
    "v_mfma_f32_16x16x32_bf16 v[42:45], v[126:129], v[102:105], v[42:45]\n" \
    "ds_read_b64_tr_b16 v[146:147], v156 offset:32768\n" \
    "v_mfma_f32_16x16x32_bf16 v[62:65], v[102:105], v[130:133], v[62:65]\n" \
    "ds_read_b64_tr_b16 v[148:149], v156 offset:34816\n" \
    "v_mfma_f32_16x16x32_bf16 v[82:85], v[134:137], v[102:105], v[82:85]\n" \
    "ds_read_b64_tr_b16 v[150:151], v157 offset:32768\n" \
    "s_waitcnt lgkmcnt(9)\n" \
    "v_mfma_f32_16x16x32_bf16 v[22:25], v[122:125], v[102:105], v[22:25]\n" \
    "ds_read_b128 v[102:105], %[ap1] offset:20480\n" \
    "s_waitcnt lgkmcnt(9)\n" \
    "v_mfma_f32_16x16x32_bf16 v[46:49], v[126:129], v[106:109], v[46:49]\n" \
    "ds_read_b64_tr_b16 v[152:153], v157 offset:34816\n" \
    "v_mfma_f32_16x16x32_bf16 v[66:69], v[106:109], v[130:133], v[66:69]\n" \
    "ds_read_b64_tr_b16 v[138:139], v154 offset:32768\n" \
    "v_mfma_f32_16x16x32_bf16 v[86:89], v[134:137], v[106:109], v[86:89]\n" \
    "ds_read_b64_tr_b16 v[140:141], v154 offset:34816\n" \
    "v_mfma_f32_16x16x32_bf16 v[26:29], v[122:125], v[106:109], v[26:29]\n" \
    "ds_read_b128 v[106:109], %[ap1] offset:22528\n" \
    "s_waitcnt lgkmcnt(11)\n" \
    "v_mfma_f32_16x16x32_bf16 v[50:53], v[126:129], v[110:113], v[50:53]\n" \
    "ds_read_b128 v[118:121], %[ap1] offset:26624\n" \
    "v_mfma_f32_16x16x32_bf16 v[70:73], v[110:113], v[130:133], v[70:73]\n" \
    "v_mfma_f32_16x16x32_bf16 v[90:93], v[134:137], v[110:113], v[90:93]\n" \
    "v_mfma_f32_16x16x32_bf16 v[30:33], v[122:125], v[110:113], v[30:33]\n" \
    "ds_read_b128 v[110:113], %[ap1] offset:24576\n" \
    "v_mfma_f32_16x16x32_bf16 v[54:57], v[126:129], v[114:117], v[54:57]\n" \
    "s_barrier\n" \
    "v_mfma_f32_16x16x32_bf16 v[74:77], v[114:117], v[130:133], v[74:77]\n" \
    "v_mfma_f32_16x16x32_bf16 v[94:97], v[134:137], v[114:117], v[94:97]\n" \
    "v_mfma_f32_16x16x32_bf16 v[34:37], v[122:125], v[114:117], v[34:37]\n" \
    "s_waitcnt lgkmcnt(10)\n" \
    "v_mfma_f32_16x16x32_bf16 v[38:41], v[142:145], v[98:101], v[38:41]\n" \
    "ds_read_b64_tr_b16 v[126:127], %[wp1] offset:0\n" \
    "s_waitcnt lgkmcnt(9)\n" \
    "v_mfma_f32_16x16x32_bf16 v[58:61], v[98:101], v[146:149], v[58:61]\n" \
    "ds_read_b64_tr_b16 v[128:129], %[wp1] offset:2048\n" \
    "s_waitcnt lgkmcnt(7)\n" \
    "v_mfma_f32_16x16x32_bf16 v[78:81], v[150:153], v[98:101], v[78:81]\n" \
    "ds_read_b128 v[98:101], %[ap0] offset:53248\n" \
    "v_mfma_f32_16x16x32_bf16 v[42:45], v[142:145], v[102:105], v[42:45]\n" \
    "ds_read_b64_tr_b16 v[130:131], %[wp2] offset:0\n" \
    "v_mfma_f32_16x16x32_bf16 v[62:65], v[102:105], v[146:149], v[62:65]\n" \
    "ds_read_b64_tr_b16 v[132:133], %[wp2] offset:2048\n" \
    "v_mfma_f32_16x16x32_bf16 v[82:85], v[150:153], v[102:105], v[82:85]\n" \
    "ds_read_b64_tr_b16 v[134:135], %[wp3] offset:0\n" \
    "s_waitcnt lgkmcnt(9)\n" \
    "v_mfma_f32_16x16x32_bf16 v[22:25], v[138:141], v[102:105], v[22:25]\n" \
    "ds_read_b128 v[102:105], %[ap0] offset:55296\n" \
    "s_waitcnt lgkmcnt(9)\n" \
    "v_mfma_f32_16x16x32_bf16 v[46:49], v[142:145], v[106:109], v[46:49]\n" \
    "ds_read_b64_tr_b16 v[136:137], %[wp3] offset:2048\n" \
    "v_mfma_f32_16x16x32_bf16 v[66:69], v[106:109], v[146:149], v[66:69]\n" \
    "ds_read_b64_tr_b16 v[122:123], %[wp0] offset:0\n" \
    "v_mfma_f32_16x16x32_bf16 v[86:89], v[150:153], v[106:109], v[86:89]\n" \
    "ds_read_b64_tr_b16 v[124:125], %[wp0] offset:2048\n" \
    "v_mfma_f32_16x16x32_bf16 v[26:29], v[138:141], v[106:109], v[26:29]\n" \
    "ds_read_b128 v[106:109], %[ap0] offset:57344\n" \
    "s_waitcnt lgkmcnt(11)\n" \
    "v_mfma_f32_16x16x32_bf16 v[50:53], v[142:145], v[110:113], v[50:53]\n" \
    "ds_read_b128 v[114:117], %[ap0] offset:61440\n" \
    "v_mfma_f32_16x16x32_bf16 v[70:73], v[110:113], v[146:149], v[70:73]\n" \
    "v_mfma_f32_16x16x32_bf16 v[90:93], v[150:153], v[110:113], v[90:93]\n" \
    "v_mfma_f32_16x16x32_bf16 v[30:33], v[138:141], v[110:113], v[30:33]\n" \
    "ds_read_b128 v[110:113], %[ap0] offset:59392\n" \
    "v_mfma_f32_16x16x32_bf16 v[54:57], v[142:145], v[118:121], v[54:57]\n" \
    "s_barrier\n" \
    "v_mfma_f32_16x16x32_bf16 v[74:77], v[118:121], v[146:149], v[74:77]\n" \
    "v_mfma_f32_16x16x32_bf16 v[94:97], v[150:153], v[118:121], v[94:97]\n" \
    "v_mfma_f32_16x16x32_bf16 v[34:37], v[138:141], v[118:121], v[34:37]\n" \
    "s_waitcnt lgkmcnt(10)\n" \
    "v_mfma_f32_16x16x32_bf16 v[38:41], v[126:129], v[98:101], v[38:41]\n" \
    "ds_read_b64_tr_b16 v[142:143], %[wp1] offset:34816\n" \
    "s_waitcnt lgkmcnt(9)\n" \
    "v_mfma_f32_16x16x32_bf16 v[58:61], v[98:101], v[130:133], v[58:61]\n" \
    "ds_read_b64_tr_b16 v[144:145], %[wp1] offset:36864\n" \
    "s_waitcnt lgkmcnt(7)\n" \
    "v_mfma_f32_16x16x32_bf16 v[78:81], v[134:137], v[98:101], v[78:81]\n" \
    "ds_read_b128 v[98:101], %[ap1] offset:53248\n" \
    "v_mfma_f32_16x16x32_bf16 v[42:45], v[126:129], v[102:105], v[42:45]\n" \
    "ds_read_b64_tr_b16 v[146:147], %[wp2] offset:34816\n" \
    "v_mfma_f32_16x16x32_bf16 v[62:65], v[102:105], v[130:133], v[62:65]\n" \
    "ds_read_b64_tr_b16 v[148:149], %[wp2] offset:36864\n" \
    "v_mfma_f32_16x16x32_bf16 v[82:85], v[134:137], v[102:105], v[82:85]\n" \
    "ds_read_b64_tr_b16 v[150:151], %[wp3] offset:34816\n" \
    "s_waitcnt lgkmcnt(9)\n" \
    "v_mfma_f32_16x16x32_bf16 v[22:25], v[122:125], v[102:105], v[22:25]\n" \
    "ds_read_b128 v[102:105], %[ap1] offset:55296\n" \
    "s_waitcnt lgkmcnt(9)\n" \
    "v_mfma_f32_16x16x32_bf16 v[46:49], v[126:129], v[106:109], v[46:49]\n" \
    "ds_read_b64_tr_b16 v[152:153], %[wp3] offset:36864\n" \
    "v_mfma_f32_16x16x32_bf16 v[66:69], v[106:109], v[130:133], v[66:69]\n" \
    "ds_read_b64_tr_b16 v[138:139], %[wp0] offset:34816\n" \
    "v_mfma_f32_16x16x32_bf16 v[86:89], v[134:137], v[106:109], v[86:89]\n" \
    "ds_read_b64_tr_b16 v[140:141], %[wp0] offset:36864\n" \
    "v_mfma_f32_16x16x32_bf16 v[26:29], v[122:125], v[106:109], v[26:29]\n" \
    "ds_read_b128 v[106:109], %[ap1] offset:57344\n" \
    "s_waitcnt lgkmcnt(11)\n" \
    "v_mfma_f32_16x16x32_bf16 v[50:53], v[126:129], v[110:113], v[50:53]\n" \
    "ds_read_b128 v[118:121], %[ap1] offset:61440\n" \
    "v_mfma_f32_16x16x32_bf16 v[70:73], v[110:113], v[130:133], v[70:73]\n" \
    "v_mfma_f32_16x16x32_bf16 v[90:93], v[134:137], v[110:113], v[90:93]\n" \
    "v_mfma_f32_16x16x32_bf16 v[30:33], v[122:125], v[110:113], v[30:33]\n" \
    "ds_read_b128 v[110:113], %[ap1] offset:59392\n" \
    "v_mfma_f32_16x16x32_bf16 v[54:57], v[126:129], v[114:117], v[54:57]\n" \
    "s_barrier\n" \
    "v_mfma_f32_16x16x32_bf16 v[74:77], v[114:117], v[130:133], v[74:77]\n" \
    "v_mfma_f32_16x16x32_bf16 v[94:97], v[134:137], v[114:117], v[94:97]\n" \
    "v_mfma_f32_16x16x32_bf16 v[34:37], v[122:125], v[114:117], v[34:37]\n" \
    "s_waitcnt lgkmcnt(10)\n" \
    "v_mfma_f32_16x16x32_bf16 v[38:41], v[142:145], v[98:101], v[38:41]\n" \
    "ds_read_b64_tr_b16 v[126:127], %[wp1] offset:51200\n" \
    "s_waitcnt lgkmcnt(9)\n" \
    "v_mfma_f32_16x16x32_bf16 v[58:61], v[98:101], v[146:149], v[58:61]\n" \
    "ds_read_b64_tr_b16 v[128:129], %[wp1] offset:53248\n" \
    "s_waitcnt lgkmcnt(7)\n" \
    "v_mfma_f32_16x16x32_bf16 v[78:81], v[150:153], v[98:101], v[78:81]\n" \
    "ds_read_b128 v[98:101], %[ap0] offset:0\n" \
    "v_mfma_f32_16x16x32_bf16 v[42:45], v[142:145], v[102:105], v[42:45]\n" \
    "ds_read_b64_tr_b16 v[130:131], %[wp2] offset:51200\n" \
    "v_mfma_f32_16x16x32_bf16 v[62:65], v[102:105], v[146:149], v[62:65]\n" \
    "ds_read_b64_tr_b16 v[132:133], %[wp2] offset:53248\n" \
    "v_mfma_f32_16x16x32_bf16 v[82:85], v[150:153], v[102:105], v[82:85]\n" \
    "ds_read_b64_tr_b16 v[134:135], %[wp3] offset:51200\n" \
    "s_waitcnt lgkmcnt(9)\n" \
    "v_mfma_f32_16x16x32_bf16 v[22:25], v[138:141], v[102:105], v[22:25]\n" \
    "ds_read_b128 v[102:105], %[ap0] offset:2048\n" \
    "s_waitcnt lgkmcnt(9)\n" \
    "v_mfma_f32_16x16x32_bf16 v[46:49], v[142:145], v[106:109], v[46:49]\n" \
    "ds_read_b64_tr_b16 v[136:137], %[wp3] offset:53248\n" \
    "v_mfma_f32_16x16x32_bf16 v[66:69], v[106:109], v[146:149], v[66:69]\n" \
    "ds_read_b64_tr_b16 v[122:123], %[wp0] offset:51200\n" \
    "v_mfma_f32_16x16x32_bf16 v[86:89], v[150:153], v[106:109], v[86:89]\n" \
    "ds_read_b64_tr_b16 v[124:125], %[wp0] offset:53248\n" \
    "v_mfma_f32_16x16x32_bf16 v[26:29], v[138:141], v[106:109], v[26:29]\n" \
    "ds_read_b128 v[106:109], %[ap0] offset:4096\n" \
    "s_waitcnt lgkmcnt(11)\n" \
    "v_mfma_f32_16x16x32_bf16 v[50:53], v[142:145], v[110:113], v[50:53]\n" \
    "ds_read_b128 v[114:117], %[ap0] offset:8192\n" \
    "v_mfma_f32_16x16x32_bf16 v[70:73], v[110:113], v[146:149], v[70:73]\n" \
    "v_mfma_f32_16x16x32_bf16 v[90:93], v[150:153], v[110:113], v[90:93]\n" \
    "v_mfma_f32_16x16x32_bf16 v[30:33], v[138:141], v[110:113], v[30:33]\n" \
    "ds_read_b128 v[110:113], %[ap0] offset:6144\n" \
    "v_mfma_f32_16x16x32_bf16 v[54:57], v[142:145], v[118:121], v[54:57]\n" \
    "s_barrier\n" \
    "v_mfma_f32_16x16x32_bf16 v[74:77], v[118:121], v[146:149], v[74:77]\n" \
    "v_mfma_f32_16x16x32_bf16 v[94:97], v[150:153], v[118:121], v[94:97]\n" \
    "v_mfma_f32_16x16x32_bf16 v[34:37], v[138:141], v[118:121], v[34:37]\n" \
    "s_waitcnt lgkmcnt(10)\n" \
    "v_mfma_f32_16x16x32_bf16 v[38:41], v[126:129], v[98:101], v[38:41]\n" \
    "ds_read_b64_tr_b16 v[142:143], v155 offset:0\n" \
    "s_waitcnt lgkmcnt(9)\n" \
    "v_mfma_f32_16x16x32_bf16 v[58:61], v[98:101], v[130:133], v[58:61]\n" \
    "ds_read_b64_tr_b16 v[144:145], v155 offset:2048\n" \
    "s_waitcnt lgkmcnt(7)\n" \
    "v_mfma_f32_16x16x32_bf16 v[78:81], v[134:137], v[98:101], v[78:81]\n" \
    "ds_read_b128 v[98:101], %[ap1] offset:0\n" \
    "v_mfma_f32_16x16x32_bf16 v[42:45], v[126:129], v[102:105], v[42:45]\n" \
    "ds_read_b64_tr_b16 v[146:147], v156 offset:0\n" \
    "v_mfma_f32_16x16x32_bf16 v[62:65], v[102:105], v[130:133], v[62:65]\n" \
    "ds_read_b64_tr_b16 v[148:149], v156 offset:2048\n" \
    "v_mfma_f32_16x16x32_bf16 v[82:85], v[134:137], v[102:105], v[82:85]\n" \
    "ds_read_b64_tr_b16 v[150:151], v157 offset:0\n" \
    "s_waitcnt lgkmcnt(9)\n" \
    "v_mfma_f32_16x16x32_bf16 v[22:25], v[122:125], v[102:105], v[22:25]\n" \
    "ds_read_b128 v[102:105], %[ap1] offset:2048\n" \
    "s_waitcnt lgkmcnt(9)\n" \
    "v_mfma_f32_16x16x32_bf16 v[46:49], v[126:129], v[106:109], v[46:49]\n" \
    "ds_read_b64_tr_b16 v[152:153], v157 offset:2048\n" \
    "v_mfma_f32_16x16x32_bf16 v[66:69], v[106:109], v[130:133], v[66:69]\n" \
    "ds_read_b64_tr_b16 v[138:139], v154 offset:0\n" \
    "v_mfma_f32_16x16x32_bf16 v[86:89], v[134:137], v[106:109], v[86:89]\n" \
    "ds_read_b64_tr_b16 v[140:141], v154 offset:2048\n" \
    "v_mfma_f32_16x16x32_bf16 v[26:29], v[122:125], v[106:109], v[26:29]\n" \
    "ds_read_b128 v[106:109], %[ap1] offset:4096\n" \
    "s_waitcnt lgkmcnt(11)\n" \
    "v_mfma_f32_16x16x32_bf16 v[50:53], v[126:129], v[110:113], v[50:53]\n" \
    "ds_read_b128 v[118:121], %[ap1] offset:8192\n" \
    "v_mfma_f32_16x16x32_bf16 v[70:73], v[110:113], v[130:133], v[70:73]\n" \
    "v_mfma_f32_16x16x32_bf16 v[90:93], v[134:137], v[110:113], v[90:93]\n" \
    "v_mfma_f32_16x16x32_bf16 v[30:33], v[122:125], v[110:113], v[30:33]\n" \
    "ds_read_b128 v[110:113], %[ap1] offset:6144\n" \
    "v_mfma_f32_16x16x32_bf16 v[54:57], v[126:129], v[114:117], v[54:57]\n" \
    "s_barrier\n" \
    "v_mfma_f32_16x16x32_bf16 v[74:77], v[114:117], v[130:133], v[74:77]\n" \
    "v_mfma_f32_16x16x32_bf16 v[94:97], v[134:137], v[114:117], v[94:97]\n" \
    "v_mfma_f32_16x16x32_bf16 v[34:37], v[122:125], v[114:117], v[34:37]\n" \
    "s_waitcnt lgkmcnt(10)\n" \
    "v_mfma_f32_16x16x32_bf16 v[38:41], v[142:145], v[98:101], v[38:41]\n" \
    "ds_read_b64_tr_b16 v[126:127], v155 offset:16384\n" \
    "s_waitcnt lgkmcnt(9)\n" \
    "v_mfma_f32_16x16x32_bf16 v[58:61], v[98:101], v[146:149], v[58:61]\n" \
    "ds_read_b64_tr_b16 v[128:129], v155 offset:18432\n" \
    "s_waitcnt lgkmcnt(7)\n" \
    "v_mfma_f32_16x16x32_bf16 v[78:81], v[150:153], v[98:101], v[78:81]\n" \
    "ds_read_b128 v[98:101], %[ap0] offset:18432\n" \
    "v_mfma_f32_16x16x32_bf16 v[42:45], v[142:145], v[102:105], v[42:45]\n" \
    "ds_read_b64_tr_b16 v[130:131], v156 offset:16384\n" \
    "v_mfma_f32_16x16x32_bf16 v[62:65], v[102:105], v[146:149], v[62:65]\n" \
    "ds_read_b64_tr_b16 v[132:133], v156 offset:18432\n" \
    "v_mfma_f32_16x16x32_bf16 v[82:85], v[150:153], v[102:105], v[82:85]\n" \
    "ds_read_b64_tr_b16 v[134:135], v157 offset:16384\n" \
    "s_waitcnt lgkmcnt(9)\n" \
    "v_mfma_f32_16x16x32_bf16 v[22:25], v[138:141], v[102:105], v[22:25]\n" \
    "ds_read_b128 v[102:105], %[ap0] offset:20480\n" \
    "s_waitcnt lgkmcnt(9)\n" \
    "v_mfma_f32_16x16x32_bf16 v[46:49], v[142:145], v[106:109], v[46:49]\n" \
    "ds_read_b64_tr_b16 v[136:137], v157 offset:18432\n" \
    "v_mfma_f32_16x16x32_bf16 v[66:69], v[106:109], v[146:149], v[66:69]\n" \
    "ds_read_b64_tr_b16 v[122:123], v154 offset:16384\n" \
    "v_mfma_f32_16x16x32_bf16 v[86:89], v[150:153], v[106:109], v[86:89]\n" \
    "ds_read_b64_tr_b16 v[124:125], v154 offset:18432\n" \
    "v_mfma_f32_16x16x32_bf16 v[26:29], v[138:141], v[106:109], v[26:29]\n" \
    "ds_read_b128 v[106:109], %[ap0] offset:22528\n" \
    "s_waitcnt lgkmcnt(11)\n" \
    "v_mfma_f32_16x16x32_bf16 v[50:53], v[142:145], v[110:113], v[50:53]\n" \
    "ds_read_b128 v[114:117], %[ap0] offset:26624\n" \
    "v_mfma_f32_16x16x32_bf16 v[70:73], v[110:113], v[146:149], v[70:73]\n" \
    "v_mfma_f32_16x16x32_bf16 v[90:93], v[150:153], v[110:113], v[90:93]\n" \
    "v_mfma_f32_16x16x32_bf16 v[30:33], v[138:141], v[110:113], v[30:33]\n" \
    "ds_read_b128 v[110:113], %[ap0] offset:24576\n" \
    "v_mfma_f32_16x16x32_bf16 v[54:57], v[142:145], v[118:121], v[54:57]\n" \
    "s_barrier\n" \
    "v_mfma_f32_16x16x32_bf16 v[74:77], v[118:121], v[146:149], v[74:77]\n" \
    "v_mfma_f32_16x16x32_bf16 v[94:97], v[150:153], v[118:121], v[94:97]\n" \
    "v_mfma_f32_16x16x32_bf16 v[34:37], v[138:141], v[118:121], v[34:37]\n" \
    "s_waitcnt lgkmcnt(10)\n" \
    "v_mfma_f32_16x16x32_bf16 v[38:41], v[126:129], v[98:101], v[38:41]\n" \
    "ds_read_b64_tr_b16 v[142:143], v155 offset:32768\n" \
    "s_waitcnt lgkmcnt(9)\n" \
    "v_mfma_f32_16x16x32_bf16 v[58:61], v[98:101], v[130:133], v[58:61]\n" \
    "ds_read_b64_tr_b16 v[144:145], v155 offset:34816\n" \
    "s_waitcnt lgkmcnt(7)\n" \
    "v_mfma_f32_16x16x32_bf16 v[78:81], v[134:137], v[98:101], v[78:81]\n" \
    "ds_read_b128 v[98:101], %[ap1] offset:18432\n" \
    "v_mfma_f32_16x16x32_bf16 v[42:45], v[126:129], v[102:105], v[42:45]\n" \
    "ds_read_b64_tr_b16 v[146:147], v156 offset:32768\n" \
    "v_mfma_f32_16x16x32_bf16 v[62:65], v[102:105], v[130:133], v[62:65]\n" \
    "ds_read_b64_tr_b16 v[148:149], v156 offset:34816\n" \
    "v_mfma_f32_16x16x32_bf16 v[82:85], v[134:137], v[102:105], v[82:85]\n" \
    "ds_read_b64_tr_b16 v[150:151], v157 offset:32768\n" \
    "s_waitcnt lgkmcnt(9)\n" \
    "v_mfma_f32_16x16x32_bf16 v[22:25], v[122:125], v[102:105], v[22:25]\n" \
    "ds_read_b128 v[102:105], %[ap1] offset:20480\n" \
    "s_waitcnt lgkmcnt(9)\n" \
    "v_mfma_f32_16x16x32_bf16 v[46:49], v[126:129], v[106:109], v[46:49]\n" \
    "ds_read_b64_tr_b16 v[152:153], v157 offset:34816\n" \
    "v_mfma_f32_16x16x32_bf16 v[66:69], v[106:109], v[130:133], v[66:69]\n" \
    "ds_read_b64_tr_b16 v[138:139], v154 offset:32768\n" \
    "v_mfma_f32_16x16x32_bf16 v[86:89], v[134:137], v[106:109], v[86:89]\n" \
    "ds_read_b64_tr_b16 v[140:141], v154 offset:34816\n" \
    "v_mfma_f32_16x16x32_bf16 v[26:29], v[122:125], v[106:109], v[26:29]\n" \
    "ds_read_b128 v[106:109], %[ap1] offset:22528\n" \
    "s_waitcnt lgkmcnt(11)\n" \
    "v_mfma_f32_16x16x32_bf16 v[50:53], v[126:129], v[110:113], v[50:53]\n" \
    "ds_read_b128 v[118:121], %[ap1] offset:26624\n" \
    "v_mfma_f32_16x16x32_bf16 v[70:73], v[110:113], v[130:133], v[70:73]\n" \
    "v_mfma_f32_16x16x32_bf16 v[90:93], v[134:137], v[110:113], v[90:93]\n" \
    "v_mfma_f32_16x16x32_bf16 v[30:33], v[122:125], v[110:113], v[30:33]\n" \
    "ds_read_b128 v[110:113], %[ap1] offset:24576\n" \
    "v_mov_b32 v161, 16\n" \
    "ds_write_b32 %[fr], v161\n" \
    "v_mfma_f32_16x16x32_bf16 v[54:57], v[126:129], v[114:117], v[54:57]\n" \
    "s_barrier\n" \
    "v_mfma_f32_16x16x32_bf16 v[74:77], v[114:117], v[130:133], v[74:77]\n" \
    "v_mfma_f32_16x16x32_bf16 v[94:97], v[134:137], v[114:117], v[94:97]\n" \
    "v_mfma_f32_16x16x32_bf16 v[34:37], v[122:125], v[114:117], v[34:37]\n" \
    "s_waitcnt lgkmcnt(11)\n" \
    "v_mfma_f32_16x16x32_bf16 v[38:41], v[142:145], v[98:101], v[38:41]\n" \
    "ds_read_b64_tr_b16 v[126:127], %[wp1] offset:0\n" \
    "s_waitcnt lgkmcnt(10)\n" \
    "v_mfma_f32_16x16x32_bf16 v[58:61], v[98:101], v[146:149], v[58:61]\n" \
    "ds_read_b64_tr_b16 v[128:129], %[wp1] offset:2048\n" \
    "s_waitcnt lgkmcnt(8)\n" \
    "v_mfma_f32_16x16x32_bf16 v[78:81], v[150:153], v[98:101], v[78:81]\n" \
    "ds_read_b128 v[98:101], %[ap0] offset:53248\n" \
    "v_mfma_f32_16x16x32_bf16 v[42:45], v[142:145], v[102:105], v[42:45]\n" \
    "ds_read_b64_tr_b16 v[130:131], %[wp2] offset:0\n" \
    "v_mfma_f32_16x16x32_bf16 v[62:65], v[102:105], v[146:149], v[62:65]\n" \
    "ds_read_b64_tr_b16 v[132:133], %[wp2] offset:2048\n" \
    "v_mfma_f32_16x16x32_bf16 v[82:85], v[150:153], v[102:105], v[82:85]\n" \
    "ds_read_b64_tr_b16 v[134:135], %[wp3] offset:0\n" \
    "s_waitcnt lgkmcnt(10)\n" \
    "v_mfma_f32_16x16x32_bf16 v[22:25], v[138:141], v[102:105], v[22:25]\n" \
    "ds_read_b128 v[102:105], %[ap0] offset:55296\n" \
    "s_waitcnt lgkmcnt(10)\n" \
    "v_mfma_f32_16x16x32_bf16 v[46:49], v[142:145], v[106:109], v[46:49]\n" \
    "ds_read_b64_tr_b16 v[136:137], %[wp3] offset:2048\n" \
    "v_mfma_f32_16x16x32_bf16 v[66:69], v[106:109], v[146:149], v[66:69]\n" \
    "ds_read_b64_tr_b16 v[122:123], %[wp0] offset:0\n" \
    "v_mfma_f32_16x16x32_bf16 v[86:89], v[150:153], v[106:109], v[86:89]\n" \
    "ds_read_b64_tr_b16 v[124:125], %[wp0] offset:2048\n" \
    "v_mfma_f32_16x16x32_bf16 v[26:29], v[138:141], v[106:109], v[26:29]\n" \
    "ds_read_b128 v[106:109], %[ap0] offset:57344\n" \
    "s_waitcnt lgkmcnt(12)\n" \
    "v_mfma_f32_16x16x32_bf16 v[50:53], v[142:145], v[110:113], v[50:53]\n" \
    "ds_read_b128 v[114:117], %[ap0] offset:61440\n" \
    "v_mfma_f32_16x16x32_bf16 v[70:73], v[110:113], v[146:149], v[70:73]\n" \
    "v_mfma_f32_16x16x32_bf16 v[90:93], v[150:153], v[110:113], v[90:93]\n" \
    "v_mfma_f32_16x16x32_bf16 v[30:33], v[138:141], v[110:113], v[30:33]\n" \
    "ds_read_b128 v[110:113], %[ap0] offset:59392\n" \
    "v_mov_b32 v161, 17\n" \
    "ds_write_b32 %[fr], v161\n" \
    "v_mfma_f32_16x16x32_bf16 v[54:57], v[142:145], v[118:121], v[54:57]\n" \
    "s_barrier\n" \
    "ds_read_b64_tr_b16 v[142:143], %[wp1] offset:34816\n" \
    "ds_read_b64_tr_b16 v[144:145], %[wp1] offset:36864\n" \
    "v_mfma_f32_16x16x32_bf16 v[74:77], v[118:121], v[146:149], v[74:77]\n" \
    "ds_read_b64_tr_b16 v[146:147], %[wp2] offset:34816\n" \
    "ds_read_b64_tr_b16 v[148:149], %[wp2] offset:36864\n" \
    "v_mfma_f32_16x16x32_bf16 v[94:97], v[150:153], v[118:121], v[94:97]\n" \
    "ds_read_b64_tr_b16 v[150:151], %[wp3] offset:34816\n" \
    "ds_read_b64_tr_b16 v[152:153], %[wp3] offset:36864\n" \
    "v_mfma_f32_16x16x32_bf16 v[34:37], v[138:141], v[118:121], v[34:37]\n" \
    "ds_read_b128 v[118:121], %[ap1] offset:61440\n" \
    "ds_read_b64_tr_b16 v[138:139], %[wp0] offset:34816\n" \
    "ds_read_b64_tr_b16 v[140:141], %[wp0] offset:36864\n" \
    "s_waitcnt lgkmcnt(11)\n" \
    "v_mfma_f32_16x16x32_bf16 v[54:57], v[126:129], v[114:117], v[54:57]\n" \
    "v_add_u32 %[wp0], %[tabc], %[l16]\n" \
    "v_cndmask_b32_e64 %[wp2], 0, -1, s[68:69]\n" \
    "v_mfma_f32_16x16x32_bf16 v[74:77], v[114:117], v[130:133], v[74:77]\n" \
    "v_add_u32 %[wp1], %[hxc], %[l16]\n" \
    "v_lshrrev_b32 %[wp3], 2, %[l16]\n" \
    "v_mfma_f32_16x16x32_bf16 v[94:97], v[134:137], v[114:117], v[94:97]\n" \
    "v_add_u32 %[wp3], %[hxd], %[wp3]\n" \
    "v_mfma_f32_16x16x32_bf16 v[34:37], v[122:125], v[114:117], v[34:37]\n" \
    "s_waitcnt lgkmcnt(10)\n" \
    "v_mfma_f32_16x16x32_bf16 v[50:53], v[126:129], v[110:113], v[50:53]\n" \
    "v_mfma_f32_16x16x32_bf16 v[70:73], v[110:113], v[130:133], v[70:73]\n" \
    "v_mfma_f32_16x16x32_bf16 v[90:93], v[134:137], v[110:113], v[90:93]\n" \
    "v_mfma_f32_16x16x32_bf16 v[30:33], v[122:125], v[110:113], v[30:33]\n" \
    "ds_read_b128 v[110:113], %[ap1] offset:59392\n" \
    "s_waitcnt lgkmcnt(3)\n" \
    "v_mfma_f32_16x16x32_bf16 v[54:57], v[142:145], v[118:121], v[54:57]\n" \
    "v_cndmask_b32_e64 v154, %[zr], %[vxb], s[60:61]\n" \
    "v_cndmask_b32_e64 v155, %[zr], %[vxb], s[62:63]\n" \
    "v_mfma_f32_16x16x32_bf16 v[74:77], v[118:121], v[146:149], v[74:77]\n" \
    "v_cndmask_b32_e64 v156, %[zr], %[vxb], s[64:65]\n" \
    "v_cndmask_b32_e64 v157, %[zr], %[vxb], s[66:67]\n" \
    "v_mfma_f32_16x16x32_bf16 v[94:97], v[150:153], v[118:121], v[94:97]\n" \
    "s_waitcnt lgkmcnt(1)\n" \
    "v_mfma_f32_16x16x32_bf16 v[34:37], v[138:141], v[118:121], v[34:37]\n" \
    "v_mfma_f32_16x16x32_bf16 v[46:49], v[126:129], v[106:109], v[46:49]\n" \
    "v_mfma_f32_16x16x32_bf16 v[66:69], v[106:109], v[130:133], v[66:69]\n" \
    "v_cvt_pk_bf16_f32 v54, v54, v55\n" \
    "v_cvt_pk_bf16_f32 v55, v56, v57\n" \
    "v_mfma_f32_16x16x32_bf16 v[86:89], v[134:137], v[106:109], v[86:89]\n" \
    "v_cvt_pk_bf16_f32 v120, v74, v75\n" \
    "v_cvt_pk_bf16_f32 v121, v76, v77\n" \
    "v_mfma_f32_16x16x32_bf16 v[26:29], v[122:125], v[106:109], v[26:29]\n" \
    "ds_read_b128 v[106:109], %[ap1] offset:57344\n" \
    "v_cvt_pk_bf16_f32 v94, v94, v95\n" \
    "v_cvt_pk_bf16_f32 v56, v96, v97\n" \
    "ds_write2_b32 %[scw], v94, v56 offset0:128 offset1:129\n" \
    "s_waitcnt lgkmcnt(2)\n" \
    "v_mfma_f32_16x16x32_bf16 v[50:53], v[142:145], v[110:113], v[50:53]\n" \
    "v_permlane16_swap_b32 v94, v56\n" \
    "v_mov_b32 v161, 1\n" \
    "ds_write_b64 %[wp1], v[54:55]\n" \
    "ds_write_b64 %[wp1], v[120:121] offset:8\n" \
    "ds_write_b32 %[wp3], v56\n" \
    "ds_write_b32 %[fr], v161 offset:144\n" \
    "v_mfma_f32_16x16x32_bf16 v[70:73], v[110:113], v[146:149], v[70:73]\n" \
    "v_cvt_pk_bf16_f32 v34, v34, v35\n" \
    "v_cvt_pk_bf16_f32 v35, v36, v37\n" \
    "v_mfma_f32_16x16x32_bf16 v[90:93], v[150:153], v[110:113], v[90:93]\n" \
    "v_and_b32 v36, v94, %[wp2]\n" \
    "v_mov_b32 v37, 0\n" \
    "v_mov_b32 v57, 0\n" \
    "v_mfma_f32_16x16x32_bf16 v[30:33], v[138:141], v[110:113], v[30:33]\n" \
    "v_mfma_f32_16x16x32_bf16 v[42:45], v[126:129], v[102:105], v[42:45]\n" \
    "v_mfma_f32_16x16x32_bf16 v[62:65], v[102:105], v[130:133], v[62:65]\n" \
    "v_cvt_pk_bf16_f32 v50, v50, v51\n" \
    "v_cvt_pk_bf16_f32 v51, v52, v53\n" \
    "v_mfma_f32_16x16x32_bf16 v[82:85], v[134:137], v[102:105], v[82:85]\n" \
    "v_cvt_pk_bf16_f32 v118, v70, v71\n" \
    "v_cvt_pk_bf16_f32 v119, v72, v73\n" \
    "v_mfma_f32_16x16x32_bf16 v[22:25], v[122:125], v[102:105], v[22:25]\n" \
    "ds_read_b128 v[102:105], %[ap1] offset:55296\n" \
    "v_cvt_pk_bf16_f32 v90, v90, v91\n" \
    "v_cvt_pk_bf16_f32 v52, v92, v93\n" \
    "ds_write2_b32 %[scw], v90, v52 offset0:96 offset1:97\n" \
    "s_waitcnt lgkmcnt(7)\n" \
    "v_mfma_f32_16x16x32_bf16 v[46:49], v[142:145], v[106:109], v[46:49]\n" \
    "v_permlane16_swap_b32 v90, v52\n" \
    "v_mfma_f32_16x16x32_bf16 v[66:69], v[106:109], v[146:149], v[66:69]\n" \
    "v_cvt_pk_bf16_f32 v30, v30, v31\n" \
    "v_cvt_pk_bf16_f32 v31, v32, v33\n" \
    "v_mfma_f32_16x16x32_bf16 v[86:89], v[150:153], v[106:109], v[86:89]\n" \
    "v_and_b32 v32, v90, %[wp2]\n" \
    "v_mov_b32 v33, 0\n" \
    "v_mov_b32 v53, 0\n" \
    "v_mfma_f32_16x16x32_bf16 v[26:29], v[138:141], v[106:109], v[26:29]\n" \
    "v_mfma_f32_16x16x32_bf16 v[38:41], v[126:129], v[98:101], v[38:41]\n" \
    "v_mfma_f32_16x16x32_bf16 v[58:61], v[98:101], v[130:133], v[58:61]\n" \
    "v_cvt_pk_bf16_f32 v46, v46, v47\n" \
    "v_cvt_pk_bf16_f32 v47, v48, v49\n" \
    "v_mfma_f32_16x16x32_bf16 v[78:81], v[134:137], v[98:101], v[78:81]\n" \
    "ds_read_b128 v[98:101], %[ap1] offset:53248\n" \
    "v_mov_b32 v161, 18\n" \
    "ds_write_b32 %[fr], v161\n" \
    "ds_read_b128 v[126:129], %[wp0] offset:1024\n" \
    "ds_read_b128 v[122:125], %[wp0]\n" \
    "ds_read_b128 v[130:133], %[wp0] offset:2048\n" \
    "v_cvt_pk_bf16_f32 v116, v66, v67\n" \
    "v_cvt_pk_bf16_f32 v117, v68, v69\n" \
    "s_waitcnt lgkmcnt(6)\n" \
    "v_mfma_f32_16x16x32_bf16 v[42:45], v[142:145], v[102:105], v[42:45]\n" \
    "v_cvt_pk_bf16_f32 v86, v86, v87\n" \
    "v_cvt_pk_bf16_f32 v48, v88, v89\n" \
    "ds_write2_b32 %[scw], v86, v48 offset0:64 offset1:65\n" \
    "v_mfma_f32_16x16x32_bf16 v[62:65], v[102:105], v[146:149], v[62:65]\n" \
    "v_permlane16_swap_b32 v86, v48\n" \
    "v_mfma_f32_16x16x32_bf16 v[82:85], v[150:153], v[102:105], v[82:85]\n" \
    "v_cvt_pk_bf16_f32 v26, v26, v27\n" \
    "v_cvt_pk_bf16_f32 v27, v28, v29\n" \
    "v_mfma_f32_16x16x32_bf16 v[22:25], v[138:141], v[102:105], v[22:25]\n" \
    "v_and_b32 v28, v86, %[wp2]\n" \
    "v_mov_b32 v29, 0\n" \
    "v_mov_b32 v49, 0\n" \
    "s_waitcnt lgkmcnt(5)\n" \
    "v_mfma_f32_16x16x32_bf16 v[38:41], v[142:145], v[98:101], v[38:41]\n" \
    "v_mfma_f32_16x16x32_bf16 v[58:61], v[98:101], v[146:149], v[58:61]\n" \
    "v_cvt_pk_bf16_f32 v42, v42, v43\n" \
    "v_cvt_pk_bf16_f32 v43, v44, v45\n" \
    "v_mfma_f32_16x16x32_bf16 v[78:81], v[150:153], v[98:101], v[78:81]\n" \
    "ds_read_b32 v159, %[zr] offset:1332\n" \
    "s_setprio 0\n" \
    "s_waitcnt lgkmcnt(4)\n" \
    "v_mfma_f32_16x16x32_bf16 v[74:77], v[54:57], v[34:37], v[126:129]\n" \
    "v_cvt_pk_bf16_f32 v82, v82, v83\n" \
    "v_cvt_pk_bf16_f32 v44, v84, v85\n" \
    "ds_write2_b32 %[scw], v82, v44 offset0:32 offset1:33\n" \
    "s_waitcnt lgkmcnt(4)\n" \
    "v_mfma_f32_16x16x32_bf16 v[94:97], v[50:53], v[34:37], v[122:125]\n" \
    "v_cvt_pk_bf16_f32 v114, v62, v63\n" \
    "v_cvt_pk_bf16_f32 v115, v64, v65\n" \
    "v_permlane16_swap_b32 v82, v44\n" \
    "v_mfma_f32_16x16x32_bf16 v[70:73], v[50:53], v[30:33], v[126:129]\n" \
    "v_cvt_pk_bf16_f32 v22, v22, v23\n" \
    "v_cvt_pk_bf16_f32 v23, v24, v25\n" \
    "v_and_b32 v24, v82, %[wp2]\n" \
    "v_mov_b32 v25, 0\n" \
    "v_mov_b32 v45, 0\n" \
    "v_mfma_f32_16x16x32_bf16 v[90:93], v[46:49], v[30:33], v[122:125]\n" \
    "v_cvt_pk_bf16_f32 v38, v38, v39\n" \
    "v_cvt_pk_bf16_f32 v39, v40, v41\n" \
    "v_cvt_pk_bf16_f32 v78, v78, v79\n" \
    "v_cvt_pk_bf16_f32 v40, v80, v81\n" \
    "ds_write2_b32 %[scw], v78, v40 offset0:0 offset1:1\n" \
    "v_mfma_f32_16x16x32_bf16 v[66:69], v[46:49], v[26:29], v[126:129]\n" \
    "v_mov_b32 v41, 0\n" \
    "v_permlane16_swap_b32 v78, v40\n" \
    "v_maximum3_f32 v144, v74, v75, v75\n" \
    "v_maximum3_f32 v145, v76, v77, v77\n" \
    "v_mfma_f32_16x16x32_bf16 v[86:89], v[42:45], v[26:29], v[122:125]\n" \
    "v_cvt_pk_bf16_f32 v112, v58, v59\n" \
    "v_cvt_pk_bf16_f32 v113, v60, v61\n" \
    "ds_read_b64_tr_b16 v[106:107], v157 offset:384\n" \
    "ds_read_b64_tr_b16 v[108:109], v157 offset:512\n" \
    "v_mfma_f32_16x16x32_bf16 v[62:65], v[42:45], v[22:25], v[126:129]\n" \
    "v_maximum3_f32 v144, v94, v95, v144\n" \
    "v_maximum3_f32 v145, v96, v97, v145\n" \
    "ds_read_b64_tr_b16 v[102:103], v156 offset:256\n" \
    "ds_read_b64_tr_b16 v[104:105], v156 offset:384\n" \
    "s_cmp_eq_u32 %[rb2], 0\n" \
    "s_cbranch_scc1 Lz_%=\n" \
    "v_mfma_f32_16x16x32_bf16 v[82:85], v[38:41], v[22:25], v[122:125]\n" \
    "s_branch Ly_%=\n" \
    "Lz_%=:\n" \
    "s_nop 7\n" \
    "v_mov_b32 v82, 0xff61b1e6\n" \
    "v_mov_b32 v83, 0xff61b1e6\n" \
    "v_mov_b32 v84, 0xff61b1e6\n" \
    "v_mov_b32 v85, 0xff61b1e6\n" \
    "Ly_%=:\n" \
    "v_maximum3_f32 v142, v70, v71, v71\n" \
    "v_maximum3_f32 v143, v72, v73, v73\n" \
    "ds_read_b64_tr_b16 v[98:99], v155 offset:128\n" \
    "ds_read_b64_tr_b16 v[100:101], v155 offset:256\n" \
    "v_maximum3_f32 v142, v90, v91, v142\n" \
    "v_maximum3_f32 v143, v92, v93, v143\n" \
    "ds_read_b64_tr_b16 v[134:135], v154 offset:0\n" \
    "ds_read_b64_tr_b16 v[136:137], v154 offset:128\n" \
    "v_maximum3_f32 v149, v144, v145, v145\n" \
    "v_maximum3_f32 v140, v66, v67, v67\n" \
    "v_maximum3_f32 v141, v68, v69, v69\n" \
    "v_maximum3_f32 v148, v142, v143, v143\n" \
    "v_maximum3_f32 v140, v86, v87, v140\n" \
    "v_maximum3_f32 v141, v88, v89, v141\n" \
    "v_maximum3_f32 v138, v62, v63, v63\n" \
    "v_maximum3_f32 v139, v64, v65, v65\n" \
    "v_maximum3_f32 v147, v140, v141, v141\n" \
    "v_maximum3_f32 v138, v82, v83, v138\n" \
    "v_maximum3_f32 v139, v84, v85, v139\n" \
    "v_maximum3_f32 v146, v138, v139, v139\n" \
    "s_waitcnt lgkmcnt(8)\n" \
    "s_branch Lend_%=\n" \
    "Llate_%=:\n" \
    "s_barrier\n" \
    "ds_read_b64_tr_b16 v[126:127], %[wp1] offset:0\n" \
    "ds_read_b64_tr_b16 v[128:129], %[wp1] offset:2048\n" \
    "ds_read_b128 v[102:105], %[ap0] offset:55296\n" \
    "ds_read_b64_tr_b16 v[130:131], %[wp2] offset:0\n" \
    "ds_read_b64_tr_b16 v[132:133], %[wp2] offset:2048\n" \
    "ds_read_b64_tr_b16 v[134:135], %[wp3] offset:0\n" \
    "ds_read_b64_tr_b16 v[136:137], %[wp3] offset:2048\n" \
    "ds_read_b64_tr_b16 v[122:123], %[wp0] offset:0\n" \
    "ds_read_b64_tr_b16 v[124:125], %[wp0] offset:2048\n" \
    "ds_read_b128 v[106:109], %[ap0] offset:57344\n" \
    "ds_read_b128 v[110:113], %[ap0] offset:59392\n" \
    "ds_read_b128 v[114:117], %[ap0] offset:61440\n" \
    "s_waitcnt lgkmcnt(9)\n" \
    "v_mfma_f32_16x16x32_bf16 v[42:45], v[126:129], v[102:105], 0\n" \
    "ds_read_b64_tr_b16 v[142:143], %[wp1] offset:34816\n" \
    "s_waitcnt lgkmcnt(8)\n" \
    "v_mfma_f32_16x16x32_bf16 v[62:65], v[102:105], v[130:133], 0\n" \
    "ds_read_b64_tr_b16 v[144:145], %[wp1] offset:36864\n" \
    "s_waitcnt lgkmcnt(7)\n" \
    "v_mfma_f32_16x16x32_bf16 v[82:85], v[134:137], v[102:105], 0\n" \
    "ds_read_b64_tr_b16 v[146:147], %[wp2] offset:34816\n" \
    "s_waitcnt lgkmcnt(6)\n" \
    "v_mfma_f32_16x16x32_bf16 v[22:25], v[122:125], v[102:105], 0\n" \
    "ds_read_b128 v[102:105], %[ap1] offset:55296\n" \
    "s_waitcnt lgkmcnt(6)\n" \
    "v_mfma_f32_16x16x32_bf16 v[46:49], v[126:129], v[106:109], 0\n" \
    "ds_read_b64_tr_b16 v[148:149], %[wp2] offset:36864\n" \
    "v_mfma_f32_16x16x32_bf16 v[66:69], v[106:109], v[130:133], 0\n" \
    "ds_read_b64_tr_b16 v[150:151], %[wp3] offset:34816\n" \
    "v_mfma_f32_16x16x32_bf16 v[86:89], v[134:137], v[106:109], 0\n" \
    "ds_read_b64_tr_b16 v[152:153], %[wp3] offset:36864\n" \
    "v_mfma_f32_16x16x32_bf16 v[26:29], v[122:125], v[106:109], 0\n" \
    "ds_read_b128 v[106:109], %[ap1] offset:57344\n" \
    "s_waitcnt lgkmcnt(9)\n" \
    "v_mfma_f32_16x16x32_bf16 v[50:53], v[126:129], v[110:113], 0\n" \
    "ds_read_b64_tr_b16 v[138:139], %[wp0] offset:34816\n" \
    "v_mfma_f32_16x16x32_bf16 v[70:73], v[110:113], v[130:133], 0\n" \
    "ds_read_b64_tr_b16 v[140:141], %[wp0] offset:36864\n" \
    "v_mfma_f32_16x16x32_bf16 v[90:93], v[134:137], v[110:113], 0\n" \
    "ds_read_b128 v[118:121], %[ap1] offset:61440\n" \
    "v_mfma_f32_16x16x32_bf16 v[30:33], v[122:125], v[110:113], 0\n" \
    "ds_read_b128 v[110:113], %[ap1] offset:59392\n" \
    "s_waitcnt lgkmcnt(12)\n" \
    "v_mfma_f32_16x16x32_bf16 v[54:57], v[126:129], v[114:117], 0\n" \
    "s_barrier\n" \
    "v_mfma_f32_16x16x32_bf16 v[74:77], v[114:117], v[130:133], 0\n" \
    "v_mfma_f32_16x16x32_bf16 v[94:97], v[134:137], v[114:117], 0\n" \
    "v_mfma_f32_16x16x32_bf16 v[34:37], v[122:125], v[114:117], 0\n" \
    "s_waitcnt lgkmcnt(8)\n" \
    "v_mfma_f32_16x16x32_bf16 v[42:45], v[142:145], v[102:105], v[42:45]\n" \
    "ds_read_b64_tr_b16 v[126:127], %[wp1] offset:51200\n" \
    "s_waitcnt lgkmcnt(8)\n" \
    "v_mfma_f32_16x16x32_bf16 v[62:65], v[102:105], v[146:149], v[62:65]\n" \
    "ds_read_b64_tr_b16 v[128:129], %[wp1] offset:53248\n" \
    "s_waitcnt lgkmcnt(7)\n" \
    "v_mfma_f32_16x16x32_bf16 v[82:85], v[150:153], v[102:105], v[82:85]\n" \
    "ds_read_b64_tr_b16 v[130:131], %[wp2] offset:51200\n" \
    "s_waitcnt lgkmcnt(5)\n" \
    "v_mfma_f32_16x16x32_bf16 v[22:25], v[138:141], v[102:105], v[22:25]\n" \
    "ds_read_b128 v[102:105], %[ap0] offset:2048\n" \
    "v_mfma_f32_16x16x32_bf16 v[46:49], v[142:145], v[106:109], v[46:49]\n" \
    "ds_read_b64_tr_b16 v[132:133], %[wp2] offset:53248\n" \
    "v_mfma_f32_16x16x32_bf16 v[66:69], v[106:109], v[146:149], v[66:69]\n" \
    "ds_read_b64_tr_b16 v[134:135], %[wp3] offset:51200\n" \
    "v_mfma_f32_16x16x32_bf16 v[86:89], v[150:153], v[106:109], v[86:89]\n" \
    "ds_read_b64_tr_b16 v[136:137], %[wp3] offset:53248\n" \
    "v_mfma_f32_16x16x32_bf16 v[26:29], v[138:141], v[106:109], v[26:29]\n" \
    "ds_read_b128 v[106:109], %[ap0] offset:4096\n" \
    "s_waitcnt lgkmcnt(8)\n" \
    "v_mfma_f32_16x16x32_bf16 v[50:53], v[142:145], v[110:113], v[50:53]\n" \
    "ds_read_b64_tr_b16 v[122:123], %[wp0] offset:51200\n" \
    "v_mfma_f32_16x16x32_bf16 v[70:73], v[110:113], v[146:149], v[70:73]\n" \
    "ds_read_b64_tr_b16 v[124:125], %[wp0] offset:53248\n" \
    "v_mfma_f32_16x16x32_bf16 v[90:93], v[150:153], v[110:113], v[90:93]\n" \
    "ds_read_b128 v[114:117], %[ap0] offset:8192\n" \
    "v_mfma_f32_16x16x32_bf16 v[30:33], v[138:141], v[110:113], v[30:33]\n" \
    "ds_read_b128 v[110:113], %[ap0] offset:6144\n" \
    "v_mfma_f32_16x16x32_bf16 v[54:57], v[142:145], v[118:121], v[54:57]\n" \
    "s_barrier\n" \
    "v_mfma_f32_16x16x32_bf16 v[74:77], v[118:121], v[146:149], v[74:77]\n" \
    "v_mfma_f32_16x16x32_bf16 v[94:97], v[150:153], v[118:121], v[94:97]\n" \
    "v_mfma_f32_16x16x32_bf16 v[34:37], v[138:141], v[118:121], v[34:37]\n" \
    "s_waitcnt lgkmcnt(8)\n" \
    "v_mfma_f32_16x16x32_bf16 v[42:45], v[126:129], v[102:105], v[42:45]\n" \
    "ds_read_b64_tr_b16 v[142:143], v155 offset:0\n" \
    "s_waitcnt lgkmcnt(8)\n" \
    "v_mfma_f32_16x16x32_bf16 v[62:65], v[102:105], v[130:133], v[62:65]\n" \
    "ds_read_b64_tr_b16 v[144:145], v155 offset:2048\n" \
    "s_waitcnt lgkmcnt(7)\n" \
    "v_mfma_f32_16x16x32_bf16 v[82:85], v[134:137], v[102:105], v[82:85]\n" \
    "ds_read_b64_tr_b16 v[146:147], v156 offset:0\n" \
    "s_waitcnt lgkmcnt(5)\n" \
    "v_mfma_f32_16x16x32_bf16 v[22:25], v[122:125], v[102:105], v[22:25]\n" \
    "ds_read_b128 v[102:105], %[ap1] offset:2048\n" \
    "v_mfma_f32_16x16x32_bf16 v[46:49], v[126:129], v[106:109], v[46:49]\n" \
    "ds_read_b64_tr_b16 v[148:149], v156 offset:2048\n" \
    "v_mfma_f32_16x16x32_bf16 v[66:69], v[106:109], v[130:133], v[66:69]\n" \
    "ds_read_b64_tr_b16 v[150:151], v157 offset:0\n" \
    "v_mfma_f32_16x16x32_bf16 v[86:89], v[134:137], v[106:109], v[86:89]\n" \
    "ds_read_b64_tr_b16 v[152:153], v157 offset:2048\n" \
    "v_mfma_f32_16x16x32_bf16 v[26:29], v[122:125], v[106:109], v[26:29]\n" \
    "ds_read_b128 v[106:109], %[ap1] offset:4096\n" \
    "s_waitcnt lgkmcnt(8)\n" \
    "v_mfma_f32_16x16x32_bf16 v[50:53], v[126:129], v[110:113], v[50:53]\n" \
    "ds_read_b64_tr_b16 v[138:139], v154 offset:0\n" \
    "v_mfma_f32_16x16x32_bf16 v[70:73], v[110:113], v[130:133], v[70:73]\n" \
    "ds_read_b64_tr_b16 v[140:141], v154 offset:2048\n" \
    "v_mfma_f32_16x16x32_bf16 v[90:93], v[134:137], v[110:113], v[90:93]\n" \
    "ds_read_b128 v[118:121], %[ap1] offset:8192\n" \
    "v_mfma_f32_16x16x32_bf16 v[30:33], v[122:125], v[110:113], v[30:33]\n" \
    "ds_read_b128 v[110:113], %[ap1] offset:6144\n" \
    "v_mfma_f32_16x16x32_bf16 v[54:57], v[126:129], v[114:117], v[54:57]\n" \
    "s_barrier\n" \
    "v_mfma_f32_16x16x32_bf16 v[74:77], v[114:117], v[130:133], v[74:77]\n" \
    "v_mfma_f32_16x16x32_bf16 v[94:97], v[134:137], v[114:117], v[94:97]\n" \
    "v_mfma_f32_16x16x32_bf16 v[34:37], v[122:125], v[114:117], v[34:37]\n" \
    "s_waitcnt lgkmcnt(8)\n" \
    "v_mfma_f32_16x16x32_bf16 v[42:45], v[142:145], v[102:105], v[42:45]\n" \
    "ds_read_b64_tr_b16 v[126:127], v155 offset:16384\n" \
    "s_waitcnt lgkmcnt(8)\n" \
    "v_mfma_f32_16x16x32_bf16 v[62:65], v[102:105], v[146:149], v[62:65]\n" \
    "ds_read_b64_tr_b16 v[128:129], v155 offset:18432\n" \
    "s_waitcnt lgkmcnt(7)\n" \
    "v_mfma_f32_16x16x32_bf16 v[82:85], v[150:153], v[102:105], v[82:85]\n" \
    "ds_read_b64_tr_b16 v[130:131], v156 offset:16384\n" \
    "s_waitcnt lgkmcnt(5)\n" \
    "v_mfma_f32_16x16x32_bf16 v[22:25], v[138:141], v[102:105], v[22:25]\n" \
    "ds_read_b128 v[102:105], %[ap0] offset:20480\n" \
    "v_mfma_f32_16x16x32_bf16 v[46:49], v[142:145], v[106:109], v[46:49]\n" \
    "ds_read_b64_tr_b16 v[132:133], v156 offset:18432\n" \
    "v_mfma_f32_16x16x32_bf16 v[66:69], v[106:109], v[146:149], v[66:69]\n" \
    "ds_read_b64_tr_b16 v[134:135], v157 offset:16384\n" \
    "v_mfma_f32_16x16x32_bf16 v[86:89], v[150:153], v[106:109], v[86:89]\n" \
    "ds_read_b64_tr_b16 v[136:137], v157 offset:18432\n" \
    "v_mfma_f32_16x16x32_bf16 v[26:29], v[138:141], v[106:109], v[26:29]\n" \
    "ds_read_b128 v[106:109], %[ap0] offset:22528\n" \
    "s_waitcnt lgkmcnt(8)\n" \
    "v_mfma_f32_16x16x32_bf16 v[50:53], v[142:145], v[110:113], v[50:53]\n" \
    "ds_read_b64_tr_b16 v[122:123], v154 offset:16384\n" \
    "v_mfma_f32_16x16x32_bf16 v[70:73], v[110:113], v[146:149], v[70:73]\n" \
    "ds_read_b64_tr_b16 v[124:125], v154 offset:18432\n" \
    "v_mfma_f32_16x16x32_bf16 v[90:93], v[150:153], v[110:113], v[90:93]\n" \
    "ds_read_b128 v[114:117], %[ap0] offset:26624\n" \
    "v_mfma_f32_16x16x32_bf16 v[30:33], v[138:141], v[110:113], v[30:33]\n" \
    "ds_read_b128 v[110:113], %[ap0] offset:24576\n" \
    "v_mfma_f32_16x16x32_bf16 v[54:57], v[142:145], v[118:121], v[54:57]\n" \
    "s_barrier\n" \
    "v_mfma_f32_16x16x32_bf16 v[74:77], v[118:121], v[146:149], v[74:77]\n" \
    "v_mfma_f32_16x16x32_bf16 v[94:97], v[150:153], v[118:121], v[94:97]\n" \
    "v_mfma_f32_16x16x32_bf16 v[34:37], v[138:141], v[118:121], v[34:37]\n" \
    "s_waitcnt lgkmcnt(8)\n" \
    "v_mfma_f32_16x16x32_bf16 v[42:45], v[126:129], v[102:105], v[42:45]\n" \
    "ds_read_b64_tr_b16 v[142:143], v155 offset:32768\n" \
    "s_waitcnt lgkmcnt(8)\n" \
    "v_mfma_f32_16x16x32_bf16 v[62:65], v[102:105], v[130:133], v[62:65]\n" \
    "ds_read_b64_tr_b16 v[144:145], v155 offset:34816\n" \
    "s_waitcnt lgkmcnt(7)\n" \
    "v_mfma_f32_16x16x32_bf16 v[82:85], v[134:137], v[102:105], v[82:85]\n" \
    "ds_read_b64_tr_b16 v[146:147], v156 offset:32768\n" \
    "s_waitcnt lgkmcnt(5)\n" \
    "v_mfma_f32_16x16x32_bf16 v[22:25], v[122:125], v[102:105], v[22:25]\n" \
    "ds_read_b128 v[102:105], %[ap1] offset:20480\n" \
    "v_mfma_f32_16x16x32_bf16 v[46:49], v[126:129], v[106:109], v[46:49]\n" \
    "ds_read_b64_tr_b16 v[148:149], v156 offset:34816\n" \
    "v_mfma_f32_16x16x32_bf16 v[66:69], v[106:109], v[130:133], v[66:69]\n" \
    "ds_read_b64_tr_b16 v[150:151], v157 offset:32768\n" \
    "v_mfma_f32_16x16x32_bf16 v[86:89], v[134:137], v[106:109], v[86:89]\n" \
    "ds_read_b64_tr_b16 v[152:153], v157 offset:34816\n" \
    "v_mfma_f32_16x16x32_bf16 v[26:29], v[122:125], v[106:109], v[26:29]\n" \
    "ds_read_b128 v[106:109], %[ap1] offset:22528\n" \
    "s_waitcnt lgkmcnt(8)\n" \
    "v_mfma_f32_16x16x32_bf16 v[50:53], v[126:129], v[110:113], v[50:53]\n" \
    "ds_read_b64_tr_b16 v[138:139], v154 offset:32768\n" \
    "v_mfma_f32_16x16x32_bf16 v[70:73], v[110:113], v[130:133], v[70:73]\n" \
    "ds_read_b64_tr_b16 v[140:141], v154 offset:34816\n" \
    "v_mfma_f32_16x16x32_bf16 v[90:93], v[134:137], v[110:113], v[90:93]\n" \
    "ds_read_b128 v[118:121], %[ap1] offset:26624\n" \
    "v_mfma_f32_16x16x32_bf16 v[30:33], v[122:125], v[110:113], v[30:33]\n" \
    "ds_read_b128 v[110:113], %[ap1] offset:24576\n" \
    "v_mfma_f32_16x16x32_bf16 v[54:57], v[126:129], v[114:117], v[54:57]\n" \
    "s_barrier\n" \
    "v_mfma_f32_16x16x32_bf16 v[74:77], v[114:117], v[130:133], v[74:77]\n" \
    "v_mfma_f32_16x16x32_bf16 v[94:97], v[134:137], v[114:117], v[94:97]\n" \
    "v_mfma_f32_16x16x32_bf16 v[34:37], v[122:125], v[114:117], v[34:37]\n" \
    "s_waitcnt lgkmcnt(8)\n" \
    "v_mfma_f32_16x16x32_bf16 v[42:45], v[142:145], v[102:105], v[42:45]\n" \
    "ds_read_b64_tr_b16 v[126:127], %[wp1] offset:0\n" \
    "s_waitcnt lgkmcnt(8)\n" \
    "v_mfma_f32_16x16x32_bf16 v[62:65], v[102:105], v[146:149], v[62:65]\n" \
    "ds_read_b64_tr_b16 v[128:129], %[wp1] offset:2048\n" \
    "s_waitcnt lgkmcnt(7)\n" \
    "v_mfma_f32_16x16x32_bf16 v[82:85], v[150:153], v[102:105], v[82:85]\n" \
    "ds_read_b64_tr_b16 v[130:131], %[wp2] offset:0\n" \
    "s_waitcnt lgkmcnt(5)\n" \
    "v_mfma_f32_16x16x32_bf16 v[22:25], v[138:141], v[102:105], v[22:25]\n" \
    "ds_read_b128 v[102:105], %[ap0] offset:55296\n" \
    "v_mfma_f32_16x16x32_bf16 v[46:49], v[142:145], v[106:109], v[46:49]\n" \
    "ds_read_b64_tr_b16 v[132:133], %[wp2] offset:2048\n" \
    "v_mfma_f32_16x16x32_bf16 v[66:69], v[106:109], v[146:149], v[66:69]\n" \
    "ds_read_b64_tr_b16 v[134:135], %[wp3] offset:0\n" \
    "v_mfma_f32_16x16x32_bf16 v[86:89], v[150:153], v[106:109], v[86:89]\n" \
    "ds_read_b64_tr_b16 v[136:137], %[wp3] offset:2048\n" \
    "v_mfma_f32_16x16x32_bf16 v[26:29], v[138:141], v[106:109], v[26:29]\n" \
    "ds_read_b128 v[106:109], %[ap0] offset:57344\n" \
    "s_waitcnt lgkmcnt(8)\n" \
    "v_mfma_f32_16x16x32_bf16 v[50:53], v[142:145], v[110:113], v[50:53]\n" \
    "ds_read_b64_tr_b16 v[122:123], %[wp0] offset:0\n" \
    "v_mfma_f32_16x16x32_bf16 v[70:73], v[110:113], v[146:149], v[70:73]\n" \
    "ds_read_b64_tr_b16 v[124:125], %[wp0] offset:2048\n" \
    "v_mfma_f32_16x16x32_bf16 v[90:93], v[150:153], v[110:113], v[90:93]\n" \
    "ds_read_b128 v[114:117], %[ap0] offset:61440\n" \
    "v_mfma_f32_16x16x32_bf16 v[30:33], v[138:141], v[110:113], v[30:33]\n" \
    "ds_read_b128 v[110:113], %[ap0] offset:59392\n" \
    "v_mfma_f32_16x16x32_bf16 v[54:57], v[142:145], v[118:121], v[54:57]\n" \
    "s_barrier\n" \
    "v_mfma_f32_16x16x32_bf16 v[74:77], v[118:121], v[146:149], v[74:77]\n" \
    "v_mfma_f32_16x16x32_bf16 v[94:97], v[150:153], v[118:121], v[94:97]\n" \
    "v_mfma_f32_16x16x32_bf16 v[34:37], v[138:141], v[118:121], v[34:37]\n" \
    "s_waitcnt lgkmcnt(8)\n" \
    "v_mfma_f32_16x16x32_bf16 v[42:45], v[126:129], v[102:105], v[42:45]\n" \
    "ds_read_b64_tr_b16 v[142:143], %[wp1] offset:34816\n" \
    "s_waitcnt lgkmcnt(8)\n" \
    "v_mfma_f32_16x16x32_bf16 v[62:65], v[102:105], v[130:133], v[62:65]\n" \
    "ds_read_b64_tr_b16 v[144:145], %[wp1] offset:36864\n" \
    "s_waitcnt lgkmcnt(7)\n" \
    "v_mfma_f32_16x16x32_bf16 v[82:85], v[134:137], v[102:105], v[82:85]\n" \
    "ds_read_b64_tr_b16 v[146:147], %[wp2] offset:34816\n" \
    "s_waitcnt lgkmcnt(5)\n" \
    "v_mfma_f32_16x16x32_bf16 v[22:25], v[122:125], v[102:105], v[22:25]\n" \
    "ds_read_b128 v[102:105], %[ap1] offset:55296\n" \
    "v_mfma_f32_16x16x32_bf16 v[46:49], v[126:129], v[106:109], v[46:49]\n" \
    "ds_read_b64_tr_b16 v[148:149], %[wp2] offset:36864\n" \
    "v_mfma_f32_16x16x32_bf16 v[66:69], v[106:109], v[130:133], v[66:69]\n" \
    "ds_read_b64_tr_b16 v[150:151], %[wp3] offset:34816\n" \
    "v_mfma_f32_16x16x32_bf16 v[86:89], v[134:137], v[106:109], v[86:89]\n" \
    "ds_read_b64_tr_b16 v[152:153], %[wp3] offset:36864\n" \
    "v_mfma_f32_16x16x32_bf16 v[26:29], v[122:125], v[106:109], v[26:29]\n" \
    "ds_read_b128 v[106:109], %[ap1] offset:57344\n" \
    "s_waitcnt lgkmcnt(8)\n" \
    "v_mfma_f32_16x16x32_bf16 v[50:53], v[126:129], v[110:113], v[50:53]\n" \
    "ds_read_b64_tr_b16 v[138:139], %[wp0] offset:34816\n" \
    "v_mfma_f32_16x16x32_bf16 v[70:73], v[110:113], v[130:133], v[70:73]\n" \
    "ds_read_b64_tr_b16 v[140:141], %[wp0] offset:36864\n" \
    "v_mfma_f32_16x16x32_bf16 v[90:93], v[134:137], v[110:113], v[90:93]\n" \
    "ds_read_b128 v[118:121], %[ap1] offset:61440\n" \
    "v_mfma_f32_16x16x32_bf16 v[30:33], v[122:125], v[110:113], v[30:33]\n" \
    "ds_read_b128 v[110:113], %[ap1] offset:59392\n" \
    "v_mfma_f32_16x16x32_bf16 v[54:57], v[126:129], v[114:117], v[54:57]\n" \
    "s_barrier\n" \
    "v_mfma_f32_16x16x32_bf16 v[74:77], v[114:117], v[130:133], v[74:77]\n" \
    "v_mfma_f32_16x16x32_bf16 v[94:97], v[134:137], v[114:117], v[94:97]\n" \
    "v_mfma_f32_16x16x32_bf16 v[34:37], v[122:125], v[114:117], v[34:37]\n" \
    "s_waitcnt lgkmcnt(8)\n" \
    "v_mfma_f32_16x16x32_bf16 v[42:45], v[142:145], v[102:105], v[42:45]\n" \
    "ds_read_b64_tr_b16 v[126:127], %[wp1] offset:51200\n" \
    "s_waitcnt lgkmcnt(8)\n" \
    "v_mfma_f32_16x16x32_bf16 v[62:65], v[102:105], v[146:149], v[62:65]\n" \
    "ds_read_b64_tr_b16 v[128:129], %[wp1] offset:53248\n" \
    "s_waitcnt lgkmcnt(7)\n" \
    "v_mfma_f32_16x16x32_bf16 v[82:85], v[150:153], v[102:105], v[82:85]\n" \
    "ds_read_b64_tr_b16 v[130:131], %[wp2] offset:51200\n" \
    "s_waitcnt lgkmcnt(5)\n" \
    "v_mfma_f32_16x16x32_bf16 v[22:25], v[138:141], v[102:105], v[22:25]\n" \
    "ds_read_b128 v[102:105], %[ap0] offset:2048\n" \
    "v_mfma_f32_16x16x32_bf16 v[46:49], v[142:145], v[106:109], v[46:49]\n" \
    "ds_read_b64_tr_b16 v[132:133], %[wp2] offset:53248\n" \
    "v_mfma_f32_16x16x32_bf16 v[66:69], v[106:109], v[146:149], v[66:69]\n" \
    "ds_read_b64_tr_b16 v[134:135], %[wp3] offset:51200\n" \
    "v_mfma_f32_16x16x32_bf16 v[86:89], v[150:153], v[106:109], v[86:89]\n" \
    "ds_read_b64_tr_b16 v[136:137], %[wp3] offset:53248\n" \
    "v_mfma_f32_16x16x32_bf16 v[26:29], v[138:141], v[106:109], v[26:29]\n" \
    "ds_read_b128 v[106:109], %[ap0] offset:4096\n" \
    "s_waitcnt lgkmcnt(8)\n" \
    "v_mfma_f32_16x16x32_bf16 v[50:53], v[142:145], v[110:113], v[50:53]\n" \
    "ds_read_b64_tr_b16 v[122:123], %[wp0] offset:51200\n" \
    "v_mfma_f32_16x16x32_bf16 v[70:73], v[110:113], v[146:149], v[70:73]\n" \
    "ds_read_b64_tr_b16 v[124:125], %[wp0] offset:53248\n" \
    "v_mfma_f32_16x16x32_bf16 v[90:93], v[150:153], v[110:113], v[90:93]\n" \
    "ds_read_b128 v[114:117], %[ap0] offset:8192\n" \
    "v_mfma_f32_16x16x32_bf16 v[30:33], v[138:141], v[110:113], v[30:33]\n" \
    "ds_read_b128 v[110:113], %[ap0] offset:6144\n" \
    "v_mfma_f32_16x16x32_bf16 v[54:57], v[142:145], v[118:121], v[54:57]\n" \
    "s_barrier\n" \
    "v_mfma_f32_16x16x32_bf16 v[74:77], v[118:121], v[146:149], v[74:77]\n" \
    "v_mfma_f32_16x16x32_bf16 v[94:97], v[150:153], v[118:121], v[94:97]\n" \
    "v_mfma_f32_16x16x32_bf16 v[34:37], v[138:141], v[118:121], v[34:37]\n" \
    "s_waitcnt lgkmcnt(8)\n" \
    "v_mfma_f32_16x16x32_bf16 v[42:45], v[126:129], v[102:105], v[42:45]\n" \
    "ds_read_b64_tr_b16 v[142:143], v155 offset:0\n" \
    "s_waitcnt lgkmcnt(8)\n" \
    "v_mfma_f32_16x16x32_bf16 v[62:65], v[102:105], v[130:133], v[62:65]\n" \
    "ds_read_b64_tr_b16 v[144:145], v155 offset:2048\n" \
    "s_waitcnt lgkmcnt(7)\n" \
    "v_mfma_f32_16x16x32_bf16 v[82:85], v[134:137], v[102:105], v[82:85]\n" \
    "ds_read_b64_tr_b16 v[146:147], v156 offset:0\n" \
    "s_waitcnt lgkmcnt(5)\n" \
    "v_mfma_f32_16x16x32_bf16 v[22:25], v[122:125], v[102:105], v[22:25]\n" \
    "ds_read_b128 v[102:105], %[ap1] offset:2048\n" \
    "v_mfma_f32_16x16x32_bf16 v[46:49], v[126:129], v[106:109], v[46:49]\n" \
    "ds_read_b64_tr_b16 v[148:149], v156 offset:2048\n" \
    "v_mfma_f32_16x16x32_bf16 v[66:69], v[106:109], v[130:133], v[66:69]\n" \
    "ds_read_b64_tr_b16 v[150:151], v157 offset:0\n" \
    "v_mfma_f32_16x16x32_bf16 v[86:89], v[134:137], v[106:109], v[86:89]\n" \
    "ds_read_b64_tr_b16 v[152:153], v157 offset:2048\n" \
    "v_mfma_f32_16x16x32_bf16 v[26:29], v[122:125], v[106:109], v[26:29]\n" \
    "ds_read_b128 v[106:109], %[ap1] offset:4096\n" \
    "s_waitcnt lgkmcnt(8)\n" \
    "v_mfma_f32_16x16x32_bf16 v[50:53], v[126:129], v[110:113], v[50:53]\n" \
    "ds_read_b64_tr_b16 v[138:139], v154 offset:0\n" \
    "v_mfma_f32_16x16x32_bf16 v[70:73], v[110:113], v[130:133], v[70:73]\n" \
    "ds_read_b64_tr_b16 v[140:141], v154 offset:2048\n" \
    "v_mfma_f32_16x16x32_bf16 v[90:93], v[134:137], v[110:113], v[90:93]\n" \
    "ds_read_b128 v[118:121], %[ap1] offset:8192\n" \
    "v_mfma_f32_16x16x32_bf16 v[30:33], v[122:125], v[110:113], v[30:33]\n" \
    "ds_read_b128 v[110:113], %[ap1] offset:6144\n" \
    "v_mfma_f32_16x16x32_bf16 v[54:57], v[126:129], v[114:117], v[54:57]\n" \
    "s_barrier\n" \
    "v_mfma_f32_16x16x32_bf16 v[74:77], v[114:117], v[130:133], v[74:77]\n" \
    "v_mfma_f32_16x16x32_bf16 v[94:97], v[134:137], v[114:117], v[94:97]\n" \
    "v_mfma_f32_16x16x32_bf16 v[34:37], v[122:125], v[114:117], v[34:37]\n" \
    "s_waitcnt lgkmcnt(8)\n" \
    "v_mfma_f32_16x16x32_bf16 v[42:45], v[142:145], v[102:105], v[42:45]\n" \
    "ds_read_b64_tr_b16 v[126:127], v155 offset:16384\n" \
    "s_waitcnt lgkmcnt(8)\n" \
    "v_mfma_f32_16x16x32_bf16 v[62:65], v[102:105], v[146:149], v[62:65]\n" \
    "ds_read_b64_tr_b16 v[128:129], v155 offset:18432\n" \
    "s_waitcnt lgkmcnt(7)\n" \
    "v_mfma_f32_16x16x32_bf16 v[82:85], v[150:153], v[102:105], v[82:85]\n" \
    "ds_read_b64_tr_b16 v[130:131], v156 offset:16384\n" \
    "s_waitcnt lgkmcnt(5)\n" \
    "v_mfma_f32_16x16x32_bf16 v[22:25], v[138:141], v[102:105], v[22:25]\n" \
    "ds_read_b128 v[102:105], %[ap0] offset:20480\n" \
    "v_mfma_f32_16x16x32_bf16 v[46:49], v[142:145], v[106:109], v[46:49]\n" \
    "ds_read_b64_tr_b16 v[132:133], v156 offset:18432\n" \
    "v_mfma_f32_16x16x32_bf16 v[66:69], v[106:109], v[146:149], v[66:69]\n" \
    "ds_read_b64_tr_b16 v[134:135], v157 offset:16384\n" \
    "v_mfma_f32_16x16x32_bf16 v[86:89], v[150:153], v[106:109], v[86:89]\n" \
    "ds_read_b64_tr_b16 v[136:137], v157 offset:18432\n" \
    "v_mfma_f32_16x16x32_bf16 v[26:29], v[138:141], v[106:109], v[26:29]\n" \
    "ds_read_b128 v[106:109], %[ap0] offset:22528\n" \
    "s_waitcnt lgkmcnt(8)\n" \
    "v_mfma_f32_16x16x32_bf16 v[50:53], v[142:145], v[110:113], v[50:53]\n" \
    "ds_read_b64_tr_b16 v[122:123], v154 offset:16384\n" \
    "v_mfma_f32_16x16x32_bf16 v[70:73], v[110:113], v[146:149], v[70:73]\n" \
    "ds_read_b64_tr_b16 v[124:125], v154 offset:18432\n" \
    "v_mfma_f32_16x16x32_bf16 v[90:93], v[150:153], v[110:113], v[90:93]\n" \
    "ds_read_b128 v[114:117], %[ap0] offset:26624\n" \
    "v_mfma_f32_16x16x32_bf16 v[30:33], v[138:141], v[110:113], v[30:33]\n" \
    "ds_read_b128 v[110:113], %[ap0] offset:24576\n" \
    "v_mfma_f32_16x16x32_bf16 v[54:57], v[142:145], v[118:121], v[54:57]\n" \
    "s_barrier\n" \
    "v_mfma_f32_16x16x32_bf16 v[74:77], v[118:121], v[146:149], v[74:77]\n" \
    "v_mfma_f32_16x16x32_bf16 v[94:97], v[150:153], v[118:121], v[94:97]\n" \
    "v_mfma_f32_16x16x32_bf16 v[34:37], v[138:141], v[118:121], v[34:37]\n" \
    "s_waitcnt lgkmcnt(8)\n" \
    "v_mfma_f32_16x16x32_bf16 v[42:45], v[126:129], v[102:105], v[42:45]\n" \
    "ds_read_b64_tr_b16 v[142:143], v155 offset:32768\n" \
    "s_waitcnt lgkmcnt(8)\n" \
    "v_mfma_f32_16x16x32_bf16 v[62:65], v[102:105], v[130:133], v[62:65]\n" \
    "ds_read_b64_tr_b16 v[144:145], v155 offset:34816\n" \
    "s_waitcnt lgkmcnt(7)\n" \
    "v_mfma_f32_16x16x32_bf16 v[82:85], v[134:137], v[102:105], v[82:85]\n" \
    "ds_read_b64_tr_b16 v[146:147], v156 offset:32768\n" \
    "s_waitcnt lgkmcnt(5)\n" \
    "v_mfma_f32_16x16x32_bf16 v[22:25], v[122:125], v[102:105], v[22:25]\n" \
    "ds_read_b128 v[102:105], %[ap1] offset:20480\n" \
    "v_mfma_f32_16x16x32_bf16 v[46:49], v[126:129], v[106:109], v[46:49]\n" \
    "ds_read_b64_tr_b16 v[148:149], v156 offset:34816\n" \
    "v_mfma_f32_16x16x32_bf16 v[66:69], v[106:109], v[130:133], v[66:69]\n" \
    "ds_read_b64_tr_b16 v[150:151], v157 offset:32768\n" \
    "v_mfma_f32_16x16x32_bf16 v[86:89], v[134:137], v[106:109], v[86:89]\n" \
    "ds_read_b64_tr_b16 v[152:153], v157 offset:34816\n" \
    "v_mfma_f32_16x16x32_bf16 v[26:29], v[122:125], v[106:109], v[26:29]\n" \
    "ds_read_b128 v[106:109], %[ap1] offset:22528\n" \
    "s_waitcnt lgkmcnt(8)\n" \
    "v_mfma_f32_16x16x32_bf16 v[50:53], v[126:129], v[110:113], v[50:53]\n" \
    "ds_read_b64_tr_b16 v[138:139], v154 offset:32768\n" \
    "v_mfma_f32_16x16x32_bf16 v[70:73], v[110:113], v[130:133], v[70:73]\n" \
    "ds_read_b64_tr_b16 v[140:141], v154 offset:34816\n" \
    "v_mfma_f32_16x16x32_bf16 v[90:93], v[134:137], v[110:113], v[90:93]\n" \
    "ds_read_b128 v[118:121], %[ap1] offset:26624\n" \
    "v_mfma_f32_16x16x32_bf16 v[30:33], v[122:125], v[110:113], v[30:33]\n" \
    "ds_read_b128 v[110:113], %[ap1] offset:24576\n" \
    "v_mfma_f32_16x16x32_bf16 v[54:57], v[126:129], v[114:117], v[54:57]\n" \
    "s_barrier\n" \
    "v_mfma_f32_16x16x32_bf16 v[74:77], v[114:117], v[130:133], v[74:77]\n" \
    "v_mfma_f32_16x16x32_bf16 v[94:97], v[134:137], v[114:117], v[94:97]\n" \
    "v_mfma_f32_16x16x32_bf16 v[34:37], v[122:125], v[114:117], v[34:37]\n" \
    "s_waitcnt lgkmcnt(8)\n" \
    "v_mfma_f32_16x16x32_bf16 v[42:45], v[142:145], v[102:105], v[42:45]\n" \
    "ds_read_b64_tr_b16 v[126:127], %[wp1] offset:0\n" \
    "s_waitcnt lgkmcnt(8)\n" \
    "v_mfma_f32_16x16x32_bf16 v[62:65], v[102:105], v[146:149], v[62:65]\n" \
    "ds_read_b64_tr_b16 v[128:129], %[wp1] offset:2048\n" \
    "s_waitcnt lgkmcnt(7)\n" \
    "v_mfma_f32_16x16x32_bf16 v[82:85], v[150:153], v[102:105], v[82:85]\n" \
    "ds_read_b64_tr_b16 v[130:131], %[wp2] offset:0\n" \
    "s_waitcnt lgkmcnt(5)\n" \
    "v_mfma_f32_16x16x32_bf16 v[22:25], v[138:141], v[102:105], v[22:25]\n" \
    "ds_read_b128 v[102:105], %[ap0] offset:55296\n" \
    "v_mfma_f32_16x16x32_bf16 v[46:49], v[142:145], v[106:109], v[46:49]\n" \
    "ds_read_b64_tr_b16 v[132:133], %[wp2] offset:2048\n" \
    "v_mfma_f32_16x16x32_bf16 v[66:69], v[106:109], v[146:149], v[66:69]\n" \
    "ds_read_b64_tr_b16 v[134:135], %[wp3] offset:0\n" \
    "v_mfma_f32_16x16x32_bf16 v[86:89], v[150:153], v[106:109], v[86:89]\n" \
    "ds_read_b64_tr_b16 v[136:137], %[wp3] offset:2048\n" \
    "v_mfma_f32_16x16x32_bf16 v[26:29], v[138:141], v[106:109], v[26:29]\n" \
    "ds_read_b128 v[106:109], %[ap0] offset:57344\n" \
    "s_waitcnt lgkmcnt(8)\n" \
    "v_mfma_f32_16x16x32_bf16 v[50:53], v[142:145], v[110:113], v[50:53]\n" \
    "ds_read_b64_tr_b16 v[122:123], %[wp0] offset:0\n" \
    "v_mfma_f32_16x16x32_bf16 v[70:73], v[110:113], v[146:149], v[70:73]\n" \
    "ds_read_b64_tr_b16 v[124:125], %[wp0] offset:2048\n" \
    "v_mfma_f32_16x16x32_bf16 v[90:93], v[150:153], v[110:113], v[90:93]\n" \
    "ds_read_b128 v[114:117], %[ap0] offset:61440\n" \
    "v_mfma_f32_16x16x32_bf16 v[30:33], v[138:141], v[110:113], v[30:33]\n" \
    "ds_read_b128 v[110:113], %[ap0] offset:59392\n" \
    "v_mfma_f32_16x16x32_bf16 v[54:57], v[142:145], v[118:121], v[54:57]\n" \
    "s_barrier\n" \
    "v_mfma_f32_16x16x32_bf16 v[74:77], v[118:121], v[146:149], v[74:77]\n" \
    "v_mfma_f32_16x16x32_bf16 v[94:97], v[150:153], v[118:121], v[94:97]\n" \
    "v_mfma_f32_16x16x32_bf16 v[34:37], v[138:141], v[118:121], v[34:37]\n" \
    "s_waitcnt lgkmcnt(8)\n" \
    "v_mfma_f32_16x16x32_bf16 v[42:45], v[126:129], v[102:105], v[42:45]\n" \
    "ds_read_b64_tr_b16 v[142:143], %[wp1] offset:34816\n" \
    "s_waitcnt lgkmcnt(8)\n" \
    "v_mfma_f32_16x16x32_bf16 v[62:65], v[102:105], v[130:133], v[62:65]\n" \
    "ds_read_b64_tr_b16 v[144:145], %[wp1] offset:36864\n" \
    "s_waitcnt lgkmcnt(7)\n" \
    "v_mfma_f32_16x16x32_bf16 v[82:85], v[134:137], v[102:105], v[82:85]\n" \
    "ds_read_b64_tr_b16 v[146:147], %[wp2] offset:34816\n" \
    "s_waitcnt lgkmcnt(5)\n" \
    "v_mfma_f32_16x16x32_bf16 v[22:25], v[122:125], v[102:105], v[22:25]\n" \
    "ds_read_b128 v[102:105], %[ap1] offset:55296\n" \
    "v_mfma_f32_16x16x32_bf16 v[46:49], v[126:129], v[106:109], v[46:49]\n" \
    "ds_read_b64_tr_b16 v[148:149], %[wp2] offset:36864\n" \
    "v_mfma_f32_16x16x32_bf16 v[66:69], v[106:109], v[130:133], v[66:69]\n" \
    "ds_read_b64_tr_b16 v[150:151], %[wp3] offset:34816\n" \
    "v_mfma_f32_16x16x32_bf16 v[86:89], v[134:137], v[106:109], v[86:89]\n" \
    "ds_read_b64_tr_b16 v[152:153], %[wp3] offset:36864\n" \
    "v_mfma_f32_16x16x32_bf16 v[26:29], v[122:125], v[106:109], v[26:29]\n" \
    "ds_read_b128 v[106:109], %[ap1] offset:57344\n" \
    "s_waitcnt lgkmcnt(8)\n" \
    "v_mfma_f32_16x16x32_bf16 v[50:53], v[126:129], v[110:113], v[50:53]\n" \
    "ds_read_b64_tr_b16 v[138:139], %[wp0] offset:34816\n" \
    "v_mfma_f32_16x16x32_bf16 v[70:73], v[110:113], v[130:133], v[70:73]\n" \
    "ds_read_b64_tr_b16 v[140:141], %[wp0] offset:36864\n" \
    "v_mfma_f32_16x16x32_bf16 v[90:93], v[134:137], v[110:113], v[90:93]\n" \
    "ds_read_b128 v[118:121], %[ap1] offset:61440\n" \
    "v_mfma_f32_16x16x32_bf16 v[30:33], v[122:125], v[110:113], v[30:33]\n" \
    "ds_read_b128 v[110:113], %[ap1] offset:59392\n" \
    "v_mfma_f32_16x16x32_bf16 v[54:57], v[126:129], v[114:117], v[54:57]\n" \
    "s_barrier\n" \
    "v_mfma_f32_16x16x32_bf16 v[74:77], v[114:117], v[130:133], v[74:77]\n" \
    "v_mfma_f32_16x16x32_bf16 v[94:97], v[134:137], v[114:117], v[94:97]\n" \
    "v_mfma_f32_16x16x32_bf16 v[34:37], v[122:125], v[114:117], v[34:37]\n" \
    "s_waitcnt lgkmcnt(8)\n" \
    "v_mfma_f32_16x16x32_bf16 v[42:45], v[142:145], v[102:105], v[42:45]\n" \
    "ds_read_b64_tr_b16 v[126:127], %[wp1] offset:51200\n" \
    "s_waitcnt lgkmcnt(8)\n" \
    "v_mfma_f32_16x16x32_bf16 v[62:65], v[102:105], v[146:149], v[62:65]\n" \
    "ds_read_b64_tr_b16 v[128:129], %[wp1] offset:53248\n" \
    "s_waitcnt lgkmcnt(7)\n" \
    "v_mfma_f32_16x16x32_bf16 v[82:85], v[150:153], v[102:105], v[82:85]\n" \
    "ds_read_b64_tr_b16 v[130:131], %[wp2] offset:51200\n" \
    "s_waitcnt lgkmcnt(5)\n" \
    "v_mfma_f32_16x16x32_bf16 v[22:25], v[138:141], v[102:105], v[22:25]\n" \
    "ds_read_b128 v[102:105], %[ap0] offset:2048\n" \
    "v_mfma_f32_16x16x32_bf16 v[46:49], v[142:145], v[106:109], v[46:49]\n" \
    "ds_read_b64_tr_b16 v[132:133], %[wp2] offset:53248\n" \
    "v_mfma_f32_16x16x32_bf16 v[66:69], v[106:109], v[146:149], v[66:69]\n" \
    "ds_read_b64_tr_b16 v[134:135], %[wp3] offset:51200\n" \
    "v_mfma_f32_16x16x32_bf16 v[86:89], v[150:153], v[106:109], v[86:89]\n" \
    "ds_read_b64_tr_b16 v[136:137], %[wp3] offset:53248\n" \
    "v_mfma_f32_16x16x32_bf16 v[26:29], v[138:141], v[106:109], v[26:29]\n" \
    "ds_read_b128 v[106:109], %[ap0] offset:4096\n" \
    "s_waitcnt lgkmcnt(8)\n" \
    "v_mfma_f32_16x16x32_bf16 v[50:53], v[142:145], v[110:113], v[50:53]\n" \
    "ds_read_b64_tr_b16 v[122:123], %[wp0] offset:51200\n" \
    "v_mfma_f32_16x16x32_bf16 v[70:73], v[110:113], v[146:149], v[70:73]\n" \
    "ds_read_b64_tr_b16 v[124:125], %[wp0] offset:53248\n" \
    "v_mfma_f32_16x16x32_bf16 v[90:93], v[150:153], v[110:113], v[90:93]\n" \
    "ds_read_b128 v[114:117], %[ap0] offset:8192\n" \
    "v_mfma_f32_16x16x32_bf16 v[30:33], v[138:141], v[110:113], v[30:33]\n" \
    "ds_read_b128 v[110:113], %[ap0] offset:6144\n" \
    "v_mfma_f32_16x16x32_bf16 v[54:57], v[142:145], v[118:121], v[54:57]\n" \
    "s_barrier\n" \
    "v_mfma_f32_16x16x32_bf16 v[74:77], v[118:121], v[146:149], v[74:77]\n" \
    "v_mfma_f32_16x16x32_bf16 v[94:97], v[150:153], v[118:121], v[94:97]\n" \
    "v_mfma_f32_16x16x32_bf16 v[34:37], v[138:141], v[118:121], v[34:37]\n" \
    "s_waitcnt lgkmcnt(8)\n" \
    "v_mfma_f32_16x16x32_bf16 v[42:45], v[126:129], v[102:105], v[42:45]\n" \
    "ds_read_b64_tr_b16 v[142:143], v155 offset:0\n" \
    "s_waitcnt lgkmcnt(8)\n" \
    "v_mfma_f32_16x16x32_bf16 v[62:65], v[102:105], v[130:133], v[62:65]\n" \
    "ds_read_b64_tr_b16 v[144:145], v155 offset:2048\n" \
    "s_waitcnt lgkmcnt(7)\n" \
    "v_mfma_f32_16x16x32_bf16 v[82:85], v[134:137], v[102:105], v[82:85]\n" \
    "ds_read_b64_tr_b16 v[146:147], v156 offset:0\n" \
    "s_waitcnt lgkmcnt(5)\n" \
    "v_mfma_f32_16x16x32_bf16 v[22:25], v[122:125], v[102:105], v[22:25]\n" \
    "ds_read_b128 v[102:105], %[ap1] offset:2048\n" \
    "v_mfma_f32_16x16x32_bf16 v[46:49], v[126:129], v[106:109], v[46:49]\n" \
    "ds_read_b64_tr_b16 v[148:149], v156 offset:2048\n" \
    "v_mfma_f32_16x16x32_bf16 v[66:69], v[106:109], v[130:133], v[66:69]\n" \
    "ds_read_b64_tr_b16 v[150:151], v157 offset:0\n" \
    "v_mfma_f32_16x16x32_bf16 v[86:89], v[134:137], v[106:109], v[86:89]\n" \
    "ds_read_b64_tr_b16 v[152:153], v157 offset:2048\n" \
    "v_mfma_f32_16x16x32_bf16 v[26:29], v[122:125], v[106:109], v[26:29]\n" \
    "ds_read_b128 v[106:109], %[ap1] offset:4096\n" \
    "s_waitcnt lgkmcnt(8)\n" \
    "v_mfma_f32_16x16x32_bf16 v[50:53], v[126:129], v[110:113], v[50:53]\n" \
    "ds_read_b64_tr_b16 v[138:139], v154 offset:0\n" \
    "v_mfma_f32_16x16x32_bf16 v[70:73], v[110:113], v[130:133], v[70:73]\n" \
    "ds_read_b64_tr_b16 v[140:141], v154 offset:2048\n" \
    "v_mfma_f32_16x16x32_bf16 v[90:93], v[134:137], v[110:113], v[90:93]\n" \
    "ds_read_b128 v[118:121], %[ap1] offset:8192\n" \
    "v_mfma_f32_16x16x32_bf16 v[30:33], v[122:125], v[110:113], v[30:33]\n" \
    "ds_read_b128 v[110:113], %[ap1] offset:6144\n" \
    "v_mfma_f32_16x16x32_bf16 v[54:57], v[126:129], v[114:117], v[54:57]\n" \
    "s_barrier\n" \
    "v_mfma_f32_16x16x32_bf16 v[74:77], v[114:117], v[130:133], v[74:77]\n" \
    "v_mfma_f32_16x16x32_bf16 v[94:97], v[134:137], v[114:117], v[94:97]\n" \
    "v_mfma_f32_16x16x32_bf16 v[34:37], v[122:125], v[114:117], v[34:37]\n" \
    "s_waitcnt lgkmcnt(8)\n" \
    "v_mfma_f32_16x16x32_bf16 v[42:45], v[142:145], v[102:105], v[42:45]\n" \
    "ds_read_b64_tr_b16 v[126:127], v155 offset:16384\n" \
    "s_waitcnt lgkmcnt(8)\n" \
    "v_mfma_f32_16x16x32_bf16 v[62:65], v[102:105], v[146:149], v[62:65]\n" \
    "ds_read_b64_tr_b16 v[128:129], v155 offset:18432\n" \
    "s_waitcnt lgkmcnt(7)\n" \
    "v_mfma_f32_16x16x32_bf16 v[82:85], v[150:153], v[102:105], v[82:85]\n" \
    "ds_read_b64_tr_b16 v[130:131], v156 offset:16384\n" \
    "s_waitcnt lgkmcnt(5)\n" \
    "v_mfma_f32_16x16x32_bf16 v[22:25], v[138:141], v[102:105], v[22:25]\n" \
    "ds_read_b128 v[102:105], %[ap0] offset:20480\n" \
    "v_mfma_f32_16x16x32_bf16 v[46:49], v[142:145], v[106:109], v[46:49]\n" \
    "ds_read_b64_tr_b16 v[132:133], v156 offset:18432\n" \
    "v_mfma_f32_16x16x32_bf16 v[66:69], v[106:109], v[146:149], v[66:69]\n" \
    "ds_read_b64_tr_b16 v[134:135], v157 offset:16384\n" \
    "v_mfma_f32_16x16x32_bf16 v[86:89], v[150:153], v[106:109], v[86:89]\n" \
    "ds_read_b64_tr_b16 v[136:137], v157 offset:18432\n" \
    "v_mfma_f32_16x16x32_bf16 v[26:29], v[138:141], v[106:109], v[26:29]\n" \
    "ds_read_b128 v[106:109], %[ap0] offset:22528\n" \
    "s_waitcnt lgkmcnt(8)\n" \
    "v_mfma_f32_16x16x32_bf16 v[50:53], v[142:145], v[110:113], v[50:53]\n" \
    "ds_read_b64_tr_b16 v[122:123], v154 offset:16384\n" \
    "v_mfma_f32_16x16x32_bf16 v[70:73], v[110:113], v[146:149], v[70:73]\n" \
    "ds_read_b64_tr_b16 v[124:125], v154 offset:18432\n" \
    "v_mfma_f32_16x16x32_bf16 v[90:93], v[150:153], v[110:113], v[90:93]\n" \
    "ds_read_b128 v[114:117], %[ap0] offset:26624\n" \
    "v_mfma_f32_16x16x32_bf16 v[30:33], v[138:141], v[110:113], v[30:33]\n" \
    "ds_read_b128 v[110:113], %[ap0] offset:24576\n" \
    "v_mfma_f32_16x16x32_bf16 v[54:57], v[142:145], v[118:121], v[54:57]\n" \
    "s_barrier\n" \
    "v_mfma_f32_16x16x32_bf16 v[74:77], v[118:121], v[146:149], v[74:77]\n" \
    "v_mfma_f32_16x16x32_bf16 v[94:97], v[150:153], v[118:121], v[94:97]\n" \
    "v_mfma_f32_16x16x32_bf16 v[34:37], v[138:141], v[118:121], v[34:37]\n" \
    "s_waitcnt lgkmcnt(8)\n" \
    "v_mfma_f32_16x16x32_bf16 v[42:45], v[126:129], v[102:105], v[42:45]\n" \
    "ds_read_b64_tr_b16 v[142:143], v155 offset:32768\n" \
    "s_waitcnt lgkmcnt(8)\n" \
    "v_mfma_f32_16x16x32_bf16 v[62:65], v[102:105], v[130:133], v[62:65]\n" \
    "ds_read_b64_tr_b16 v[144:145], v155 offset:34816\n" \
    "s_waitcnt lgkmcnt(7)\n" \
    "v_mfma_f32_16x16x32_bf16 v[82:85], v[134:137], v[102:105], v[82:85]\n" \
    "ds_read_b64_tr_b16 v[146:147], v156 offset:32768\n" \
    "s_waitcnt lgkmcnt(5)\n" \
    "v_mfma_f32_16x16x32_bf16 v[22:25], v[122:125], v[102:105], v[22:25]\n" \
    "ds_read_b128 v[102:105], %[ap1] offset:20480\n" \
    "v_mfma_f32_16x16x32_bf16 v[46:49], v[126:129], v[106:109], v[46:49]\n" \
    "ds_read_b64_tr_b16 v[148:149], v156 offset:34816\n" \
    "v_mfma_f32_16x16x32_bf16 v[66:69], v[106:109], v[130:133], v[66:69]\n" \
    "ds_read_b64_tr_b16 v[150:151], v157 offset:32768\n" \
    "v_mfma_f32_16x16x32_bf16 v[86:89], v[134:137], v[106:109], v[86:89]\n" \
    "ds_read_b64_tr_b16 v[152:153], v157 offset:34816\n" \
    "v_mfma_f32_16x16x32_bf16 v[26:29], v[122:125], v[106:109], v[26:29]\n" \
    "ds_read_b128 v[106:109], %[ap1] offset:22528\n" \
    "s_waitcnt lgkmcnt(8)\n" \
    "v_mfma_f32_16x16x32_bf16 v[50:53], v[126:129], v[110:113], v[50:53]\n" \
    "ds_read_b64_tr_b16 v[138:139], v154 offset:32768\n" \
    "v_mfma_f32_16x16x32_bf16 v[70:73], v[110:113], v[130:133], v[70:73]\n" \
    "ds_read_b64_tr_b16 v[140:141], v154 offset:34816\n" \
    "v_mfma_f32_16x16x32_bf16 v[90:93], v[134:137], v[110:113], v[90:93]\n" \
    "ds_read_b128 v[118:121], %[ap1] offset:26624\n" \
    "v_mfma_f32_16x16x32_bf16 v[30:33], v[122:125], v[110:113], v[30:33]\n" \
    "ds_read_b128 v[110:113], %[ap1] offset:24576\n" \
    "v_mov_b32 v161, 16\n" \
    "ds_write_b32 %[fr], v161\n" \
    "v_mfma_f32_16x16x32_bf16 v[54:57], v[126:129], v[114:117], v[54:57]\n" \
    "s_barrier\n" \
    "v_mfma_f32_16x16x32_bf16 v[74:77], v[114:117], v[130:133], v[74:77]\n" \
    "v_mfma_f32_16x16x32_bf16 v[94:97], v[134:137], v[114:117], v[94:97]\n" \
    "v_mfma_f32_16x16x32_bf16 v[34:37], v[122:125], v[114:117], v[34:37]\n" \
    "s_waitcnt lgkmcnt(9)\n" \
    "v_mfma_f32_16x16x32_bf16 v[42:45], v[142:145], v[102:105], v[42:45]\n" \
    "ds_read_b64_tr_b16 v[126:127], %[wp1] offset:0\n" \
    "s_waitcnt lgkmcnt(9)\n" \
    "v_mfma_f32_16x16x32_bf16 v[62:65], v[102:105], v[146:149], v[62:65]\n" \
    "ds_read_b64_tr_b16 v[128:129], %[wp1] offset:2048\n" \
    "s_waitcnt lgkmcnt(8)\n" \
    "v_mfma_f32_16x16x32_bf16 v[82:85], v[150:153], v[102:105], v[82:85]\n" \
    "ds_read_b64_tr_b16 v[130:131], %[wp2] offset:0\n" \
    "s_waitcnt lgkmcnt(6)\n" \
    "v_mfma_f32_16x16x32_bf16 v[22:25], v[138:141], v[102:105], v[22:25]\n" \
    "ds_read_b128 v[102:105], %[ap0] offset:55296\n" \
    "v_mfma_f32_16x16x32_bf16 v[46:49], v[142:145], v[106:109], v[46:49]\n" \
    "ds_read_b64_tr_b16 v[132:133], %[wp2] offset:2048\n" \
    "v_mfma_f32_16x16x32_bf16 v[66:69], v[106:109], v[146:149], v[66:69]\n" \
    "ds_read_b64_tr_b16 v[134:135], %[wp3] offset:0\n" \
    "v_mfma_f32_16x16x32_bf16 v[86:89], v[150:153], v[106:109], v[86:89]\n" \
    "ds_read_b64_tr_b16 v[136:137], %[wp3] offset:2048\n" \
    "v_mfma_f32_16x16x32_bf16 v[26:29], v[138:141], v[106:109], v[26:29]\n" \
    "ds_read_b128 v[106:109], %[ap0] offset:57344\n" \
    "s_waitcnt lgkmcnt(9)\n" \
    "v_mfma_f32_16x16x32_bf16 v[50:53], v[142:145], v[110:113], v[50:53]\n" \
    "ds_read_b64_tr_b16 v[122:123], %[wp0] offset:0\n" \
    "v_mfma_f32_16x16x32_bf16 v[70:73], v[110:113], v[146:149], v[70:73]\n" \
    "ds_read_b64_tr_b16 v[124:125], %[wp0] offset:2048\n" \
    "v_mfma_f32_16x16x32_bf16 v[90:93], v[150:153], v[110:113], v[90:93]\n" \
    "ds_read_b128 v[114:117], %[ap0] offset:61440\n" \
    "v_mfma_f32_16x16x32_bf16 v[30:33], v[138:141], v[110:113], v[30:33]\n" \
    "ds_read_b128 v[110:113], %[ap0] offset:59392\n" \
    "v_mov_b32 v161, 17\n" \
    "ds_write_b32 %[fr], v161\n" \
    "v_mfma_f32_16x16x32_bf16 v[54:57], v[142:145], v[118:121], v[54:57]\n" \
    "s_barrier\n" \
    "ds_read_b64_tr_b16 v[142:143], %[wp1] offset:34816\n" \
    "ds_read_b64_tr_b16 v[144:145], %[wp1] offset:36864\n" \
    "v_mfma_f32_16x16x32_bf16 v[74:77], v[118:121], v[146:149], v[74:77]\n" \
    "ds_read_b64_tr_b16 v[146:147], %[wp2] offset:34816\n" \
    "ds_read_b64_tr_b16 v[148:149], %[wp2] offset:36864\n" \
    "v_mfma_f32_16x16x32_bf16 v[94:97], v[150:153], v[118:121], v[94:97]\n" \
    "ds_read_b64_tr_b16 v[150:151], %[wp3] offset:34816\n" \
    "ds_read_b64_tr_b16 v[152:153], %[wp3] offset:36864\n" \
    "v_mfma_f32_16x16x32_bf16 v[34:37], v[138:141], v[118:121], v[34:37]\n" \
    "ds_read_b128 v[118:121], %[ap1] offset:61440\n" \
    "ds_read_b64_tr_b16 v[138:139], %[wp0] offset:34816\n" \
    "ds_read_b64_tr_b16 v[140:141], %[wp0] offset:36864\n" \
    "s_waitcnt lgkmcnt(11)\n" \
    "v_mfma_f32_16x16x32_bf16 v[54:57], v[126:129], v[114:117], v[54:57]\n" \
    "v_add_u32 %[wp0], %[tabc], %[l16]\n" \
    "v_cndmask_b32_e64 %[wp2], 0, -1, s[68:69]\n" \
    "v_mfma_f32_16x16x32_bf16 v[74:77], v[114:117], v[130:133], v[74:77]\n" \
    "v_add_u32 %[wp1], %[hxc], %[l16]\n" \
    "v_lshrrev_b32 %[wp3], 2, %[l16]\n" \
    "v_mfma_f32_16x16x32_bf16 v[94:97], v[134:137], v[114:117], v[94:97]\n" \
    "v_add_u32 %[wp3], %[hxd], %[wp3]\n" \
    "v_mfma_f32_16x16x32_bf16 v[34:37], v[122:125], v[114:117], v[34:37]\n" \
    "s_waitcnt lgkmcnt(10)\n" \
    "v_mfma_f32_16x16x32_bf16 v[50:53], v[126:129], v[110:113], v[50:53]\n" \
    "v_mfma_f32_16x16x32_bf16 v[70:73], v[110:113], v[130:133], v[70:73]\n" \
    "v_mfma_f32_16x16x32_bf16 v[90:93], v[134:137], v[110:113], v[90:93]\n" \
    "v_mfma_f32_16x16x32_bf16 v[30:33], v[122:125], v[110:113], v[30:33]\n" \
    "ds_read_b128 v[110:113], %[ap1] offset:59392\n" \
    "s_waitcnt lgkmcnt(3)\n" \
    "v_mfma_f32_16x16x32_bf16 v[54:57], v[142:145], v[118:121], v[54:57]\n" \
    "v_cndmask_b32_e64 v154, %[zr], %[vxb], s[60:61]\n" \
    "v_cndmask_b32_e64 v155, %[zr], %[vxb], s[62:63]\n" \
    "v_mfma_f32_16x16x32_bf16 v[74:77], v[118:121], v[146:149], v[74:77]\n" \
    "v_cndmask_b32_e64 v156, %[zr], %[vxb], s[64:65]\n" \
    "v_cndmask_b32_e64 v157, %[zr], %[vxb], s[66:67]\n" \
    "v_mfma_f32_16x16x32_bf16 v[94:97], v[150:153], v[118:121], v[94:97]\n" \
    "v_mov_b32 v41, 0\n" \
    "s_waitcnt lgkmcnt(1)\n" \
    "v_mfma_f32_16x16x32_bf16 v[34:37], v[138:141], v[118:121], v[34:37]\n" \
    "v_mfma_f32_16x16x32_bf16 v[46:49], v[126:129], v[106:109], v[46:49]\n" \
    "v_mfma_f32_16x16x32_bf16 v[66:69], v[106:109], v[130:133], v[66:69]\n" \
    "v_cvt_pk_bf16_f32 v54, v54, v55\n" \
    "v_cvt_pk_bf16_f32 v55, v56, v57\n" \
    "v_mfma_f32_16x16x32_bf16 v[86:89], v[134:137], v[106:109], v[86:89]\n" \
    "v_cvt_pk_bf16_f32 v120, v74, v75\n" \
    "v_cvt_pk_bf16_f32 v121, v76, v77\n" \
    "v_mfma_f32_16x16x32_bf16 v[26:29], v[122:125], v[106:109], v[26:29]\n" \
    "ds_read_b128 v[106:109], %[ap1] offset:57344\n" \
    "v_cvt_pk_bf16_f32 v94, v94, v95\n" \
    "v_cvt_pk_bf16_f32 v56, v96, v97\n" \
    "ds_write2_b32 %[scw], v94, v56 offset0:128 offset1:129\n" \
    "s_waitcnt lgkmcnt(2)\n" \
    "v_mfma_f32_16x16x32_bf16 v[50:53], v[142:145], v[110:113], v[50:53]\n" \
    "v_permlane16_swap_b32 v94, v56\n" \
    "v_mfma_f32_16x16x32_bf16 v[70:73], v[110:113], v[146:149], v[70:73]\n" \
    "v_cvt_pk_bf16_f32 v34, v34, v35\n" \
    "v_cvt_pk_bf16_f32 v35, v36, v37\n" \
    "v_mfma_f32_16x16x32_bf16 v[90:93], v[150:153], v[110:113], v[90:93]\n" \
    "v_and_b32 v36, v94, %[wp2]\n" \
    "v_mov_b32 v37, 0\n" \
    "v_mov_b32 v57, 0\n" \
    "v_mfma_f32_16x16x32_bf16 v[30:33], v[138:141], v[110:113], v[30:33]\n" \
    "v_mfma_f32_16x16x32_bf16 v[42:45], v[126:129], v[102:105], v[42:45]\n" \
    "v_mfma_f32_16x16x32_bf16 v[62:65], v[102:105], v[130:133], v[62:65]\n" \
    "v_cvt_pk_bf16_f32 v50, v50, v51\n" \
    "v_cvt_pk_bf16_f32 v51, v52, v53\n" \
    "v_mfma_f32_16x16x32_bf16 v[82:85], v[134:137], v[102:105], v[82:85]\n" \
    "v_cvt_pk_bf16_f32 v118, v70, v71\n" \
    "v_cvt_pk_bf16_f32 v119, v72, v73\n" \
    "v_mfma_f32_16x16x32_bf16 v[22:25], v[122:125], v[102:105], v[22:25]\n" \
    "ds_read_b128 v[102:105], %[ap1] offset:55296\n" \
    "v_mov_b32 v161, 18\n" \
    "ds_write_b32 %[fr], v161\n" \
    "ds_read_b128 v[126:129], %[wp0] offset:1024\n" \
    "ds_read_b128 v[122:125], %[wp0]\n" \
    "ds_read_b128 v[130:133], %[wp0] offset:2048\n" \
    "v_cvt_pk_bf16_f32 v90, v90, v91\n" \
    "v_cvt_pk_bf16_f32 v52, v92, v93\n" \
    "ds_write2_b32 %[scw], v90, v52 offset0:96 offset1:97\n" \
    "s_waitcnt lgkmcnt(7)\n" \
    "v_mfma_f32_16x16x32_bf16 v[46:49], v[142:145], v[106:109], v[46:49]\n" \
    "v_permlane16_swap_b32 v90, v52\n" \
    "ds_read_b32 v160, %[fr] offset:128\n" \
    "v_mfma_f32_16x16x32_bf16 v[66:69], v[106:109], v[146:149], v[66:69]\n" \
    "v_cvt_pk_bf16_f32 v30, v30, v31\n" \
    "v_cvt_pk_bf16_f32 v31, v32, v33\n" \
    "v_mfma_f32_16x16x32_bf16 v[86:89], v[150:153], v[106:109], v[86:89]\n" \
    "v_and_b32 v32, v90, %[wp2]\n" \
    "v_mov_b32 v33, 0\n" \
    "v_mov_b32 v53, 0\n" \
    "v_mfma_f32_16x16x32_bf16 v[26:29], v[138:141], v[106:109], v[26:29]\n" \
    "s_waitcnt lgkmcnt(6)\n" \
    "v_mfma_f32_16x16x32_bf16 v[42:45], v[142:145], v[102:105], v[42:45]\n" \
    "v_mfma_f32_16x16x32_bf16 v[62:65], v[102:105], v[146:149], v[62:65]\n" \
    "v_cvt_pk_bf16_f32 v46, v46, v47\n" \
    "v_cvt_pk_bf16_f32 v47, v48, v49\n" \
    "v_mfma_f32_16x16x32_bf16 v[82:85], v[150:153], v[102:105], v[82:85]\n" \
    "v_cvt_pk_bf16_f32 v116, v66, v67\n" \
    "v_cvt_pk_bf16_f32 v117, v68, v69\n" \
    "v_mfma_f32_16x16x32_bf16 v[22:25], v[138:141], v[102:105], v[22:25]\n" \
    "ds_read_b32 v159, %[zr] offset:1332\n" \
    "s_setprio 0\n" \
    "s_waitcnt lgkmcnt(1)\n" \
    "v_readfirstlane_b32 %[tmp], v160\n" \
    "s_cmp_lg_u32 %[tmp], 0\n" \
    "s_cbranch_scc1 Lhx_%=\n" \
    "s_mov_b32 %[cnt], 0\n" \
    "Lhxs_%=:\n" \
    "ds_read_b32 v160, %[fr] offset:128\n" \
    "s_add_u32 %[cnt], %[cnt], 1\n" \
    "s_waitcnt lgkmcnt(0)\n" \
    "v_readfirstlane_b32 %[tmp], v160\n" \
    "s_cmp_ge_u32 %[cnt], %[cap]\n" \
    "s_cbranch_scc1 Lhx_%=\n" \
    "s_cmp_eq_u32 %[tmp], 0\n" \
    "s_cbranch_scc1 Lhxs_%=\n" \
    "Lhx_%=:\n" \
    "ds_read_b64 v[38:39], %[wp1]\n" \
    "ds_read_b32 v40, %[wp3]\n" \
    "ds_read_b64 v[112:113], %[wp1] offset:8\n" \
    "v_mfma_f32_16x16x32_bf16 v[74:77], v[54:57], v[34:37], v[126:129]\n" \
    "v_cvt_pk_bf16_f32 v86, v86, v87\n" \
    "v_cvt_pk_bf16_f32 v48, v88, v89\n" \
    "ds_write2_b32 %[scw], v86, v48 offset0:64 offset1:65\n" \
    "v_mfma_f32_16x16x32_bf16 v[94:97], v[50:53], v[34:37], v[122:125]\n" \
    "v_cvt_pk_bf16_f32 v42, v42, v43\n" \
    "v_cvt_pk_bf16_f32 v43, v44, v45\n" \
    "v_permlane16_swap_b32 v86, v48\n" \
    "v_mfma_f32_16x16x32_bf16 v[70:73], v[50:53], v[30:33], v[126:129]\n" \
    "v_cvt_pk_bf16_f32 v26, v26, v27\n" \
    "v_cvt_pk_bf16_f32 v27, v28, v29\n" \
    "v_and_b32 v28, v86, %[wp2]\n" \
    "v_mov_b32 v29, 0\n" \
    "v_mov_b32 v49, 0\n" \
    "s_nop 1\n" \
    "v_mfma_f32_16x16x32_bf16 v[90:93], v[46:49], v[30:33], v[122:125]\n" \
    "v_cvt_pk_bf16_f32 v82, v82, v83\n" \
    "v_cvt_pk_bf16_f32 v44, v84, v85\n" \
    "ds_write2_b32 %[scw], v82, v44 offset0:32 offset1:33\n" \
    "v_cvt_pk_bf16_f32 v114, v62, v63\n" \
    "v_cvt_pk_bf16_f32 v115, v64, v65\n" \
    "v_mfma_f32_16x16x32_bf16 v[66:69], v[46:49], v[26:29], v[126:129]\n" \
    "v_mov_b32 v45, 0\n" \
    "v_permlane16_swap_b32 v82, v44\n" \
    "v_maximum3_f32 v144, v74, v75, v75\n" \
    "s_nop 0\n" \
    "v_mfma_f32_16x16x32_bf16 v[86:89], v[42:45], v[26:29], v[122:125]\n" \
    "v_cvt_pk_bf16_f32 v22, v22, v23\n" \
    "v_cvt_pk_bf16_f32 v23, v24, v25\n" \
    "v_and_b32 v24, v82, %[wp2]\n" \
    "v_mov_b32 v25, 0\n" \
    "v_maximum3_f32 v145, v76, v77, v77\n" \
    "ds_read_b64_tr_b16 v[106:107], v157 offset:384\n" \
    "ds_read_b64_tr_b16 v[108:109], v157 offset:512\n" \
    "v_mfma_f32_16x16x32_bf16 v[62:65], v[42:45], v[22:25], v[126:129]\n" \
    "v_maximum3_f32 v144, v94, v95, v144\n" \
    "v_maximum3_f32 v145, v96, v97, v145\n" \
    "s_waitcnt lgkmcnt(5)\n" \
    "v_mfma_f32_16x16x32_bf16 v[82:85], v[38:41], v[22:25], v[122:125]\n" \
    "ds_read_b64_tr_b16 v[102:103], v156 offset:256\n" \
    "ds_read_b64_tr_b16 v[104:105], v156 offset:384\n" \
    "v_maximum3_f32 v142, v70, v71, v71\n" \
    "v_maximum3_f32 v143, v72, v73, v73\n" \
    "ds_read_b64_tr_b16 v[98:99], v155 offset:128\n" \
    "ds_read_b64_tr_b16 v[100:101], v155 offset:256\n" \
    "v_maximum3_f32 v142, v90, v91, v142\n" \
    "v_maximum3_f32 v143, v92, v93, v143\n" \
    "ds_read_b64_tr_b16 v[134:135], v154 offset:0\n" \
    "ds_read_b64_tr_b16 v[136:137], v154 offset:128\n" \
    "v_maximum3_f32 v149, v144, v145, v145\n" \
    "v_maximum3_f32 v140, v66, v67, v67\n" \
    "v_maximum3_f32 v141, v68, v69, v69\n" \
    "v_maximum3_f32 v148, v142, v143, v143\n" \
    "v_maximum3_f32 v140, v86, v87, v140\n" \
    "v_maximum3_f32 v141, v88, v89, v141\n" \
    "v_maximum3_f32 v138, v62, v63, v63\n" \
    "v_maximum3_f32 v139, v64, v65, v65\n" \
    "v_maximum3_f32 v147, v140, v141, v141\n" \
    "v_maximum3_f32 v138, v82, v83, v138\n" \
    "v_maximum3_f32 v139, v84, v85, v139\n" \
    "v_maximum3_f32 v146, v138, v139, v139\n" \
    "s_waitcnt lgkmcnt(8)\n" \
    "Lend_%=:\n" \
    "s_nop 0\n" \
    "v_permlane16_swap_b32 v146, v147\n" \
    "v_permlane16_swap_b32 v148, v149\n" \
    "v_maximum3_f32 v146, v146, v147, v147\n" \
    "v_maximum3_f32 v148, v148, v149, v149\n" \
    "s_nop 1\n" \
    "v_permlane32_swap_b32 v146, v148\n" \
    "v_maximum3_f32 v146, v146, v148, v148\n" \
    "v_mul_f32 v146, 0x3ea52b5b, v146\n" \
    "v_mov_b32 v148, v146\n" \
    "s_nop 1\n" \
    "v_permlane32_swap_b32 v146, v148\n" \
    "v_mov_b32 v147, v146\n" \
    "v_mov_b32 v149, v148\n" \
    "s_nop 0\n" \
    "v_permlane16_swap_b32 v146, v147\n" \
    "v_permlane16_swap_b32 v148, v149\n" \
    "v_fma_f32 v138, v138, %[sc], -v146\n" \
    "v_fma_f32 v139, v139, %[sc], -v146\n" \
    "v_fma_f32 v140, v140, %[sc], -v147\n" \
    "v_fma_f32 v141, v141, %[sc], -v147\n" \
    "v_exp_f32 v138, v138\n" \
    "v_exp_f32 v139, v139\n" \
    "v_fma_f32 v142, v142, %[sc], -v148\n" \
    "v_fma_f32 v143, v143, %[sc], -v148\n" \
    "v_exp_f32 v140, v140\n" \
    "v_exp_f32 v141, v141\n" \
    "v_fma_f32 v144, v144, %[sc], -v149\n" \
    "v_fma_f32 v145, v145, %[sc], -v149\n" \
    "v_exp_f32 v142, v142\n" \
    "v_exp_f32 v143, v143\n" \
    "v_add_f32 v146, v138, v139\n" \
    "v_exp_f32 v144, v144\n" \
    "v_exp_f32 v145, v145\n" \
    "v_add_f32 v147, v140, v141\n" \
    "v_add_f32 v148, v142, v143\n" \
    "v_add_f32 v149, v144, v145\n" \
    "v_permlane16_swap_b32 v146, v147\n" \
    "s_nop 0\n" \
    "v_permlane16_swap_b32 v148, v149\n" \
    "v_add_f32 v146, v146, v147\n" \
    "v_add_f32 v148, v148, v149\n" \
    "s_nop 1\n" \
    "v_permlane32_swap_b32 v146, v148\n" \
    "v_add_f32 v146, v146, v148\n" \
    "v_rcp_f32 v146, v146\n" \
    "v_add_u32 v150, 40960, %[oo]\n" \
    "v_mov_b32 v148, v146\n" \
    "s_nop 1\n" \
    "v_permlane32_swap_b32 v146, v148\n" \
    "v_mov_b32 v147, v146\n" \
    "v_mov_b32 v149, v148\n" \
    "s_nop 0\n" \
    "v_permlane16_swap_b32 v146, v147\n" \
    "v_permlane16_swap_b32 v148, v149\n" \
    "v_mul_f32 v144, v144, v149\n" \
    "v_mul_f32 v145, v145, v149\n" \
    "v_cvt_pk_bf16_f32 v144, v144, v145\n" \
    "v_mul_f32 v142, v142, v148\n" \
    "v_mul_f32 v143, v143, v148\n" \
    "v_cvt_pk_bf16_f32 v142, v142, v143\n" \
    "v_perm_b32 v34, v144, v144, v130\n" \
    "v_perm_b32 v35, v144, v144, v131\n" \
    "v_perm_b32 v36, v144, v144, v132\n" \
    "v_perm_b32 v37, v144, v144, v133\n" \
    "v_mul_f32 v140, v140, v147\n" \
    "v_mul_f32 v141, v141, v147\n" \
    "v_cvt_pk_bf16_f32 v140, v140, v141\n" \
    "v_mfma_f32_16x16x32_bf16 v[74:77], v[118:121], v[34:37], 0\n" \
    "s_waitcnt lgkmcnt(6)\n" \
    "v_mfma_f32_16x16x32_bf16 v[94:97], v[106:109], v[34:37], 0\n" \
    "v_perm_b32 v30, v142, v142, v130\n" \
    "v_perm_b32 v31, v142, v142, v131\n" \
    "v_perm_b32 v32, v142, v142, v132\n" \
    "v_perm_b32 v33, v142, v142, v133\n" \
    "v_mul_f32 v138, v138, v146\n" \
    "v_mul_f32 v139, v139, v146\n" \
    "v_cvt_pk_bf16_f32 v138, v138, v139\n" \
    "v_mfma_f32_16x16x32_bf16 v[70:73], v[116:119], v[30:33], 0\n" \
    "s_waitcnt lgkmcnt(4)\n" \
    "v_mfma_f32_16x16x32_bf16 v[94:97], v[102:105], v[30:33], v[94:97]\n" \
    "v_perm_b32 v26, v140, v140, v130\n" \
    "v_perm_b32 v27, v140, v140, v131\n" \
    "v_perm_b32 v28, v140, v140, v132\n" \
    "v_perm_b32 v29, v140, v140, v133\n" \
    "s_nop 1\n" \
    "v_mfma_f32_16x16x32_bf16 v[66:69], v[114:117], v[26:29], 0\n" \
    "s_waitcnt lgkmcnt(2)\n" \
    "v_mfma_f32_16x16x32_bf16 v[94:97], v[98:101], v[26:29], v[94:97]\n" \
    "v_perm_b32 v22, v138, v138, v130\n" \
    "v_perm_b32 v23, v138, v138, v131\n" \
    "v_perm_b32 v24, v138, v138, v132\n" \
    "v_perm_b32 v25, v138, v138, v133\n" \
    "s_nop 1\n" \
    "v_mfma_f32_16x16x32_bf16 v[62:65], v[112:115], v[22:25], 0\n" \
    "s_waitcnt lgkmcnt(0)\n" \
    "v_mfma_f32_16x16x32_bf16 v[94:97], v[134:137], v[22:25], v[94:97]\n" \
    "v_readfirstlane_b32 %[tmp], v159\n" \
    "s_cmp_lg_u32 %[tmp], 0\n" \
    "s_cbranch_scc1 Lse_%=\n" \
    "s_mov_b32 %[cnt], 0\n" \
    "Lses_%=:\n" \
    "ds_read_b32 v159, %[zr] offset:1332\n" \
    "s_add_u32 %[cnt], %[cnt], 1\n" \
    "s_waitcnt lgkmcnt(0)\n" \
    "v_readfirstlane_b32 %[tmp], v159\n" \
    "s_cmp_ge_u32 %[cnt], %[cap]\n" \
    "s_cbranch_scc1 Lse_%=\n" \
    "s_cmp_eq_u32 %[tmp], 0\n" \
    "s_cbranch_scc1 Lses_%=\n" \
    "Lse_%=:\n" \
    "v_cvt_pk_bf16_f32 v52, v74, v75\n" \
    "v_cvt_pk_bf16_f32 v53, v76, v77\n" \
    "v_cvt_pk_bf16_f32 v50, v70, v71\n" \
    "v_cvt_pk_bf16_f32 v51, v72, v73\n" \
    "v_cvt_pk_bf16_f32 v44, v66, v67\n" \
    "v_cvt_pk_bf16_f32 v45, v68, v69\n" \
    "v_permlane16_swap_b32 v50, v52\n" \
    "v_permlane16_swap_b32 v51, v53\n" \
    "v_cvt_pk_bf16_f32 v42, v62, v63\n" \
    "v_cvt_pk_bf16_f32 v43, v64, v65\n" \
    "global_store_dwordx4 v150, v[50:53], %[wsp] sc1\n" \
    "v_cvt_pk_bf16_f32 v94, v94, v95\n" \
    "v_cvt_pk_bf16_f32 v95, v96, v97\n" \
    "v_permlane16_swap_b32 v42, v44\n" \
    "v_permlane16_swap_b32 v43, v45\n" \
    "global_store_dwordx4 %[oo], v[42:45], %[wsp] sc1\n" \
    "global_store_dwordx2 %[ox], v[94:95], %[wsp] sc1\n" \
    "s_nop 1\n"
#define G_CLOB "v22", "v23", "v24", "v25", "v26", "v27", "v28", "v29", "v30", "v31", "v32", "v33", "v34", "v35", "v36", "v37", "v38", "v39", "v40", "v41", "v42", "v43", "v44", "v45", "v46", "v47", "v48", "v49", "v50", "v51", "v52", "v53", "v54", "v55", "v56", "v57", "v58", "v59", "v60", "v61", "v62", "v63", "v64", "v65", "v66", "v67", "v68", "v69", "v70", "v71", "v72", "v73", "v74", "v75", "v76", "v77", "v78", "v79", "v80", "v81", "v82", "v83", "v84", "v85", "v86", "v87", "v88", "v89", "v90", "v91", "v92", "v93", "v94", "v95", "v96", "v97", "v98", "v99", "v100", "v101", "v102", "v103", "v104", "v105", "v106", "v107", "v108", "v109", "v110", "v111", "v112", "v113", "v114", "v115", "v116", "v117", "v118", "v119", "v120", "v121", "v122", "v123", "v124", "v125", "v126", "v127", "v128", "v129", "v130", "v131", "v132", "v133", "v134", "v135", "v136", "v137", "v138", "v139", "v140", "v141", "v142", "v143", "v144", "v145", "v146", "v147", "v148", "v149", "v150", "v151", "v152", "v153", "v154", "v155", "v156", "v157", "v158", "v159", "v160", "v161", "s60", "s61", "s62", "s63", "s64", "s65", "s66", "s67", "s68", "s69", "s70", "s71", "vcc", "scc", "memory"
#define P_ASM \
    "s_cmp_eq_u32 %[role], 1\n" \
    "s_cbranch_scc0 Lr0_%=\n" \
    "s_waitcnt vmcnt(8)\n" \
    "v_mov_b32 v157, 1\n" \
    "s_mov_b64 exec, 1\n" \
    "ds_add_u32 %[ctl], v157 offset:184\n" \
    "s_mov_b64 exec, -1\n" \
    "Lr0_%=:\n" \
    "s_mov_b32 %[cnt], 0\n" \
    "La_%=:\n" \
    "ds_read_b32 v157, %[wa] offset:0\n" \
    "s_add_u32 %[cnt], %[cnt], 1\n" \
    "s_waitcnt lgkmcnt(0)\n" \
    "v_readfirstlane_b32 %[tmp], v157\n" \
    "s_cmp_ge_u32 %[tmp], %[won]\n" \
    "s_cbranch_scc1 Lax_%=\n" \
    "s_sleep 2\n" \
    "s_cmp_lt_u32 %[cnt], %[cap]\n" \
    "s_cbranch_scc1 La_%=\n" \
    "Lax_%=:\n" \
    "ds_read_b64_tr_b16 v[16:17], %[pc] offset:0\n" \
    "ds_read_b64_tr_b16 v[18:19], %[pd] offset:0\n" \
    "ds_read_b64_tr_b16 v[20:21], %[pc] offset:32\n" \
    "ds_read_b64_tr_b16 v[22:23], %[pd] offset:32\n" \
    "ds_read_b64_tr_b16 v[24:25], %[pc] offset:64\n" \
    "ds_read_b64_tr_b16 v[26:27], %[pd] offset:64\n" \
    "ds_read_b64_tr_b16 v[28:29], %[pc] offset:96\n" \
    "ds_read_b64_tr_b16 v[30:31], %[pd] offset:96\n" \
    "ds_read_b64_tr_b16 v[32:33], %[pc] offset:128\n" \
    "ds_read_b64_tr_b16 v[34:35], %[pd] offset:128\n" \
    "ds_read_b64_tr_b16 v[36:37], %[pc] offset:5120\n" \
    "ds_read_b64_tr_b16 v[38:39], %[pd] offset:5120\n" \
    "ds_read_b64_tr_b16 v[40:41], %[pc] offset:5152\n" \
    "ds_read_b64_tr_b16 v[42:43], %[pd] offset:5152\n" \
    "ds_read_b64_tr_b16 v[44:45], %[pc] offset:5184\n" \
    "ds_read_b64_tr_b16 v[46:47], %[pd] offset:5184\n" \
    "ds_read_b64_tr_b16 v[48:49], %[pc] offset:5216\n" \
    "ds_read_b64_tr_b16 v[50:51], %[pd] offset:5216\n" \
    "ds_read_b64_tr_b16 v[52:53], %[pc] offset:5248\n" \
    "ds_read_b64_tr_b16 v[54:55], %[pd] offset:5248\n" \
    "ds_read_b64_tr_b16 v[56:57], %[pc] offset:10240\n" \
    "ds_read_b64_tr_b16 v[58:59], %[pd] offset:10240\n" \
    "ds_read_b64_tr_b16 v[60:61], %[pc] offset:10272\n" \
    "ds_read_b64_tr_b16 v[62:63], %[pd] offset:10272\n" \
    "ds_read_b64_tr_b16 v[64:65], %[pc] offset:10304\n" \
    "ds_read_b64_tr_b16 v[66:67], %[pd] offset:10304\n" \
    "ds_read_b64_tr_b16 v[68:69], %[pc] offset:10336\n" \
    "ds_read_b64_tr_b16 v[70:71], %[pd] offset:10336\n" \
    "ds_read_b64_tr_b16 v[72:73], %[pc] offset:10368\n" \
    "ds_read_b64_tr_b16 v[74:75], %[pd] offset:10368\n" \
    "ds_read_b64_tr_b16 v[76:77], %[pc] offset:15360\n" \
    "ds_read_b64_tr_b16 v[78:79], %[pd] offset:15360\n" \
    "ds_read_b64_tr_b16 v[80:81], %[pc] offset:15392\n" \
    "ds_read_b64_tr_b16 v[82:83], %[pd] offset:15392\n" \
    "ds_read_b64_tr_b16 v[84:85], %[pc] offset:15424\n" \
    "ds_read_b64_tr_b16 v[86:87], %[pd] offset:15424\n" \
    "ds_read_b64_tr_b16 v[88:89], %[pc] offset:15456\n" \
    "ds_read_b64_tr_b16 v[90:91], %[pd] offset:15456\n" \
    "ds_read_b64_tr_b16 v[92:93], %[pc] offset:15488\n" \
    "ds_read_b64_tr_b16 v[94:95], %[pd] offset:15488\n" \
    "ds_read_b64_tr_b16 v[96:97], %[pc] offset:20480\n" \
    "ds_read_b64_tr_b16 v[98:99], %[pd] offset:20480\n" \
    "ds_read_b64_tr_b16 v[100:101], %[pc] offset:20512\n" \
    "ds_read_b64_tr_b16 v[102:103], %[pd] offset:20512\n" \
    "ds_read_b64_tr_b16 v[104:105], %[pc] offset:20544\n" \
    "ds_read_b64_tr_b16 v[106:107], %[pd] offset:20544\n" \
    "ds_read_b64_tr_b16 v[108:109], %[pc] offset:20576\n" \
    "ds_read_b64_tr_b16 v[110:111], %[pd] offset:20576\n" \
    "ds_read_b64_tr_b16 v[112:113], %[pc] offset:20608\n" \
    "ds_read_b64_tr_b16 v[114:115], %[pd] offset:20608\n" \
    "v_mov_b32 v157, 1\n" \
    "s_mov_b64 exec, 1\n" \
    "ds_add_u32 %[ctl], v157 offset:96\n" \
    "s_mov_b64 exec, -1\n" \
    "s_cmp_eq_u32 %[role], 1\n" \
    "s_cbranch_scc0 Lr1_%=\n" \
    "s_waitcnt vmcnt(0)\n" \
    "v_mov_b32 v157, 1\n" \
    "s_mov_b64 exec, 1\n" \
    "ds_add_u32 %[ctl], v157 offset:56\n" \
    "s_mov_b64 exec, -1\n" \
    "Lr1_%=:\n" \
    "s_cmp_eq_u32 %[role], 2\n" \
    "s_cbranch_scc0 Lr2_%=\n" \
    "s_mov_b32 %[cnt], 0\n" \
    "Lq_%=:\n" \
    "ds_read_b32 v157, %[ctl] offset:96\n" \
    "s_add_u32 %[cnt], %[cnt], 1\n" \
    "s_waitcnt lgkmcnt(0)\n" \
    "v_readfirstlane_b32 %[tmp], v157\n" \
    "s_cmp_ge_u32 %[tmp], 8\n" \
    "s_cbranch_scc1 Lqx_%=\n" \
    "s_sleep 1\n" \
    "s_cmp_lt_u32 %[cnt], %[cap]\n" \
    "s_cbranch_scc1 Lq_%=\n" \
    "Lqx_%=:\n" \
    "s_mov_b32 m0, %[qa]\n" \
    "s_nop 0\n" \
    "global_load_lds_dwordx4 %[q0], %[qs]\n" \
    "s_add_u32 m0, %[qa], 1024\n" \
    "s_nop 0\n" \
    "global_load_lds_dwordx4 %[q1], %[qs]\n" \
    "s_add_u32 m0, %[qa], 2048\n" \
    "s_mov_b32 exec_hi, 0\n" \
    "global_load_lds_dwordx4 %[q2], %[qs]\n" \
    "s_mov_b32 exec_hi, -1\n" \
    "Lr2_%=:\n" \
    "s_mov_b32 %[cnt], 0\n" \
    "Lb_%=:\n" \
    "ds_read_b32 v157, %[ctl] offset:100\n" \
    "s_add_u32 %[cnt], %[cnt], 1\n" \
    "s_waitcnt lgkmcnt(0)\n" \
    "v_readfirstlane_b32 %[tmp], v157\n" \
    "s_cmp_ge_u32 %[tmp], 1\n" \
    "s_cbranch_scc1 Lbx_%=\n" \
    "s_sleep 1\n" \
    "s_cmp_lt_u32 %[cnt], %[cap]\n" \
    "s_cbranch_scc1 Lb_%=\n" \
    "Lbx_%=:\n" \
    "s_mov_b32 %[cnt], 0\n" \
    "Lc_%=:\n" \
    "ds_read_b32 v157, %[ctl] offset:48\n" \
    "s_add_u32 %[cnt], %[cnt], 1\n" \
    "s_waitcnt lgkmcnt(0)\n" \
    "v_readfirstlane_b32 %[tmp], v157\n" \
    "s_cmp_ge_u32 %[tmp], %[ost]\n" \
    "s_cbranch_scc1 Lcx_%=\n" \
    "s_sleep 1\n" \
    "s_cmp_lt_u32 %[cnt], %[cap]\n" \
    "s_cbranch_scc1 Lc_%=\n" \
    "Lcx_%=:\n" \
    "s_mov_b32 %[cnt], 0\n" \
    "Ld_%=:\n" \
    "global_load_dword v156, %[wat], %[wsp] sc1\n" \
    "s_add_u32 %[cnt], %[cnt], 1\n" \
    "s_waitcnt vmcnt(0)\n" \
    "v_cmp_eq_u32 vcc, %[so], v156\n" \
    "s_mov_b32 %[wm], vcc_lo\n" \
    "s_and_b32 %[tmp], %[wm], 3\n" \
    "s_cbranch_scc0 Ldx_%=\n" \
    "s_sleep 4\n" \
    "s_cmp_lt_u32 %[cnt], %[cap]\n" \
    "s_cbranch_scc1 Ld_%=\n" \
    "Ldx_%=:\n" \
    "s_cmp_eq_u32 %[role], 2\n" \
    "s_cbranch_scc0 Lr3_%=\n" \
    "v_mov_b32 v157, 1\n" \
    "s_mov_b64 exec, 1\n" \
    "ds_add_u32 %[ctl], v157 offset:188\n" \
    "s_mov_b64 exec, -1\n" \
    "Lr3_%=:\n" \
    "s_setprio 1\n" \
    "global_load_dwordx4 v[116:119], %[ofo], %[wsp] offset:0 sc1\n" \
    "global_load_dwordx4 v[120:123], %[ofo], %[wsp] offset:1024 sc1\n" \
    "global_load_dwordx4 v[124:127], %[ofo], %[wsp] offset:2048 sc1\n" \
    "global_load_dwordx4 v[128:131], %[ofo], %[wsp] offset:3072 sc1\n" \
    "global_load_dwordx2 v[132:133], %[of4], %[wsp] sc1\n" \
    "global_load_dwordx2 v[134:135], %[of4], %[wsp] offset:128 sc1\n" \
    "ds_read_b32 v157, %[ctl] offset:96\n" \
    "s_mov_b32 %[ah], 1\n" \
    "s_and_b32 %[tmp], %[wm], 12\n" \
    "s_cbranch_scc1 Ln_%=\n" \
    "s_waitcnt vmcnt(5)\n" \
    "v_mfma_f32_16x16x32_bf16 v[136:139], v[16:19], v[116:119], 0\n" \
    "v_mfma_f32_16x16x32_bf16 v[140:143], v[20:23], v[116:119], 0\n" \
    "v_mfma_f32_16x16x32_bf16 v[144:147], v[24:27], v[116:119], 0\n" \
    "v_mfma_f32_16x16x32_bf16 v[148:151], v[28:31], v[116:119], 0\n" \
    "v_mfma_f32_16x16x32_bf16 v[152:155], v[32:35], v[116:119], 0\n" \
    "global_load_dwordx4 v[116:119], %[ofo], %[ws2] offset:0 sc1\n" \
    "s_waitcnt vmcnt(5)\n" \
    "v_mfma_f32_16x16x32_bf16 v[136:139], v[36:39], v[120:123], v[136:139]\n" \
    "v_mfma_f32_16x16x32_bf16 v[140:143], v[40:43], v[120:123], v[140:143]\n" \
    "v_mfma_f32_16x16x32_bf16 v[144:147], v[44:47], v[120:123], v[144:147]\n" \
    "v_mfma_f32_16x16x32_bf16 v[148:151], v[48:51], v[120:123], v[148:151]\n" \
    "v_mfma_f32_16x16x32_bf16 v[152:155], v[52:55], v[120:123], v[152:155]\n" \
    "global_load_dwordx4 v[120:123], %[ofo], %[ws2] offset:1024 sc1\n" \
    "s_waitcnt vmcnt(5)\n" \
    "v_mfma_f32_16x16x32_bf16 v[136:139], v[56:59], v[124:127], v[136:139]\n" \
    "v_mfma_f32_16x16x32_bf16 v[140:143], v[60:63], v[124:127], v[140:143]\n" \
    "v_mfma_f32_16x16x32_bf16 v[144:147], v[64:67], v[124:127], v[144:147]\n" \
    "v_mfma_f32_16x16x32_bf16 v[148:151], v[68:71], v[124:127], v[148:151]\n" \
    "v_mfma_f32_16x16x32_bf16 v[152:155], v[72:75], v[124:127], v[152:155]\n" \
    "global_load_dwordx4 v[124:127], %[ofo], %[ws2] offset:2048 sc1\n" \
    "s_waitcnt vmcnt(5)\n" \
    "v_mfma_f32_16x16x32_bf16 v[136:139], v[76:79], v[128:131], v[136:139]\n" \
    "v_mfma_f32_16x16x32_bf16 v[140:143], v[80:83], v[128:131], v[140:143]\n" \
    "v_mfma_f32_16x16x32_bf16 v[144:147], v[84:87], v[128:131], v[144:147]\n" \
    "v_mfma_f32_16x16x32_bf16 v[148:151], v[88:91], v[128:131], v[148:151]\n" \
    "v_mfma_f32_16x16x32_bf16 v[152:155], v[92:95], v[128:131], v[152:155]\n" \
    "global_load_dwordx4 v[128:131], %[ofo], %[ws2] offset:3072 sc1\n" \
    "s_waitcnt vmcnt(4)\n" \
    "v_mfma_f32_16x16x32_bf16 v[136:139], v[96:99], v[132:135], v[136:139]\n" \
    "v_mfma_f32_16x16x32_bf16 v[140:143], v[100:103], v[132:135], v[140:143]\n" \
    "v_mfma_f32_16x16x32_bf16 v[144:147], v[104:107], v[132:135], v[144:147]\n" \
    "v_mfma_f32_16x16x32_bf16 v[148:151], v[108:111], v[132:135], v[148:151]\n" \
    "v_mfma_f32_16x16x32_bf16 v[152:155], v[112:115], v[132:135], v[152:155]\n" \
    "global_load_dwordx2 v[132:133], %[of4], %[ws2] sc1\n" \
    "global_load_dwordx2 v[134:135], %[of4], %[ws2] offset:128 sc1\n" \
    "s_branch Lj_%=\n" \
    "Ln_%=:\n" \
    "s_waitcnt vmcnt(5)\n" \
    "global_load_dword v156, %[wat], %[wsp] sc1\n" \
    "s_waitcnt vmcnt(6)\n" \
    "v_mfma_f32_16x16x32_bf16 v[136:139], v[16:19], v[116:119], 0\n" \
    "v_mfma_f32_16x16x32_bf16 v[140:143], v[20:23], v[116:119], 0\n" \
    "v_mfma_f32_16x16x32_bf16 v[144:147], v[24:27], v[116:119], 0\n" \
    "v_mfma_f32_16x16x32_bf16 v[148:151], v[28:31], v[116:119], 0\n" \
    "v_mfma_f32_16x16x32_bf16 v[152:155], v[32:35], v[116:119], 0\n" \
    "s_waitcnt vmcnt(5)\n" \
    "v_mfma_f32_16x16x32_bf16 v[136:139], v[36:39], v[120:123], v[136:139]\n" \
    "v_mfma_f32_16x16x32_bf16 v[140:143], v[40:43], v[120:123], v[140:143]\n" \
    "v_mfma_f32_16x16x32_bf16 v[144:147], v[44:47], v[120:123], v[144:147]\n" \
    "v_mfma_f32_16x16x32_bf16 v[148:151], v[48:51], v[120:123], v[148:151]\n" \
    "v_mfma_f32_16x16x32_bf16 v[152:155], v[52:55], v[120:123], v[152:155]\n" \
    "s_waitcnt vmcnt(4)\n" \
    "v_mfma_f32_16x16x32_bf16 v[136:139], v[56:59], v[124:127], v[136:139]\n" \
    "v_mfma_f32_16x16x32_bf16 v[140:143], v[60:63], v[124:127], v[140:143]\n" \
    "v_mfma_f32_16x16x32_bf16 v[144:147], v[64:67], v[124:127], v[144:147]\n" \
    "v_mfma_f32_16x16x32_bf16 v[148:151], v[68:71], v[124:127], v[148:151]\n" \
    "v_mfma_f32_16x16x32_bf16 v[152:155], v[72:75], v[124:127], v[152:155]\n" \
    "s_waitcnt vmcnt(3)\n" \
    "v_mfma_f32_16x16x32_bf16 v[136:139], v[76:79], v[128:131], v[136:139]\n" \
    "v_mfma_f32_16x16x32_bf16 v[140:143], v[80:83], v[128:131], v[140:143]\n" \
    "v_mfma_f32_16x16x32_bf16 v[144:147], v[84:87], v[128:131], v[144:147]\n" \
    "v_mfma_f32_16x16x32_bf16 v[148:151], v[88:91], v[128:131], v[148:151]\n" \
    "v_mfma_f32_16x16x32_bf16 v[152:155], v[92:95], v[128:131], v[152:155]\n" \
    "s_waitcnt vmcnt(1)\n" \
    "v_mfma_f32_16x16x32_bf16 v[136:139], v[96:99], v[132:135], v[136:139]\n" \
    "v_mfma_f32_16x16x32_bf16 v[140:143], v[100:103], v[132:135], v[140:143]\n" \
    "v_mfma_f32_16x16x32_bf16 v[144:147], v[104:107], v[132:135], v[144:147]\n" \
    "v_mfma_f32_16x16x32_bf16 v[148:151], v[108:111], v[132:135], v[148:151]\n" \
    "v_mfma_f32_16x16x32_bf16 v[152:155], v[112:115], v[132:135], v[152:155]\n" \
    "s_waitcnt vmcnt(0)\n" \
    "v_cmp_eq_u32 vcc, %[so], v156\n" \
    "s_mov_b32 %[wm], vcc_lo\n" \
    "s_mov_b32 %[ah], 0\n" \
    "s_and_b32 %[tmp], %[wm], 12\n" \
    "s_cbranch_scc1 Lj_%=\n" \
    "s_mov_b32 %[ah], 1\n" \
    "global_load_dwordx4 v[116:119], %[ofo], %[ws2] offset:0 sc1\n" \
    "global_load_dwordx4 v[120:123], %[ofo], %[ws2] offset:1024 sc1\n" \
    "global_load_dwordx4 v[124:127], %[ofo], %[ws2] offset:2048 sc1\n" \
    "global_load_dwordx4 v[128:131], %[ofo], %[ws2] offset:3072 sc1\n" \
    "global_load_dwordx2 v[132:133], %[of4], %[ws2] sc1\n" \
    "global_load_dwordx2 v[134:135], %[of4], %[ws2] offset:128 sc1\n" \
    "Lj_%=:\n" \
    "s_waitcnt lgkmcnt(0)\n" \
    "v_readfirstlane_b32 %[tmp], v157\n" \
    "s_cmp_ge_u32 %[tmp], 8\n" \
    "s_cbranch_scc1 Lex_%=\n" \
    "s_mov_b32 %[cnt], 0\n" \
    "Le_%=:\n" \
    "ds_read_b32 v157, %[ctl] offset:96\n" \
    "s_add_u32 %[cnt], %[cnt], 1\n" \
    "s_waitcnt lgkmcnt(0)\n" \
    "v_readfirstlane_b32 %[tmp], v157\n" \
    "s_cmp_ge_u32 %[tmp], 8\n" \
    "s_cbranch_scc1 Lex_%=\n" \
    "s_sleep 1\n" \
    "s_cmp_lt_u32 %[cnt], %[cap]\n" \
    "s_cbranch_scc1 Le_%=\n" \
    "Lex_%=:\n" \
    "s_nop 3\n" \
    "v_cmp_u_f32 vcc, v136, v136\n" \
    "s_cbranch_vccz Lfx_%=\n" \
    "s_waitcnt vmcnt(0)\n" \
    "s_mov_b32 %[ah], 0\n" \
    "s_mov_b32 %[cnt], 0\n" \
    "Lf_%=:\n" \
    "global_load_dwordx4 v[116:119], %[ofo], %[wsp] offset:0 sc1\n" \
    "global_load_dwordx4 v[120:123], %[ofo], %[wsp] offset:1024 sc1\n" \
    "global_load_dwordx4 v[124:127], %[ofo], %[wsp] offset:2048 sc1\n" \
    "global_load_dwordx4 v[128:131], %[ofo], %[wsp] offset:3072 sc1\n" \
    "global_load_dwordx2 v[132:133], %[of4], %[wsp] sc1\n" \
    "global_load_dwordx2 v[134:135], %[of4], %[wsp] offset:128 sc1\n" \
    "s_add_u32 %[cnt], %[cnt], 1\n" \
    "s_waitcnt vmcnt(0)\n" \
    "v_xor_b32 v157, %[so], v116\n" \
    "v_xor_b32 v158, %[so], v117\n" \
    "v_min_u32 v157, v157, v158\n" \
    "v_xor_b32 v158, %[so], v118\n" \
    "v_min_u32 v157, v157, v158\n" \
    "v_xor_b32 v158, %[so], v119\n" \
    "v_min_u32 v157, v157, v158\n" \
    "v_xor_b32 v158, %[so], v120\n" \
    "v_min_u32 v157, v157, v158\n" \
    "v_xor_b32 v158, %[so], v121\n" \
    "v_min_u32 v157, v157, v158\n" \
    "v_xor_b32 v158, %[so], v122\n" \
    "v_min_u32 v157, v157, v158\n" \
    "v_xor_b32 v158, %[so], v123\n" \
    "v_min_u32 v157, v157, v158\n" \
    "v_xor_b32 v158, %[so], v124\n" \
    "v_min_u32 v157, v157, v158\n" \
    "v_xor_b32 v158, %[so], v125\n" \
    "v_min_u32 v157, v157, v158\n" \
    "v_xor_b32 v158, %[so], v126\n" \
    "v_min_u32 v157, v157, v158\n" \
    "v_xor_b32 v158, %[so], v127\n" \
    "v_min_u32 v157, v157, v158\n" \
    "v_xor_b32 v158, %[so], v128\n" \
    "v_min_u32 v157, v157, v158\n" \
    "v_xor_b32 v158, %[so], v129\n" \
    "v_min_u32 v157, v157, v158\n" \
    "v_xor_b32 v158, %[so], v130\n" \
    "v_min_u32 v157, v157, v158\n" \
    "v_xor_b32 v158, %[so], v131\n" \
    "v_min_u32 v157, v157, v158\n" \
    "v_xor_b32 v158, %[so], v132\n" \
    "v_min_u32 v157, v157, v158\n" \
    "v_xor_b32 v158, %[so], v133\n" \
    "v_min_u32 v157, v157, v158\n" \
    "v_xor_b32 v158, %[so], v134\n" \
    "v_min_u32 v157, v157, v158\n" \
    "v_xor_b32 v158, %[so], v135\n" \
    "v_min_u32 v157, v157, v158\n" \
    "v_cmp_eq_u32 vcc, 0, v157\n" \
    "s_cmp_lt_u32 %[cnt], %[cap]\n" \
    "s_cbranch_vccz Lfy_%=\n" \
    "s_cbranch_scc1 Lf_%=\n" \
    "Lfy_%=:\n" \
    "s_waitcnt vmcnt(0)\n" \
    "v_mfma_f32_16x16x32_bf16 v[136:139], v[16:19], v[116:119], 0\n" \
    "v_mfma_f32_16x16x32_bf16 v[140:143], v[20:23], v[116:119], 0\n" \
    "v_mfma_f32_16x16x32_bf16 v[144:147], v[24:27], v[116:119], 0\n" \
    "v_mfma_f32_16x16x32_bf16 v[148:151], v[28:31], v[116:119], 0\n" \
    "v_mfma_f32_16x16x32_bf16 v[152:155], v[32:35], v[116:119], 0\n" \
    "s_waitcnt vmcnt(0)\n" \
    "v_mfma_f32_16x16x32_bf16 v[136:139], v[36:39], v[120:123], v[136:139]\n" \
    "v_mfma_f32_16x16x32_bf16 v[140:143], v[40:43], v[120:123], v[140:143]\n" \
    "v_mfma_f32_16x16x32_bf16 v[144:147], v[44:47], v[120:123], v[144:147]\n" \
    "v_mfma_f32_16x16x32_bf16 v[148:151], v[48:51], v[120:123], v[148:151]\n" \
    "v_mfma_f32_16x16x32_bf16 v[152:155], v[52:55], v[120:123], v[152:155]\n" \
    "s_waitcnt vmcnt(0)\n" \
    "v_mfma_f32_16x16x32_bf16 v[136:139], v[56:59], v[124:127], v[136:139]\n" \
    "v_mfma_f32_16x16x32_bf16 v[140:143], v[60:63], v[124:127], v[140:143]\n" \
    "v_mfma_f32_16x16x32_bf16 v[144:147], v[64:67], v[124:127], v[144:147]\n" \
    "v_mfma_f32_16x16x32_bf16 v[148:151], v[68:71], v[124:127], v[148:151]\n" \
    "v_mfma_f32_16x16x32_bf16 v[152:155], v[72:75], v[124:127], v[152:155]\n" \
    "s_waitcnt vmcnt(0)\n" \
    "v_mfma_f32_16x16x32_bf16 v[136:139], v[76:79], v[128:131], v[136:139]\n" \
    "v_mfma_f32_16x16x32_bf16 v[140:143], v[80:83], v[128:131], v[140:143]\n" \
    "v_mfma_f32_16x16x32_bf16 v[144:147], v[84:87], v[128:131], v[144:147]\n" \
    "v_mfma_f32_16x16x32_bf16 v[148:151], v[88:91], v[128:131], v[148:151]\n" \
    "v_mfma_f32_16x16x32_bf16 v[152:155], v[92:95], v[128:131], v[152:155]\n" \
    "s_waitcnt vmcnt(0)\n" \
    "v_mfma_f32_16x16x32_bf16 v[136:139], v[96:99], v[132:135], v[136:139]\n" \
    "v_mfma_f32_16x16x32_bf16 v[140:143], v[100:103], v[132:135], v[140:143]\n" \
    "v_mfma_f32_16x16x32_bf16 v[144:147], v[104:107], v[132:135], v[144:147]\n" \
    "v_mfma_f32_16x16x32_bf16 v[148:151], v[108:111], v[132:135], v[148:151]\n" \
    "v_mfma_f32_16x16x32_bf16 v[152:155], v[112:115], v[132:135], v[152:155]\n" \
    "s_nop 3\n" \
    "Lfx_%=:\n" \
    "v_mov_b32 v157, 1\n" \
    "ds_write_b128 %[xa0], v[136:139] offset:0\n" \
    "ds_write_b128 %[xa0], v[140:143] offset:1024\n" \
    "ds_write_b128 %[xa0], v[144:147] offset:2048\n" \
    "ds_write_b128 %[xa0], v[148:151] offset:3072\n" \
    "ds_write_b128 %[xa0], v[152:155] offset:4096\n" \
    "ds_write_b32 %[pf], v157 offset:0\n" \
    "s_cmp_lg_u32 %[ah], 0\n" \
    "s_cbranch_scc1 Lk_%=\n" \
    "s_and_b32 %[tmp], %[wm], 12\n" \
    "s_cbranch_scc0 Lhx_%=\n" \
    "s_mov_b32 %[cnt], 0\n" \
    "Lh_%=:\n" \
    "global_load_dword v156, %[wat], %[wsp] sc1\n" \
    "s_add_u32 %[cnt], %[cnt], 1\n" \
    "s_waitcnt vmcnt(0)\n" \
    "v_cmp_eq_u32 vcc, %[so], v156\n" \
    "s_mov_b32 %[wm], vcc_lo\n" \
    "s_and_b32 %[tmp], %[wm], 12\n" \
    "s_cbranch_scc0 Lhx_%=\n" \
    "s_sleep 4\n" \
    "s_cmp_lt_u32 %[cnt], %[cap]\n" \
    "s_cbranch_scc1 Lh_%=\n" \
    "Lhx_%=:\n" \
    "global_load_dwordx4 v[116:119], %[ofo], %[ws2] offset:0 sc1\n" \
    "global_load_dwordx4 v[120:123], %[ofo], %[ws2] offset:1024 sc1\n" \
    "global_load_dwordx4 v[124:127], %[ofo], %[ws2] offset:2048 sc1\n" \
    "global_load_dwordx4 v[128:131], %[ofo], %[ws2] offset:3072 sc1\n" \
    "global_load_dwordx2 v[132:133], %[of4], %[ws2] sc1\n" \
    "global_load_dwordx2 v[134:135], %[of4], %[ws2] offset:128 sc1\n" \
    "Lk_%=:\n" \
    "s_waitcnt vmcnt(5)\n" \
    "v_mfma_f32_16x16x32_bf16 v[136:139], v[16:19], v[116:119], 0\n" \
    "v_mfma_f32_16x16x32_bf16 v[140:143], v[20:23], v[116:119], 0\n" \
    "v_mfma_f32_16x16x32_bf16 v[144:147], v[24:27], v[116:119], 0\n" \
    "v_mfma_f32_16x16x32_bf16 v[148:151], v[28:31], v[116:119], 0\n" \
    "v_mfma_f32_16x16x32_bf16 v[152:155], v[32:35], v[116:119], 0\n" \
    "s_waitcnt vmcnt(4)\n" \
    "v_mfma_f32_16x16x32_bf16 v[136:139], v[36:39], v[120:123], v[136:139]\n" \
    "v_mfma_f32_16x16x32_bf16 v[140:143], v[40:43], v[120:123], v[140:143]\n" \
    "v_mfma_f32_16x16x32_bf16 v[144:147], v[44:47], v[120:123], v[144:147]\n" \
    "v_mfma_f32_16x16x32_bf16 v[148:151], v[48:51], v[120:123], v[148:151]\n" \
    "v_mfma_f32_16x16x32_bf16 v[152:155], v[52:55], v[120:123], v[152:155]\n" \
    "s_waitcnt vmcnt(3)\n" \
    "v_mfma_f32_16x16x32_bf16 v[136:139], v[56:59], v[124:127], v[136:139]\n" \
    "v_mfma_f32_16x16x32_bf16 v[140:143], v[60:63], v[124:127], v[140:143]\n" \
    "v_mfma_f32_16x16x32_bf16 v[144:147], v[64:67], v[124:127], v[144:147]\n" \
    "v_mfma_f32_16x16x32_bf16 v[148:151], v[68:71], v[124:127], v[148:151]\n" \
    "v_mfma_f32_16x16x32_bf16 v[152:155], v[72:75], v[124:127], v[152:155]\n" \
    "s_waitcnt vmcnt(2)\n" \
    "v_mfma_f32_16x16x32_bf16 v[136:139], v[76:79], v[128:131], v[136:139]\n" \
    "v_mfma_f32_16x16x32_bf16 v[140:143], v[80:83], v[128:131], v[140:143]\n" \
    "v_mfma_f32_16x16x32_bf16 v[144:147], v[84:87], v[128:131], v[144:147]\n" \
    "v_mfma_f32_16x16x32_bf16 v[148:151], v[88:91], v[128:131], v[148:151]\n" \
    "v_mfma_f32_16x16x32_bf16 v[152:155], v[92:95], v[128:131], v[152:155]\n" \
    "s_waitcnt vmcnt(0)\n" \
    "v_mfma_f32_16x16x32_bf16 v[136:139], v[96:99], v[132:135], v[136:139]\n" \
    "v_mfma_f32_16x16x32_bf16 v[140:143], v[100:103], v[132:135], v[140:143]\n" \
    "v_mfma_f32_16x16x32_bf16 v[144:147], v[104:107], v[132:135], v[144:147]\n" \
    "v_mfma_f32_16x16x32_bf16 v[148:151], v[108:111], v[132:135], v[148:151]\n" \
    "v_mfma_f32_16x16x32_bf16 v[152:155], v[112:115], v[132:135], v[152:155]\n" \
    "s_nop 3\n" \
    "v_cmp_u_f32 vcc, v136, v136\n" \
    "s_cbranch_vccz Lix_%=\n" \
    "s_waitcnt vmcnt(0)\n" \
    "s_mov_b32 %[ah], 0\n" \
    "s_mov_b32 %[cnt], 0\n" \
    "Li_%=:\n" \
    "global_load_dwordx4 v[116:119], %[ofo], %[ws2] offset:0 sc1\n" \
    "global_load_dwordx4 v[120:123], %[ofo], %[ws2] offset:1024 sc1\n" \
    "global_load_dwordx4 v[124:127], %[ofo], %[ws2] offset:2048 sc1\n" \
    "global_load_dwordx4 v[128:131], %[ofo], %[ws2] offset:3072 sc1\n" \
    "global_load_dwordx2 v[132:133], %[of4], %[ws2] sc1\n" \
    "global_load_dwordx2 v[134:135], %[of4], %[ws2] offset:128 sc1\n" \
    "s_add_u32 %[cnt], %[cnt], 1\n" \
    "s_waitcnt vmcnt(0)\n" \
    "v_xor_b32 v157, %[so], v116\n" \
    "v_xor_b32 v158, %[so], v117\n" \
    "v_min_u32 v157, v157, v158\n" \
    "v_xor_b32 v158, %[so], v118\n" \
    "v_min_u32 v157, v157, v158\n" \
    "v_xor_b32 v158, %[so], v119\n" \
    "v_min_u32 v157, v157, v158\n" \
    "v_xor_b32 v158, %[so], v120\n" \
    "v_min_u32 v157, v157, v158\n" \
    "v_xor_b32 v158, %[so], v121\n" \
    "v_min_u32 v157, v157, v158\n" \
    "v_xor_b32 v158, %[so], v122\n" \
    "v_min_u32 v157, v157, v158\n" \
    "v_xor_b32 v158, %[so], v123\n" \
    "v_min_u32 v157, v157, v158\n" \
    "v_xor_b32 v158, %[so], v124\n" \
    "v_min_u32 v157, v157, v158\n" \
    "v_xor_b32 v158, %[so], v125\n" \
    "v_min_u32 v157, v157, v158\n" \
    "v_xor_b32 v158, %[so], v126\n" \
    "v_min_u32 v157, v157, v158\n" \
    "v_xor_b32 v158, %[so], v127\n" \
    "v_min_u32 v157, v157, v158\n" \
    "v_xor_b32 v158, %[so], v128\n" \
    "v_min_u32 v157, v157, v158\n" \
    "v_xor_b32 v158, %[so], v129\n" \
    "v_min_u32 v157, v157, v158\n" \
    "v_xor_b32 v158, %[so], v130\n" \
    "v_min_u32 v157, v157, v158\n" \
    "v_xor_b32 v158, %[so], v131\n" \
    "v_min_u32 v157, v157, v158\n" \
    "v_xor_b32 v158, %[so], v132\n" \
    "v_min_u32 v157, v157, v158\n" \
    "v_xor_b32 v158, %[so], v133\n" \
    "v_min_u32 v157, v157, v158\n" \
    "v_xor_b32 v158, %[so], v134\n" \
    "v_min_u32 v157, v157, v158\n" \
    "v_xor_b32 v158, %[so], v135\n" \
    "v_min_u32 v157, v157, v158\n" \
    "v_cmp_eq_u32 vcc, 0, v157\n" \
    "s_cmp_lt_u32 %[cnt], %[cap]\n" \
    "s_cbranch_vccz Liy_%=\n" \
    "s_cbranch_scc1 Li_%=\n" \
    "Liy_%=:\n" \
    "s_waitcnt vmcnt(0)\n" \
    "v_mfma_f32_16x16x32_bf16 v[136:139], v[16:19], v[116:119], 0\n" \
    "v_mfma_f32_16x16x32_bf16 v[140:143], v[20:23], v[116:119], 0\n" \
    "v_mfma_f32_16x16x32_bf16 v[144:147], v[24:27], v[116:119], 0\n" \
    "v_mfma_f32_16x16x32_bf16 v[148:151], v[28:31], v[116:119], 0\n" \
    "v_mfma_f32_16x16x32_bf16 v[152:155], v[32:35], v[116:119], 0\n" \
    "s_waitcnt vmcnt(0)\n" \
    "v_mfma_f32_16x16x32_bf16 v[136:139], v[36:39], v[120:123], v[136:139]\n" \
    "v_mfma_f32_16x16x32_bf16 v[140:143], v[40:43], v[120:123], v[140:143]\n" \
    "v_mfma_f32_16x16x32_bf16 v[144:147], v[44:47], v[120:123], v[144:147]\n" \
    "v_mfma_f32_16x16x32_bf16 v[148:151], v[48:51], v[120:123], v[148:151]\n" \
    "v_mfma_f32_16x16x32_bf16 v[152:155], v[52:55], v[120:123], v[152:155]\n" \
    "s_waitcnt vmcnt(0)\n" \
    "v_mfma_f32_16x16x32_bf16 v[136:139], v[56:59], v[124:127], v[136:139]\n" \
    "v_mfma_f32_16x16x32_bf16 v[140:143], v[60:63], v[124:127], v[140:143]\n" \
    "v_mfma_f32_16x16x32_bf16 v[144:147], v[64:67], v[124:127], v[144:147]\n" \
    "v_mfma_f32_16x16x32_bf16 v[148:151], v[68:71], v[124:127], v[148:151]\n" \
    "v_mfma_f32_16x16x32_bf16 v[152:155], v[72:75], v[124:127], v[152:155]\n" \
    "s_waitcnt vmcnt(0)\n" \
    "v_mfma_f32_16x16x32_bf16 v[136:139], v[76:79], v[128:131], v[136:139]\n" \
    "v_mfma_f32_16x16x32_bf16 v[140:143], v[80:83], v[128:131], v[140:143]\n" \
    "v_mfma_f32_16x16x32_bf16 v[144:147], v[84:87], v[128:131], v[144:147]\n" \
    "v_mfma_f32_16x16x32_bf16 v[148:151], v[88:91], v[128:131], v[148:151]\n" \
    "v_mfma_f32_16x16x32_bf16 v[152:155], v[92:95], v[128:131], v[152:155]\n" \
    "s_waitcnt vmcnt(0)\n" \
    "v_mfma_f32_16x16x32_bf16 v[136:139], v[96:99], v[132:135], v[136:139]\n" \
    "v_mfma_f32_16x16x32_bf16 v[140:143], v[100:103], v[132:135], v[140:143]\n" \
    "v_mfma_f32_16x16x32_bf16 v[144:147], v[104:107], v[132:135], v[144:147]\n" \
    "v_mfma_f32_16x16x32_bf16 v[148:151], v[108:111], v[132:135], v[148:151]\n" \
    "v_mfma_f32_16x16x32_bf16 v[152:155], v[112:115], v[132:135], v[152:155]\n" \
    "s_nop 3\n" \
    "Lix_%=:\n" \
    "v_mov_b32 v157, 1\n" \
    "s_nop 1\n" \
    "ds_write_b128 %[xa1], v[136:139] offset:0\n" \
    "ds_write_b128 %[xa1], v[140:143] offset:1024\n" \
    "ds_write_b128 %[xa1], v[144:147] offset:2048\n" \
    "ds_write_b128 %[xa1], v[148:151] offset:3072\n" \
    "ds_write_b128 %[xa1], v[152:155] offset:4096\n" \
    "ds_write_b32 %[pf], v157 offset:32\n" \
    "s_nop 0\n"
#define P_CLOB "v16", "v17", "v18", "v19", "v20", "v21", "v22", "v23", "v24", "v25", "v26", "v27", "v28", "v29", "v30", "v31", "v32", "v33", "v34", "v35", "v36", "v37", "v38", "v39", "v40", "v41", "v42", "v43", "v44", "v45", "v46", "v47", "v48", "v49", "v50", "v51", "v52", "v53", "v54", "v55", "v56", "v57", "v58", "v59", "v60", "v61", "v62", "v63", "v64", "v65", "v66", "v67", "v68", "v69", "v70", "v71", "v72", "v73", "v74", "v75", "v76", "v77", "v78", "v79", "v80", "v81", "v82", "v83", "v84", "v85", "v86", "v87", "v88", "v89", "v90", "v91", "v92", "v93", "v94", "v95", "v96", "v97", "v98", "v99", "v100", "v101", "v102", "v103", "v104", "v105", "v106", "v107", "v108", "v109", "v110", "v111", "v112", "v113", "v114", "v115", "v116", "v117", "v118", "v119", "v120", "v121", "v122", "v123", "v124", "v125", "v126", "v127", "v128", "v129", "v130", "v131", "v132", "v133", "v134", "v135", "v136", "v137", "v138", "v139", "v140", "v141", "v142", "v143", "v144", "v145", "v146", "v147", "v148", "v149", "v150", "v151", "v152", "v153", "v154", "v155", "v156", "v157", "v158", "v159", "vcc", "scc", "memory"
static_assert(C_WO - L_CTRL == 56 && C_OST - L_CTRL == 48 && C_BP - L_CTRL == 96 && C_WO0 - L_CTRL == 184 && C_QR2 - L_CTRL == 188 && C_SENT2 - L_CTRL == 100, "offsets in the output stream");
static_assert(L_CTRL - G_ZR == 1280 && C_HX - C_FREE == 144 && C_SENT - L_CTRL == 52, "offsets in the projection stream");
static_assert(C_HR - L_CTRL == 144 && C_RED - L_CTRL == 84 && C_XD - L_CTRL == 80 && C_SENT - L_CTRL == 52, "offsets in the conv stream");
typedef __attribute__((ext_vector_type(16))) unsigned u32x16;
typedef __attribute__((address_space(3))) const unsigned lds_cu32;
typedef __attribute__((address_space(3))) u32x2 lds_u32x2;
typedef __attribute__((address_space(3))) u32x4 lds_u32x4;
typedef __attribute__((address_space(3))) f32x4 lds_f32x4;
#define GPTR(T, lo, hi) ((T*)(__attribute__((address_space(1))) T*)(((u64)(hi) << 32) | (u64)(lo)))
#if defined(__HIP_DEVICE_COMPILE__)
#define NOPK __attribute__((target("no-packed-fp32-ops")))
#else
#define NOPK
#endif
__global__ NOPK __launch_bounds__(768, 3) void fused_kernel(const u16* x_, const u16* kin_, const u16* vin_, const u16* shift_k_,
                                                       const u16* ssm_k_, const u16* ssm_D_, const u16* wqkv_, const u16* wo_,
                                                       const u16* gw_, char* ws_, u16* out_) {
    u32x16 ka0;
    u32x4 ka1;
    u32x2 ka2;
    asm volatile("s_load_dwordx16 %0, %3, 0\n\ts_load_dwordx4 %1, %3, 64\n\ts_load_dwordx2 %2, %3, 80"
                 : "=&s"(ka0), "=&s"(ka1), "=&s"(ka2) : "s"(__builtin_amdgcn_kernarg_segment_ptr()));
#define x GPTR(const u16, ka0[0], ka0[1])
#define kin GPTR(const u16, ka0[2], ka0[3])
#define vin GPTR(const u16, ka0[4], ka0[5])
#define shift_k GPTR(const u16, ka0[6], ka0[7])
#define ssm_k GPTR(const u16, ka0[8], ka0[9])
#define ssm_D GPTR(const u16, ka0[10], ka0[11])
#define wqkv GPTR(const u16, ka0[12], ka0[13])
#define wo GPTR(const u16, ka0[14], ka0[15])
#define gw GPTR(const u16, ka1[0], ka1[1])
#define ws GPTR(char, ka1[2], ka1[3])
#define out GPTR(u16, ka2[0], ka2[1])
#define wsr __builtin_amdgcn_make_buffer_rsrc(ws, 0, 0x4000000, 0x00020000)
    __shared__ __attribute__((aligned(16))) char smem[LDS_TOTAL];
    const int bid = __builtin_amdgcn_workgroup_id_x();
    const int tid = __builtin_amdgcn_workitem_id_x();
    int lane = tid & 63;
    const int wave = __builtin_amdgcn_readfirstlane(tid >> 6);
    const int xcd = bid & 7, r5 = bid >> 3, r = r5 & 15;
    const bool isG = r5 >= 16;
    const int hg = (xcd & 1) * 4 + (r & 3);
    const int rb2 = (xcd >> 1) * 4 + (r >> 2);
    const int cq = (xcd & 1) * 2 + (r & 1), rbv = (xcd >> 1) * 8 + (r >> 1);
    const int vid = cq * 32 + rbv;
    const int sid = rbv * 4 + cq;
    const int rbo = xcd * 4 + (r >> 3) * 2 + ((r5 >> 4) ^ GH ^ 1), oc = r5 & 7;
    const int w = wave & 3;
    const int wr = wave < 4 ? w ^ WR_X : w;
    const int rto = (rbo >> 1) * 8 + (wr >> 1) * 4 + (rbo & 1) * 2 + (wr & 1);
    const int rtq = (rbo >> 1) * 8 + (w >> 1) * 4 + (rbo & 1) * 2 + (w & 1);
    const int g = lane >> 4, n16 = lane & 15, q4 = n16 >> 2, p4 = lane & 3;
    const unsigned lb = (unsigned)(size_t)(lds_char*)smem;
    u64 tag;
    {
        u64 id = llvm_dispatch_id();
        u64 dp = (u64)(size_t)__builtin_amdgcn_dispatch_ptr();
        tag = ((u64)((unsigned)id ^ 0x5bd1e995u) << 32) | (u64)((unsigned)(dp >> 6) ^ (unsigned)(id >> 32) ^ 0x9e3779b9u);
    }
    const unsigned tlo = (unsigned)tag, thi = (unsigned)(tag >> 32);
    if (wave == 0) {
        u64 pc;
        asm volatile("s_getpc_b64 %0" : "=s"(pc));
        pc &= ~255ull;
        const unsigned cl = lane * 32 + r5;
        if (cl < CODE_LINES) GLT(cl * 128, pc);
    }

#define WO_PATS() unsigned wpm, wpo, wpm4, wpo4; { \
        const int c0_ = w * 64 + lane, r0_ = (c0_ * 6554) >> 16, k0_ = r0_ ^ ((r0_ & 8) >> 1), b0_ = oc * 160 + (c0_ - r0_ * 10) * 16; \
        const int c4_ = 256 + lane, r4_ = (c4_ * 6554) >> 16, k4_ = r4_ ^ ((r4_ & 8) >> 1), b4_ = oc * 160 + (c4_ - r4_ * 10) * 16; \
        wpm = opaque((k0_ + ((k0_ >> 4) << 3)) * (D_DIM * 2) + b0_); wpo = opaque((20 * (k0_ >> 2) + (k0_ & 3) + ((~k0_ & 4) << 2)) * (D_DIM * 2) + b0_); \
        wpm4 = opaque((k4_ + 8) * (D_DIM * 2) + b4_); wpo4 = opaque((20 * (k4_ >> 2) + (k4_ & 3) + ((~k4_ & 4) << 2)) * (D_DIM * 2) + b4_); }
#define WO_ROW(ks) (((ks) / 5) * 160 + ((ks) % 5 < 4 ? 40 * ((ks) % 5) : 0))
#define WO_K(ks) GLS((ks) % 5 == 4 ? wpo : wpm, (const char*)wo + WO_ROW(ks) * (D_DIM * 2), lb + w * 1024, WO_POS(ks));
#define SEL4(a, b, c, d) (w == 0 ? (a) : w == 1 ? (b) : w == 2 ? (c) : (d))
#define WO_X(a, b, c, d) GLS(SEL4((a) % 5 == 4, (b) % 5 == 4, (c) % 5 == 4, (d) % 5 == 4) ? wpo4 : wpm4, \
        (const char*)wo + SEL4(WO_ROW(a), WO_ROW(b), WO_ROW(c), WO_ROW(d)) * (D_DIM * 2), lb + SEL4(WO_POS(a), WO_POS(b), WO_POS(c), WO_POS(d)), 4096);
#define WO_4(a, b, c, d) { WO_K(a) WO_K(b) WO_K(c) WO_K(d) WO_X(a, b, c, d) }
#define RD_FLAGS() ((lane & 31) < 8 ? WS_HF + ((rbo >> 1) * 8 + (lane & 7)) * 128 : WS_SF + ((lane & 31) < 24 ? oc * 16 + (lane & 31) - 8 : (lane & 3) * 32 + (rbo & ~1) + ((lane >> 2) & 1)) * 128)
    const int kr = KROT;
#define T_C(i) __builtin_amdgcn_readfirstlane((kr + (i)) >= 10 ? kr + (i) - 10 : kr + (i))
#define T_WV() ({ const int j_ = min(n16, 14); (unsigned)((r * 4 + g) * (NQKV * 2) + j_ * 128 + ((j_ >= 5) + (j_ >= 10) + (xcd & 1)) * 640); })
#define T_XV() ((unsigned)(((xcd >> 1) * 512 + r * 32 + (lane & 31)) * (D_DIM * 2)))
#define T_W(wv, i) GLT(wv, (const char*)wqkv + (size_t)T_C(i) * (64 * NQKV * 2))
#define T_X(xv, i) { const int c0_ = T_C(i), c1_ = T_C((i) + 1); GLT((xv) + (lane < 32 ? c0_ : c1_) * 128, x); }
#define T_PAIR(i) { T_W(wv, i); T_X(xv_, i); T_W(wv, (i) + 1); }

    unsigned e0;
    if (isG) {
        const unsigned t0_ = T_WV() + (kr + (w >> 1)) * (64 * NQKV * 2);
        const unsigned t1_ = ((xcd >> 1) * 512 + (w == 1 ? r * 32 + (lane & 31) : r - ((xcd >> 1) ? 16 : 0))) * (D_DIM * 2) + (w == 1 ? kr + (lane >> 5) : min(lane, 9)) * 128;
        e0 = (w & 1) ? t1_ : t0_;
    } else {
        const unsigned c = (wave >= 5 ? wave - 5 : wave) * 64 + lane, row = (c * 3277) >> 16;
        e0 = rbv * (64 * D_DIM * 2) + cq * 320 + row * (D_DIM * 2 - 320) + c * 16;
    }
    asm volatile("" : "+v"(e0));
    asm volatile("s_waitcnt lgkmcnt(0)" : "+s"(ka0), "+s"(ka1), "+s"(ka2) :: "memory");
    if (isG) {
        if ((wave >> 2) == 1) GLT(e0, (w & 1) ? x : wqkv);
        BARRIER();
    }
    u32x2 skw;
    if (!isG && wave < 10) {
        const int tens = wave >= 5, i5 = wave - 5 * tens;
        const unsigned lw = lb + (tens ? V_VST : V_KST) + i5 * 1024;
        const char* kb = (const char*)(tens ? vin : kin);
        __builtin_amdgcn_s_sleep(V_DELAY);
        GLSP(e0, kb, lw, 0);
        GLSP(e0, kb + 16 * D_DIM * 2, lw, 5152);
        GLSP(e0, kb + 32 * D_DIM * 2, lw, 2 * 5152);
        GLSP(e0, kb + 48 * D_DIM * 2, lw, 3 * 5152);
        asm volatile("global_load_dwordx2 %0, %1, %2" : "=v"(skw) : "v"((cq * 160 + wave * 16 + n16) * 8), "s"(shift_k) : "memory");
        if (wave == 0) { if (lane < 60) GLS(e0, kb - (rbv ? 3 * D_DIM * 2 : 0), lb, V_KST - 32 - 960); }
    }
    if (wave == 11) {
        if (lane < 16) *(u32x4*)(smem + L_CTRL + lane * 16) = (u32x4){0u, 0u, 0u, 0u};
        asm volatile("s_waitcnt lgkmcnt(0)" ::: "memory");
    }
#define Y_OFF (WS_Y + rbo * (D_DIM * 128) + oc * 10240 + w * 1024)
#define Y_FIRST() u32x4 yv[3]; float g1; { yv[0] = __builtin_amdgcn_raw_buffer_load_b128(wsr, lane * 16, Y_OFF, 16); yv[1] = __builtin_amdgcn_raw_buffer_load_b128(wsr, lane * 16, Y_OFF + 4096, 16); \
        yv[2] = __builtin_amdgcn_raw_buffer_load_b128(wsr, lane * 16, Y_OFF + (w < 2 ? 8192 : 0), 16); \
        g1 = __uint_as_float(__builtin_amdgcn_raw_buffer_load_b32(wsr, (lane & 15) * 4, WS_G + rtq * 64, 16)); }
#define RELANE() const int lane_ = opaque(lane); const int g = lane_ >> 4, n16 = lane_ & 15, q4 = n16 >> 2, p4 = lane_ & 3; \
        const unsigned qa = lb + (isG ? G_Q : V_Q) + w * 2560 + n16 * 160 + 8 * g
#define SENT4(v) __any(max(max((v)[0], (v)[1]), max((v)[2], (v)[3])) == 0xffffffffu)
#define Q_READ(base) f32x4 gq[5]; { const int l_ = opaque(lane); _Pragma("unroll") for (int ct = 0; ct < 5; ++ct) { \
            const u32x2 q2 = *(const lds_u32x2*)(size_t)(lb + (base) + w * 2560 + (l_ & 15) * 160 + 8 * (l_ >> 4) + ct * 32); \
            gq[ct] = (f32x4){lo_f(q2[0]), hi_f(q2[0]), lo_f(q2[1]), hi_f(q2[1])}; } \
        asm volatile("" : "+v"(gq[0]), "+v"(gq[1]), "+v"(gq[2]), "+v"(gq[3]), "+v"(gq[4]) :: "memory"); }
#define P_STAGE() { \
        RELANE(); \
        const unsigned l_y = lb + (isG ? G_Y : V_Y); \
        const unsigned yw = l_y + (w * 8 + (lane >> 3)) * Y_PITCH + (lane & 7) * 16; \
        const unsigned yr = l_y + (4 * g + q4) * Y_PITCH + w * 32 + p4 * 8; \
_Pragma("nounroll") \
        for (int it = 0; it < SPIN_CAP; ++it) { \
            const bool b0 = SENT4(yv[0]), b1 = SENT4(yv[1]), b2 = SENT4(yv[2]); \
            if (!(b0 | b1 | b2)) break; \
            if (it || Y_SL0) __builtin_amdgcn_s_sleep(Y_SLEEP); \
            asm volatile("" ::: "memory"); \
            if (b0) yv[0] = __builtin_amdgcn_raw_buffer_load_b128(wsr, lane * 16, Y_OFF, 16); \
            if (b1) yv[1] = __builtin_amdgcn_raw_buffer_load_b128(wsr, lane * 16, Y_OFF + 4096, 16); \
            if (b2) yv[2] = __builtin_amdgcn_raw_buffer_load_b128(wsr, lane * 16, Y_OFF + (w < 2 ? 8192 : 0), 16); \
        } \
        STAMP(8); \
        *(lds_u32x4*)(size_t)yw = yv[0]; \
        *(lds_u32x4*)(size_t)(yw + 32 * Y_PITCH) = yv[1]; \
        if (w < 2) *(lds_u32x4*)(size_t)(yw + 64 * Y_PITCH) = yv[2]; \
        lds_add(lb + C_Y, lane); \
_Pragma("unroll") \
        for (int ct = 0; ct < 5; ++ct) gq[ct] *= g1; \
        asm volatile("" : "+v"(gq[0]), "+v"(gq[1]), "+v"(gq[2]), "+v"(gq[3]), "+v"(gq[4])); \
        spin_ge(lb + C_Y, 4); \
        u32x2 y2[5]; \
_Pragma("unroll") \
        for (int ct = 0; ct < 5; ++ct) y2[ct] = __builtin_bit_cast(u32x2, TRRD((lds_char*)(size_t)(yr + ct * 16 * Y_PITCH))); \
_Pragma("unroll") \
        for (int ct = 0; ct < 5; ++ct) gq[ct] *= (f32x4){lo_f(y2[ct][0]), hi_f(y2[ct][0]), lo_f(y2[ct][1]), hi_f(y2[ct][1])}; \
        const float g0 = 1.f - g1; \
        const unsigned pa = lb + PART(w) + lane_ * 16; \
        const auto outr = __builtin_amdgcn_make_buffer_rsrc(out, 0, S_LEN * D_DIM * 2, 0x00020000); \
        unsigned ob = ((rtq * 16 + n16) * D_DIM + oc * 80) * 2, ob2 = ob + (64 + 4 * g) * 2; \
        ob += ((g & 1) * 16 + (g >> 1) * 8) * 2; \
        asm volatile("" : "+v"(gq[0]), "+v"(gq[1]), "+v"(gq[2]), "+v"(gq[3]), "+v"(gq[4]), "+v"(ob), "+v"(ob2)); \
        STAMP(9); \
          \
        u32x4 pf; \
_Pragma("nounroll") \
        for (int it = 0; it < SPIN_CAP; ++it) { \
            pf = lds_v128(lb + C_PF + w * 16); \
            if (__builtin_amdgcn_readfirstlane((pf[0] & pf[1]) | (pf[2] & pf[3]))) break; \
            __builtin_amdgcn_s_sleep(1); \
        } \
        const unsigned h1 = __builtin_amdgcn_readfirstlane(pf[0] & pf[1]) ? 0 : 10240; \
        f32x4 sa[5]; \
_Pragma("unroll") \
        for (int ct = 0; ct < 5; ++ct) sa[ct] = *(const lds_f32x4*)(size_t)(pa + h1 + ct * 1024) + *(const lds_f32x4*)(size_t)(pa + h1 + 5120 + ct * 1024); \
        asm volatile("" : "+v"(sa[0]), "+v"(sa[1]), "+v"(sa[2]), "+v"(sa[3]), "+v"(sa[4])); \
_Pragma("nounroll") \
        for (int it = 0; it < SPIN_CAP; ++it) { \
            if (__builtin_amdgcn_readfirstlane((pf[0] & pf[1]) & (pf[2] & pf[3]))) break; \
            pf = lds_v128(lb + C_PF + w * 16); \
        } \
        asm volatile("" ::: "memory"); \
        STAMP(13); \
        unsigned pk[5][2]; \
_Pragma("unroll") \
        for (int ct = 0; ct < 5; ++ct) { \
            const f32x4 sb = *(const lds_f32x4*)(size_t)(pa + (10240 - h1) + ct * 1024) + *(const lds_f32x4*)(size_t)(pa + (10240 - h1) + 5120 + ct * 1024); \
            const f32x4 fin = gq[ct] + g0 * (sa[ct] + sb); \
            pk[ct][0] = pack2(fin[0], fin[1]); \
            pk[ct][1] = pack2(fin[2], fin[3]); \
        } \
_Pragma("unroll") \
        for (int p = 0; p < 2; ++p) { \
            const auto r0 = __builtin_amdgcn_permlane16_swap(pk[2 * p][0], pk[2 * p + 1][0], false, false); \
            const auto r1 = __builtin_amdgcn_permlane16_swap(pk[2 * p][1], pk[2 * p + 1][1], false, false); \
            __builtin_amdgcn_raw_buffer_store_b128((u32x4){r0[0], r1[0], r0[1], r1[1]}, outr, ob + p * 64, 0, OUT_AUX); \
        } \
        __builtin_amdgcn_raw_buffer_store_b64((u32x2){pk[4][0], pk[4][1]}, outr, ob2, 0, OUT_AUX); \
        FLUSH(); \
        return; }

    if (isG) {
      if (wave >= 4) {
        const int half = (wave >> 2) - 1;
        if (half) __builtin_amdgcn_s_setprio(C1_PRIO); else __builtin_amdgcn_s_setprio(C0_PRIO);
        const int rb = rb2 * 2 + half;
        unsigned ap[2], wp[4];
#pragma unroll
        for (int sub = 0; sub < 2; ++sub) ap[sub] = lb + half * 8192 + n16 * 128 + (((sub * 4 + g) ^ ((n16 >> 1) & 7)) * 16);
#pragma unroll
        for (int i = 0; i < 4; ++i) {
            const int part = i < 3 ? i : (p4 < 3 ? p4 : 2);
            const int Q = i < 3 ? 5 * w + (w & 1) + p4 : 5 * w + ((w & 1) ? 0 : 4);
            const int c2 = (Q >> 1) - 2 * (hg & 1);
            const int cc = ((unsigned)c2 < 8u ? part * 8 + c2 : 24 + part * 2 + (c2 & 1)) ^ (2 * ((g & 1) * 4 + q4));
            wp[i] = lb + W_OFF(0) + (8 * g + q4) * W_PITCH + cc * 16 + (Q & 1) * 8;
        }
        const int qi = n16, par = lane & 1;
        if (wave == 11) {
            f32x4 cm0, cm1;
            u32x4 psel;
#pragma unroll
            for (int e = 0; e < 4; ++e) {
                const bool own = 4 * g + e <= qi, pm = (e & 1) == par;
                cm0[e] = pm && !own ? 0.f : -3.0e38f;
                cm1[e] = pm && own ? 0.f : -3.0e38f;
                const bool ow = 4 * g + par + 2 * (e & 1) <= qi;
                const unsigned by = (e & 1) ? 0x0302u : 0x0100u;
                psel[e] = (ow == (e >= 2)) ? (par ? (by << 16) | 0x0c0cu : 0x0c0c0000u | by) : 0x0c0c0c0cu;
            }
            *(lds_f32x4*)(size_t)(lb + G_TAB + lane * 16) = cm0;
            *(lds_f32x4*)(size_t)(lb + G_TAB + 1024 + lane * 16) = cm1;
            *(lds_u32x4*)(size_t)(lb + G_TAB + 2048 + lane * 16) = psel;
            if (lane < 5) *(lds_u32x2*)(size_t)(lb + G_ZR + lane * 128) = (u32x2){0u, 0u};
        }
        const unsigned vxw = lb + G_VX + w * 1152;
        const unsigned scw = g == 2 ? vxw + half * 512 + qi * 8 : lb + G_VXD;
        const int hj = (hg & 1) * 4 + w;
        const unsigned oo = WS_O + ((rb * 4 + (g & 1)) * 20 + (hg >> 1) * 5 + (hj >> 1)) * 1024 + (((w & 1) * 2 + (g >> 1)) * 16 + qi) * 16;
        const unsigned ox = WS_O + ((rb * 4 + g) * 20 + (hg >> 1) * 5 + 4) * 1024 + hj * 128 + qi * 8;
        unsigned c0_, c1_, l16 = lane * 16;
        STAMP(6);
        CSTAMP(0);
        asm volatile(G_ASM
                     : [cnt] "=&s"(c0_), [tmp] "=&s"(c1_), [wp0] "+v"(wp[0]), [wp1] "+v"(wp[1]), [wp2] "+v"(wp[2]), [wp3] "+v"(wp[3]), [l16] "+v"(l16)
                     : [ap0] "v"(ap[0]), [ap1] "v"(ap[1]), [scw] "v"(scw), [oo] "v"(oo), [ox] "v"(ox),
                       [vxb] "v"(vxw + half * 512 + (4 * g + q4) * 8), [zr] "v"(lb + G_ZR), [fr] "v"(lb + C_FREE + (wave - 4) * 4),
                       [tabc] "s"(lb + G_TAB), [hxc] "s"(lb + G_HX + w * 1024), [hxd] "s"(lb + G_HXO + w * 256),
                       [wsp] "s"(ws), [half] "s"(half), [rb2] "s"(rb2), [cap] "s"(SPIN_CAP), [sc] "s"(0x3ea52b5bu),
                       [thi] "s"(thi), [hfo] "s"(w == HINT_W ? WS_H + ((rb2 * 8 + hg) * 2 + half) * 128 : 0)
                     : G_CLOB);
        lane = l16 >> 4;
        CSTAMP(1);
        __builtin_amdgcn_s_setprio(T_PRIO);
        if (half) {
            if (GY_DELAY > 0) { lds_add(lb + C_OST1, lane); __builtin_amdgcn_s_sleep(GY_DELAY); }
            spin_ge(lb + C_SENT2, 1);
            Y_FIRST();
            if (GY_DELAY == 0) lds_add(lb + C_OST1, lane);
            STAMP(4);
            spin_ge(lb + C_WO, 4);
            spin_all(lb + C_FREE + (lane & 7) * 4, 18);
            Q_READ(G_Q);
            P_STAGE();
        }
        lds_add(lb + C_OST, lane);
        STAMP(4);
      } else {
        __builtin_amdgcn_s_setprio(L_PRIO);
        unsigned ev[9];
        {
            const int hi = lane >> 5, pos = lane & 31;
#pragma unroll
            for (int v = 0; v < 2; ++v) {
                const int row = 2 * w + hi + 8 * v;
                int L = pos ^ (2 * (v * 4 + (row & 3)));
                L = L > 29 ? L - 2 : L;
                const int part = L < 24 ? L >> 3 : (L - 24) >> 1;
                const int chunk = L < 24 ? (L & 7) + 2 * (hg & 1) : (L & 1) + ((hg & 1) ? 0 : 8);
                ev[v] = row * (NQKV * 2) + chunk * 16 + part * 1280 + hg * 160;
                ev[v + 2] = ev[v] + 16 * NQKV * 2;
            }
            const int ch = (lane & 7) ^ ((4 * (w & 1) + g) & 7);
#pragma unroll
            for (int j = 0; j < 5; ++j) {
                int t = rb2 * 128 - 16 + 8 * w + (lane >> 3) + 32 * j;
                t = t < 0 ? 0 : t;
                t = t > S_LEN - 1 ? S_LEN - 1 : t;
                ev[4 + j] = t * (D_DIM * 2) + ch * 16;
            }
        }
        SB();
        if (L_DELAY > 0) __builtin_amdgcn_s_sleep(L_DELAY);
        STAMP(5);
        if (w < 2) ring_issue<5, 0, ring_need(-1, 5)>(ev, x, wqkv, kr, lb + w * 1024); else ring_issue<4, 0, ring_need(-1, 4)>(ev, x, wqkv, kr, lb + w * 1024);
        SB();
        unsigned qf[3];
        WO_PATS();
#pragma unroll
        for (int j = 0; j < 3; ++j) { const int c = min(j * 64 + lane, 159); const int row = c / 10, ch = c - row * 10; qf[j] = opaque(row * (D_DIM * 2) + ch * 16); }
        const unsigned sfo = opaque(RD_FLAGS());
        const unsigned sf1 = opaque(WS_HF + (rb2 * 8 + (lane & 7)) * 128);
        const unsigned fra = opaque(lb + C_FREE + (lane & 7) * 4);
        SB();
        if (w < 2) {
            ring_issue<5, ring_need(-1, 5), ring_cum(-1, 5)>(ev, x, wqkv, kr, lb + w * 1024);
            waitvm<ring_cum(-1, 5) - ring_need(-1, 5)>();
            STAMP(6);
            BARRIER();
            ring_run<5, 0>(ev, x, wqkv, kr, lb + w * 1024);
        } else {
            ring_issue<4, ring_need(-1, 4), ring_cum(-1, 4)>(ev, x, wqkv, kr, lb + w * 1024);
            waitvm<ring_cum(-1, 4) - ring_need(-1, 4)>();
            BARRIER();
            ring_run<4, 0>(ev, x, wqkv, kr, lb + w * 1024);
        }
        STAMP(2);
#define WO_POS(ks) ((ks) < 10 ? G_R0 + (ks) * 5120 : G_R1 + ((ks) - 10) * 5120)
        static_assert(JL == 17, "last barrier");
        WO_4(0, 1, 2, 3)
        WAIT_VM(9);
        BARRIER();
        WO_4(4, 5, 10, 11)
        WAIT_VM(10);
        BARRIER();
        __builtin_amdgcn_s_setprio(WO_PRIO);
        u32x2 f1 = {0u, 0u};
#define SF1_ASK() if (w == 2) asm volatile("global_load_dwordx2 %0, %1, %2 sc1" : "=v"(f1) : "v"(sf1), "s"(ws) : "memory");
#define SF1_SEE() if (w == 2) { asm volatile("s_waitcnt vmcnt(5)" : "+v"(f1) :: "memory"); if (__all(f1[0] == tlo && f1[1] == thi)) lds_st32(lb + C_SENT, 1); }
        if (SF1_AT == 0) SF1_ASK()
        WO_4(12, 6, 7, 8)
        if (SF1_AT == 0) SF1_SEE()
        spin_all(fra, 16);
        if (SF1_AT == 1) SF1_ASK()
        WO_4(9, 13, 14, 15)
        if (SF1_AT == 1) SF1_SEE()
        WO_K(16)
        spin_all(fra, 17);
        WO_K(17) WO_K(18) WO_K(19) WO_X(16, 17, 18, 19)
#undef WO_POS
#pragma unroll
        for (int j = 0; j < 3; ++j)
            if (j < 2 || lane < 32) GLS(qf[j], (const char*)x + (size_t)(rtq * 16) * (D_DIM * 2) + oc * 160, lb + w * 2560, G_Q + j * 1024);
        STAMP(3);
        if (w == 2 && !__all(f1[0] == tlo && f1[1] == thi)) {
            poll_flags(wsr, sf1, tlo, thi);
            lds_st32(lb + C_SENT, 1);
        }
        u32x2 fv = {0u, 0u};
        if (w == 3) {
            asm volatile("global_load_dwordx2 %0, %1, %2 sc1" : "=v"(fv) : "v"(sfo), "s"(ws) : "memory");
            WAIT_VM(1);
        } else {
            WAIT_VM(0);
        }
        lds_add(lb + C_WO, lane);
        if (w == 3) {
            asm volatile("s_waitcnt vmcnt(0)" : "+v"(fv) :: "memory");
            if (!__all(fv[0] == tlo && fv[1] == thi)) poll_flags(wsr, sfo, tlo, thi);
            lds_st32(lb + C_SENT2, 1);
        }
        STAMP(4);
      }
    } else {
        __builtin_amdgcn_s_setprio(3);
        const int c5 = wave < 4 ? wave : 4;
        const int d = vid * 5 + c5;
        unsigned Dw = *(const __attribute__((address_space(4))) unsigned*)(size_t)(ssm_D + (d & ~1));
        unsigned hp0 = *(const __attribute__((address_space(4))) unsigned*)(size_t)(ssm_k + (vid * 5 + 4) * S_LEN + w * 512 - 2);
        BARRIER();
        if (wave <= 4) hp0 = 0;
        asm volatile("" : "+s"(Dw), "+s"(hp0));
#define WO_POS(ks) (V_WO + (ks) * 5120)
#define KV_DONE() do { if (lds_add_rtn(lb + C_KVD, lane) == 9) { \
            if (lane == 0) __hip_atomic_store((u64*)(ws + WS_XF + vid * 128), tag, __ATOMIC_RELAXED, __HIP_MEMORY_SCOPE_AGENT); lds_add(lb + C_KVD, lane); } } while (0)
        if (wave == 10) {
            const unsigned wv = T_WV(), xv_ = T_XV();
            T_PAIR(2);
            T_PAIR(4);
            if (WO_MALL) GLT((unsigned)((vid * 50 + min(lane, 49)) * 128), wo);
        }
        if (wave < 10) {
            const int m0 = (g & 1) * 2 + (g >> 1);
            const unsigned kp = opaque(lb + V_KST + (8 * m0 + q4) * 320 + (m0 >> 1) * 32 + wave * 32 + p4 * 8);
            const unsigned km = opaque(kp - 1280 - ((m0 & 1) ? 0 : 32));
            const unsigned sw_ = opaque(lb + V_STG + wave * 2048 + n16 * 128 + ((m0 + n16) & 7) * 16);
            const unsigned sr_ = opaque(lb + V_STG + wave * 2048 + (lane >> 3) * 128 + ((lane + (lane >> 3)) & 7) * 16);
            const unsigned e1 = opaque(WS_KV + (cq * 160 + wave * 16 + (lane >> 3)) * KV_PITCH + rbv * 128 + (lane & 7) * 16), e2 = opaque(e1 + 8 * KV_PITCH);
            asm volatile("s_waitcnt vmcnt(0)" : "+v"(skw) :: "memory");
            lds_add(lb + C_KV, lane);
            const f32x2 s0 = {lo_f(skw[0]), lo_f(skw[0])}, s1 = {hi_f(skw[0]), hi_f(skw[0])}, s2 = {lo_f(skw[1]), lo_f(skw[1])}, s3 = {hi_f(skw[1]), hi_f(skw[1])};
            spin_ge(lb + C_KV, 10);
            STAMP(2);
            u32x2 kq[2][3], vq[2][2];
#pragma unroll
            for (int i = 0; i < 2; ++i) {
                kq[i][0] = __builtin_bit_cast(u32x2, TRRD((lds_char*)(size_t)(km + i * 10304)));
#pragma unroll
                for (int e = 0; e < 2; ++e) kq[i][e + 1] = __builtin_bit_cast(u32x2, TRRD((lds_char*)(size_t)(kp + i * 10304 + e * 1280)));
#pragma unroll
                for (int e = 0; e < 2; ++e) vq[i][e] = __builtin_bit_cast(u32x2, TRRD((lds_char*)(size_t)(kp + i * 10304 + (V_VST - V_KST) + e * 1280)));
            }
            if (rbv == 0 && m0 == 0) kq[0][0] = (u32x2){0u, 0u};
            f32x2 kf[12], res[8];
#pragma unroll
            for (int e = 1; e < 12; ++e) {
                const unsigned u0 = kq[0][e >> 2][(e >> 1) & 1], u1 = kq[1][e >> 2][(e >> 1) & 1];
                kf[e] = (e & 1) ? (f32x2){hi_f(u0), hi_f(u1)} : (f32x2){lo_f(u0), lo_f(u1)};
            }
#pragma unroll
            for (int e = 0; e < 8; ++e) {
                const unsigned u0 = vq[0][e >> 2][(e >> 1) & 1], u1 = vq[1][e >> 2][(e >> 1) & 1];
                f32x2 a = kf[e + 1] * s3;
                a = kf[e + 2] * s2 + a;
                a = kf[e + 3] * s1 + a;
                a = kf[e + 4] * s0 + a;
                res[e] = a * ((e & 1) ? (f32x2){hi_f(u0), hi_f(u1)} : (f32x2){lo_f(u0), lo_f(u1)});
            }
#pragma unroll
            for (int i = 0; i < 2; ++i) {
                u32x4 pk;
#pragma unroll
                for (int e = 0; e < 4; ++e) pk[e] = pack2(res[2 * e][i], res[2 * e + 1][i]);
                *(lds_vu32x4*)(size_t)(sw_ ^ (i * 64)) = pk;
            }
            const u32x4 pk0 = lds_v128(sr_), pk1 = lds_v128(sr_ + 1024);
            __builtin_amdgcn_raw_buffer_store_b128(pk0, wsr, e1, 0, 16);
            __builtin_amdgcn_raw_buffer_store_b128(pk1, wsr, e2, 0, 16);
            lds_add(lb + C_KVST, lane);
            STAMP(3);
        }
        if (wave >= 8) {
            const unsigned wv = T_WV(), xv_ = T_XV();
            WO_PATS();
            if (wave == 10) {
            } else if (wave == 11) {
                if (lane < 56) {
#pragma unroll
                    for (int c5 = 0; c5 < 5; ++c5)
                        *(lds_u32x4*)(size_t)(lb + (lane < 16 ? V_KV(c5) + lane * 16 : V_F + c5 * XC_BYTES + 15 * XC_ROW + 256 + (lane < 36 ? XC_COPY + (lane - 16) * 16 : XC_B + 4 + (lane - 36) * 16))) = (u32x4){0u, 0u, 0u, 0u};
                }
                const unsigned x4o = opaque(WS_KV + d * KV_PITCH + lane * 16);
                spin_ge(lb + C_KVD, 11);
                if (POLL_DELAY > 0) __builtin_amdgcn_s_sleep(POLL_DELAY);
                const unsigned xfo = WS_XF + (cq * 32 + (lane & 31)) * 128;
                const u32x2 fv = __builtin_amdgcn_raw_buffer_load_b64(wsr, xfo, 0, 16);
                if (!__all(fv[0] == tlo && fv[1] == thi)) {
                    lds_st32(lb + C_GO, 1);
                    poll_flags(wsr, xfo, tlo, thi);
                }
                asm volatile("s_mov_b32 m0, %2\n\ts_nop 0\n\tglobal_load_lds_dwordx4 %0, %1 sc1\n\tglobal_load_lds_dwordx4 %0, %1 offset:1024 sc1\n\t"
                             "global_load_lds_dwordx4 %0, %1 offset:2048 sc1\n\tglobal_load_lds_dwordx4 %0, %1 offset:3072 sc1" ::"v"(x4o), "s"(ws), "s"(lb + V_KV4 + 256) : "memory");
                lds_add(lb + C_KVR, lane);
                STAMP(2);
                WAIT_VM(0);
                lds_st32(lb + C_HR, 4);
                STAMP(3);
                spin_ge(lb + C_XD, 4);
                lds_st32(lb + C_GO, 1);
            } else {
                WAIT_VM(0);
                KV_DONE();
                STAMP(11);
                spin_ge(lb + OS_W, OS_N);
                const u32x4 sv = {S_O, S_O, S_O, S_O};
#pragma unroll
                for (int i = 0; i < 10; ++i) {
                    if (i >= SENT_PACE) waitvm<SENT_PACE - 1>();
                    __builtin_amdgcn_raw_buffer_store_b128(sv, wsr, WS_O + sid * 20480 + (wave & 1) * 10240 + lane * 16 + i * 1024, 0, 16);
                }
                WAIT_VM(0);
                if (lds_add_rtn(lb + C_YS, lane) == 1) {
                    if (lane == 0) __hip_atomic_store((u64*)(ws + WS_HF + sid * 128), tag, __ATOMIC_RELAXED, __HIP_MEMORY_SCOPE_AGENT);
                }
                STAMP(12);
            }
            if (wave == 10) {
                spin_ge(lb + GO_W, GO_N);
                T_PAIR(6);
                if (T4_EARLY) WAIT_VM(1); else spin_ge(lb + GO2_W, GO2_N);
                T_PAIR(8);
            }
            spin_mid(lb + C_XD, 4);
            if (wave == WT_WAVE) {
#pragma unroll
                for (int i = 0; i < 4; ++i) {
                    const int l_ = r * 250 + min(i * 64 + lane, 249);
                    GLT((unsigned)((l_ < 800 ? l_ : l_ + 2400) * 128), wo);
                }
            }
            STAMP(5);
            u32x2 fv = {0u, 0u};
            const unsigned sfo = RD_FLAGS();
            if (r < 8) WO_4(2, 3, 4, 5) else WO_4(6, 7, 8, 9)
            if (WO_PACE >= 0) waitvm<(WO_PACE < 0 ? 0 : WO_PACE)>();
            if (r < 8) WO_4(6, 7, 8, 9) else WO_4(2, 3, 4, 5)
            WO_K(10) WO_K(11) WO_K(12)
            if (w < 3) WO_X(10, 11, 12, 12)
            STAMP(4);
            if (wave == SF_WAVE) {
                spin_mid(lb + SF_W, SF_N);
                asm volatile("global_load_dwordx2 %0, %1, %2 sc1" : "=v"(fv) : "v"(sfo), "s"(ws) : "memory");
                WAIT_VM(1);
            } else {
                WAIT_VM(0);
            }
            lds_add(lb + C_WO, lane);
            if (wave == SF_WAVE) {
                asm volatile("s_waitcnt vmcnt(0)" : "+v"(fv) :: "memory");
                if (!__all(fv[0] == tlo && fv[1] == thi)) poll_flags(wsr, sfo, tlo, thi);
                lds_st32(lb + C_SENT2, 1);
                STAMP(3);
            }
            STAMP(6);
        } else {
            const unsigned xb = lb + V_F + c5 * XC_BYTES;
            const unsigned hb = lb + V_KV(c5) + 256;
            const float Dd = (d & 1) ? hi_f(Dw) : lo_f(Dw);
            u32x4 hv[4];
            const unsigned l16 = lane * 16;
            {
                const char* hsrc = (const char*)ssm_k + (size_t)d * (S_LEN * 2) + (wave < 4 ? 0 : w * 1024);
                spin_ge(lb + C_KVST, 10);
#define H_LD(i) asm volatile("global_load_dwordx4 %0, %1, %2 offset:%3" H_POL : "=v"(hv[i]) : "v"(l16), "s"(hsrc), "n"((i) * 1024) : "memory")
                H_LD(0);
                if (wave < 4) { H_LD(1); H_LD(2); H_LD(3); WAIT_VM(4); } else WAIT_VM(1);
                KV_DONE();
                STAMP(4);
            }
            {
                const unsigned da = xb + (3 - g) * XC_ROW + (15 - n16) * 16 + (wave < 4 ? 0 : -w * (4 * XC_ROW));
                u32x4 ra[4], rb[4];
                asm volatile("s_waitcnt vmcnt(0)" : "+v"(hv[0]), "+v"(hv[1]), "+v"(hv[2]), "+v"(hv[3]) :: "memory");
#define H_PREV(i) __builtin_amdgcn_update_dpp((i) == 0 ? hp0 : __builtin_amdgcn_readlane(hv[(i) == 0 ? 0 : (i) - 1][3], 63), hv[i][3], 0x138, 0xf, 0xf, false)
#define BFI16(lo, hi) (((lo) & 0xffffu) | ((hi) & 0xffff0000u))
#define H_PREP(i) { ra[i] = (u32x4){__builtin_amdgcn_alignbit(hv[i][3], hv[i][3], 16), __builtin_amdgcn_alignbit(hv[i][2], hv[i][2], 16), __builtin_amdgcn_alignbit(hv[i][1], hv[i][1], 16), __builtin_amdgcn_alignbit(hv[i][0], hv[i][0], 16)}; \
                    rb[i] = (u32x4){BFI16(hv[i][3], hv[i][2]), BFI16(hv[i][2], hv[i][1]), BFI16(hv[i][1], hv[i][0]), BFI16(hv[i][0], H_PREV(i))}; \
                    *(lds_u32x4*)(size_t)(da + (3 - (i)) * (4 * XC_ROW) + XC_COPY) = ra[i]; *(lds_u32x4*)(size_t)(da + (3 - (i)) * (4 * XC_ROW) + XC_B + 4) = rb[i]; }
#define H_DUP(i) { *(lds_u32x4*)(size_t)(da + (3 - (i)) * (4 * XC_ROW) + XC_COPY - 32) = ra[i]; *(lds_u32x4*)(size_t)(da + (3 - (i)) * (4 * XC_ROW) + XC_B + 4 - 32) = rb[i]; }
                H_PREP(0)
                if (wave < 4) {
                    H_PREP(1) H_PREP(2) H_PREP(3)
                    if (n16 == 15) { H_DUP(0) H_DUP(1) H_DUP(2) if (g < 3) H_DUP(3) }
                } else {
                    if (n16 == 15 && (w < 3 || g < 3)) H_DUP(0)
                }
            }
            STAMP(9);
#define Y_ADDR(j0) const int l2_ = opaque(lane), gg = l2_ >> 4, mm = (l2_ & 3) | ((l2_ & 4) << 1) | ((l2_ & 8) >> 1); \
            const unsigned kp = hb + 2 * (256 * (j0) + 128 * (gg >> 1) + 8 * mm + 4 * (gg & 1)); \
            const int ts = 256 * ((j0) + (gg & 1)) + 128 * (gg >> 1) + 8 * mm; \
            const unsigned yo = WS_Y + d * 128 + ((ts >> 7) * 2 + ((ts >> 5) & 1)) * (D_DIM * 128) + (((ts >> 6) & 1) * 32 + (ts & 31)) * 2;
            if (wave < 4) {
                Y_ADDR(0)
                const int b1 = n16 >> 3, phi = n16 & 7;
                const unsigned xp = xb + ((phi & 1) ? XC_COPY - 4 : XC_B) + (1 - b1) * XC_ROW + 2 * (8 - 2 * (phi >> 1) + 8 * g);
                const unsigned hp = hb + 2 * (8 * (mm + g) - 120);
                const int sx4 = (4 - w) * XC_BYTES + (w >> 1) * XC_ROW + (w & 1) * 128, sh4 = V_KV4 - w * KV_BYTES + w * 128;
                unsigned c0_, c1_, scr = lb + V_SCR + w * 8192 + l16;
                __builtin_amdgcn_s_setprio(CONV_PRIO);
                spin_ge(lb + C_KVR, 5);
                STAMP(6);
                CSTAMP(0);
                asm volatile(CONV_ASM
                             : [cnt] "=&s"(c0_), [tmp] "=&s"(c1_), [scr] "+v"(scr)
                             : [xp] "v"(xp), [hp] "v"(hp), [sx4] "s"(sx4), [sh4] "s"(sh4), [kvl] "s"(hb), [kvr] "v"(kp),
                               [ctl] "v"(lb + L_CTRL), [dd] "s"(Dd), [cap] "s"(SPIN_CAP), [xo] "v"(WS_KV + d * KV_PITCH + l16), [wsp] "s"(ws), [yo] "v"(yo)
                             : CONV_CLOB);
                __builtin_amdgcn_s_setprio(3);
                lane = ((scr - lb - V_SCR) >> 4) & 63;
                CSTAMP(1);
                lds_add(lb + C_CONVN, lane);
                STAMP(12);
                if (VY_DELAY > 0) __builtin_amdgcn_s_sleep(VY_DELAY);
                spin_ge(lb + C_SENT2, 1);
                Y_FIRST();
                STAMP(7);
                spin_ge(lb + C_QR2, 4);
                Q_READ(V_Q2);
                spin_ge(lb + C_CONVN, 4);
                P_STAGE();
            } else {
                lds_add(lb + C_KVR, lane);
                Y_ADDR(2 * w)
                WO_PATS();
                {
                    const int grow = (xcd >> 1) * 512 + r * 32 + (xcd & 1) * 16 + w * 4 + g;
                    const unsigned go = opaque(grow * (D_DIM * 2) + n16 * 16);
                    if (GATE_LATE) spin_mid(lb + C_XD, 4);
                    u32x4 gx[5], gv[5];
#pragma unroll
                    for (int i = 0; i < 5; ++i) {
                        gx[i] = *(const u32x4*)((const char*)x + go + i * 256);
                        gv[i] = *(const u32x4*)((const char*)gw + n16 * 16 + i * 256);
                    }
                    float sa[4] = {0.f, 0.f, 0.f, 0.f};
#pragma unroll
                    for (int i = 0; i < 5; ++i)
#pragma unroll
                        for (int e = 0; e < 4; ++e) sa[e] = __builtin_fmaf(hi_f(gx[i][e]), hi_f(gv[i][e]), __builtin_fmaf(lo_f(gx[i][e]), lo_f(gv[i][e]), sa[e]));
                    float sacc = (sa[0] + sa[1]) + (sa[2] + sa[3]);
                    sacc += __builtin_amdgcn_update_dpp(0.f, sacc, 0x128, 0xf, 0xf, true);
                    sacc += __builtin_amdgcn_update_dpp(0.f, sacc, 0x124, 0xf, 0xf, true);
                    sacc += __builtin_amdgcn_update_dpp(0.f, sacc, 0x122, 0xf, 0xf, true);
                    sacc += __builtin_amdgcn_update_dpp(0.f, sacc, 0x121, 0xf, 0xf, true);
                    const float gres = rbf(__builtin_amdgcn_rcpf(1.f + __expf(-rbf(sacc))));
                    if (n16 == 0) __builtin_amdgcn_raw_buffer_store_b32(__float_as_uint(gres), wsr, WS_G + grow * 4, 0, 16);
                    spin_mid(lb + SENT_W, SENT_N);
                    const u32x4 ones = {0xffffffffu, 0xffffffffu, 0xffffffffu, 0xffffffffu};
#pragma unroll
                    for (int i = 0; i < 5; ++i) {
                        const int c_ = (w * 5 + i) * 64 + lane, b_ = (c_ * 1639) >> 16;
                        __builtin_amdgcn_raw_buffer_store_b128(ones, wsr, WS_Y + vid * 640 + b_ * (D_DIM * 128 - 640) + c_ * 16, 0, 16);
                    }
                    WAIT_VM(0);
                    if (lds_add_rtn(lb + C_SFC, lane) == 3) {
                        lds_st32(lb + C_SENT, 1);
                        if (lane == 0) __hip_atomic_store((u64*)(ws + WS_SF + vid * 128), tag, __ATOMIC_RELAXED, __HIP_MEMORY_SCOPE_AGENT);
                    }
                    STAMP(6);
                }
                spin_mid(lb + C_RED, 4);
                STAMP(5);
                f32x4 acc[2];
#pragma unroll
                for (int e = 0; e < 2; ++e) {
                    const u32x2 kk = *(const lds_u32x2*)(size_t)(kp + e * 512);
                    acc[e] = (f32x4){lo_f(kk[0]) * Dd, hi_f(kk[0]) * Dd, lo_f(kk[1]) * Dd, hi_f(kk[1]) * Dd};
#pragma unroll
                    for (int o = 0; o < 4; ++o) acc[e] += *(const lds_f32x4*)(size_t)(lb + V_SCR + o * 8192 + (2 * w + e) * 1024 + l16);
                }
                lds_add(lb + C_RED2, lane);
                const auto r0 = __builtin_amdgcn_permlane16_swap(min(pack2(acc[0][0], acc[0][1]), 0xfffffffeu), min(pack2(acc[1][0], acc[1][1]), 0xfffffffeu), false, false);
                const auto r1 = __builtin_amdgcn_permlane16_swap(min(pack2(acc[0][2], acc[0][3]), 0xfffffffeu), min(pack2(acc[1][2], acc[1][3]), 0xfffffffeu), false, false);
                STAMP(7);
                spin_ge(lb + C_RED2, 4);
                WO_4(0, 1, 13, 14) WO_4(15, 16, 17, 18) WO_K(19)
                if (w == 0) WO_X(19, 19, 19, 19)
                if (__builtin_amdgcn_readfirstlane(lds_ld32(lb + C_SENT))) {
                    __builtin_amdgcn_raw_buffer_store_b128((u32x4){r0[0], r1[0], r0[1], r1[1]}, wsr, yo, 0, 16);
                    WAIT_VM(1);
                    lds_add(lb + C_WO, lane);
                } else {
                    WAIT_VM(0);
                    lds_add(lb + C_WO, lane);
                    spin_mid(lb + C_SENT, 1);
                    __builtin_amdgcn_raw_buffer_store_b128((u32x4){r0[0], r1[0], r0[1], r1[1]}, wsr, yo, 0, 16);
                }
            }
            STAMP(12);
        }
    }

    __builtin_amdgcn_s_setprio(T_PRIO);
    {
        RELANE();
        const int kh = (wave >> 2) == (isG ? 0 : 1), kq = kh * 2 + (w & 1), pp = w >> 1;
        const int rte = (rbo >> 1) * 8 + (rbo & 1) * 2 + pp;
        const unsigned ofo = WS_O + (rte * 20 + kq * 5) * 1024 + lane_ * 16;
        const int hsw = (g & 1) * 4;
        const unsigned lofs = lb + (8 * g + hsw + q4) * 160 + p4 * 8 + (w & 1) * 25600;
        const unsigned pc = lofs + (isG ? (kh ? G_R1 : G_R0) : V_WO + kh * 51200);
        const unsigned pd = pc + (4 - 2 * hsw) * 160;
        const unsigned ob4 = WS_O + (rte * 20 + kq * 5 + 4) * 1024;
        const unsigned wo_ = ob4 + (lane_ & 2) * 40960 + (lane_ & 1) * 512 + WATCH_J * 128;
        const unsigned xa = lb + kq * 5120 + lane_ * 16;
        unsigned c0_, c1_, c2_, c3_, qg[3];
#pragma unroll
        for (int j = 0; j < 3; ++j) { const int c = j * 64 + (j < 2 ? lane_ : lane_ & 31), row = (c * 6554) >> 16; qg[j] = row * (D_DIM * 2 - 160) + c * 16; }
        asm volatile(P_ASM
                     : [cnt] "=&s"(c0_), [tmp] "=&s"(c1_), [wm] "=&s"(c2_), [ah] "=&s"(c3_)
                     : [pc] "v"(pc), [pd] "v"(pd), [ofo] "v"(ofo), [of4] "v"(ob4 + g * 256 + n16 * 8), [wat] "v"(wo_), [xa0] "v"(xa + PART(pp)), [xa1] "v"(xa + PART(2 + pp)),
                       [pf] "v"(lb + C_PF + pp * 16 + kq * 4), [ctl] "v"(lb + L_CTRL), [wsp] "s"(ws), [ws2] "s"(ws + 4 * 20480), [won] "s"(isG ? 4 : 8),
                       [wa] "v"(lb + C_WO), [role] "s"(wave >= 8 ? 2 : 0), [qa] "s"(lb + V_Q2 + w * 2560),
                       [q0] "v"(qg[0]), [q1] "v"(qg[1]), [q2] "v"(qg[2]), [qs] "s"((const char*)x + (size_t)(rtq * 16) * (D_DIM * 2) + oc * 160),
                       [ost] "s"(isG ? G_WST : V_WST0), [cap] "s"(SPIN_CAP), [so] "s"(S_O), [thi] "s"(thi)
                     : P_CLOB);
        FLUSH();
    }
}

#undef x
#undef kin
#undef vin
#undef shift_k
#undef ssm_k
#undef ssm_D
#undef wqkv
#undef wo
#undef gw
#undef ws
#undef out
#undef wsr
extern "C" void kernel_launch(void* const* d_in, const int* in_sizes, int n_in, void* d_out, int out_size,
                              void* d_ws, size_t ws_size, hipStream_t stream) {
    const u16* q = (const u16*)d_in[0];
    const u16* k = (const u16*)d_in[1];
    const u16* v = (const u16*)d_in[2];
    const u16* shift_k = (const u16*)d_in[3];
    const u16* ssm_k = (const u16*)d_in[4];
    const u16* ssm_D = (const u16*)d_in[5];
    const u16* wqkv = (const u16*)d_in[7];
    const u16* wo = (const u16*)d_in[8];
    const u16* gw = (const u16*)d_in[9];
    fused_kernel<<<256, 768, 0, stream>>>(q, k, v, shift_k, ssm_k, ssm_D, wqkv, wo, gw, (char*)d_ws, (u16*)d_out);
}
```

```cpp
#define V_DELAY 6
#define MAP1 1
#define HINT_AT 9
#define G_OWN 0
#define OUT_AUX 2
#define L_DELAY 0
#define O_ROT 0
#define Y_SLEEP 3
#define POLL_DELAY 16
#define CONV_PRIO 3
#define SENT_PACE 3
#define GY_DELAY 12
#define VY_DELAY 10
#define NO_PK 1
#define T4_EARLY 1
#define CODE_LINES 402
#include <hip/hip_runtime.h>
#include <hip/hip_bf16.h>
#include <stdint.h>

#define S_LEN 2048
#define D_DIM 640
#define NQKV 1920

typedef unsigned short u16;
typedef unsigned long long u64;
typedef __attribute__((ext_vector_type(8))) short bf16x8;
typedef __attribute__((ext_vector_type(4))) short s16x4;
typedef __attribute__((ext_vector_type(4))) float f32x4;
typedef __attribute__((ext_vector_type(2))) float f32x2;
typedef __attribute__((ext_vector_type(4))) unsigned u32x4;
typedef __attribute__((ext_vector_type(2))) unsigned u32x2;
typedef __attribute__((ext_vector_type(2))) __bf16 bf16x2_t;
typedef __attribute__((address_space(3))) s16x4 lds_s16x4;
typedef __attribute__((address_space(3))) const s16x4 lds_cs16x4;
typedef __attribute__((address_space(3))) const bf16x8 lds_cbf16x8;
typedef __attribute__((address_space(3))) char lds_char;

#define __float_as_uint(f) __builtin_bit_cast(unsigned, (float)(f))
#define __uint_as_float(u) __builtin_bit_cast(float, (unsigned)(u))
#define __any(c) (__builtin_amdgcn_ballot_w64(c) != 0)
#define __all(c) (__builtin_amdgcn_ballot_w64(!(c)) == 0)
extern "C" __device__ unsigned long long llvm_dispatch_id() __asm("llvm.amdgcn.dispatch.id");

__device__ __forceinline__ float bf2f(u16 x) { return __uint_as_float(((unsigned)x) << 16); }
__device__ __forceinline__ u16 f2bf(float f) {
    __hip_bfloat16 b = __float2bfloat16(f);
    return *reinterpret_cast<u16*>(&b);
}
__device__ __forceinline__ float rbf(float f) { return bf2f(f2bf(f)); }
__device__ __forceinline__ float lo_f(unsigned u) { return __uint_as_float(u << 16); }
__device__ __forceinline__ float hi_f(unsigned u) { return __uint_as_float(u & 0xffff0000u); }
__device__ __forceinline__ unsigned pack2(float a, float b) {
    return __builtin_bit_cast(unsigned, __builtin_convertvector((f32x2){a, b}, bf16x2_t));
}
__device__ __forceinline__ float dot2(unsigned a, unsigned b, float c) {
    return __builtin_amdgcn_fdot2_f32_bf16(__builtin_bit_cast(bf16x2_t, a), __builtin_bit_cast(bf16x2_t, b), c, false);
}

#define SB() __builtin_amdgcn_sched_barrier(0)
#define WAIT_VM(n) asm volatile("s_waitcnt vmcnt(" #n ")" ::: "memory")
#define RAW_BARRIER() do { asm volatile("s_waitcnt lgkmcnt(0)" ::: "memory"); __builtin_amdgcn_s_barrier(); } while (0)

#define KV_PITCH 4224
#define WS_KV 4096
#define WS_O (WS_KV + D_DIM * KV_PITCH)
#define WS_Y (WS_O + D_DIM * S_LEN * 2)
#define WS_G (WS_Y + D_DIM * S_LEN * 2)
#define WS_XF (WS_G + S_LEN * 4)
#define WS_SF (WS_XF + 128 * 128)
#define WS_HF (WS_SF + 128 * 128)
#define WS_H (WS_HF + 128 * 128)
#define WS_END (WS_H + 256 * 128)
#define WS_DBG (WS_END + 4096)

#define A_SLOT 18432
#define W_SLOT 16384
#define W_PITCH 512
#define NW 6
#define A_OFF(s) ((s) == 0 ? 53248 : ((s) - 1) * A_SLOT)
#define W_OFF(s) ((s) == 0 ? 36864 : 71680 + ((s) - 1) * W_SLOT)
#define G_R1 0
#define G_R0 88064
#define G_XCH 53248
#define G_Y (G_XCH + 20480)
#define G_Q (G_R0 + 51200)
#define G_HX (G_Q + 10240)
#define G_VX 153600
#define G_TAB (G_VX + 4608)
#define G_HXO (G_TAB + 3072)
#define G_ZR (G_HXO + 1024)
#define G_VXD (G_ZR + 64)
#define KV_BYTES 4352
#define V_F (4 * KV_BYTES)
#define XC_ROW 288
#define XC_B 12
#define XC_COPY 4944
#define XC_BYTES (XC_COPY + 17 * XC_ROW)
#define V_KV4 (V_F + 5 * XC_BYTES)
#define V_KV(c) ((c) < 4 ? (c) * KV_BYTES : V_KV4)
#define V_WO (V_KV4 + KV_BYTES - 10240)
#define V_KST (V_WO + 10240 + 1312)
#define V_VST (V_KST + 20608)
#define V_STG 113536
#define V_QS (V_WO + 10240)
#define V_SCR 130560
#define V_XCH 0
#define V_Q 20480
#define V_Y (V_Q + 10240)
#define Y_PITCH 160
#define L_TOUCH 163328
#define L_CTRL 163584
#define LDS_TOTAL 163840
static_assert(V_VST + 20608 <= V_STG && V_STG % 128 == 0 && V_STG + 20480 <= L_TOUCH && V_WO >= V_F + 4 * XC_BYTES && V_WO + 10 * 10240 <= L_TOUCH &&
              V_WO + 6 * 10240 <= V_SCR && V_SCR + 32768 <= L_TOUCH && V_Y + 80 * Y_PITCH <= V_WO && V_WO % 16 == 0, "lds");
static_assert(W_OFF(5) + W_SLOT == G_VX && G_HX + 4096 == G_VX && G_ZR + 656 <= L_TOUCH && G_Y + 80 * Y_PITCH <= G_R0 && G_TAB % 16 == 0, "lds");
#define C_FREE (L_CTRL + 16)
#define C_OST (L_CTRL + 48)
#define C_SENT (L_CTRL + 52)
#define C_WO (L_CTRL + 56)
#define C_KV (L_CTRL + 60)
#define C_KVST (L_CTRL + 64)
#define C_CONVN (L_CTRL + 68)
#define C_Y (L_CTRL + 72)
#define C_KVR (L_CTRL + 76)
#define C_XD (L_CTRL + 80)
#define C_RED (L_CTRL + 84)
#define C_ORDY (L_CTRL + 88)
#define C_QR (L_CTRL + 92)
#define C_XCH (L_CTRL + 96)
#define C_PRE (L_CTRL + 112)
#define C_SFC (L_CTRL + 136)
#define C_GO (L_CTRL + 140)
#define C_HR (L_CTRL + 144)
#define C_RED2 (L_CTRL + 148)
#define C_KVD (L_CTRL + 152)
#define C_WT (L_CTRL + 156)
#define C_HX (L_CTRL + 160)
#define C_OST1 (L_CTRL + 176)
#define C_HS (L_CTRL + 180)
#define C_WO0 (L_CTRL + 184)
#define C_QR2 (L_CTRL + 188)
#define V_Q2 (V_WO + 81920)
#define C_SENT2 (L_CTRL + 100)
#define C_YS (L_CTRL + 104)
#define C_BP C_XCH
#define C_PF (L_CTRL + 192)
#define PART(slot) (isG ? ((slot) < 2 ? G_R1 + (slot) * 20480 : G_R0 + ((slot) - 2) * 20480) : V_WO + (slot) * 20480)

#define KNOB(n, v)
#define SPIN_CAP 4000000
#define GH 0
#define WO_PRIO 0
#define T_PRIO 3
#define C0_PRIO 2
#define C1_PRIO 1
#define L_PRIO 3
#define O_SLEEP 4
#define O_DELAY 0
#define POLL_SLEEP 2
#define W_SLEEP 4
#define X_SLEEP 2
#define X_DELAY 0
#define NBUF 4
#define WATCH_RT 2
#define JL 17
#define RING_E0 70
#define RING_F 105
#define WOT_SPLIT 0
#define GO_W C_KVD
#define GO_N 11
#define GATE_LATE 0
#define WOT_PACE 3
#define SENT_W C_XD
#define SENT_N 4
#define NSENT 20
#define T0_PRIO T_PRIO
#define G_WST 4
#define V_WST 4
#define V_WST0 0
#define WR_X 2
#define HALO_AT 0
#define H_SLEEP 2
#define HS_WAIT 1
#define H_DELAY 0
#define LAST_PRIO 3
#define WATCH_C 16
#define GO2_W C_XD
#define GO2_N 4
#define H_POL ""
#define KV_POL ""
#define WATCH_J 0
#define SF_EARLY 2
#define HINT_W 3
#define WT_WAVE 11
#define WO_PACE 1
#define SF_WAVE 11
#define YS_LATE 1
#define SF_W C_RED2
#define SF_N 4
#define SO_AT 17
#define OS_W C_KVD
#define OS_N 11
#define SF1_AT 0
#define GW_DELAY 0
#define O_AHEAD 4
#define PASS_PRIO 1
#define G_OST 8
#define V_ODELAY 0
#define W_LATE 1
#define Y_SL0 0
#define SF_DELAY 8
#define WO_MALL 1
#define LATE_AT C_LOOP
#define LATE_N 4
#define KROT ((0x27057250u >> (xcd * 4)) & 15)

#define STAMP(id)
#define FLUSH()
#define CSTAMP(i)

__device__ __forceinline__ unsigned lds_ld32(unsigned a) {
    unsigned v;
    asm volatile("ds_read_b32 %0, %1\n\ts_waitcnt lgkmcnt(0)" : "=v"(v) : "v"(a) : "memory");
    return v;
}
__device__ __forceinline__ unsigned lds_min4(unsigned a) {
    u32x4 v;
    asm volatile("ds_read_b128 %0, %1\n\ts_waitcnt lgkmcnt(0)" : "=v"(v) : "v"(a) : "memory");
    return min(min(v[0], v[1]), min(v[2], v[3]));
}
__device__ __forceinline__ unsigned lds_min8(unsigned a) {
    u32x4 v, u;
    asm volatile("ds_read_b128 %0, %2\n\tds_read_b128 %1, %2 offset:16\n\ts_waitcnt lgkmcnt(0)" : "=&v"(v), "=&v"(u) : "v"(a) : "memory");
    return min(min(min(v[0], v[1]), min(v[2], v[3])), min(min(u[0], u[1]), min(u[2], u[3])));
}
__device__ __forceinline__ void lds_st32(unsigned a, unsigned v) {
    asm volatile("ds_write_b32 %0, %1" ::"v"(a), "v"(v) : "memory");
}
__device__ __forceinline__ unsigned lds_add_rtn(unsigned a, int lane) {
    unsigned old = 0;
    if (lane == 0) {
        unsigned one = 1;
        asm volatile("ds_add_rtn_u32 %0, %1, %2\n\ts_waitcnt lgkmcnt(0)" : "=v"(old) : "v"(a), "v"(one) : "memory");
    }
    return __builtin_amdgcn_readfirstlane(old);
}
__device__ __forceinline__ void lds_add(unsigned a, int lane, unsigned n = 1) {
    if (lane == 0) asm volatile("ds_add_u32 %0, %1" ::"v"(a), "v"(n) : "memory");
}
__device__ __forceinline__ void spin_min4(unsigned a, unsigned target) {
#pragma nounroll
    for (int i = 0; i < SPIN_CAP; ++i) {
        if (__builtin_amdgcn_readfirstlane(lds_min4(a)) >= target) break;
        __builtin_amdgcn_s_sleep(1);
    }
}
__device__ __forceinline__ void spin_min8(unsigned a, unsigned target) {
#pragma nounroll
    for (int i = 0; i < SPIN_CAP; ++i) {
        if (__builtin_amdgcn_readfirstlane(lds_min8(a)) >= target) break;
        __builtin_amdgcn_s_sleep(1);
    }
}
__device__ __forceinline__ void spin_ge(unsigned a, unsigned target) {
#pragma nounroll
    for (int i = 0; i < SPIN_CAP; ++i) {
        if (__builtin_amdgcn_readfirstlane(lds_ld32(a)) >= target) break;
        __builtin_amdgcn_s_sleep(1);
    }
}
typedef __attribute__((address_space(3))) volatile u32x4 lds_vu32x4;
typedef __attribute__((address_space(3))) volatile unsigned lds_vu32_t;
__device__ __forceinline__ u32x4 lds_v128(unsigned a) { return *(lds_vu32x4*)(size_t)a; }
__device__ __forceinline__ unsigned lds_v32(unsigned a) { return *(lds_vu32_t*)(size_t)a; }
__device__ __forceinline__ float fmax_raw(float a, float b) { return __builtin_elementwise_maximum(a, b); }
__device__ __forceinline__ float fmax3_raw(float a, float b, float c) { return __builtin_elementwise_maximum(__builtin_elementwise_maximum(a, b), c); }
__device__ __forceinline__ unsigned opaque(unsigned v) { asm volatile("" : "+v"(v)); return v; }
__device__ __forceinline__ bf16x8 cat8(s16x4 a, s16x4 b) { return __builtin_shufflevector(a, b, 0, 1, 2, 3, 4, 5, 6, 7); }

__device__ __forceinline__ void poll_flags(__amdgpu_buffer_rsrc_t rs, unsigned off, unsigned tlo, unsigned thi) {
#pragma nounroll
    for (int i = 0; i < SPIN_CAP; ++i) {
        asm volatile("" ::: "memory");
        const u32x2 v = __builtin_amdgcn_raw_buffer_load_b64(rs, off, 0, 16);
        if (__all(v[0] == tlo && v[1] == thi)) break;
        __builtin_amdgcn_s_sleep(POLL_SLEEP);
    }
    __builtin_amdgcn_fence(__ATOMIC_ACQUIRE, "wavefront");
}

__device__ __forceinline__ void spin_slow(unsigned a, unsigned target) {
#pragma nounroll
    for (int i = 0; i < SPIN_CAP; ++i) {
        if (__builtin_amdgcn_readfirstlane(lds_ld32(a)) >= target) break;
        __builtin_amdgcn_s_sleep(6);
    }
}
__device__ __forceinline__ void spin_mid(unsigned a, unsigned target) {
#pragma nounroll
    for (int i = 0; i < SPIN_CAP; ++i) {
        if (__builtin_amdgcn_readfirstlane(lds_ld32(a)) >= target) break;
        __builtin_amdgcn_s_sleep(3);
    }
}
__device__ __forceinline__ void spin_all(unsigned a, unsigned target) {
#pragma nounroll
    for (int i = 0; i < SPIN_CAP; ++i) {
        if (__all(lds_ld32(a) >= target)) break;
        __builtin_amdgcn_s_sleep(1);
    }
}

template <int N> __device__ __forceinline__ void waitvm() { asm volatile("s_waitcnt vmcnt(%0)" ::"n"(N) : "memory"); }
#define BARRIER() asm volatile("s_barrier" ::: "memory")
#define GLS(voff, sbase, lw, ldsoff) asm volatile("s_add_u32 m0, %2, %3\n\ts_nop 0\n\tglobal_load_lds_dwordx4 %0, %1" ::"v"(voff), "s"(sbase), "s"(lw), "n"(ldsoff) : "memory", "scc")
#define GLSP(voff, sbase, lw, ldsoff) asm volatile("s_add_u32 m0, %2, %3\n\ts_nop 0\n\tglobal_load_lds_dwordx4 %0, %1" KV_POL ::"v"(voff), "s"(sbase), "s"(lw), "n"(ldsoff) : "memory", "scc")
#define GLT(voff, sbase) asm volatile("s_mov_b32 m0, %2\n\ts_nop 0\n\tglobal_load_lds_dword %0, %1" ::"v"(voff), "s"(sbase), "s"(lb + L_TOUCH) : "memory")
#define TRRD(p) __builtin_amdgcn_ds_read_tr16_b64_v4i16((lds_s16x4*)(p))

constexpr int ring_total(int NA) { return 10 * (8 + NA); }
constexpr int ring_need(int J, int NA) { return J < 0 ? NA + 8 : J >= JL ? 10 * (8 + NA) : (J & 1) ? (NA + 8) * ((J + 3) / 2) : (NA + 8) * ((J + 2) / 2) + 4 + NA; }
constexpr int ring_isA(int p, int NA) { return p < NA ? 1 : p < NA + 8 ? 0 : (p % (8 + NA) >= 4 && p % (8 + NA) < 4 + NA); }
constexpr int ring_tile(int p, int NA) { return p < NA ? 0 : p < NA + 8 ? (p - NA) / 4 : ring_isA(p, NA) ? p / (8 + NA) : 2 * (p / (8 + NA)) + (p % (8 + NA) >= 4); }
constexpr int ring_sub(int p, int NA) { return p < NA ? p : p < NA + 8 ? (p - NA) % 4 : p % (8 + NA) < 4 ? p % (8 + NA) : p % (8 + NA) < 4 + NA ? p % (8 + NA) - 4 : p % (8 + NA) - 4 - NA; }
constexpr int ring_cum(int J, int NA) {
    int c = J >= (JL < 16 ? JL : 16) ? ring_total(NA) : NA + 8 + (8 + NA) * (2 * RING_E0 + RING_F * (J + 1)) / 200;
    if (c > ring_total(NA)) c = ring_total(NA);
    if (c < ring_need(J, NA)) c = ring_need(J, NA);
    while (ring_isA(c - 1, NA) ? 2 * (ring_tile(c - 1, NA) - 3) + 1 > J - 1 : ring_tile(c - 1, NA) - NW > J - 1) --c;
    return c;
}
static_assert(ring_cum(16, 5) == ring_total(5) && ring_cum(16, 4) == ring_total(4), "ring");
template <int NA, int P0, int P1>
__device__ __forceinline__ void ring_issue(const unsigned (&ev)[9], const void* xp, const void* wp, int kr, unsigned lw) {
    if constexpr (P0 < P1) {
        constexpr int t = ring_tile(P0, NA), sub = ring_sub(P0, NA);
        if constexpr (ring_isA(P0, NA)) {
            const int c = __builtin_amdgcn_readfirstlane(kr + t >= 10 ? kr + t - 10 : kr + t);
            GLS(ev[4 + sub], (const char*)xp + c * 128, lw, A_OFF(t % 3) + sub * 4096);
        } else {
            const int c = __builtin_amdgcn_readfirstlane(2 * kr + t >= 20 ? 2 * kr + t - 20 : 2 * kr + t);
            GLS(ev[sub], (const char*)wp + (size_t)c * (32 * NQKV * 2), lw, W_OFF(t % NW) + sub * 4096);
        }
        ring_issue<NA, P0 + 1, P1>(ev, xp, wp, kr, lw);
    }
}
#define S_O 0xff81ff81u
template <int NA, int J>
__device__ __forceinline__ void ring_run(const unsigned (&ev)[9], const void* xp, const void* wp, int kr, unsigned lw) {
    ring_issue<NA, ring_cum(J - 1, NA), ring_cum(J, NA)>(ev, xp, wp, kr, lw);
    if constexpr (J < 16) {
        waitvm<ring_cum(J, NA) - ring_need(J, NA)>();
        BARRIER();
        ring_run<NA, J + 1>(ev, xp, wp, kr, lw);
    }
}

#define CONV_ASM \
    "s_mov_b32 m0, %[kvl]\n" \
    "v_mov_b32 v50, 1\n" \
    "global_load_lds_dwordx4 %[xo], %[wsp] offset:0 sc1\n" \
    "global_load_lds_dwordx4 %[xo], %[wsp] offset:1024 sc1\n" \
    "global_load_lds_dwordx4 %[xo], %[wsp] offset:2048 sc1\n" \
    "global_load_lds_dwordx4 %[xo], %[wsp] offset:3072 sc1\n" \
    "s_mov_b64 exec, 1\n" \
    "ds_add_u32 %[ctl], v50 offset:80\n" \
    "s_mov_b64 exec, -1\n" \
    "v_add_u32 v84, %[sx4], %[xp]\n" \
    "v_add_u32 v51, %[sh4], %[hp]\n" \
    "v_add_u32 v160, 0, %[xp]\n" \
    "v_add_u32 v161, 1152, %[xp]\n" \
    "v_add_u32 v85, 1152, v84\n" \
    "v_add_u32 v162, 2304, %[xp]\n" \
    "v_add_u32 v86, 2304, v84\n" \
    "v_add_u32 v163, 3456, %[xp]\n" \
    "v_add_u32 v87, 3456, v84\n" \
    "ds_read2_b32 v[88:89], v87 offset0:144 offset1:145\n" \
    "ds_read2_b32 v[90:91], v87 offset0:146 offset1:147\n" \
    "ds_read2_b32 v[92:93], v87 offset0:0 offset1:1\n" \
    "ds_read2_b32 v[94:95], v87 offset0:2 offset1:3\n" \
    "ds_read2_b32 v[96:97], v86 offset0:144 offset1:145\n" \
    "ds_read2_b32 v[98:99], v86 offset0:146 offset1:147\n" \
    "ds_read2_b32 v[100:101], v86 offset0:0 offset1:1\n" \
    "ds_read2_b32 v[102:103], v86 offset0:2 offset1:3\n" \
    "ds_read2_b32 v[104:105], v85 offset0:144 offset1:145\n" \
    "ds_read2_b32 v[106:107], v85 offset0:146 offset1:147\n" \
    "ds_read2_b32 v[108:109], v85 offset0:0 offset1:1\n" \
    "ds_read2_b32 v[110:111], v85 offset0:2 offset1:3\n" \
    "ds_read2_b32 v[112:113], v84 offset0:144 offset1:145\n" \
    "ds_read2_b32 v[114:115], v84 offset0:146 offset1:147\n" \
    "ds_read2_b32 v[116:117], v84 offset0:0 offset1:1\n" \
    "ds_read2_b32 v[118:119], v84 offset0:2 offset1:3\n" \
    "s_mov_b32 %[cnt], 0\n" \
    "Lsp_%=:\n" \
    "ds_read_b32 v50, %[ctl] offset:144\n" \
    "s_add_u32 %[cnt], %[cnt], 1\n" \
    "s_waitcnt lgkmcnt(0)\n" \
    "v_readfirstlane_b32 %[tmp], v50\n" \
    "s_cmp_ge_u32 %[cnt], %[cap]\n" \
    "s_cbranch_scc1 Lgo_%=\n" \
    "s_cmp_lt_u32 %[tmp], 4\n" \
    "s_cbranch_scc1 Lsp_%=\n" \
    "Lgo_%=:\n" \
    "ds_read_b128 v[148:151], v51 offset:3584\n" \
    "ds_read_b128 v[144:147], v51 offset:3072\n" \
    "ds_read_b128 v[140:143], v51 offset:2560\n" \
    "ds_read_b128 v[136:139], v51 offset:2048\n" \
    "ds_read_b128 v[132:135], v51 offset:1536\n" \
    "ds_read_b128 v[128:131], v51 offset:1024\n" \
    "ds_read_b128 v[124:127], v51 offset:512\n" \
    "ds_read_b128 v[120:123], v51 offset:0\n" \
    "s_waitcnt lgkmcnt(7)\n" \
    "v_mfma_f32_16x16x32_bf16 v[80:83], v[88:91], v[148:151], 0\n" \
    "ds_read_b128 v[148:151], v51 offset:3648\n" \
    "s_waitcnt lgkmcnt(7)\n" \
    "v_mfma_f32_16x16x32_bf16 v[76:79], v[88:91], v[144:147], 0\n" \
    "ds_read_b128 v[156:159], v51 offset:576\n" \
    "s_waitcnt lgkmcnt(7)\n" \
    "v_mfma_f32_16x16x32_bf16 v[72:75], v[88:91], v[140:143], 0\n" \
    "ds_read_b128 v[152:155], v51 offset:64\n" \
    "s_waitcnt lgkmcnt(7)\n" \
    "v_mfma_f32_16x16x32_bf16 v[68:71], v[88:91], v[136:139], 0\n" \
    "s_waitcnt lgkmcnt(6)\n" \
    "v_mfma_f32_16x16x32_bf16 v[64:67], v[88:91], v[132:135], 0\n" \
    "s_waitcnt lgkmcnt(5)\n" \
    "v_mfma_f32_16x16x32_bf16 v[60:63], v[88:91], v[128:131], 0\n" \
    "s_waitcnt lgkmcnt(4)\n" \
    "v_mfma_f32_16x16x32_bf16 v[56:59], v[88:91], v[124:127], 0\n" \
    "s_waitcnt lgkmcnt(3)\n" \
    "v_mfma_f32_16x16x32_bf16 v[52:55], v[88:91], v[120:123], 0\n" \
    "ds_read2_b32 v[88:89], v87 offset0:160 offset1:161\n" \
    "v_mfma_f32_16x16x32_bf16 v[80:83], v[92:95], v[144:147], v[80:83]\n" \
    "ds_read2_b32 v[90:91], v87 offset0:162 offset1:163\n" \
    "v_mfma_f32_16x16x32_bf16 v[76:79], v[92:95], v[140:143], v[76:79]\n" \
    "ds_read_b128 v[144:147], v51 offset:3136\n" \
    "v_mfma_f32_16x16x32_bf16 v[72:75], v[92:95], v[136:139], v[72:75]\n" \
    "v_mfma_f32_16x16x32_bf16 v[68:71], v[92:95], v[132:135], v[68:71]\n" \
    "v_mfma_f32_16x16x32_bf16 v[64:67], v[92:95], v[128:131], v[64:67]\n" \
    "v_mfma_f32_16x16x32_bf16 v[60:63], v[92:95], v[124:127], v[60:63]\n" \
    "v_mfma_f32_16x16x32_bf16 v[56:59], v[92:95], v[120:123], v[56:59]\n" \
    "ds_read2_b32 v[92:93], v87 offset0:16 offset1:17\n" \
    "v_mfma_f32_16x16x32_bf16 v[80:83], v[96:99], v[140:143], v[80:83]\n" \
    "ds_read_b128 v[140:143], v51 offset:2624\n" \
    "v_mfma_f32_16x16x32_bf16 v[76:79], v[96:99], v[136:139], v[76:79]\n" \
    "ds_read2_b32 v[94:95], v87 offset0:18 offset1:19\n" \
    "v_mfma_f32_16x16x32_bf16 v[72:75], v[96:99], v[132:135], v[72:75]\n" \
    "v_mfma_f32_16x16x32_bf16 v[68:71], v[96:99], v[128:131], v[68:71]\n" \
    "v_mfma_f32_16x16x32_bf16 v[64:67], v[96:99], v[124:127], v[64:67]\n" \
    "v_mfma_f32_16x16x32_bf16 v[60:63], v[96:99], v[120:123], v[60:63]\n" \
    "ds_read2_b32 v[96:97], v86 offset0:160 offset1:161\n" \
    "v_mfma_f32_16x16x32_bf16 v[80:83], v[100:103], v[136:139], v[80:83]\n" \
    "ds_read_b128 v[136:139], v51 offset:2112\n" \
    "v_mfma_f32_16x16x32_bf16 v[76:79], v[100:103], v[132:135], v[76:79]\n" \
    "ds_read2_b32 v[98:99], v86 offset0:162 offset1:163\n" \
    "v_mfma_f32_16x16x32_bf16 v[72:75], v[100:103], v[128:131], v[72:75]\n" \
    "v_mfma_f32_16x16x32_bf16 v[68:71], v[100:103], v[124:127], v[68:71]\n" \
    "v_mfma_f32_16x16x32_bf16 v[64:67], v[100:103], v[120:123], v[64:67]\n" \
    "ds_read2_b32 v[100:101], v86 offset0:16 offset1:17\n" \
    "v_mfma_f32_16x16x32_bf16 v[80:83], v[104:107], v[132:135], v[80:83]\n" \
    "ds_read_b128 v[132:135], v51 offset:1600\n" \
    "v_mfma_f32_16x16x32_bf16 v[76:79], v[104:107], v[128:131], v[76:79]\n" \
    "ds_read2_b32 v[102:103], v86 offset0:18 offset1:19\n" \
    "v_mfma_f32_16x16x32_bf16 v[72:75], v[104:107], v[124:127], v[72:75]\n" \
    "v_mfma_f32_16x16x32_bf16 v[68:71], v[104:107], v[120:123], v[68:71]\n" \
    "ds_read2_b32 v[104:105], v85 offset0:160 offset1:161\n" \
    "v_mfma_f32_16x16x32_bf16 v[80:83], v[108:111], v[128:131], v[80:83]\n" \
    "ds_read_b128 v[128:131], v51 offset:1088\n" \
    "v_mfma_f32_16x16x32_bf16 v[76:79], v[108:111], v[124:127], v[76:79]\n" \
    "ds_read2_b32 v[106:107], v85 offset0:162 offset1:163\n" \
    "v_mfma_f32_16x16x32_bf16 v[72:75], v[108:111], v[120:123], v[72:75]\n" \
    "ds_read2_b32 v[108:109], v85 offset0:16 offset1:17\n" \
    "v_mfma_f32_16x16x32_bf16 v[80:83], v[112:115], v[124:127], v[80:83]\n" \
    "ds_read2_b32 v[110:111], v85 offset0:18 offset1:19\n" \
    "v_mfma_f32_16x16x32_bf16 v[76:79], v[112:115], v[120:123], v[76:79]\n" \
    "ds_read2_b32 v[112:113], v84 offset0:160 offset1:161\n" \
    "v_mfma_f32_16x16x32_bf16 v[80:83], v[116:119], v[120:123], v[80:83]\n" \
    "ds_read2_b32 v[114:115], v84 offset0:162 offset1:163\n" \
    "s_waitcnt lgkmcnt(15)\n" \
    "v_mfma_f32_16x16x32_bf16 v[80:83], v[88:91], v[148:151], v[80:83]\n" \
    "ds_read2_b32 v[116:117], v84 offset0:16 offset1:17\n" \
    "v_mfma_f32_16x16x32_bf16 v[76:79], v[88:91], v[144:147], v[76:79]\n" \
    "ds_read2_b32 v[118:119], v84 offset0:18 offset1:19\n" \
    "s_waitcnt lgkmcnt(15)\n" \
    "v_mfma_f32_16x16x32_bf16 v[72:75], v[88:91], v[140:143], v[72:75]\n" \
    "s_waitcnt vmcnt(0)\n" \
    "ds_read_b128 v[148:151], %[hp] offset:3584\n" \
    "s_waitcnt lgkmcnt(14)\n" \
    "v_mfma_f32_16x16x32_bf16 v[68:71], v[88:91], v[136:139], v[68:71]\n" \
    "ds_read_b128 v[124:127], %[hp] offset:512\n" \
    "s_waitcnt lgkmcnt(12)\n" \
    "v_mfma_f32_16x16x32_bf16 v[64:67], v[88:91], v[132:135], v[64:67]\n" \
    "ds_read_b128 v[120:123], %[hp] offset:0\n" \
    "s_waitcnt lgkmcnt(10)\n" \
    "v_mfma_f32_16x16x32_bf16 v[60:63], v[88:91], v[128:131], v[60:63]\n" \
    "v_mfma_f32_16x16x32_bf16 v[56:59], v[88:91], v[156:159], v[56:59]\n" \
    "v_mfma_f32_16x16x32_bf16 v[52:55], v[88:91], v[152:155], v[52:55]\n" \
    "ds_read2_b32 v[88:89], v163 offset0:144 offset1:145\n" \
    "v_mfma_f32_16x16x32_bf16 v[80:83], v[92:95], v[144:147], v[80:83]\n" \
    "ds_read2_b32 v[90:91], v163 offset0:146 offset1:147\n" \
    "v_mfma_f32_16x16x32_bf16 v[76:79], v[92:95], v[140:143], v[76:79]\n" \
    "ds_read_b128 v[144:147], %[hp] offset:3072\n" \
    "v_mfma_f32_16x16x32_bf16 v[72:75], v[92:95], v[136:139], v[72:75]\n" \
    "v_mfma_f32_16x16x32_bf16 v[68:71], v[92:95], v[132:135], v[68:71]\n" \
    "ds_write_b128 %[scr], v[52:55] offset:0\n" \
    "v_mfma_f32_16x16x32_bf16 v[64:67], v[92:95], v[128:131], v[64:67]\n" \
    "ds_read_b64 v[86:87], %[kvr] offset:0\n" \
    "v_mfma_f32_16x16x32_bf16 v[60:63], v[92:95], v[156:159], v[60:63]\n" \
    "v_mfma_f32_16x16x32_bf16 v[56:59], v[92:95], v[152:155], v[56:59]\n" \
    "ds_read2_b32 v[92:93], v163 offset0:0 offset1:1\n" \
    "v_mfma_f32_16x16x32_bf16 v[80:83], v[96:99], v[140:143], v[80:83]\n" \
    "ds_read_b128 v[140:143], %[hp] offset:2560\n" \
    "v_mfma_f32_16x16x32_bf16 v[76:79], v[96:99], v[136:139], v[76:79]\n" \
    "ds_read2_b32 v[94:95], v163 offset0:2 offset1:3\n" \
    "v_mfma_f32_16x16x32_bf16 v[72:75], v[96:99], v[132:135], v[72:75]\n" \
    "v_mfma_f32_16x16x32_bf16 v[68:71], v[96:99], v[128:131], v[68:71]\n" \
    "ds_write_b128 %[scr], v[56:59] offset:1024\n" \
    "v_mfma_f32_16x16x32_bf16 v[64:67], v[96:99], v[156:159], v[64:67]\n" \
    "v_mfma_f32_16x16x32_bf16 v[60:63], v[96:99], v[152:155], v[60:63]\n" \
    "ds_read2_b32 v[96:97], v162 offset0:144 offset1:145\n" \
    "v_mfma_f32_16x16x32_bf16 v[80:83], v[100:103], v[136:139], v[80:83]\n" \
    "ds_read_b128 v[136:139], %[hp] offset:2048\n" \
    "v_mfma_f32_16x16x32_bf16 v[76:79], v[100:103], v[132:135], v[76:79]\n" \
    "ds_read2_b32 v[98:99], v162 offset0:146 offset1:147\n" \
    "v_mfma_f32_16x16x32_bf16 v[72:75], v[100:103], v[128:131], v[72:75]\n" \
    "v_mfma_f32_16x16x32_bf16 v[68:71], v[100:103], v[156:159], v[68:71]\n" \
    "s_waitcnt lgkmcnt(7)\n" \
    "v_lshlrev_b32 v84, 16, v86\n" \
    "ds_write_b128 %[scr], v[60:63] offset:2048\n" \
    "v_mfma_f32_16x16x32_bf16 v[64:67], v[100:103], v[152:155], v[64:67]\n" \
    "ds_read2_b32 v[100:101], v162 offset0:0 offset1:1\n" \
    "v_mfma_f32_16x16x32_bf16 v[80:83], v[104:107], v[132:135], v[80:83]\n" \
    "ds_read_b128 v[132:135], %[hp] offset:1536\n" \
    "v_and_b32 v85, 0xffff0000, v86\n" \
    "v_mfma_f32_16x16x32_bf16 v[76:79], v[104:107], v[128:131], v[76:79]\n" \
    "ds_read2_b32 v[102:103], v162 offset0:2 offset1:3\n" \
    "v_mfma_f32_16x16x32_bf16 v[72:75], v[104:107], v[156:159], v[72:75]\n" \
    "v_lshlrev_b32 v86, 16, v87\n" \
    "v_mfma_f32_16x16x32_bf16 v[68:71], v[104:107], v[152:155], v[68:71]\n" \
    "ds_read2_b32 v[104:105], v161 offset0:144 offset1:145\n" \
    "ds_write_b128 %[scr], v[64:67] offset:3072\n" \
    "v_mfma_f32_16x16x32_bf16 v[80:83], v[108:111], v[128:131], v[80:83]\n" \
    "ds_read_b128 v[128:131], %[hp] offset:1024\n" \
    "v_and_b32 v87, 0xffff0000, v87\n" \
    "v_mfma_f32_16x16x32_bf16 v[76:79], v[108:111], v[156:159], v[76:79]\n" \
    "ds_read2_b32 v[106:107], v161 offset0:146 offset1:147\n" \
    "v_mfma_f32_16x16x32_bf16 v[72:75], v[108:111], v[152:155], v[72:75]\n" \
    "ds_read2_b32 v[108:109], v161 offset0:0 offset1:1\n" \
    "v_mfma_f32_16x16x32_bf16 v[80:83], v[112:115], v[156:159], v[80:83]\n" \
    "ds_read2_b32 v[110:111], v161 offset0:2 offset1:3\n" \
    "ds_write_b128 %[scr], v[68:71] offset:4096\n" \
    "v_mfma_f32_16x16x32_bf16 v[76:79], v[112:115], v[152:155], v[76:79]\n" \
    "ds_read2_b32 v[112:113], v160 offset0:144 offset1:145\n" \
    "v_mfma_f32_16x16x32_bf16 v[80:83], v[116:119], v[152:155], v[80:83]\n" \
    "ds_read2_b32 v[114:115], v160 offset0:146 offset1:147\n" \
    "v_mfma_f32_16x16x32_bf16 v[44:47], v[88:91], v[148:151], 0\n" \
    "ds_read2_b32 v[116:117], v160 offset0:0 offset1:1\n" \
    "ds_write_b128 %[scr], v[72:75] offset:5120\n" \
    "v_mfma_f32_16x16x32_bf16 v[40:43], v[88:91], v[144:147], 0\n" \
    "ds_read2_b32 v[118:119], v160 offset0:2 offset1:3\n" \
    "s_waitcnt lgkmcnt(15)\n" \
    "v_mfma_f32_16x16x32_bf16 v[36:39], v[88:91], v[140:143], 0\n" \
    "ds_read_b128 v[148:151], %[hp] offset:3648\n" \
    "ds_write_b128 %[scr], v[76:79] offset:6144\n" \
    "v_mfma_f32_16x16x32_bf16 v[32:35], v[88:91], v[136:139], 0\n" \
    "ds_read_b128 v[156:159], %[hp] offset:576\n" \
    "s_waitcnt lgkmcnt(15)\n" \
    "v_mfma_f32_16x16x32_bf16 v[28:31], v[88:91], v[132:135], 0\n" \
    "ds_read_b128 v[152:155], %[hp] offset:64\n" \
    "ds_write_b128 %[scr], v[80:83] offset:7168\n" \
    "v_mov_b32 v50, 1\n" \
    "s_mov_b64 exec, 1\n" \
    "ds_add_u32 %[ctl], v50 offset:84\n" \
    "s_mov_b64 exec, -1\n" \
    "s_setprio 0\n" \
    "s_waitcnt lgkmcnt(15)\n" \
    "v_mfma_f32_16x16x32_bf16 v[24:27], v[88:91], v[128:131], 0\n" \
    "v_mfma_f32_16x16x32_bf16 v[20:23], v[88:91], v[124:127], 0\n" \
    "v_mfma_f32_16x16x32_bf16 v[16:19], v[88:91], v[120:123], 0\n" \
    "ds_read2_b32 v[88:89], v163 offset0:160 offset1:161\n" \
    "v_mfma_f32_16x16x32_bf16 v[44:47], v[92:95], v[144:147], v[44:47]\n" \
    "ds_read2_b32 v[90:91], v163 offset0:162 offset1:163\n" \
    "v_mfma_f32_16x16x32_bf16 v[40:43], v[92:95], v[140:143], v[40:43]\n" \
    "ds_read_b128 v[144:147], %[hp] offset:3136\n" \
    "v_mfma_f32_16x16x32_bf16 v[36:39], v[92:95], v[136:139], v[36:39]\n" \
    "v_fmac_f32 v16, %[dd], v84\n" \
    "v_fmac_f32 v17, %[dd], v85\n" \
    "v_mfma_f32_16x16x32_bf16 v[32:35], v[92:95], v[132:135], v[32:35]\n" \
    "v_fmac_f32 v18, %[dd], v86\n" \
    "v_fmac_f32 v19, %[dd], v87\n" \
    "v_mfma_f32_16x16x32_bf16 v[28:31], v[92:95], v[128:131], v[28:31]\n" \
    "ds_read_b64 v[86:87], %[kvr] offset:512\n" \
    "v_mfma_f32_16x16x32_bf16 v[24:27], v[92:95], v[124:127], v[24:27]\n" \
    "v_mfma_f32_16x16x32_bf16 v[20:23], v[92:95], v[120:123], v[20:23]\n" \
    "ds_read2_b32 v[92:93], v163 offset0:16 offset1:17\n" \
    "v_mfma_f32_16x16x32_bf16 v[44:47], v[96:99], v[140:143], v[44:47]\n" \
    "ds_read_b128 v[140:143], %[hp] offset:2624\n" \
    "v_mfma_f32_16x16x32_bf16 v[40:43], v[96:99], v[136:139], v[40:43]\n" \
    "ds_read2_b32 v[94:95], v163 offset0:18 offset1:19\n" \
    "v_mfma_f32_16x16x32_bf16 v[36:39], v[96:99], v[132:135], v[36:39]\n" \
    "v_mfma_f32_16x16x32_bf16 v[32:35], v[96:99], v[128:131], v[32:35]\n" \
    "v_mfma_f32_16x16x32_bf16 v[28:31], v[96:99], v[124:127], v[28:31]\n" \
    "v_mfma_f32_16x16x32_bf16 v[24:27], v[96:99], v[120:123], v[24:27]\n" \
    "ds_read2_b32 v[96:97], v162 offset0:160 offset1:161\n" \
    "v_mfma_f32_16x16x32_bf16 v[44:47], v[100:103], v[136:139], v[44:47]\n" \
    "ds_read_b128 v[136:139], %[hp] offset:2112\n" \
    "v_mfma_f32_16x16x32_bf16 v[40:43], v[100:103], v[132:135], v[40:43]\n" \
    "ds_read2_b32 v[98:99], v162 offset0:162 offset1:163\n" \
    "v_mfma_f32_16x16x32_bf16 v[36:39], v[100:103], v[128:131], v[36:39]\n" \
    "v_mfma_f32_16x16x32_bf16 v[32:35], v[100:103], v[124:127], v[32:35]\n" \
    "s_waitcnt lgkmcnt(6)\n" \
    "v_lshlrev_b32 v84, 16, v86\n" \
    "s_setprio 3\n" \
    "v_mfma_f32_16x16x32_bf16 v[28:31], v[100:103], v[120:123], v[28:31]\n" \
    "ds_read2_b32 v[100:101], v162 offset0:16 offset1:17\n" \
    "v_mfma_f32_16x16x32_bf16 v[44:47], v[104:107], v[132:135], v[44:47]\n" \
    "ds_read_b128 v[132:135], %[hp] offset:1600\n" \
    "v_and_b32 v85, 0xffff0000, v86\n" \
    "v_mfma_f32_16x16x32_bf16 v[40:43], v[104:107], v[128:131], v[40:43]\n" \
    "ds_read2_b32 v[102:103], v162 offset0:18 offset1:19\n" \
    "v_mfma_f32_16x16x32_bf16 v[36:39], v[104:107], v[124:127], v[36:39]\n" \
    "v_lshlrev_b32 v86, 16, v87\n" \
    "v_mfma_f32_16x16x32_bf16 v[32:35], v[104:107], v[120:123], v[32:35]\n" \
    "ds_read2_b32 v[104:105], v161 offset0:160 offset1:161\n" \
    "v_mfma_f32_16x16x32_bf16 v[44:47], v[108:111], v[128:131], v[44:47]\n" \
    "ds_read_b128 v[128:131], %[hp] offset:1088\n" \
    "v_and_b32 v87, 0xffff0000, v87\n" \
    "v_mfma_f32_16x16x32_bf16 v[40:43], v[108:111], v[124:127], v[40:43]\n" \
    "ds_read2_b32 v[106:107], v161 offset0:162 offset1:163\n" \
    "v_mfma_f32_16x16x32_bf16 v[36:39], v[108:111], v[120:123], v[36:39]\n" \
    "ds_read2_b32 v[108:109], v161 offset0:16 offset1:17\n" \
    "v_mfma_f32_16x16x32_bf16 v[44:47], v[112:115], v[124:127], v[44:47]\n" \
    "ds_read2_b32 v[110:111], v161 offset0:18 offset1:19\n" \
    "v_mfma_f32_16x16x32_bf16 v[40:43], v[112:115], v[120:123], v[40:43]\n" \
    "ds_read2_b32 v[112:113], v160 offset0:160 offset1:161\n" \
    "v_mfma_f32_16x16x32_bf16 v[44:47], v[116:119], v[120:123], v[44:47]\n" \
    "ds_read2_b32 v[114:115], v160 offset0:162 offset1:163\n" \
    "v_mfma_f32_16x16x32_bf16 v[44:47], v[88:91], v[148:151], v[44:47]\n" \
    "ds_read2_b32 v[116:117], v160 offset0:16 offset1:17\n" \
    "v_mfma_f32_16x16x32_bf16 v[40:43], v[88:91], v[144:147], v[40:43]\n" \
    "ds_read2_b32 v[118:119], v160 offset0:18 offset1:19\n" \
    "s_waitcnt lgkmcnt(15)\n" \
    "v_mfma_f32_16x16x32_bf16 v[36:39], v[88:91], v[140:143], v[36:39]\n" \
    "ds_read_b128 v[148:151], %[hp] offset:3712\n" \
    "s_waitcnt lgkmcnt(14)\n" \
    "v_mfma_f32_16x16x32_bf16 v[32:35], v[88:91], v[136:139], v[32:35]\n" \
    "ds_read_b128 v[124:127], %[hp] offset:640\n" \
    "s_waitcnt lgkmcnt(12)\n" \
    "v_mfma_f32_16x16x32_bf16 v[28:31], v[88:91], v[132:135], v[28:31]\n" \
    "ds_read_b128 v[120:123], %[hp] offset:128\n" \
    "s_waitcnt lgkmcnt(10)\n" \
    "v_mfma_f32_16x16x32_bf16 v[24:27], v[88:91], v[128:131], v[24:27]\n" \
    "v_mfma_f32_16x16x32_bf16 v[20:23], v[88:91], v[156:159], v[20:23]\n" \
    "v_mfma_f32_16x16x32_bf16 v[16:19], v[88:91], v[152:155], v[16:19]\n" \
    "ds_read2_b32 v[88:89], v163 offset0:176 offset1:177\n" \
    "v_mfma_f32_16x16x32_bf16 v[44:47], v[92:95], v[144:147], v[44:47]\n" \
    "ds_read2_b32 v[90:91], v163 offset0:178 offset1:179\n" \
    "v_mfma_f32_16x16x32_bf16 v[40:43], v[92:95], v[140:143], v[40:43]\n" \
    "ds_read_b128 v[144:147], %[hp] offset:3200\n" \
    "v_fmac_f32 v20, %[dd], v84\n" \
    "v_fmac_f32 v21, %[dd], v85\n" \
    "v_mfma_f32_16x16x32_bf16 v[36:39], v[92:95], v[136:139], v[36:39]\n" \
    "v_fmac_f32 v22, %[dd], v86\n" \
    "v_fmac_f32 v23, %[dd], v87\n" \
    "v_mfma_f32_16x16x32_bf16 v[32:35], v[92:95], v[132:135], v[32:35]\n" \
    "v_mfma_f32_16x16x32_bf16 v[28:31], v[92:95], v[128:131], v[28:31]\n" \
    "ds_read_b64 v[86:87], %[kvr] offset:1024\n" \
    "v_mfma_f32_16x16x32_bf16 v[24:27], v[92:95], v[156:159], v[24:27]\n" \
    "v_mfma_f32_16x16x32_bf16 v[20:23], v[92:95], v[152:155], v[20:23]\n" \
    "ds_read2_b32 v[92:93], v163 offset0:32 offset1:33\n" \
    "v_mfma_f32_16x16x32_bf16 v[44:47], v[96:99], v[140:143], v[44:47]\n" \
    "ds_read_b128 v[140:143], %[hp] offset:2688\n" \
    "v_mfma_f32_16x16x32_bf16 v[40:43], v[96:99], v[136:139], v[40:43]\n" \
    "ds_read2_b32 v[94:95], v163 offset0:34 offset1:35\n" \
    "v_mfma_f32_16x16x32_bf16 v[36:39], v[96:99], v[132:135], v[36:39]\n" \
    "v_mfma_f32_16x16x32_bf16 v[32:35], v[96:99], v[128:131], v[32:35]\n" \
    "v_mfma_f32_16x16x32_bf16 v[28:31], v[96:99], v[156:159], v[28:31]\n" \
    "v_mfma_f32_16x16x32_bf16 v[24:27], v[96:99], v[152:155], v[24:27]\n" \
    "ds_read2_b32 v[96:97], v162 offset0:176 offset1:177\n" \
    "v_mfma_f32_16x16x32_bf16 v[44:47], v[100:103], v[136:139], v[44:47]\n" \
    "ds_read_b128 v[136:139], %[hp] offset:2176\n" \
    "v_mfma_f32_16x16x32_bf16 v[40:43], v[100:103], v[132:135], v[40:43]\n" \
    "ds_read2_b32 v[98:99], v162 offset0:178 offset1:179\n" \
    "v_mfma_f32_16x16x32_bf16 v[36:39], v[100:103], v[128:131], v[36:39]\n" \
    "v_mfma_f32_16x16x32_bf16 v[32:35], v[100:103], v[156:159], v[32:35]\n" \
    "s_waitcnt lgkmcnt(6)\n" \
    "v_lshlrev_b32 v84, 16, v86\n" \
    "v_mfma_f32_16x16x32_bf16 v[28:31], v[100:103], v[152:155], v[28:31]\n" \
    "ds_read2_b32 v[100:101], v162 offset0:32 offset1:33\n" \
    "v_mfma_f32_16x16x32_bf16 v[44:47], v[104:107], v[132:135], v[44:47]\n" \
    "ds_read_b128 v[132:135], %[hp] offset:1664\n" \
    "v_and_b32 v85, 0xffff0000, v86\n" \
    "v_mfma_f32_16x16x32_bf16 v[40:43], v[104:107], v[128:131], v[40:43]\n" \
    "ds_read2_b32 v[102:103], v162 offset0:34 offset1:35\n" \
    "v_mfma_f32_16x16x32_bf16 v[36:39], v[104:107], v[156:159], v[36:39]\n" \
    "v_lshlrev_b32 v86, 16, v87\n" \
    "v_mfma_f32_16x16x32_bf16 v[32:35], v[104:107], v[152:155], v[32:35]\n" \
    "ds_read2_b32 v[104:105], v161 offset0:176 offset1:177\n" \
    "v_mfma_f32_16x16x32_bf16 v[44:47], v[108:111], v[128:131], v[44:47]\n" \
    "ds_read_b128 v[128:131], %[hp] offset:1152\n" \
    "v_and_b32 v87, 0xffff0000, v87\n" \
    "v_mfma_f32_16x16x32_bf16 v[40:43], v[108:111], v[156:159], v[40:43]\n" \
    "ds_read2_b32 v[106:107], v161 offset0:178 offset1:179\n" \
    "v_mfma_f32_16x16x32_bf16 v[36:39], v[108:111], v[152:155], v[36:39]\n" \
    "ds_read2_b32 v[108:109], v161 offset0:32 offset1:33\n" \
    "v_mfma_f32_16x16x32_bf16 v[44:47], v[112:115], v[156:159], v[44:47]\n" \
    "ds_read2_b32 v[110:111], v161 offset0:34 offset1:35\n" \
    "v_mfma_f32_16x16x32_bf16 v[40:43], v[112:115], v[152:155], v[40:43]\n" \
    "ds_read2_b32 v[112:113], v160 offset0:176 offset1:177\n" \
    "v_mfma_f32_16x16x32_bf16 v[44:47], v[116:119], v[152:155], v[44:47]\n" \
    "ds_read2_b32 v[114:115], v160 offset0:178 offset1:179\n" \
    "v_mfma_f32_16x16x32_bf16 v[44:47], v[88:91], v[148:151], v[44:47]\n" \
    "ds_read2_b32 v[116:117], v160 offset0:32 offset1:33\n" \
    "v_mfma_f32_16x16x32_bf16 v[40:43], v[88:91], v[144:147], v[40:43]\n" \
    "ds_read2_b32 v[118:119], v160 offset0:34 offset1:35\n" \
    "s_waitcnt lgkmcnt(15)\n" \
    "v_mfma_f32_16x16x32_bf16 v[36:39], v[88:91], v[140:143], v[36:39]\n" \
    "ds_read_b128 v[148:151], %[hp] offset:3776\n" \
    "s_waitcnt lgkmcnt(14)\n" \
    "v_mfma_f32_16x16x32_bf16 v[32:35], v[88:91], v[136:139], v[32:35]\n" \
    "ds_read_b128 v[156:159], %[hp] offset:704\n" \
    "s_waitcnt lgkmcnt(12)\n" \
    "v_mfma_f32_16x16x32_bf16 v[28:31], v[88:91], v[132:135], v[28:31]\n" \
    "ds_read_b128 v[152:155], %[hp] offset:192\n" \
    "s_waitcnt lgkmcnt(10)\n" \
    "v_mfma_f32_16x16x32_bf16 v[24:27], v[88:91], v[128:131], v[24:27]\n" \
    "v_mfma_f32_16x16x32_bf16 v[20:23], v[88:91], v[124:127], v[20:23]\n" \
    "v_mfma_f32_16x16x32_bf16 v[16:19], v[88:91], v[120:123], v[16:19]\n" \
    "ds_read2_b32 v[88:89], v163 offset0:192 offset1:193\n" \
    "v_mfma_f32_16x16x32_bf16 v[44:47], v[92:95], v[144:147], v[44:47]\n" \
    "ds_read2_b32 v[90:91], v163 offset0:194 offset1:195\n" \
    "v_fmac_f32 v24, %[dd], v84\n" \
    "v_fmac_f32 v25, %[dd], v85\n" \
    "v_mfma_f32_16x16x32_bf16 v[40:43], v[92:95], v[140:143], v[40:43]\n" \
    "ds_read_b128 v[144:147], %[hp] offset:3264\n" \
    "v_fmac_f32 v26, %[dd], v86\n" \
    "v_fmac_f32 v27, %[dd], v87\n" \
    "v_mfma_f32_16x16x32_bf16 v[36:39], v[92:95], v[136:139], v[36:39]\n" \
    "v_mfma_f32_16x16x32_bf16 v[32:35], v[92:95], v[132:135], v[32:35]\n" \
    "v_mfma_f32_16x16x32_bf16 v[28:31], v[92:95], v[128:131], v[28:31]\n" \
    "ds_read_b64 v[86:87], %[kvr] offset:1536\n" \
    "v_mfma_f32_16x16x32_bf16 v[24:27], v[92:95], v[124:127], v[24:27]\n" \
    "v_mfma_f32_16x16x32_bf16 v[20:23], v[92:95], v[120:123], v[20:23]\n" \
    "ds_read2_b32 v[92:93], v163 offset0:48 offset1:49\n" \
    "v_mfma_f32_16x16x32_bf16 v[44:47], v[96:99], v[140:143], v[44:47]\n" \
    "ds_read_b128 v[140:143], %[hp] offset:2752\n" \
    "v_mfma_f32_16x16x32_bf16 v[40:43], v[96:99], v[136:139], v[40:43]\n" \
    "ds_read2_b32 v[94:95], v163 offset0:50 offset1:51\n" \
    "v_mfma_f32_16x16x32_bf16 v[36:39], v[96:99], v[132:135], v[36:39]\n" \
    "v_mfma_f32_16x16x32_bf16 v[32:35], v[96:99], v[128:131], v[32:35]\n" \
    "v_mfma_f32_16x16x32_bf16 v[28:31], v[96:99], v[124:127], v[28:31]\n" \
    "v_mfma_f32_16x16x32_bf16 v[24:27], v[96:99], v[120:123], v[24:27]\n" \
    "ds_read2_b32 v[96:97], v162 offset0:192 offset1:193\n" \
    "v_mfma_f32_16x16x32_bf16 v[44:47], v[100:103], v[136:139], v[44:47]\n" \
    "ds_read_b128 v[136:139], %[hp] offset:2240\n" \
    "v_mfma_f32_16x16x32_bf16 v[40:43], v[100:103], v[132:135], v[40:43]\n" \
    "ds_read2_b32 v[98:99], v162 offset0:194 offset1:195\n" \
    "v_mfma_f32_16x16x32_bf16 v[36:39], v[100:103], v[128:131], v[36:39]\n" \
    "v_mfma_f32_16x16x32_bf16 v[32:35], v[100:103], v[124:127], v[32:35]\n" \
    "s_waitcnt lgkmcnt(6)\n" \
    "v_lshlrev_b32 v84, 16, v86\n" \
    "v_mfma_f32_16x16x32_bf16 v[28:31], v[100:103], v[120:123], v[28:31]\n" \
    "ds_read2_b32 v[100:101], v162 offset0:48 offset1:49\n" \
    "v_mfma_f32_16x16x32_bf16 v[44:47], v[104:107], v[132:135], v[44:47]\n" \
    "ds_read_b128 v[132:135], %[hp] offset:1728\n" \
    "v_and_b32 v85, 0xffff0000, v86\n" \
    "v_mfma_f32_16x16x32_bf16 v[40:43], v[104:107], v[128:131], v[40:43]\n" \
    "ds_read2_b32 v[102:103], v162 offset0:50 offset1:51\n" \
    "v_mfma_f32_16x16x32_bf16 v[36:39], v[104:107], v[124:127], v[36:39]\n" \
    "v_lshlrev_b32 v86, 16, v87\n" \
    "v_mfma_f32_16x16x32_bf16 v[32:35], v[104:107], v[120:123], v[32:35]\n" \
    "ds_read2_b32 v[104:105], v161 offset0:192 offset1:193\n" \
    "v_mfma_f32_16x16x32_bf16 v[44:47], v[108:111], v[128:131], v[44:47]\n" \
    "ds_read_b128 v[128:131], %[hp] offset:1216\n" \
    "v_and_b32 v87, 0xffff0000, v87\n" \
    "v_mfma_f32_16x16x32_bf16 v[40:43], v[108:111], v[124:127], v[40:43]\n" \
    "ds_read2_b32 v[106:107], v161 offset0:194 offset1:195\n" \
    "v_mfma_f32_16x16x32_bf16 v[36:39], v[108:111], v[120:123], v[36:39]\n" \
    "ds_read2_b32 v[108:109], v161 offset0:48 offset1:49\n" \
    "v_mfma_f32_16x16x32_bf16 v[44:47], v[112:115], v[124:127], v[44:47]\n" \
    "ds_read2_b32 v[110:111], v161 offset0:50 offset1:51\n" \
    "v_mfma_f32_16x16x32_bf16 v[40:43], v[112:115], v[120:123], v[40:43]\n" \
    "ds_read2_b32 v[112:113], v160 offset0:192 offset1:193\n" \
    "v_mfma_f32_16x16x32_bf16 v[44:47], v[116:119], v[120:123], v[44:47]\n" \
    "ds_read2_b32 v[114:115], v160 offset0:194 offset1:195\n" \
    "v_mfma_f32_16x16x32_bf16 v[44:47], v[88:91], v[148:151], v[44:47]\n" \
    "ds_read2_b32 v[116:117], v160 offset0:48 offset1:49\n" \
    "v_mfma_f32_16x16x32_bf16 v[40:43], v[88:91], v[144:147], v[40:43]\n" \
    "ds_read2_b32 v[118:119], v160 offset0:50 offset1:51\n" \
    "s_waitcnt lgkmcnt(15)\n" \
    "v_mfma_f32_16x16x32_bf16 v[36:39], v[88:91], v[140:143], v[36:39]\n" \
    "ds_read_b128 v[148:151], %[hp] offset:3840\n" \
    "s_waitcnt lgkmcnt(14)\n" \
    "v_mfma_f32_16x16x32_bf16 v[32:35], v[88:91], v[136:139], v[32:35]\n" \
    "ds_read_b128 v[124:127], %[hp] offset:768\n" \
    "s_waitcnt lgkmcnt(12)\n" \
    "v_mfma_f32_16x16x32_bf16 v[28:31], v[88:91], v[132:135], v[28:31]\n" \
    "ds_read_b128 v[120:123], %[hp] offset:256\n" \
    "s_waitcnt lgkmcnt(10)\n" \
    "v_mfma_f32_16x16x32_bf16 v[24:27], v[88:91], v[128:131], v[24:27]\n" \
    "v_mfma_f32_16x16x32_bf16 v[20:23], v[88:91], v[156:159], v[20:23]\n" \
    "v_mfma_f32_16x16x32_bf16 v[16:19], v[88:91], v[152:155], v[16:19]\n" \
    "ds_read2_b32 v[88:89], v163 offset0:216 offset1:217\n" \
    "v_fmac_f32 v28, %[dd], v84\n" \
    "v_fmac_f32 v29, %[dd], v85\n" \
    "v_mfma_f32_16x16x32_bf16 v[44:47], v[92:95], v[144:147], v[44:47]\n" \
    "ds_read2_b32 v[90:91], v163 offset0:218 offset1:219\n" \
    "v_fmac_f32 v30, %[dd], v86\n" \
    "v_fmac_f32 v31, %[dd], v87\n" \
    "v_mfma_f32_16x16x32_bf16 v[40:43], v[92:95], v[140:143], v[40:43]\n" \
    "ds_read_b128 v[144:147], %[hp] offset:3328\n" \
    "v_mfma_f32_16x16x32_bf16 v[36:39], v[92:95], v[136:139], v[36:39]\n" \
    "v_mfma_f32_16x16x32_bf16 v[32:35], v[92:95], v[132:135], v[32:35]\n" \
    "v_mfma_f32_16x16x32_bf16 v[28:31], v[92:95], v[128:131], v[28:31]\n" \
    "ds_read_b64 v[86:87], %[kvr] offset:2048\n" \
    "v_mfma_f32_16x16x32_bf16 v[24:27], v[92:95], v[156:159], v[24:27]\n" \
    "v_mfma_f32_16x16x32_bf16 v[20:23], v[92:95], v[152:155], v[20:23]\n" \
    "ds_read2_b32 v[92:93], v163 offset0:72 offset1:73\n" \
    "v_mfma_f32_16x16x32_bf16 v[44:47], v[96:99], v[140:143], v[44:47]\n" \
    "ds_read_b128 v[140:143], %[hp] offset:2816\n" \
    "v_mfma_f32_16x16x32_bf16 v[40:43], v[96:99], v[136:139], v[40:43]\n" \
    "ds_read2_b32 v[94:95], v163 offset0:74 offset1:75\n" \
    "v_mfma_f32_16x16x32_bf16 v[36:39], v[96:99], v[132:135], v[36:39]\n" \
    "v_mfma_f32_16x16x32_bf16 v[32:35], v[96:99], v[128:131], v[32:35]\n" \
    "v_mfma_f32_16x16x32_bf16 v[28:31], v[96:99], v[156:159], v[28:31]\n" \
    "v_mfma_f32_16x16x32_bf16 v[24:27], v[96:99], v[152:155], v[24:27]\n" \
    "ds_read2_b32 v[96:97], v162 offset0:216 offset1:217\n" \
    "v_mfma_f32_16x16x32_bf16 v[44:47], v[100:103], v[136:139], v[44:47]\n" \
    "ds_read_b128 v[136:139], %[hp] offset:2304\n" \
    "v_mfma_f32_16x16x32_bf16 v[40:43], v[100:103], v[132:135], v[40:43]\n" \
    "ds_read2_b32 v[98:99], v162 offset0:218 offset1:219\n" \
    "v_mfma_f32_16x16x32_bf16 v[36:39], v[100:103], v[128:131], v[36:39]\n" \
    "v_mfma_f32_16x16x32_bf16 v[32:35], v[100:103], v[156:159], v[32:35]\n" \
    "s_waitcnt lgkmcnt(6)\n" \
    "v_lshlrev_b32 v84, 16, v86\n" \
    "v_mfma_f32_16x16x32_bf16 v[28:31], v[100:103], v[152:155], v[28:31]\n" \
    "ds_read2_b32 v[100:101], v162 offset0:72 offset1:73\n" \
    "v_mfma_f32_16x16x32_bf16 v[44:47], v[104:107], v[132:135], v[44:47]\n" \
    "ds_read_b128 v[132:135], %[hp] offset:1792\n" \
    "v_and_b32 v85, 0xffff0000, v86\n" \
    "v_mfma_f32_16x16x32_bf16 v[40:43], v[104:107], v[128:131], v[40:43]\n" \
    "ds_read2_b32 v[102:103], v162 offset0:74 offset1:75\n" \
    "v_mfma_f32_16x16x32_bf16 v[36:39], v[104:107], v[156:159], v[36:39]\n" \
    "v_lshlrev_b32 v86, 16, v87\n" \
    "v_mfma_f32_16x16x32_bf16 v[32:35], v[104:107], v[152:155], v[32:35]\n" \
    "ds_read2_b32 v[104:105], v161 offset0:216 offset1:217\n" \
    "v_mfma_f32_16x16x32_bf16 v[44:47], v[108:111], v[128:131], v[44:47]\n" \
    "ds_read_b128 v[128:131], %[hp] offset:1280\n" \
    "v_and_b32 v87, 0xffff0000, v87\n" \
    "v_mfma_f32_16x16x32_bf16 v[40:43], v[108:111], v[156:159], v[40:43]\n" \
    "ds_read2_b32 v[106:107], v161 offset0:218 offset1:219\n" \
    "v_mfma_f32_16x16x32_bf16 v[36:39], v[108:111], v[152:155], v[36:39]\n" \
    "ds_read2_b32 v[108:109], v161 offset0:72 offset1:73\n" \
    "v_mfma_f32_16x16x32_bf16 v[44:47], v[112:115], v[156:159], v[44:47]\n" \
    "ds_read2_b32 v[110:111], v161 offset0:74 offset1:75\n" \
    "v_mfma_f32_16x16x32_bf16 v[40:43], v[112:115], v[152:155], v[40:43]\n" \
    "ds_read2_b32 v[112:113], v160 offset0:216 offset1:217\n" \
    "v_mfma_f32_16x16x32_bf16 v[44:47], v[116:119], v[152:155], v[44:47]\n" \
    "ds_read2_b32 v[114:115], v160 offset0:218 offset1:219\n" \
    "v_add_u32 v160, 288, v160\n" \
    "v_add_u32 v161, 288, v161\n" \
    "v_add_u32 v162, 288, v162\n" \
    "v_add_u32 v163, 288, v163\n" \
    "v_mfma_f32_16x16x32_bf16 v[44:47], v[88:91], v[148:151], v[44:47]\n" \
    "ds_read2_b32 v[116:117], v160 offset0:0 offset1:1\n" \
    "v_mfma_f32_16x16x32_bf16 v[40:43], v[88:91], v[144:147], v[40:43]\n" \
    "ds_read2_b32 v[118:119], v160 offset0:2 offset1:3\n" \
    "s_waitcnt lgkmcnt(15)\n" \
    "v_mfma_f32_16x16x32_bf16 v[36:39], v[88:91], v[140:143], v[36:39]\n" \
    "ds_read_b128 v[148:151], %[hp] offset:3904\n" \
    "s_waitcnt lgkmcnt(14)\n" \
    "v_mfma_f32_16x16x32_bf16 v[32:35], v[88:91], v[136:139], v[32:35]\n" \
    "ds_read_b128 v[156:159], %[hp] offset:832\n" \
    "s_waitcnt lgkmcnt(12)\n" \
    "v_mfma_f32_16x16x32_bf16 v[28:31], v[88:91], v[132:135], v[28:31]\n" \
    "ds_read_b128 v[152:155], %[hp] offset:320\n" \
    "s_waitcnt lgkmcnt(10)\n" \
    "v_mfma_f32_16x16x32_bf16 v[24:27], v[88:91], v[128:131], v[24:27]\n" \
    "v_mfma_f32_16x16x32_bf16 v[20:23], v[88:91], v[124:127], v[20:23]\n" \
    "v_fmac_f32 v32, %[dd], v84\n" \
    "v_fmac_f32 v33, %[dd], v85\n" \
    "v_mfma_f32_16x16x32_bf16 v[16:19], v[88:91], v[120:123], v[16:19]\n" \
    "ds_read2_b32 v[88:89], v163 offset0:160 offset1:161\n" \
    "v_fmac_f32 v34, %[dd], v86\n" \
    "v_fmac_f32 v35, %[dd], v87\n" \
    "v_mfma_f32_16x16x32_bf16 v[44:47], v[92:95], v[144:147], v[44:47]\n" \
    "ds_read2_b32 v[90:91], v163 offset0:162 offset1:163\n" \
    "v_mfma_f32_16x16x32_bf16 v[40:43], v[92:95], v[140:143], v[40:43]\n" \
    "ds_read_b128 v[144:147], %[hp] offset:3392\n" \
    "v_mfma_f32_16x16x32_bf16 v[36:39], v[92:95], v[136:139], v[36:39]\n" \
    "v_mfma_f32_16x16x32_bf16 v[32:35], v[92:95], v[132:135], v[32:35]\n" \
    "v_mfma_f32_16x16x32_bf16 v[28:31], v[92:95], v[128:131], v[28:31]\n" \
    "ds_read_b64 v[86:87], %[kvr] offset:2560\n" \
    "v_mfma_f32_16x16x32_bf16 v[24:27], v[92:95], v[124:127], v[24:27]\n" \
    "v_mfma_f32_16x16x32_bf16 v[20:23], v[92:95], v[120:123], v[20:23]\n" \
    "ds_read2_b32 v[92:93], v163 offset0:16 offset1:17\n" \
    "v_mfma_f32_16x16x32_bf16 v[44:47], v[96:99], v[140:143], v[44:47]\n" \
    "ds_read_b128 v[140:143], %[hp] offset:2880\n" \
    "v_mfma_f32_16x16x32_bf16 v[40:43], v[96:99], v[136:139], v[40:43]\n" \
    "ds_read2_b32 v[94:95], v163 offset0:18 offset1:19\n" \
    "v_mfma_f32_16x16x32_bf16 v[36:39], v[96:99], v[132:135], v[36:39]\n" \
    "v_mfma_f32_16x16x32_bf16 v[32:35], v[96:99], v[128:131], v[32:35]\n" \
    "v_mfma_f32_16x16x32_bf16 v[28:31], v[96:99], v[124:127], v[28:31]\n" \
    "v_mfma_f32_16x16x32_bf16 v[24:27], v[96:99], v[120:123], v[24:27]\n" \
    "ds_read2_b32 v[96:97], v162 offset0:160 offset1:161\n" \
    "v_mfma_f32_16x16x32_bf16 v[44:47], v[100:103], v[136:139], v[44:47]\n" \
    "ds_read_b128 v[136:139], %[hp] offset:2368\n" \
    "v_mfma_f32_16x16x32_bf16 v[40:43], v[100:103], v[132:135], v[40:43]\n" \
    "ds_read2_b32 v[98:99], v162 offset0:162 offset1:163\n" \
    "v_mfma_f32_16x16x32_bf16 v[36:39], v[100:103], v[128:131], v[36:39]\n" \
    "v_mfma_f32_16x16x32_bf16 v[32:35], v[100:103], v[124:127], v[32:35]\n" \
    "s_waitcnt lgkmcnt(6)\n" \
    "v_lshlrev_b32 v84, 16, v86\n" \
    "v_mfma_f32_16x16x32_bf16 v[28:31], v[100:103], v[120:123], v[28:31]\n" \
    "ds_read2_b32 v[100:101], v162 offset0:16 offset1:17\n" \
    "v_mfma_f32_16x16x32_bf16 v[44:47], v[104:107], v[132:135], v[44:47]\n" \
    "ds_read_b128 v[132:135], %[hp] offset:1856\n" \
    "v_and_b32 v85, 0xffff0000, v86\n" \
    "v_mfma_f32_16x16x32_bf16 v[40:43], v[104:107], v[128:131], v[40:43]\n" \
    "ds_read2_b32 v[102:103], v162 offset0:18 offset1:19\n" \
    "v_mfma_f32_16x16x32_bf16 v[36:39], v[104:107], v[124:127], v[36:39]\n" \
    "v_lshlrev_b32 v86, 16, v87\n" \
    "v_mfma_f32_16x16x32_bf16 v[32:35], v[104:107], v[120:123], v[32:35]\n" \
    "ds_read2_b32 v[104:105], v161 offset0:160 offset1:161\n" \
    "v_mfma_f32_16x16x32_bf16 v[44:47], v[108:111], v[128:131], v[44:47]\n" \
    "ds_read_b128 v[128:131], %[hp] offset:1344\n" \
    "v_and_b32 v87, 0xffff0000, v87\n" \
    "v_mfma_f32_16x16x32_bf16 v[40:43], v[108:111], v[124:127], v[40:43]\n" \
    "ds_read2_b32 v[106:107], v161 offset0:162 offset1:163\n" \
    "v_mfma_f32_16x16x32_bf16 v[36:39], v[108:111], v[120:123], v[36:39]\n" \
    "ds_read2_b32 v[108:109], v161 offset0:16 offset1:17\n" \
    "v_mfma_f32_16x16x32_bf16 v[44:47], v[112:115], v[124:127], v[44:47]\n" \
    "ds_read2_b32 v[110:111], v161 offset0:18 offset1:19\n" \
    "v_mfma_f32_16x16x32_bf16 v[40:43], v[112:115], v[120:123], v[40:43]\n" \
    "ds_read2_b32 v[112:113], v160 offset0:160 offset1:161\n" \
    "v_mfma_f32_16x16x32_bf16 v[44:47], v[116:119], v[120:123], v[44:47]\n" \
    "ds_read2_b32 v[114:115], v160 offset0:162 offset1:163\n" \
    "v_mfma_f32_16x16x32_bf16 v[44:47], v[88:91], v[148:151], v[44:47]\n" \
    "ds_read2_b32 v[116:117], v160 offset0:16 offset1:17\n" \
    "v_mfma_f32_16x16x32_bf16 v[40:43], v[88:91], v[144:147], v[40:43]\n" \
    "ds_read2_b32 v[118:119], v160 offset0:18 offset1:19\n" \
    "s_waitcnt lgkmcnt(15)\n" \
    "v_mfma_f32_16x16x32_bf16 v[36:39], v[88:91], v[140:143], v[36:39]\n" \
    "ds_read_b128 v[148:151], %[hp] offset:3968\n" \
    "s_waitcnt lgkmcnt(14)\n" \
    "v_mfma_f32_16x16x32_bf16 v[32:35], v[88:91], v[136:139], v[32:35]\n" \
    "ds_read_b128 v[124:127], %[hp] offset:896\n" \
    "s_waitcnt lgkmcnt(12)\n" \
    "v_mfma_f32_16x16x32_bf16 v[28:31], v[88:91], v[132:135], v[28:31]\n" \
    "ds_read_b128 v[120:123], %[hp] offset:384\n" \
    "s_waitcnt lgkmcnt(10)\n" \
    "v_mfma_f32_16x16x32_bf16 v[24:27], v[88:91], v[128:131], v[24:27]\n" \
    "v_fmac_f32 v36, %[dd], v84\n" \
    "v_fmac_f32 v37, %[dd], v85\n" \
    "v_mfma_f32_16x16x32_bf16 v[20:23], v[88:91], v[156:159], v[20:23]\n" \
    "v_fmac_f32 v38, %[dd], v86\n" \
    "v_fmac_f32 v39, %[dd], v87\n" \
    "v_mfma_f32_16x16x32_bf16 v[16:19], v[88:91], v[152:155], v[16:19]\n" \
    "ds_read2_b32 v[88:89], v163 offset0:176 offset1:177\n" \
    "v_mfma_f32_16x16x32_bf16 v[44:47], v[92:95], v[144:147], v[44:47]\n" \
    "ds_read2_b32 v[90:91], v163 offset0:178 offset1:179\n" \
    "v_mfma_f32_16x16x32_bf16 v[40:43], v[92:95], v[140:143], v[40:43]\n" \
    "ds_read_b128 v[144:147], %[hp] offset:3456\n" \
    "v_mfma_f32_16x16x32_bf16 v[36:39], v[92:95], v[136:139], v[36:39]\n" \
    "v_mfma_f32_16x16x32_bf16 v[32:35], v[92:95], v[132:135], v[32:35]\n" \
    "v_mfma_f32_16x16x32_bf16 v[28:31], v[92:95], v[128:131], v[28:31]\n" \
    "ds_read_b64 v[86:87], %[kvr] offset:3072\n" \
    "v_mfma_f32_16x16x32_bf16 v[24:27], v[92:95], v[156:159], v[24:27]\n" \
    "v_mfma_f32_16x16x32_bf16 v[20:23], v[92:95], v[152:155], v[20:23]\n" \
    "ds_read2_b32 v[92:93], v163 offset0:32 offset1:33\n" \
    "v_mfma_f32_16x16x32_bf16 v[44:47], v[96:99], v[140:143], v[44:47]\n" \
    "ds_read_b128 v[140:143], %[hp] offset:2944\n" \
    "v_mfma_f32_16x16x32_bf16 v[40:43], v[96:99], v[136:139], v[40:43]\n" \
    "ds_read2_b32 v[94:95], v163 offset0:34 offset1:35\n" \
    "v_mfma_f32_16x16x32_bf16 v[36:39], v[96:99], v[132:135], v[36:39]\n" \
    "v_mfma_f32_16x16x32_bf16 v[32:35], v[96:99], v[128:131], v[32:35]\n" \
    "v_mfma_f32_16x16x32_bf16 v[28:31], v[96:99], v[156:159], v[28:31]\n" \
    "v_mfma_f32_16x16x32_bf16 v[24:27], v[96:99], v[152:155], v[24:27]\n" \
    "ds_read2_b32 v[96:97], v162 offset0:176 offset1:177\n" \
    "v_mfma_f32_16x16x32_bf16 v[44:47], v[100:103], v[136:139], v[44:47]\n" \
    "ds_read_b128 v[136:139], %[hp] offset:2432\n" \
    "v_mfma_f32_16x16x32_bf16 v[40:43], v[100:103], v[132:135], v[40:43]\n" \
    "ds_read2_b32 v[98:99], v162 offset0:178 offset1:179\n" \
    "v_mfma_f32_16x16x32_bf16 v[36:39], v[100:103], v[128:131], v[36:39]\n" \
    "v_mfma_f32_16x16x32_bf16 v[32:35], v[100:103], v[156:159], v[32:35]\n" \
    "s_waitcnt lgkmcnt(6)\n" \
    "v_lshlrev_b32 v84, 16, v86\n" \
    "v_mfma_f32_16x16x32_bf16 v[28:31], v[100:103], v[152:155], v[28:31]\n" \
    "ds_read2_b32 v[100:101], v162 offset0:32 offset1:33\n" \
    "v_mfma_f32_16x16x32_bf16 v[44:47], v[104:107], v[132:135], v[44:47]\n" \
    "ds_read_b128 v[132:135], %[hp] offset:1920\n" \
    "v_and_b32 v85, 0xffff0000, v86\n" \
    "v_mfma_f32_16x16x32_bf16 v[40:43], v[104:107], v[128:131], v[40:43]\n" \
    "ds_read2_b32 v[102:103], v162 offset0:34 offset1:35\n" \
    "v_mfma_f32_16x16x32_bf16 v[36:39], v[104:107], v[156:159], v[36:39]\n" \
    "v_lshlrev_b32 v86, 16, v87\n" \
    "v_mfma_f32_16x16x32_bf16 v[32:35], v[104:107], v[152:155], v[32:35]\n" \
    "ds_read2_b32 v[104:105], v161 offset0:176 offset1:177\n" \
    "v_mfma_f32_16x16x32_bf16 v[44:47], v[108:111], v[128:131], v[44:47]\n" \
    "ds_read_b128 v[128:131], %[hp] offset:1408\n" \
    "v_and_b32 v87, 0xffff0000, v87\n" \
    "v_mfma_f32_16x16x32_bf16 v[40:43], v[108:111], v[156:159], v[40:43]\n" \
    "ds_read2_b32 v[106:107], v161 offset0:178 offset1:179\n" \
    "v_mfma_f32_16x16x32_bf16 v[36:39], v[108:111], v[152:155], v[36:39]\n" \
    "ds_read2_b32 v[108:109], v161 offset0:32 offset1:33\n" \
    "v_mfma_f32_16x16x32_bf16 v[44:47], v[112:115], v[156:159], v[44:47]\n" \
    "ds_read2_b32 v[110:111], v161 offset0:34 offset1:35\n" \
    "v_mfma_f32_16x16x32_bf16 v[40:43], v[112:115], v[152:155], v[40:43]\n" \
    "ds_read2_b32 v[112:113], v160 offset0:176 offset1:177\n" \
    "v_mfma_f32_16x16x32_bf16 v[44:47], v[116:119], v[152:155], v[44:47]\n" \
    "ds_read2_b32 v[114:115], v160 offset0:178 offset1:179\n" \
    "v_mfma_f32_16x16x32_bf16 v[44:47], v[88:91], v[148:151], v[44:47]\n" \
    "ds_read2_b32 v[116:117], v160 offset0:32 offset1:33\n" \
    "v_mfma_f32_16x16x32_bf16 v[40:43], v[88:91], v[144:147], v[40:43]\n" \
    "ds_read2_b32 v[118:119], v160 offset0:34 offset1:35\n" \
    "s_waitcnt lgkmcnt(15)\n" \
    "v_mfma_f32_16x16x32_bf16 v[36:39], v[88:91], v[140:143], v[36:39]\n" \
    "ds_read_b128 v[152:155], %[hp] offset:448\n" \
    "s_waitcnt lgkmcnt(14)\n" \
    "v_mfma_f32_16x16x32_bf16 v[32:35], v[88:91], v[136:139], v[32:35]\n" \
    "ds_read_b128 v[156:159], %[hp] offset:960\n" \
    "s_waitcnt lgkmcnt(12)\n" \
    "v_mfma_f32_16x16x32_bf16 v[28:31], v[88:91], v[132:135], v[28:31]\n" \
    "ds_read_b128 v[148:151], %[hp] offset:4032\n" \
    "v_fmac_f32 v40, %[dd], v84\n" \
    "v_fmac_f32 v41, %[dd], v85\n" \
    "s_waitcnt lgkmcnt(10)\n" \
    "v_mfma_f32_16x16x32_bf16 v[24:27], v[88:91], v[128:131], v[24:27]\n" \
    "v_fmac_f32 v42, %[dd], v86\n" \
    "v_fmac_f32 v43, %[dd], v87\n" \
    "v_mfma_f32_16x16x32_bf16 v[20:23], v[88:91], v[124:127], v[20:23]\n" \
    "v_mfma_f32_16x16x32_bf16 v[16:19], v[88:91], v[120:123], v[16:19]\n" \
    "ds_read2_b32 v[88:89], v163 offset0:192 offset1:193\n" \
    "v_mfma_f32_16x16x32_bf16 v[44:47], v[92:95], v[144:147], v[44:47]\n" \
    "ds_read2_b32 v[90:91], v163 offset0:194 offset1:195\n" \
    "v_mfma_f32_16x16x32_bf16 v[40:43], v[92:95], v[140:143], v[40:43]\n" \
    "ds_read_b128 v[144:147], %[hp] offset:3520\n" \
    "v_mfma_f32_16x16x32_bf16 v[36:39], v[92:95], v[136:139], v[36:39]\n" \
    "v_mfma_f32_16x16x32_bf16 v[32:35], v[92:95], v[132:135], v[32:35]\n" \
    "v_mfma_f32_16x16x32_bf16 v[28:31], v[92:95], v[128:131], v[28:31]\n" \
    "ds_read_b64 v[86:87], %[kvr] offset:3584\n" \
    "v_mfma_f32_16x16x32_bf16 v[24:27], v[92:95], v[124:127], v[24:27]\n" \
    "v_mfma_f32_16x16x32_bf16 v[20:23], v[92:95], v[120:123], v[20:23]\n" \
    "ds_read2_b32 v[92:93], v163 offset0:48 offset1:49\n" \
    "v_mfma_f32_16x16x32_bf16 v[44:47], v[96:99], v[140:143], v[44:47]\n" \
    "ds_read2_b32 v[94:95], v163 offset0:50 offset1:51\n" \
    "v_mfma_f32_16x16x32_bf16 v[40:43], v[96:99], v[136:139], v[40:43]\n" \
    "ds_read_b128 v[140:143], %[hp] offset:3008\n" \
    "v_mfma_f32_16x16x32_bf16 v[36:39], v[96:99], v[132:135], v[36:39]\n" \
    "v_mfma_f32_16x16x32_bf16 v[32:35], v[96:99], v[128:131], v[32:35]\n" \
    "v_mfma_f32_16x16x32_bf16 v[28:31], v[96:99], v[124:127], v[28:31]\n" \
    "v_mfma_f32_16x16x32_bf16 v[24:27], v[96:99], v[120:123], v[24:27]\n" \
    "ds_read2_b32 v[96:97], v162 offset0:192 offset1:193\n" \
    "v_mfma_f32_16x16x32_bf16 v[44:47], v[100:103], v[136:139], v[44:47]\n" \
    "ds_read2_b32 v[98:99], v162 offset0:194 offset1:195\n" \
    "v_mfma_f32_16x16x32_bf16 v[40:43], v[100:103], v[132:135], v[40:43]\n" \
    "ds_read_b128 v[136:139], %[hp] offset:2496\n" \
    "v_mfma_f32_16x16x32_bf16 v[36:39], v[100:103], v[128:131], v[36:39]\n" \
    "v_mfma_f32_16x16x32_bf16 v[32:35], v[100:103], v[124:127], v[32:35]\n" \
    "s_waitcnt lgkmcnt(6)\n" \
    "v_lshlrev_b32 v84, 16, v86\n" \
    "v_mfma_f32_16x16x32_bf16 v[28:31], v[100:103], v[120:123], v[28:31]\n" \
    "ds_read2_b32 v[100:101], v162 offset0:48 offset1:49\n" \
    "v_mfma_f32_16x16x32_bf16 v[44:47], v[104:107], v[132:135], v[44:47]\n" \
    "ds_read_b128 v[132:135], %[hp] offset:1984\n" \
    "v_and_b32 v85, 0xffff0000, v86\n" \
    "v_mfma_f32_16x16x32_bf16 v[40:43], v[104:107], v[128:131], v[40:43]\n" \
    "ds_read2_b32 v[102:103], v162 offset0:50 offset1:51\n" \
    "v_mfma_f32_16x16x32_bf16 v[36:39], v[104:107], v[124:127], v[36:39]\n" \
    "v_lshlrev_b32 v86, 16, v87\n" \
    "v_mfma_f32_16x16x32_bf16 v[32:35], v[104:107], v[120:123], v[32:35]\n" \
    "ds_read2_b32 v[104:105], v161 offset0:192 offset1:193\n" \
    "v_mfma_f32_16x16x32_bf16 v[44:47], v[108:111], v[128:131], v[44:47]\n" \
    "ds_read_b128 v[128:131], %[hp] offset:1472\n" \
    "v_and_b32 v87, 0xffff0000, v87\n" \
    "v_mfma_f32_16x16x32_bf16 v[40:43], v[108:111], v[124:127], v[40:43]\n" \
    "ds_read2_b32 v[106:107], v161 offset0:194 offset1:195\n" \
    "v_mfma_f32_16x16x32_bf16 v[36:39], v[108:111], v[120:123], v[36:39]\n" \
    "ds_read2_b32 v[108:109], v161 offset0:48 offset1:49\n" \
    "v_mfma_f32_16x16x32_bf16 v[44:47], v[112:115], v[124:127], v[44:47]\n" \
    "ds_read2_b32 v[110:111], v161 offset0:50 offset1:51\n" \
    "v_mfma_f32_16x16x32_bf16 v[40:43], v[112:115], v[120:123], v[40:43]\n" \
    "ds_read2_b32 v[112:113], v160 offset0:192 offset1:193\n" \
    "v_mfma_f32_16x16x32_bf16 v[44:47], v[116:119], v[120:123], v[44:47]\n" \
    "ds_read2_b32 v[114:115], v160 offset0:194 offset1:195\n" \
    "v_mfma_f32_16x16x32_bf16 v[16:19], v[88:91], v[152:155], v[16:19]\n" \
    "ds_read2_b32 v[116:117], v160 offset0:48 offset1:49\n" \
    "ds_read_b32 v50, %[ctl] offset:52\n" \
    "v_mfma_f32_16x16x32_bf16 v[20:23], v[88:91], v[156:159], v[20:23]\n" \
    "ds_read2_b32 v[118:119], v160 offset0:50 offset1:51\n" \
    "s_waitcnt lgkmcnt(15)\n" \
    "v_mfma_f32_16x16x32_bf16 v[20:23], v[92:95], v[152:155], v[20:23]\n" \
    "s_waitcnt lgkmcnt(8)\n" \
    "v_mfma_f32_16x16x32_bf16 v[24:27], v[88:91], v[128:131], v[24:27]\n" \
    "v_fmac_f32 v44, %[dd], v84\n" \
    "v_fmac_f32 v45, %[dd], v85\n" \
    "v_mfma_f32_16x16x32_bf16 v[24:27], v[92:95], v[156:159], v[24:27]\n" \
    "v_fmac_f32 v46, %[dd], v86\n" \
    "v_fmac_f32 v47, %[dd], v87\n" \
    "v_mfma_f32_16x16x32_bf16 v[24:27], v[96:99], v[152:155], v[24:27]\n" \
    "v_mfma_f32_16x16x32_bf16 v[28:31], v[88:91], v[132:135], v[28:31]\n" \
    "v_cvt_pk_bf16_f32 v52, v16, v17\n" \
    "v_cvt_pk_bf16_f32 v54, v20, v21\n" \
    "v_mfma_f32_16x16x32_bf16 v[28:31], v[92:95], v[128:131], v[28:31]\n" \
    "v_cvt_pk_bf16_f32 v53, v18, v19\n" \
    "v_cvt_pk_bf16_f32 v55, v22, v23\n" \
    "v_mfma_f32_16x16x32_bf16 v[28:31], v[96:99], v[156:159], v[28:31]\n" \
    "v_add_u32 v68, 0, %[yo]\n" \
    "v_mfma_f32_16x16x32_bf16 v[28:31], v[100:103], v[152:155], v[28:31]\n" \
    "s_waitcnt lgkmcnt(1)\n" \
    "v_readfirstlane_b32 %[tmp], v50\n" \
    "s_mov_b32 %[cnt], 0\n" \
    "s_cmp_lg_u32 %[tmp], 0\n" \
    "s_cbranch_scc1 Lok_%=\n" \
    "Ls2_%=:\n" \
    "ds_read_b32 v50, %[ctl] offset:52\n" \
    "s_add_u32 %[cnt], %[cnt], 1\n" \
    "s_waitcnt lgkmcnt(0)\n" \
    "v_readfirstlane_b32 %[tmp], v50\n" \
    "s_cmp_ge_u32 %[cnt], %[cap]\n" \
    "s_cbranch_scc1 Lok_%=\n" \
    "s_cmp_eq_u32 %[tmp], 0\n" \
    "s_cbranch_scc1 Ls2_%=\n" \
    "Lok_%=:\n" \
    "v_permlane16_swap_b32 v52, v54\n" \
    "v_mfma_f32_16x16x32_bf16 v[32:35], v[88:91], v[136:139], v[32:35]\n" \
    "v_permlane16_swap_b32 v53, v55\n" \
    "v_mfma_f32_16x16x32_bf16 v[32:35], v[92:95], v[132:135], v[32:35]\n" \
    "global_store_dwordx4 v68, v[52:55], %[wsp] sc1\n" \
    "v_mfma_f32_16x16x32_bf16 v[32:35], v[96:99], v[128:131], v[32:35]\n" \
    "v_mfma_f32_16x16x32_bf16 v[32:35], v[100:103], v[156:159], v[32:35]\n" \
    "v_cvt_pk_bf16_f32 v56, v24, v25\n" \
    "v_cvt_pk_bf16_f32 v58, v28, v29\n" \
    "v_mfma_f32_16x16x32_bf16 v[32:35], v[104:107], v[152:155], v[32:35]\n" \
    "v_cvt_pk_bf16_f32 v57, v26, v27\n" \
    "v_cvt_pk_bf16_f32 v59, v30, v31\n" \
    "v_mfma_f32_16x16x32_bf16 v[36:39], v[88:91], v[140:143], v[36:39]\n" \
    "v_add_u32 v69, 655360, %[yo]\n" \
    "v_mfma_f32_16x16x32_bf16 v[36:39], v[92:95], v[136:139], v[36:39]\n" \
    "v_permlane16_swap_b32 v56, v58\n" \
    "v_mfma_f32_16x16x32_bf16 v[36:39], v[96:99], v[132:135], v[36:39]\n" \
    "v_permlane16_swap_b32 v57, v59\n" \
    "v_mfma_f32_16x16x32_bf16 v[36:39], v[100:103], v[128:131], v[36:39]\n" \
    "global_store_dwordx4 v69, v[56:59], %[wsp] sc1\n" \
    "v_mfma_f32_16x16x32_bf16 v[36:39], v[104:107], v[156:159], v[36:39]\n" \
    "v_mfma_f32_16x16x32_bf16 v[36:39], v[108:111], v[152:155], v[36:39]\n" \
    "v_mfma_f32_16x16x32_bf16 v[40:43], v[88:91], v[144:147], v[40:43]\n" \
    "v_mfma_f32_16x16x32_bf16 v[40:43], v[92:95], v[140:143], v[40:43]\n" \
    "v_mfma_f32_16x16x32_bf16 v[40:43], v[96:99], v[136:139], v[40:43]\n" \
    "v_mfma_f32_16x16x32_bf16 v[40:43], v[100:103], v[132:135], v[40:43]\n" \
    "v_cvt_pk_bf16_f32 v60, v32, v33\n" \
    "v_cvt_pk_bf16_f32 v62, v36, v37\n" \
    "v_mfma_f32_16x16x32_bf16 v[40:43], v[104:107], v[128:131], v[40:43]\n" \
    "v_cvt_pk_bf16_f32 v61, v34, v35\n" \
    "v_cvt_pk_bf16_f32 v63, v38, v39\n" \
    "v_mfma_f32_16x16x32_bf16 v[40:43], v[108:111], v[156:159], v[40:43]\n" \
    "v_add_u32 v70, 1310720, %[yo]\n" \
    "v_mfma_f32_16x16x32_bf16 v[40:43], v[112:115], v[152:155], v[40:43]\n" \
    "v_permlane16_swap_b32 v60, v62\n" \
    "v_mfma_f32_16x16x32_bf16 v[44:47], v[88:91], v[148:151], v[44:47]\n" \
    "v_permlane16_swap_b32 v61, v63\n" \
    "v_mfma_f32_16x16x32_bf16 v[44:47], v[92:95], v[144:147], v[44:47]\n" \
    "global_store_dwordx4 v70, v[60:63], %[wsp] sc1\n" \
    "v_mfma_f32_16x16x32_bf16 v[44:47], v[96:99], v[140:143], v[44:47]\n" \
    "v_mfma_f32_16x16x32_bf16 v[44:47], v[100:103], v[136:139], v[44:47]\n" \
    "v_mfma_f32_16x16x32_bf16 v[44:47], v[104:107], v[132:135], v[44:47]\n" \
    "v_mfma_f32_16x16x32_bf16 v[44:47], v[108:111], v[128:131], v[44:47]\n" \
    "v_mfma_f32_16x16x32_bf16 v[44:47], v[112:115], v[156:159], v[44:47]\n" \
    "s_waitcnt lgkmcnt(0)\n" \
    "v_mfma_f32_16x16x32_bf16 v[44:47], v[116:119], v[152:155], v[44:47]\n" \
    "s_nop 7\n" \
    "v_cvt_pk_bf16_f32 v64, v40, v41\n" \
    "v_cvt_pk_bf16_f32 v66, v44, v45\n" \
    "v_cvt_pk_bf16_f32 v65, v42, v43\n" \
    "v_cvt_pk_bf16_f32 v67, v46, v47\n" \
    "v_add_u32 v71, 1966080, %[yo]\n" \
    "s_nop 0\n" \
    "v_permlane16_swap_b32 v64, v66\n" \
    "v_permlane16_swap_b32 v65, v67\n" \
    "global_store_dwordx4 v71, v[64:67], %[wsp] sc1\n" \
    "s_nop 1\n"
#define CONV_CLOB "v16", "v17", "v18", "v19", "v20", "v21", "v22", "v23", "v24", "v25", "v26", "v27", "v28", "v29", "v30", "v31", "v32", "v33", "v34", "v35", "v36", "v37", "v38", "v39", "v40", "v41", "v42", "v43", "v44", "v45", "v46", "v47", "v50", "v51", "v52", "v53", "v54", "v55", "v56", "v57", "v58", "v59", "v60", "v61", "v62", "v63", "v64", "v65", "v66", "v67", "v68", "v69", "v70", "v71", "v72", "v73", "v74", "v75", "v76", "v77", "v78", "v79", "v80", "v81", "v82", "v83", "v84", "v85", "v86", "v87", "v88", "v89", "v90", "v91", "v92", "v93", "v94", "v95", "v96", "v97", "v98", "v99", "v100", "v101", "v102", "v103", "v104", "v105", "v106", "v107", "v108", "v109", "v110", "v111", "v112", "v113", "v114", "v115", "v116", "v117", "v118", "v119", "v120", "v121", "v122", "v123", "v124", "v125", "v126", "v127", "v128", "v129", "v130", "v131", "v132", "v133", "v134", "v135", "v136", "v137", "v138", "v139", "v140", "v141", "v142", "v143", "v144", "v145", "v146", "v147", "v148", "v149", "v150", "v151", "v152", "v153", "v154", "v155", "v156", "v157", "v158", "v159", "v160", "v161", "v162", "v163", "scc", "memory"
#define G_ASM \
    "v_add_u32 v154, 67584, %[wp0]\n" \
    "v_add_u32 v155, 67584, %[wp1]\n" \
    "v_add_u32 v156, 67584, %[wp2]\n" \
    "v_add_u32 v157, 67584, %[wp3]\n" \
    "s_mov_b32 s60, 0x11111111\n" \
    "s_mov_b32 s61, 0x11111111\n" \
    "s_mov_b32 s62, 0x22222222\n" \
    "s_mov_b32 s63, 0x22222222\n" \
    "s_mov_b32 s64, 0x44444444\n" \
    "s_mov_b32 s65, 0x44444444\n" \
    "s_mov_b32 s66, 0x88888888\n" \
    "s_mov_b32 s67, 0x88888888\n" \
    "s_mov_b32 s68, -1\n" \
    "s_mov_b32 s69, 0\n" \
    "s_cmp_eq_u32 %[half], 0\n" \
    "s_waitcnt lgkmcnt(0)\n" \
    "s_cbranch_scc0 Llate_%=\n" \
    "s_barrier\n" \
    "ds_read_b64_tr_b16 v[126:127], %[wp1] offset:0\n" \
    "ds_read_b64_tr_b16 v[128:129], %[wp1] offset:2048\n" \
    "ds_read_b128 v[98:101], %[ap0] offset:53248\n" \
    "ds_read_b64_tr_b16 v[130:131], %[wp2] offset:0\n" \
    "ds_read_b64_tr_b16 v[132:133], %[wp2] offset:2048\n" \
    "ds_read_b64_tr_b16 v[134:135], %[wp3] offset:0\n" \
    "ds_read_b64_tr_b16 v[136:137], %[wp3] offset:2048\n" \
    "ds_read_b128 v[102:105], %[ap0] offset:55296\n" \
    "ds_read_b64_tr_b16 v[122:123], %[wp0] offset:0\n" \
    "ds_read_b64_tr_b16 v[124:125], %[wp0] offset:2048\n" \
    "ds_read_b128 v[106:109], %[ap0] offset:57344\n" \
    "ds_read_b128 v[110:113], %[ap0] offset:59392\n" \
    "ds_read_b128 v[114:117], %[ap0] offset:61440\n" \
    "s_waitcnt lgkmcnt(10)\n" \
    "v_mfma_f32_16x16x32_bf16 v[38:41], v[126:129], v[98:101], 0\n" \
    "ds_read_b64_tr_b16 v[142:143], %[wp1] offset:34816\n" \
    "s_waitcnt lgkmcnt(9)\n" \
    "v_mfma_f32_16x16x32_bf16 v[58:61], v[98:101], v[130:133], 0\n" \
    "ds_read_b64_tr_b16 v[144:145], %[wp1] offset:36864\n" \
    "s_waitcnt lgkmcnt(8)\n" \
    "v_mfma_f32_16x16x32_bf16 v[78:81], v[134:137], v[98:101], 0\n" \
    "ds_read_b128 v[98:101], %[ap1] offset:53248\n" \
    "s_waitcnt lgkmcnt(8)\n" \
    "v_mfma_f32_16x16x32_bf16 v[42:45], v[126:129], v[102:105], 0\n" \
    "ds_read_b64_tr_b16 v[146:147], %[wp2] offset:34816\n" \
    "v_mfma_f32_16x16x32_bf16 v[62:65], v[102:105], v[130:133], 0\n" \
    "ds_read_b64_tr_b16 v[148:149], %[wp2] offset:36864\n" \
    "v_mfma_f32_16x16x32_bf16 v[82:85], v[134:137], v[102:105], 0\n" \
    "ds_read_b64_tr_b16 v[150:151], %[wp3] offset:34816\n" \
    "s_waitcnt lgkmcnt(9)\n" \
    "v_mfma_f32_16x16x32_bf16 v[22:25], v[122:125], v[102:105], 0\n" \
    "ds_read_b128 v[102:105], %[ap1] offset:55296\n" \
    "s_waitcnt lgkmcnt(9)\n" \
    "v_mfma_f32_16x16x32_bf16 v[46:49], v[126:129], v[106:109], 0\n" \
    "ds_read_b64_tr_b16 v[152:153], %[wp3] offset:36864\n" \
    "v_mfma_f32_16x16x32_bf16 v[66:69], v[106:109], v[130:133], 0\n" \
    "ds_read_b64_tr_b16 v[138:139], %[wp0] offset:34816\n" \
    "v_mfma_f32_16x16x32_bf16 v[86:89], v[134:137], v[106:109], 0\n" \
    "ds_read_b64_tr_b16 v[140:141], %[wp0] offset:36864\n" \
    "v_mfma_f32_16x16x32_bf16 v[26:29], v[122:125], v[106:109], 0\n" \
    "ds_read_b128 v[106:109], %[ap1] offset:57344\n" \
    "s_waitcnt lgkmcnt(12)\n" \
    "v_mfma_f32_16x16x32_bf16 v[50:53], v[126:129], v[110:113], 0\n" \
    "ds_read_b128 v[118:121], %[ap1] offset:61440\n" \
    "v_mfma_f32_16x16x32_bf16 v[70:73], v[110:113], v[130:133], 0\n" \
    "v_mfma_f32_16x16x32_bf16 v[90:93], v[134:137], v[110:113], 0\n" \
    "v_mfma_f32_16x16x32_bf16 v[30:33], v[122:125], v[110:113], 0\n" \
    "ds_read_b128 v[110:113], %[ap1] offset:59392\n" \
    "s_waitcnt lgkmcnt(13)\n" \
    "v_mfma_f32_16x16x32_bf16 v[54:57], v[126:129], v[114:117], 0\n" \
    "s_barrier\n" \
    "v_mfma_f32_16x16x32_bf16 v[74:77], v[114:117], v[130:133], 0\n" \
    "v_mfma_f32_16x16x32_bf16 v[94:97], v[134:137], v[114:117], 0\n" \
    "v_mfma_f32_16x16x32_bf16 v[34:37], v[122:125], v[114:117], 0\n" \
    "s_waitcnt lgkmcnt(10)\n" \
    "v_mfma_f32_16x16x32_bf16 v[38:41], v[142:145], v[98:101], v[38:41]\n" \
    "ds_read_b64_tr_b16 v[126:127], %[wp1] offset:51200\n" \
    "s_waitcnt lgkmcnt(9)\n" \
    "v_mfma_f32_16x16x32_bf16 v[58:61], v[98:101], v[146:149], v[58:61]\n" \
    "ds_read_b64_tr_b16 v[128:129], %[wp1] offset:53248\n" \
    "s_waitcnt lgkmcnt(7)\n" \
    "v_mfma_f32_16x16x32_bf16 v[78:81], v[150:153], v[98:101], v[78:81]\n" \
    "ds_read_b128 v[98:101], %[ap0] offset:0\n" \
    "v_mfma_f32_16x16x32_bf16 v[42:45], v[142:145], v[102:105], v[42:45]\n" \
    "ds_read_b64_tr_b16 v[130:131], %[wp2] offset:51200\n" \
    "v_mfma_f32_16x16x32_bf16 v[62:65], v[102:105], v[146:149], v[62:65]\n" \
    "ds_read_b64_tr_b16 v[132:133], %[wp2] offset:53248\n" \
    "v_mfma_f32_16x16x32_bf16 v[82:85], v[150:153], v[102:105], v[82:85]\n" \
    "ds_read_b64_tr_b16 v[134:135], %[wp3] offset:51200\n" \
    "s_waitcnt lgkmcnt(9)\n" \
    "v_mfma_f32_16x16x32_bf16 v[22:25], v[138:141], v[102:105], v[22:25]\n" \
    "ds_read_b128 v[102:105], %[ap0] offset:2048\n" \
    "s_waitcnt lgkmcnt(9)\n" \
    "v_mfma_f32_16x16x32_bf16 v[46:49], v[142:145], v[106:109], v[46:49]\n" \
    "ds_read_b64_tr_b16 v[136:137], %[wp3] offset:53248\n" \
    "v_mfma_f32_16x16x32_bf16 v[66:69], v[106:109], v[146:149], v[66:69]\n" \
    "ds_read_b64_tr_b16 v[122:123], %[wp0] offset:51200\n" \
    "v_mfma_f32_16x16x32_bf16 v[86:89], v[150:153], v[106:109], v[86:89]\n" \
    "ds_read_b64_tr_b16 v[124:125], %[wp0] offset:53248\n" \
    "v_mfma_f32_16x16x32_bf16 v[26:29], v[138:141], v[106:109], v[26:29]\n" \
    "ds_read_b128 v[106:109], %[ap0] offset:4096\n" \
    "s_waitcnt lgkmcnt(11)\n" \
    "v_mfma_f32_16x16x32_bf16 v[50:53], v[142:145], v[110:113], v[50:53]\n" \
    "ds_read_b128 v[114:117], %[ap0] offset:8192\n" \
    "v_mfma_f32_16x16x32_bf16 v[70:73], v[110:113], v[146:149], v[70:73]\n" \
    "v_mfma_f32_16x16x32_bf16 v[90:93], v[150:153], v[110:113], v[90:93]\n" \
    "v_mfma_f32_16x16x32_bf16 v[30:33], v[138:141], v[110:113], v[30:33]\n" \
    "ds_read_b128 v[110:113], %[ap0] offset:6144\n" \
    "v_mfma_f32_16x16x32_bf16 v[54:57], v[142:145], v[118:121], v[54:57]\n" \
    "s_barrier\n" \
    "v_mfma_f32_16x16x32_bf16 v[74:77], v[118:121], v[146:149], v[74:77]\n" \
    "v_mfma_f32_16x16x32_bf16 v[94:97], v[150:153], v[118:121], v[94:97]\n" \
    "v_mfma_f32_16x16x32_bf16 v[34:37], v[138:141], v[118:121], v[34:37]\n" \
    "s_waitcnt lgkmcnt(10)\n" \
    "v_mfma_f32_16x16x32_bf16 v[38:41], v[126:129], v[98:101], v[38:41]\n" \
    "ds_read_b64_tr_b16 v[142:143], v155 offset:0\n" \
    "s_waitcnt lgkmcnt(9)\n" \
    "v_mfma_f32_16x16x32_bf16 v[58:61], v[98:101], v[130:133], v[58:61]\n" \
    "ds_read_b64_tr_b16 v[144:145], v155 offset:2048\n" \
    "s_waitcnt lgkmcnt(7)\n" \
    "v_mfma_f32_16x16x32_bf16 v[78:81], v[134:137], v[98:101], v[78:81]\n" \
    "ds_read_b128 v[98:101], %[ap1] offset:0\n" \
    "v_mfma_f32_16x16x32_bf16 v[42:45], v[126:129], v[102:105], v[42:45]\n" \
    "ds_read_b64_tr_b16 v[146:147], v156 offset:0\n" \
    "v_mfma_f32_16x16x32_bf16 v[62:65], v[102:105], v[130:133], v[62:65]\n" \
    "ds_read_b64_tr_b16 v[148:149], v156 offset:2048\n" \
    "v_mfma_f32_16x16x32_bf16 v[82:85], v[134:137], v[102:105], v[82:85]\n" \
    "ds_read_b64_tr_b16 v[150:151], v157 offset:0\n" \
    "s_waitcnt lgkmcnt(9)\n" \
    "v_mfma_f32_16x16x32_bf16 v[22:25], v[122:125], v[102:105], v[22:25]\n" \
    "ds_read_b128 v[102:105], %[ap1] offset:2048\n" \
    "s_waitcnt lgkmcnt(9)\n" \
    "v_mfma_f32_16x16x32_bf16 v[46:49], v[126:129], v[106:109], v[46:49]\n" \
    "ds_read_b64_tr_b16 v[152:153], v157 offset:2048\n" \
    "v_mfma_f32_16x16x32_bf16 v[66:69], v[106:109], v[130:133], v[66:69]\n" \
    "ds_read_b64_tr_b16 v[138:139], v154 offset:0\n" \
    "v_mfma_f32_16x16x32_bf16 v[86:89], v[134:137], v[106:109], v[86:89]\n" \
    "ds_read_b64_tr_b16 v[140:141], v154 offset:2048\n" \
    "v_mfma_f32_16x16x32_bf16 v[26:29], v[122:125], v[106:109], v[26:29]\n" \
    "ds_read_b128 v[106:109], %[ap1] offset:4096\n" \
    "s_waitcnt lgkmcnt(11)\n" \
    "v_mfma_f32_16x16x32_bf16 v[50:53], v[126:129], v[110:113], v[50:53]\n" \
    "ds_read_b128 v[118:121], %[ap1] offset:8192\n" \
    "v_mfma_f32_16x16x32_bf16 v[70:73], v[110:113], v[130:133], v[70:73]\n" \
    "v_mfma_f32_16x16x32_bf16 v[90:93], v[134:137], v[110:113], v[90:93]\n" \
    "v_mfma_f32_16x16x32_bf16 v[30:33], v[122:125], v[110:113], v[30:33]\n" \
    "ds_read_b128 v[110:113], %[ap1] offset:6144\n" \
    "v_mfma_f32_16x16x32_bf16 v[54:57], v[126:129], v[114:117], v[54:57]\n" \
    "s_barrier\n" \
    "v_mfma_f32_16x16x32_bf16 v[74:77], v[114:117], v[130:133], v[74:77]\n" \
    "v_mfma_f32_16x16x32_bf16 v[94:97], v[134:137], v[114:117], v[94:97]\n" \
    "v_mfma_f32_16x16x32_bf16 v[34:37], v[122:125], v[114:117], v[34:37]\n" \
    "s_waitcnt lgkmcnt(10)\n" \
    "v_mfma_f32_16x16x32_bf16 v[38:41], v[142:145], v[98:101], v[38:41]\n" \
    "ds_read_b64_tr_b16 v[126:127], v155 offset:16384\n" \
    "s_waitcnt lgkmcnt(9)\n" \
    "v_mfma_f32_16x16x32_bf16 v[58:61], v[98:101], v[146:149], v[58:61]\n" \
    "ds_read_b64_tr_b16 v[128:129], v155 offset:18432\n" \
    "s_waitcnt lgkmcnt(7)\n" \
    "v_mfma_f32_16x16x32_bf16 v[78:81], v[150:153], v[98:101], v[78:81]\n" \
    "ds_read_b128 v[98:101], %[ap0] offset:18432\n" \
    "v_mfma_f32_16x16x32_bf16 v[42:45], v[142:145], v[102:105], v[42:45]\n" \
    "ds_read_b64_tr_b16 v[130:131], v156 offset:16384\n" \
    "v_mfma_f32_16x16x32_bf16 v[62:65], v[102:105], v[146:149], v[62:65]\n" \
    "ds_read_b64_tr_b16 v[132:133], v156 offset:18432\n" \
    "v_mfma_f32_16x16x32_bf16 v[82:85], v[150:153], v[102:105], v[82:85]\n" \
    "ds_read_b64_tr_b16 v[134:135], v157 offset:16384\n" \
    "s_waitcnt lgkmcnt(9)\n" \
    "v_mfma_f32_16x16x32_bf16 v[22:25], v[138:141], v[102:105], v[22:25]\n" \
    "ds_read_b128 v[102:105], %[ap0] offset:20480\n" \
    "s_waitcnt lgkmcnt(9)\n" \
    "v_mfma_f32_16x16x32_bf16 v[46:49], v[142:145], v[106:109], v[46:49]\n" \
    "ds_read_b64_tr_b16 v[136:137], v157 offset:18432\n" \
    "v_mfma_f32_16x16x32_bf16 v[66:69], v[106:109], v[146:149], v[66:69]\n" \
    "ds_read_b64_tr_b16 v[122:123], v154 offset:16384\n" \
    "v_mfma_f32_16x16x32_bf16 v[86:89], v[150:153], v[106:109], v[86:89]\n" \
    "ds_read_b64_tr_b16 v[124:125], v154 offset:18432\n" \
    "v_mfma_f32_16x16x32_bf16 v[26:29], v[138:141], v[106:109], v[26:29]\n" \
    "ds_read_b128 v[106:109], %[ap0] offset:22528\n" \
    "s_waitcnt lgkmcnt(11)\n" \
    "v_mfma_f32_16x16x32_bf16 v[50:53], v[142:145], v[110:113], v[50:53]\n" \
    "ds_read_b128 v[114:117], %[ap0] offset:26624\n" \
    "v_mfma_f32_16x16x32_bf16 v[70:73], v[110:113], v[146:149], v[70:73]\n" \
    "v_mfma_f32_16x16x32_bf16 v[90:93], v[150:153], v[110:113], v[90:93]\n" \
    "v_mfma_f32_16x16x32_bf16 v[30:33], v[138:141], v[110:113], v[30:33]\n" \
    "ds_read_b128 v[110:113], %[ap0] offset:24576\n" \
    "v_mfma_f32_16x16x32_bf16 v[54:57], v[142:145], v[118:121], v[54:57]\n" \
    "s_barrier\n" \
    "v_mfma_f32_16x16x32_bf16 v[74:77], v[118:121], v[146:149], v[74:77]\n" \
    "v_mfma_f32_16x16x32_bf16 v[94:97], v[150:153], v[118:121], v[94:97]\n" \
    "v_mfma_f32_16x16x32_bf16 v[34:37], v[138:141], v[118:121], v[34:37]\n" \
    "s_waitcnt lgkmcnt(10)\n" \
    "v_mfma_f32_16x16x32_bf16 v[38:41], v[126:129], v[98:101], v[38:41]\n" \
    "ds_read_b64_tr_b16 v[142:143], v155 offset:32768\n" \
    "s_waitcnt lgkmcnt(9)\n" \
    "v_mfma_f32_16x16x32_bf16 v[58:61], v[98:101], v[130:133], v[58:61]\n" \
    "ds_read_b64_tr_b16 v[144:145], v155 offset:34816\n" \
    "s_waitcnt lgkmcnt(7)\n" \
    "v_mfma_f32_16x16x32_bf16 v[78:81], v[134:137], v[98:101], v[78:81]\n" \
    "ds_read_b128 v[98:101], %[ap1] offset:18432\n" \
    "v_mfma_f32_16x16x32_bf16 v[42:45], v[126:129], v[102:105], v[42:45]\n" \
    "ds_read_b64_tr_b16 v[146:147], v156 offset:32768\n" \
    "v_mfma_f32_16x16x32_bf16 v[62:65], v[102:105], v[130:133], v[62:65]\n" \
    "ds_read_b64_tr_b16 v[148:149], v156 offset:34816\n" \
    "v_mfma_f32_16x16x32_bf16 v[82:85], v[134:137], v[102:105], v[82:85]\n" \
    "ds_read_b64_tr_b16 v[150:151], v157 offset:32768\n" \
    "s_waitcnt lgkmcnt(9)\n" \
    "v_mfma_f32_16x16x32_bf16 v[22:25], v[122:125], v[102:105], v[22:25]\n" \
    "ds_read_b128 v[102:105], %[ap1] offset:20480\n" \
    "s_waitcnt lgkmcnt(9)\n" \
    "v_mfma_f32_16x16x32_bf16 v[46:49], v[126:129], v[106:109], v[46:49]\n" \
    "ds_read_b64_tr_b16 v[152:153], v157 offset:34816\n" \
    "v_mfma_f32_16x16x32_bf16 v[66:69], v[106:109], v[130:133], v[66:69]\n" \
    "ds_read_b64_tr_b16 v[138:139], v154 offset:32768\n" \
    "v_mfma_f32_16x16x32_bf16 v[86:89], v[134:137], v[106:109], v[86:89]\n" \
    "ds_read_b64_tr_b16 v[140:141], v154 offset:34816\n" \
    "v_mfma_f32_16x16x32_bf16 v[26:29], v[122:125], v[106:109], v[26:29]\n" \
    "ds_read_b128 v[106:109], %[ap1] offset:22528\n" \
    "s_waitcnt lgkmcnt(11)\n" \
    "v_mfma_f32_16x16x32_bf16 v[50:53], v[126:129], v[110:113], v[50:53]\n" \
    "ds_read_b128 v[118:121], %[ap1] offset:26624\n" \
    "v_mfma_f32_16x16x32_bf16 v[70:73], v[110:113], v[130:133], v[70:73]\n" \
    "v_mfma_f32_16x16x32_bf16 v[90:93], v[134:137], v[110:113], v[90:93]\n" \
    "v_mfma_f32_16x16x32_bf16 v[30:33], v[122:125], v[110:113], v[30:33]\n" \
    "ds_read_b128 v[110:113], %[ap1] offset:24576\n" \
    "v_mfma_f32_16x16x32_bf16 v[54:57], v[126:129], v[114:117], v[54:57]\n" \
    "s_barrier\n" \
    "v_mfma_f32_16x16x32_bf16 v[74:77], v[114:117], v[130:133], v[74:77]\n" \
    "v_mfma_f32_16x16x32_bf16 v[94:97], v[134:137], v[114:117], v[94:97]\n" \
    "v_mfma_f32_16x16x32_bf16 v[34:37], v[122:125], v[114:117], v[34:37]\n" \
    "s_waitcnt lgkmcnt(10)\n" \
    "v_mfma_f32_16x16x32_bf16 v[38:41], v[142:145], v[98:101], v[38:41]\n" \
    "ds_read_b64_tr_b16 v[126:127], %[wp1] offset:0\n" \
    "s_waitcnt lgkmcnt(9)\n" \
    "v_mfma_f32_16x16x32_bf16 v[58:61], v[98:101], v[146:149], v[58:61]\n" \
    "ds_read_b64_tr_b16 v[128:129], %[wp1] offset:2048\n" \
    "s_waitcnt lgkmcnt(7)\n" \
    "v_mfma_f32_16x16x32_bf16 v[78:81], v[150:153], v[98:101], v[78:81]\n" \
    "ds_read_b128 v[98:101], %[ap0] offset:53248\n" \
    "v_mfma_f32_16x16x32_bf16 v[42:45], v[142:145], v[102:105], v[42:45]\n" \
    "ds_read_b64_tr_b16 v[130:131], %[wp2] offset:0\n" \
    "v_mfma_f32_16x16x32_bf16 v[62:65], v[102:105], v[146:149], v[62:65]\n" \
    "ds_read_b64_tr_b16 v[132:133], %[wp2] offset:2048\n" \
    "v_mfma_f32_16x16x32_bf16 v[82:85], v[150:153], v[102:105], v[82:85]\n" \
    "ds_read_b64_tr_b16 v[134:135], %[wp3] offset:0\n" \
    "s_waitcnt lgkmcnt(9)\n" \
    "v_mfma_f32_16x16x32_bf16 v[22:25], v[138:141], v[102:105], v[22:25]\n" \
    "ds_read_b128 v[102:105], %[ap0] offset:55296\n" \
    "s_waitcnt lgkmcnt(9)\n" \
    "v_mfma_f32_16x16x32_bf16 v[46:49], v[142:145], v[106:109], v[46:49]\n" \
    "ds_read_b64_tr_b16 v[136:137], %[wp3] offset:2048\n" \
    "v_mfma_f32_16x16x32_bf16 v[66:69], v[106:109], v[146:149], v[66:69]\n" \
    "ds_read_b64_tr_b16 v[122:123], %[wp0] offset:0\n" \
    "v_mfma_f32_16x16x32_bf16 v[86:89], v[150:153], v[106:109], v[86:89]\n" \
    "ds_read_b64_tr_b16 v[124:125], %[wp0] offset:2048\n" \
    "v_mfma_f32_16x16x32_bf16 v[26:29], v[138:141], v[106:109], v[26:29]\n" \
    "ds_read_b128 v[106:109], %[ap0] offset:57344\n" \
    "s_waitcnt lgkmcnt(11)\n" \
    "v_mfma_f32_16x16x32_bf16 v[50:53], v[142:145], v[110:113], v[50:53]\n" \
    "ds_read_b128 v[114:117], %[ap0] offset:61440\n" \
    "v_mfma_f32_16x16x32_bf16 v[70:73], v[110:113], v[146:149], v[70:73]\n" \
    "v_mfma_f32_16x16x32_bf16 v[90:93], v[150:153], v[110:113], v[90:93]\n" \
    "v_mfma_f32_16x16x32_bf16 v[30:33], v[138:141], v[110:113], v[30:33]\n" \
    "ds_read_b128 v[110:113], %[ap0] offset:59392\n" \
    "v_mfma_f32_16x16x32_bf16 v[54:57], v[142:145], v[118:121], v[54:57]\n" \
    "s_barrier\n" \
    "v_mfma_f32_16x16x32_bf16 v[74:77], v[118:121], v[146:149], v[74:77]\n" \
    "v_mfma_f32_16x16x32_bf16 v[94:97], v[150:153], v[118:121], v[94:97]\n" \
    "v_mfma_f32_16x16x32_bf16 v[34:37], v[138:141], v[118:121], v[34:37]\n" \
    "s_waitcnt lgkmcnt(10)\n" \
    "v_mfma_f32_16x16x32_bf16 v[38:41], v[126:129], v[98:101], v[38:41]\n" \
    "ds_read_b64_tr_b16 v[142:143], %[wp1] offset:34816\n" \
    "s_waitcnt lgkmcnt(9)\n" \
    "v_mfma_f32_16x16x32_bf16 v[58:61], v[98:101], v[130:133], v[58:61]\n" \
    "ds_read_b64_tr_b16 v[144:145], %[wp1] offset:36864\n" \
    "s_waitcnt lgkmcnt(7)\n" \
    "v_mfma_f32_16x16x32_bf16 v[78:81], v[134:137], v[98:101], v[78:81]\n" \
    "ds_read_b128 v[98:101], %[ap1] offset:53248\n" \
    "v_mfma_f32_16x16x32_bf16 v[42:45], v[126:129], v[102:105], v[42:45]\n" \
    "ds_read_b64_tr_b16 v[146:147], %[wp2] offset:34816\n" \
    "v_mfma_f32_16x16x32_bf16 v[62:65], v[102:105], v[130:133], v[62:65]\n" \
    "ds_read_b64_tr_b16 v[148:149], %[wp2] offset:36864\n" \
    "v_mfma_f32_16x16x32_bf16 v[82:85], v[134:137], v[102:105], v[82:85]\n" \
    "ds_read_b64_tr_b16 v[150:151], %[wp3] offset:34816\n" \
    "s_waitcnt lgkmcnt(9)\n" \
    "v_mfma_f32_16x16x32_bf16 v[22:25], v[122:125], v[102:105], v[22:25]\n" \
    "ds_read_b128 v[102:105], %[ap1] offset:55296\n" \
    "s_waitcnt lgkmcnt(9)\n" \
    "v_mfma_f32_16x16x32_bf16 v[46:49], v[126:129], v[106:109], v[46:49]\n" \
    "ds_read_b64_tr_b16 v[152:153], %[wp3] offset:36864\n" \
    "v_mfma_f32_16x16x32_bf16 v[66:69], v[106:109], v[130:133], v[66:69]\n" \
    "ds_read_b64_tr_b16 v[138:139], %[wp0] offset:34816\n" \
    "v_mfma_f32_16x16x32_bf16 v[86:89], v[134:137], v[106:109], v[86:89]\n" \
    "ds_read_b64_tr_b16 v[140:141], %[wp0] offset:36864\n" \
    "v_mfma_f32_16x16x32_bf16 v[26:29], v[122:125], v[106:109], v[26:29]\n" \
    "ds_read_b128 v[106:109], %[ap1] offset:57344\n" \
    "s_waitcnt lgkmcnt(11)\n" \
    "v_mfma_f32_16x16x32_bf16 v[50:53], v[126:129], v[110:113], v[50:53]\n" \
    "ds_read_b128 v[118:121], %[ap1] offset:61440\n" \
    "v_mfma_f32_16x16x32_bf16 v[70:73], v[110:113], v[130:133], v[70:73]\n" \
    "v_mfma_f32_16x16x32_bf16 v[90:93], v[134:137], v[110:113], v[90:93]\n" \
    "v_mfma_f32_16x16x32_bf16 v[30:33], v[122:125], v[110:113], v[30:33]\n" \
    "ds_read_b128 v[110:113], %[ap1] offset:59392\n" \
    "v_mfma_f32_16x16x32_bf16 v[54:57], v[126:129], v[114:117], v[54:57]\n" \
    "s_barrier\n" \
    "v_mfma_f32_16x16x32_bf16 v[74:77], v[114:117], v[130:133], v[74:77]\n" \
    "v_mfma_f32_16x16x32_bf16 v[94:97], v[134:137], v[114:117], v[94:97]\n" \
    "v_mfma_f32_16x16x32_bf16 v[34:37], v[122:125], v[114:117], v[34:37]\n" \
    "s_waitcnt lgkmcnt(10)\n" \
    "v_mfma_f32_16x16x32_bf16 v[38:41], v[142:145], v[98:101], v[38:41]\n" \
    "ds_read_b64_tr_b16 v[126:127], %[wp1] offset:51200\n" \
    "s_waitcnt lgkmcnt(9)\n" \
    "v_mfma_f32_16x16x32_bf16 v[58:61], v[98:101], v[146:149], v[58:61]\n" \
    "ds_read_b64_tr_b16 v[128:129], %[wp1] offset:53248\n" \
    "s_waitcnt lgkmcnt(7)\n" \
    "v_mfma_f32_16x16x32_bf16 v[78:81], v[150:153], v[98:101], v[78:81]\n" \
    "ds_read_b128 v[98:101], %[ap0] offset:0\n" \
    "v_mfma_f32_16x16x32_bf16 v[42:45], v[142:145], v[102:105], v[42:45]\n" \
    "ds_read_b64_tr_b16 v[130:131], %[wp2] offset:51200\n" \
    "v_mfma_f32_16x16x32_bf16 v[62:65], v[102:105], v[146:149], v[62:65]\n" \
    "ds_read_b64_tr_b16 v[132:133], %[wp2] offset:53248\n" \
    "v_mfma_f32_16x16x32_bf16 v[82:85], v[150:153], v[102:105], v[82:85]\n" \
    "ds_read_b64_tr_b16 v[134:135], %[wp3] offset:51200\n" \
    "s_waitcnt lgkmcnt(9)\n" \
    "v_mfma_f32_16x16x32_bf16 v[22:25], v[138:141], v[102:105], v[22:25]\n" \
    "ds_read_b128 v[102:105], %[ap0] offset:2048\n" \
    "s_waitcnt lgkmcnt(9)\n" \
    "v_mfma_f32_16x16x32_bf16 v[46:49], v[142:145], v[106:109], v[46:49]\n" \
    "ds_read_b64_tr_b16 v[136:137], %[wp3] offset:53248\n" \
    "v_mfma_f32_16x16x32_bf16 v[66:69], v[106:109], v[146:149], v[66:69]\n" \
    "ds_read_b64_tr_b16 v[122:123], %[wp0] offset:51200\n" \
    "v_mfma_f32_16x16x32_bf16 v[86:89], v[150:153], v[106:109], v[86:89]\n" \
    "ds_read_b64_tr_b16 v[124:125], %[wp0] offset:53248\n" \
    "v_mfma_f32_16x16x32_bf16 v[26:29], v[138:141], v[106:109], v[26:29]\n" \
    "ds_read_b128 v[106:109], %[ap0] offset:4096\n" \
    "s_waitcnt lgkmcnt(11)\n" \
    "v_mfma_f32_16x16x32_bf16 v[50:53], v[142:145], v[110:113], v[50:53]\n" \
    "ds_read_b128 v[114:117], %[ap0] offset:8192\n" \
    "v_mfma_f32_16x16x32_bf16 v[70:73], v[110:113], v[146:149], v[70:73]\n" \
    "v_mfma_f32_16x16x32_bf16 v[90:93], v[150:153], v[110:113], v[90:93]\n" \
    "v_mfma_f32_16x16x32_bf16 v[30:33], v[138:141], v[110:113], v[30:33]\n" \
    "ds_read_b128 v[110:113], %[ap0] offset:6144\n" \
    "v_mfma_f32_16x16x32_bf16 v[54:57], v[142:145], v[118:121], v[54:57]\n" \
    "s_barrier\n" \
    "v_mfma_f32_16x16x32_bf16 v[74:77], v[118:121], v[146:149], v[74:77]\n" \
    "v_mfma_f32_16x16x32_bf16 v[94:97], v[150:153], v[118:121], v[94:97]\n" \
    "v_mfma_f32_16x16x32_bf16 v[34:37], v[138:141], v[118:121], v[34:37]\n" \
    "s_waitcnt lgkmcnt(10)\n" \
    "v_mfma_f32_16x16x32_bf16 v[38:41], v[126:129], v[98:101], v[38:41]\n" \
    "ds_read_b64_tr_b16 v[142:143], v155 offset:0\n" \
    "s_waitcnt lgkmcnt(9)\n" \
    "v_mfma_f32_16x16x32_bf16 v[58:61], v[98:101], v[130:133], v[58:61]\n" \
    "ds_read_b64_tr_b16 v[144:145], v155 offset:2048\n" \
    "s_waitcnt lgkmcnt(7)\n" \
    "v_mfma_f32_16x16x32_bf16 v[78:81], v[134:137], v[98:101], v[78:81]\n" \
    "ds_read_b128 v[98:101], %[ap1] offset:0\n" \
    "v_mfma_f32_16x16x32_bf16 v[42:45], v[126:129], v[102:105], v[42:45]\n" \
    "ds_read_b64_tr_b16 v[146:147], v156 offset:0\n" \
    "v_mfma_f32_16x16x32_bf16 v[62:65], v[102:105], v[130:133], v[62:65]\n" \
    "ds_read_b64_tr_b16 v[148:149], v156 offset:2048\n" \
    "v_mfma_f32_16x16x32_bf16 v[82:85], v[134:137], v[102:105], v[82:85]\n" \
    "ds_read_b64_tr_b16 v[150:151], v157 offset:0\n" \
    "s_waitcnt lgkmcnt(9)\n" \
    "v_mfma_f32_16x16x32_bf16 v[22:25], v[122:125], v[102:105], v[22:25]\n" \
    "ds_read_b128 v[102:105], %[ap1] offset:2048\n" \
    "s_waitcnt lgkmcnt(9)\n" \
    "v_mfma_f32_16x16x32_bf16 v[46:49], v[126:129], v[106:109], v[46:49]\n" \
    "ds_read_b64_tr_b16 v[152:153], v157 offset:2048\n" \
    "v_mfma_f32_16x16x32_bf16 v[66:69], v[106:109], v[130:133], v[66:69]\n" \
    "ds_read_b64_tr_b16 v[138:139], v154 offset:0\n" \
    "v_mfma_f32_16x16x32_bf16 v[86:89], v[134:137], v[106:109], v[86:89]\n" \
    "ds_read_b64_tr_b16 v[140:141], v154 offset:2048\n" \
    "v_mfma_f32_16x16x32_bf16 v[26:29], v[122:125], v[106:109], v[26:29]\n" \
    "ds_read_b128 v[106:109], %[ap1] offset:4096\n" \
    "s_waitcnt lgkmcnt(11)\n" \
    "v_mfma_f32_16x16x32_bf16 v[50:53], v[126:129], v[110:113], v[50:53]\n" \
    "ds_read_b128 v[118:121], %[ap1] offset:8192\n" \
    "v_mfma_f32_16x16x32_bf16 v[70:73], v[110:113], v[130:133], v[70:73]\n" \
    "v_mfma_f32_16x16x32_bf16 v[90:93], v[134:137], v[110:113], v[90:93]\n" \
    "v_mfma_f32_16x16x32_bf16 v[30:33], v[122:125], v[110:113], v[30:33]\n" \
    "ds_read_b128 v[110:113], %[ap1] offset:6144\n" \
    "v_mfma_f32_16x16x32_bf16 v[54:57], v[126:129], v[114:117], v[54:57]\n" \
    "s_barrier\n" \
    "v_mfma_f32_16x16x32_bf16 v[74:77], v[114:117], v[130:133], v[74:77]\n" \
    "v_mfma_f32_16x16x32_bf16 v[94:97], v[134:137], v[114:117], v[94:97]\n" \
    "v_mfma_f32_16x16x32_bf16 v[34:37], v[122:125], v[114:117], v[34:37]\n" \
    "s_waitcnt lgkmcnt(10)\n" \
    "v_mfma_f32_16x16x32_bf16 v[38:41], v[142:145], v[98:101], v[38:41]\n" \
    "ds_read_b64_tr_b16 v[126:127], v155 offset:16384\n" \
    "s_waitcnt lgkmcnt(9)\n" \
    "v_mfma_f32_16x16x32_bf16 v[58:61], v[98:101], v[146:149], v[58:61]\n" \
    "ds_read_b64_tr_b16 v[128:129], v155 offset:18432\n" \
    "s_waitcnt lgkmcnt(7)\n" \
    "v_mfma_f32_16x16x32_bf16 v[78:81], v[150:153], v[98:101], v[78:81]\n" \
    "ds_read_b128 v[98:101], %[ap0] offset:18432\n" \
    "v_mfma_f32_16x16x32_bf16 v[42:45], v[142:145], v[102:105], v[42:45]\n" \
    "ds_read_b64_tr_b16 v[130:131], v156 offset:16384\n" \
    "v_mfma_f32_16x16x32_bf16 v[62:65], v[102:105], v[146:149], v[62:65]\n" \
    "ds_read_b64_tr_b16 v[132:133], v156 offset:18432\n" \
    "v_mfma_f32_16x16x32_bf16 v[82:85], v[150:153], v[102:105], v[82:85]\n" \
    "ds_read_b64_tr_b16 v[134:135], v157 offset:16384\n" \
    "s_waitcnt lgkmcnt(9)\n" \
    "v_mfma_f32_16x16x32_bf16 v[22:25], v[138:141], v[102:105], v[22:25]\n" \
    "ds_read_b128 v[102:105], %[ap0] offset:20480\n" \
    "s_waitcnt lgkmcnt(9)\n" \
    "v_mfma_f32_16x16x32_bf16 v[46:49], v[142:145], v[106:109], v[46:49]\n" \
    "ds_read_b64_tr_b16 v[136:137], v157 offset:18432\n" \
    "v_mfma_f32_16x16x32_bf16 v[66:69], v[106:109], v[146:149], v[66:69]\n" \
    "ds_read_b64_tr_b16 v[122:123], v154 offset:16384\n" \
    "v_mfma_f32_16x16x32_bf16 v[86:89], v[150:153], v[106:109], v[86:89]\n" \
    "ds_read_b64_tr_b16 v[124:125], v154 offset:18432\n" \
    "v_mfma_f32_16x16x32_bf16 v[26:29], v[138:141], v[106:109], v[26:29]\n" \
    "ds_read_b128 v[106:109], %[ap0] offset:22528\n" \
    "s_waitcnt lgkmcnt(11)\n" \
    "v_mfma_f32_16x16x32_bf16 v[50:53], v[142:145], v[110:113], v[50:53]\n" \
    "ds_read_b128 v[114:117], %[ap0] offset:26624\n" \
    "v_mfma_f32_16x16x32_bf16 v[70:73], v[110:113], v[146:149], v[70:73]\n" \
    "v_mfma_f32_16x16x32_bf16 v[90:93], v[150:153], v[110:113], v[90:93]\n" \
    "v_mfma_f32_16x16x32_bf16 v[30:33], v[138:141], v[110:113], v[30:33]\n" \
    "ds_read_b128 v[110:113], %[ap0] offset:24576\n" \
    "v_mfma_f32_16x16x32_bf16 v[54:57], v[142:145], v[118:121], v[54:57]\n" \
    "s_barrier\n" \
    "v_mfma_f32_16x16x32_bf16 v[74:77], v[118:121], v[146:149], v[74:77]\n" \
    "v_mfma_f32_16x16x32_bf16 v[94:97], v[150:153], v[118:121], v[94:97]\n" \
    "v_mfma_f32_16x16x32_bf16 v[34:37], v[138:141], v[118:121], v[34:37]\n" \
    "s_waitcnt lgkmcnt(10)\n" \
    "v_mfma_f32_16x16x32_bf16 v[38:41], v[126:129], v[98:101], v[38:41]\n" \
    "ds_read_b64_tr_b16 v[142:143], v155 offset:32768\n" \
    "s_waitcnt lgkmcnt(9)\n" \
    "v_mfma_f32_16x16x32_bf16 v[58:61], v[98:101], v[130:133], v[58:61]\n" \
    "ds_read_b64_tr_b16 v[144:145], v155 offset:34816\n" \
    "s_waitcnt lgkmcnt(7)\n" \
    "v_mfma_f32_16x16x32_bf16 v[78:81], v[134:137], v[98:101], v[78:81]\n" \
    "ds_read_b128 v[98:101], %[ap1] offset:18432\n" \
    "v_mfma_f32_16x16x32_bf16 v[42:45], v[126:129], v[102:105], v[42:45]\n" \
    "ds_read_b64_tr_b16 v[146:147], v156 offset:32768\n" \
    "v_mfma_f32_16x16x32_bf16 v[62:65], v[102:105], v[130:133], v[62:65]\n" \
    "ds_read_b64_tr_b16 v[148:149], v156 offset:34816\n" \
    "v_mfma_f32_16x16x32_bf16 v[82:85], v[134:137], v[102:105], v[82:85]\n" \
    "ds_read_b64_tr_b16 v[150:151], v157 offset:32768\n" \
    "s_waitcnt lgkmcnt(9)\n" \
    "v_mfma_f32_16x16x32_bf16 v[22:25], v[122:125], v[102:105], v[22:25]\n" \
    "ds_read_b128 v[102:105], %[ap1] offset:20480\n" \
    "s_waitcnt lgkmcnt(9)\n" \
    "v_mfma_f32_16x16x32_bf16 v[46:49], v[126:129], v[106:109], v[46:49]\n" \
    "ds_read_b64_tr_b16 v[152:153], v157 offset:34816\n" \
    "v_mfma_f32_16x16x32_bf16 v[66:69], v[106:109], v[130:133], v[66:69]\n" \
    "ds_read_b64_tr_b16 v[138:139], v154 offset:32768\n" \
    "v_mfma_f32_16x16x32_bf16 v[86:89], v[134:137], v[106:109], v[86:89]\n" \
    "ds_read_b64_tr_b16 v[140:141], v154 offset:34816\n" \
    "v_mfma_f32_16x16x32_bf16 v[26:29], v[122:125], v[106:109], v[26:29]\n" \
    "ds_read_b128 v[106:109], %[ap1] offset:22528\n" \
    "s_waitcnt lgkmcnt(11)\n" \
    "v_mfma_f32_16x16x32_bf16 v[50:53], v[126:129], v[110:113], v[50:53]\n" \
    "ds_read_b128 v[118:121], %[ap1] offset:26624\n" \
    "v_mfma_f32_16x16x32_bf16 v[70:73], v[110:113], v[130:133], v[70:73]\n" \
    "v_mfma_f32_16x16x32_bf16 v[90:93], v[134:137], v[110:113], v[90:93]\n" \
    "v_mfma_f32_16x16x32_bf16 v[30:33], v[122:125], v[110:113], v[30:33]\n" \
    "ds_read_b128 v[110:113], %[ap1] offset:24576\n" \
    "v_mfma_f32_16x16x32_bf16 v[54:57], v[126:129], v[114:117], v[54:57]\n" \
    "s_barrier\n" \
    "v_mfma_f32_16x16x32_bf16 v[74:77], v[114:117], v[130:133], v[74:77]\n" \
    "v_mfma_f32_16x16x32_bf16 v[94:97], v[134:137], v[114:117], v[94:97]\n" \
    "v_mfma_f32_16x16x32_bf16 v[34:37], v[122:125], v[114:117], v[34:37]\n" \
    "s_waitcnt lgkmcnt(10)\n" \
    "v_mfma_f32_16x16x32_bf16 v[38:41], v[142:145], v[98:101], v[38:41]\n" \
    "ds_read_b64_tr_b16 v[126:127], %[wp1] offset:0\n" \
    "s_waitcnt lgkmcnt(9)\n" \
    "v_mfma_f32_16x16x32_bf16 v[58:61], v[98:101], v[146:149], v[58:61]\n" \
    "ds_read_b64_tr_b16 v[128:129], %[wp1] offset:2048\n" \
    "s_waitcnt lgkmcnt(7)\n" \
    "v_mfma_f32_16x16x32_bf16 v[78:81], v[150:153], v[98:101], v[78:81]\n" \
    "ds_read_b128 v[98:101], %[ap0] offset:53248\n" \
    "v_mfma_f32_16x16x32_bf16 v[42:45], v[142:145], v[102:105], v[42:45]\n" \
    "ds_read_b64_tr_b16 v[130:131], %[wp2] offset:0\n" \
    "v_mfma_f32_16x16x32_bf16 v[62:65], v[102:105], v[146:149], v[62:65]\n" \
    "ds_read_b64_tr_b16 v[132:133], %[wp2] offset:2048\n" \
    "v_mfma_f32_16x16x32_bf16 v[82:85], v[150:153], v[102:105], v[82:85]\n" \
    "ds_read_b64_tr_b16 v[134:135], %[wp3] offset:0\n" \
    "s_waitcnt lgkmcnt(9)\n" \
    "v_mfma_f32_16x16x32_bf16 v[22:25], v[138:141], v[102:105], v[22:25]\n" \
    "ds_read_b128 v[102:105], %[ap0] offset:55296\n" \
    "s_waitcnt lgkmcnt(9)\n" \
    "v_mfma_f32_16x16x32_bf16 v[46:49], v[142:145], v[106:109], v[46:49]\n" \
    "ds_read_b64_tr_b16 v[136:137], %[wp3] offset:2048\n" \
    "v_mfma_f32_16x16x32_bf16 v[66:69], v[106:109], v[146:149], v[66:69]\n" \
    "ds_read_b64_tr_b16 v[122:123], %[wp0] offset:0\n" \
    "v_mfma_f32_16x16x32_bf16 v[86:89], v[150:153], v[106:109], v[86:89]\n" \
    "ds_read_b64_tr_b16 v[124:125], %[wp0] offset:2048\n" \
    "v_mfma_f32_16x16x32_bf16 v[26:29], v[138:141], v[106:109], v[26:29]\n" \
    "ds_read_b128 v[106:109], %[ap0] offset:57344\n" \
    "s_waitcnt lgkmcnt(11)\n" \
    "v_mfma_f32_16x16x32_bf16 v[50:53], v[142:145], v[110:113], v[50:53]\n" \
    "ds_read_b128 v[114:117], %[ap0] offset:61440\n" \
    "v_mfma_f32_16x16x32_bf16 v[70:73], v[110:113], v[146:149], v[70:73]\n" \
    "v_mfma_f32_16x16x32_bf16 v[90:93], v[150:153], v[110:113], v[90:93]\n" \
    "v_mfma_f32_16x16x32_bf16 v[30:33], v[138:141], v[110:113], v[30:33]\n" \
    "ds_read_b128 v[110:113], %[ap0] offset:59392\n" \
    "v_mfma_f32_16x16x32_bf16 v[54:57], v[142:145], v[118:121], v[54:57]\n" \
    "s_barrier\n" \
    "v_mfma_f32_16x16x32_bf16 v[74:77], v[118:121], v[146:149], v[74:77]\n" \
    "v_mfma_f32_16x16x32_bf16 v[94:97], v[150:153], v[118:121], v[94:97]\n" \
    "v_mfma_f32_16x16x32_bf16 v[34:37], v[138:141], v[118:121], v[34:37]\n" \
    "s_waitcnt lgkmcnt(10)\n" \
    "v_mfma_f32_16x16x32_bf16 v[38:41], v[126:129], v[98:101], v[38:41]\n" \
    "ds_read_b64_tr_b16 v[142:143], %[wp1] offset:34816\n" \
    "s_waitcnt lgkmcnt(9)\n" \
    "v_mfma_f32_16x16x32_bf16 v[58:61], v[98:101], v[130:133], v[58:61]\n" \
    "ds_read_b64_tr_b16 v[144:145], %[wp1] offset:36864\n" \
    "s_waitcnt lgkmcnt(7)\n" \
    "v_mfma_f32_16x16x32_bf16 v[78:81], v[134:137], v[98:101], v[78:81]\n" \
    "ds_read_b128 v[98:101], %[ap1] offset:53248\n" \
    "v_mfma_f32_16x16x32_bf16 v[42:45], v[126:129], v[102:105], v[42:45]\n" \
    "ds_read_b64_tr_b16 v[146:147], %[wp2] offset:34816\n" \
    "v_mfma_f32_16x16x32_bf16 v[62:65], v[102:105], v[130:133], v[62:65]\n" \
    "ds_read_b64_tr_b16 v[148:149], %[wp2] offset:36864\n" \
    "v_mfma_f32_16x16x32_bf16 v[82:85], v[134:137], v[102:105], v[82:85]\n" \
    "ds_read_b64_tr_b16 v[150:151], %[wp3] offset:34816\n" \
    "s_waitcnt lgkmcnt(9)\n" \
    "v_mfma_f32_16x16x32_bf16 v[22:25], v[122:125], v[102:105], v[22:25]\n" \
    "ds_read_b128 v[102:105], %[ap1] offset:55296\n" \
    "s_waitcnt lgkmcnt(9)\n" \
    "v_mfma_f32_16x16x32_bf16 v[46:49], v[126:129], v[106:109], v[46:49]\n" \
    "ds_read_b64_tr_b16 v[152:153], %[wp3] offset:36864\n" \
    "v_mfma_f32_16x16x32_bf16 v[66:69], v[106:109], v[130:133], v[66:69]\n" \
    "ds_read_b64_tr_b16 v[138:139], %[wp0] offset:34816\n" \
    "v_mfma_f32_16x16x32_bf16 v[86:89], v[134:137], v[106:109], v[86:89]\n" \
    "ds_read_b64_tr_b16 v[140:141], %[wp0] offset:36864\n" \
    "v_mfma_f32_16x16x32_bf16 v[26:29], v[122:125], v[106:109], v[26:29]\n" \
    "ds_read_b128 v[106:109], %[ap1] offset:57344\n" \
    "s_waitcnt lgkmcnt(11)\n" \
    "v_mfma_f32_16x16x32_bf16 v[50:53], v[126:129], v[110:113], v[50:53]\n" \
    "ds_read_b128 v[118:121], %[ap1] offset:61440\n" \
    "v_mfma_f32_16x16x32_bf16 v[70:73], v[110:113], v[130:133], v[70:73]\n" \
    "v_mfma_f32_16x16x32_bf16 v[90:93], v[134:137], v[110:113], v[90:93]\n" \
    "v_mfma_f32_16x16x32_bf16 v[30:33], v[122:125], v[110:113], v[30:33]\n" \
    "ds_read_b128 v[110:113], %[ap1] offset:59392\n" \
    "v_mfma_f32_16x16x32_bf16 v[54:57], v[126:129], v[114:117], v[54:57]\n" \
    "s_barrier\n" \
    "v_mfma_f32_16x16x32_bf16 v[74:77], v[114:117], v[130:133], v[74:77]\n" \
    "v_mfma_f32_16x16x32_bf16 v[94:97], v[134:137], v[114:117], v[94:97]\n" \
    "v_mfma_f32_16x16x32_bf16 v[34:37], v[122:125], v[114:117], v[34:37]\n" \
    "s_waitcnt lgkmcnt(10)\n" \
    "v_mfma_f32_16x16x32_bf16 v[38:41], v[142:145], v[98:101], v[38:41]\n" \
    "ds_read_b64_tr_b16 v[126:127], %[wp1] offset:51200\n" \
    "s_waitcnt lgkmcnt(9)\n" \
    "v_mfma_f32_16x16x32_bf16 v[58:61], v[98:101], v[146:149], v[58:61]\n" \
    "ds_read_b64_tr_b16 v[128:129], %[wp1] offset:53248\n" \
    "s_waitcnt lgkmcnt(7)\n" \
    "v_mfma_f32_16x16x32_bf16 v[78:81], v[150:153], v[98:101], v[78:81]\n" \
    "ds_read_b128 v[98:101], %[ap0] offset:0\n" \
    "v_mfma_f32_16x16x32_bf16 v[42:45], v[142:145], v[102:105], v[42:45]\n" \
    "ds_read_b64_tr_b16 v[130:131], %[wp2] offset:51200\n" \
    "v_mfma_f32_16x16x32_bf16 v[62:65], v[102:105], v[146:149], v[62:65]\n" \
    "ds_read_b64_tr_b16 v[132:133], %[wp2] offset:53248\n" \
    "v_mfma_f32_16x16x32_bf16 v[82:85], v[150:153], v[102:105], v[82:85]\n" \
    "ds_read_b64_tr_b16 v[134:135], %[wp3] offset:51200\n" \
    "s_waitcnt lgkmcnt(9)\n" \
    "v_mfma_f32_16x16x32_bf16 v[22:25], v[138:141], v[102:105], v[22:25]\n" \
    "ds_read_b128 v[102:105], %[ap0] offset:2048\n" \
    "s_waitcnt lgkmcnt(9)\n" \
    "v_mfma_f32_16x16x32_bf16 v[46:49], v[142:145], v[106:109], v[46:49]\n" \
    "ds_read_b64_tr_b16 v[136:137], %[wp3] offset:53248\n" \
    "v_mfma_f32_16x16x32_bf16 v[66:69], v[106:109], v[146:149], v[66:69]\n" \
    "ds_read_b64_tr_b16 v[122:123], %[wp0] offset:51200\n" \
    "v_mfma_f32_16x16x32_bf16 v[86:89], v[150:153], v[106:109], v[86:89]\n" \
    "ds_read_b64_tr_b16 v[124:125], %[wp0] offset:53248\n" \
    "v_mfma_f32_16x16x32_bf16 v[26:29], v[138:141], v[106:109], v[26:29]\n" \
    "ds_read_b128 v[106:109], %[ap0] offset:4096\n" \
    "s_waitcnt lgkmcnt(11)\n" \
    "v_mfma_f32_16x16x32_bf16 v[50:53], v[142:145], v[110:113], v[50:53]\n" \
    "ds_read_b128 v[114:117], %[ap0] offset:8192\n" \
    "v_mfma_f32_16x16x32_bf16 v[70:73], v[110:113], v[146:149], v[70:73]\n" \
    "v_mfma_f32_16x16x32_bf16 v[90:93], v[150:153], v[110:113], v[90:93]\n" \
    "v_mfma_f32_16x16x32_bf16 v[30:33], v[138:141], v[110:113], v[30:33]\n" \
    "ds_read_b128 v[110:113], %[ap0] offset:6144\n" \
    "v_mfma_f32_16x16x32_bf16 v[54:57], v[142:145], v[118:121], v[54:57]\n" \
    "s_barrier\n" \
    "v_mfma_f32_16x16x32_bf16 v[74:77], v[118:121], v[146:149], v[74:77]\n" \
    "v_mfma_f32_16x16x32_bf16 v[94:97], v[150:153], v[118:121], v[94:97]\n" \
    "v_mfma_f32_16x16x32_bf16 v[34:37], v[138:141], v[118:121], v[34:37]\n" \
    "s_waitcnt lgkmcnt(10)\n" \
    "v_mfma_f32_16x16x32_bf16 v[38:41], v[126:129], v[98:101], v[38:41]\n" \
    "ds_read_b64_tr_b16 v[142:143], v155 offset:0\n" \
    "s_waitcnt lgkmcnt(9)\n" \
    "v_mfma_f32_16x16x32_bf16 v[58:61], v[98:101], v[130:133], v[58:61]\n" \
    "ds_read_b64_tr_b16 v[144:145], v155 offset:2048\n" \
    "s_waitcnt lgkmcnt(7)\n" \
    "v_mfma_f32_16x16x32_bf16 v[78:81], v[134:137], v[98:101], v[78:81]\n" \
    "ds_read_b128 v[98:101], %[ap1] offset:0\n" \
    "v_mfma_f32_16x16x32_bf16 v[42:45], v[126:129], v[102:105], v[42:45]\n" \
    "ds_read_b64_tr_b16 v[146:147], v156 offset:0\n" \
    "v_mfma_f32_16x16x32_bf16 v[62:65], v[102:105], v[130:133], v[62:65]\n" \
    "ds_read_b64_tr_b16 v[148:149], v156 offset:2048\n" \
    "v_mfma_f32_16x16x32_bf16 v[82:85], v[134:137], v[102:105], v[82:85]\n" \
    "ds_read_b64_tr_b16 v[150:151], v157 offset:0\n" \
    "s_waitcnt lgkmcnt(9)\n" \
    "v_mfma_f32_16x16x32_bf16 v[22:25], v[122:125], v[102:105], v[22:25]\n" \
    "ds_read_b128 v[102:105], %[ap1] offset:2048\n" \
    "s_waitcnt lgkmcnt(9)\n" \
    "v_mfma_f32_16x16x32_bf16 v[46:49], v[126:129], v[106:109], v[46:49]\n" \
    "ds_read_b64_tr_b16 v[152:153], v157 offset:2048\n" \
    "v_mfma_f32_16x16x32_bf16 v[66:69], v[106:109], v[130:133], v[66:69]\n" \
    "ds_read_b64_tr_b16 v[138:139], v154 offset:0\n" \
    "v_mfma_f32_16x16x32_bf16 v[86:89], v[134:137], v[106:109], v[86:89]\n" \
    "ds_read_b64_tr_b16 v[140:141], v154 offset:2048\n" \
    "v_mfma_f32_16x16x32_bf16 v[26:29], v[122:125], v[106:109], v[26:29]\n" \
    "ds_read_b128 v[106:109], %[ap1] offset:4096\n" \
    "s_waitcnt lgkmcnt(11)\n" \
    "v_mfma_f32_16x16x32_bf16 v[50:53], v[126:129], v[110:113], v[50:53]\n" \
    "ds_read_b128 v[118:121], %[ap1] offset:8192\n" \
    "v_mfma_f32_16x16x32_bf16 v[70:73], v[110:113], v[130:133], v[70:73]\n" \
    "v_mfma_f32_16x16x32_bf16 v[90:93], v[134:137], v[110:113], v[90:93]\n" \
    "v_mfma_f32_16x16x32_bf16 v[30:33], v[122:125], v[110:113], v[30:33]\n" \
    "ds_read_b128 v[110:113], %[ap1] offset:6144\n" \
    "v_mfma_f32_16x16x32_bf16 v[54:57], v[126:129], v[114:117], v[54:57]\n" \
    "s_barrier\n" \
    "v_mfma_f32_16x16x32_bf16 v[74:77], v[114:117], v[130:133], v[74:77]\n" \
    "v_mfma_f32_16x16x32_bf16 v[94:97], v[134:137], v[114:117], v[94:97]\n" \
    "v_mfma_f32_16x16x32_bf16 v[34:37], v[122:125], v[114:117], v[34:37]\n" \
    "s_waitcnt lgkmcnt(10)\n" \
    "v_mfma_f32_16x16x32_bf16 v[38:41], v[142:145], v[98:101], v[38:41]\n" \
    "ds_read_b64_tr_b16 v[126:127], v155 offset:16384\n" \
    "s_waitcnt lgkmcnt(9)\n" \
    "v_mfma_f32_16x16x32_bf16 v[58:61], v[98:101], v[146:149], v[58:61]\n" \
    "ds_read_b64_tr_b16 v[128:129], v155 offset:18432\n" \
    "s_waitcnt lgkmcnt(7)\n" \
    "v_mfma_f32_16x16x32_bf16 v[78:81], v[150:153], v[98:101], v[78:81]\n" \
    "ds_read_b128 v[98:101], %[ap0] offset:18432\n" \
    "v_mfma_f32_16x16x32_bf16 v[42:45], v[142:145], v[102:105], v[42:45]\n" \
    "ds_read_b64_tr_b16 v[130:131], v156 offset:16384\n" \
    "v_mfma_f32_16x16x32_bf16 v[62:65], v[102:105], v[146:149], v[62:65]\n" \
    "ds_read_b64_tr_b16 v[132:133], v156 offset:18432\n" \
    "v_mfma_f32_16x16x32_bf16 v[82:85], v[150:153], v[102:105], v[82:85]\n" \
    "ds_read_b64_tr_b16 v[134:135], v157 offset:16384\n" \
    "s_waitcnt lgkmcnt(9)\n" \
    "v_mfma_f32_16x16x32_bf16 v[22:25], v[138:141], v[102:105], v[22:25]\n" \
    "ds_read_b128 v[102:105], %[ap0] offset:20480\n" \
    "s_waitcnt lgkmcnt(9)\n" \
    "v_mfma_f32_16x16x32_bf16 v[46:49], v[142:145], v[106:109], v[46:49]\n" \
    "ds_read_b64_tr_b16 v[136:137], v157 offset:18432\n" \
    "v_mfma_f32_16x16x32_bf16 v[66:69], v[106:109], v[146:149], v[66:69]\n" \
    "ds_read_b64_tr_b16 v[122:123], v154 offset:16384\n" \
    "v_mfma_f32_16x16x32_bf16 v[86:89], v[150:153], v[106:109], v[86:89]\n" \
    "ds_read_b64_tr_b16 v[124:125], v154 offset:18432\n" \
    "v_mfma_f32_16x16x32_bf16 v[26:29], v[138:141], v[106:109], v[26:29]\n" \
    "ds_read_b128 v[106:109], %[ap0] offset:22528\n" \
    "s_waitcnt lgkmcnt(11)\n" \
    "v_mfma_f32_16x16x32_bf16 v[50:53], v[142:145], v[110:113], v[50:53]\n" \
    "ds_read_b128 v[114:117], %[ap0] offset:26624\n" \
    "v_mfma_f32_16x16x32_bf16 v[70:73], v[110:113], v[146:149], v[70:73]\n" \
    "v_mfma_f32_16x16x32_bf16 v[90:93], v[150:153], v[110:113], v[90:93]\n" \
    "v_mfma_f32_16x16x32_bf16 v[30:33], v[138:141], v[110:113], v[30:33]\n" \
    "ds_read_b128 v[110:113], %[ap0] offset:24576\n" \
    "v_mfma_f32_16x16x32_bf16 v[54:57], v[142:145], v[118:121], v[54:57]\n" \
    "s_barrier\n" \
    "v_mfma_f32_16x16x32_bf16 v[74:77], v[118:121], v[146:149], v[74:77]\n" \
    "v_mfma_f32_16x16x32_bf16 v[94:97], v[150:153], v[118:121], v[94:97]\n" \
    "v_mfma_f32_16x16x32_bf16 v[34:37], v[138:141], v[118:121], v[34:37]\n" \
    "s_waitcnt lgkmcnt(10)\n" \
    "v_mfma_f32_16x16x32_bf16 v[38:41], v[126:129], v[98:101], v[38:41]\n" \
    "ds_read_b64_tr_b16 v[142:143], v155 offset:32768\n" \
    "s_waitcnt lgkmcnt(9)\n" \
    "v_mfma_f32_16x16x32_bf16 v[58:61], v[98:101], v[130:133], v[58:61]\n" \
    "ds_read_b64_tr_b16 v[144:145], v155 offset:34816\n" \
    "s_waitcnt lgkmcnt(7)\n" \
    "v_mfma_f32_16x16x32_bf16 v[78:81], v[134:137], v[98:101], v[78:81]\n" \
    "ds_read_b128 v[98:101], %[ap1] offset:18432\n" \
    "v_mfma_f32_16x16x32_bf16 v[42:45], v[126:129], v[102:105], v[42:45]\n" \
    "ds_read_b64_tr_b16 v[146:147], v156 offset:32768\n" \
    "v_mfma_f32_16x16x32_bf16 v[62:65], v[102:105], v[130:133], v[62:65]\n" \
    "ds_read_b64_tr_b16 v[148:149], v156 offset:34816\n" \
    "v_mfma_f32_16x16x32_bf16 v[82:85], v[134:137], v[102:105], v[82:85]\n" \
    "ds_read_b64_tr_b16 v[150:151], v157 offset:32768\n" \
    "s_waitcnt lgkmcnt(9)\n" \
    "v_mfma_f32_16x16x32_bf16 v[22:25], v[122:125], v[102:105], v[22:25]\n" \
    "ds_read_b128 v[102:105], %[ap1] offset:20480\n" \
    "s_waitcnt lgkmcnt(9)\n" \
    "v_mfma_f32_16x16x32_bf16 v[46:49], v[126:129], v[106:109], v[46:49]\n" \
    "ds_read_b64_tr_b16 v[152:153], v157 offset:34816\n" \
    "v_mfma_f32_16x16x32_bf16 v[66:69], v[106:109], v[130:133], v[66:69]\n" \
    "ds_read_b64_tr_b16 v[138:139], v154 offset:32768\n" \
    "v_mfma_f32_16x16x32_bf16 v[86:89], v[134:137], v[106:109], v[86:89]\n" \
    "ds_read_b64_tr_b16 v[140:141], v154 offset:34816\n" \
    "v_mfma_f32_16x16x32_bf16 v[26:29], v[122:125], v[106:109], v[26:29]\n" \
    "ds_read_b128 v[106:109], %[ap1] offset:22528\n" \
    "s_waitcnt lgkmcnt(11)\n" \
    "v_mfma_f32_16x16x32_bf16 v[50:53], v[126:129], v[110:113], v[50:53]\n" \
    "ds_read_b128 v[118:121], %[ap1] offset:26624\n" \
    "v_mfma_f32_16x16x32_bf16 v[70:73], v[110:113], v[130:133], v[70:73]\n" \
    "v_mfma_f32_16x16x32_bf16 v[90:93], v[134:137], v[110:113], v[90:93]\n" \
    "v_mfma_f32_16x16x32_bf16 v[30:33], v[122:125], v[110:113], v[30:33]\n" \
    "ds_read_b128 v[110:113], %[ap1] offset:24576\n" \
    "v_mov_b32 v161, 16\n" \
    "ds_write_b32 %[fr], v161\n" \
    "v_mfma_f32_16x16x32_bf16 v[54:57], v[126:129], v[114:117], v[54:57]\n" \
    "s_barrier\n" \
    "v_mfma_f32_16x16x32_bf16 v[74:77], v[114:117], v[130:133], v[74:77]\n" \
    "v_mfma_f32_16x16x32_bf16 v[94:97], v[134:137], v[114:117], v[94:97]\n" \
    "v_mfma_f32_16x16x32_bf16 v[34:37], v[122:125], v[114:117], v[34:37]\n" \
    "s_waitcnt lgkmcnt(11)\n" \
    "v_mfma_f32_16x16x32_bf16 v[38:41], v[142:145], v[98:101], v[38:41]\n" \
    "ds_read_b64_tr_b16 v[126:127], %[wp1] offset:0\n" \
    "s_waitcnt lgkmcnt(10)\n" \
    "v_mfma_f32_16x16x32_bf16 v[58:61], v[98:101], v[146:149], v[58:61]\n" \
    "ds_read_b64_tr_b16 v[128:129], %[wp1] offset:2048\n" \
    "s_waitcnt lgkmcnt(8)\n" \
    "v_mfma_f32_16x16x32_bf16 v[78:81], v[150:153], v[98:101], v[78:81]\n" \
    "ds_read_b128 v[98:101], %[ap0] offset:53248\n" \
    "v_mfma_f32_16x16x32_bf16 v[42:45], v[142:145], v[102:105], v[42:45]\n" \
    "ds_read_b64_tr_b16 v[130:131], %[wp2] offset:0\n" \
    "v_mfma_f32_16x16x32_bf16 v[62:65], v[102:105], v[146:149], v[62:65]\n" \
    "ds_read_b64_tr_b16 v[132:133], %[wp2] offset:2048\n" \
    "v_mfma_f32_16x16x32_bf16 v[82:85], v[150:153], v[102:105], v[82:85]\n" \
    "ds_read_b64_tr_b16 v[134:135], %[wp3] offset:0\n" \
    "s_waitcnt lgkmcnt(10)\n" \
    "v_mfma_f32_16x16x32_bf16 v[22:25], v[138:141], v[102:105], v[22:25]\n" \
    "ds_read_b128 v[102:105], %[ap0] offset:55296\n" \
    "s_waitcnt lgkmcnt(10)\n" \
    "v_mfma_f32_16x16x32_bf16 v[46:49], v[142:145], v[106:109], v[46:49]\n" \
    "ds_read_b64_tr_b16 v[136:137], %[wp3] offset:2048\n" \
    "v_mfma_f32_16x16x32_bf16 v[66:69], v[106:109], v[146:149], v[66:69]\n" \
    "ds_read_b64_tr_b16 v[122:123], %[wp0] offset:0\n" \
    "v_mfma_f32_16x16x32_bf16 v[86:89], v[150:153], v[106:109], v[86:89]\n" \
    "ds_read_b64_tr_b16 v[124:125], %[wp0] offset:2048\n" \
    "v_mfma_f32_16x16x32_bf16 v[26:29], v[138:141], v[106:109], v[26:29]\n" \
    "ds_read_b128 v[106:109], %[ap0] offset:57344\n" \
    "s_waitcnt lgkmcnt(12)\n" \
    "v_mfma_f32_16x16x32_bf16 v[50:53], v[142:145], v[110:113], v[50:53]\n" \
    "ds_read_b128 v[114:117], %[ap0] offset:61440\n" \
    "v_mfma_f32_16x16x32_bf16 v[70:73], v[110:113], v[146:149], v[70:73]\n" \
    "v_mfma_f32_16x16x32_bf16 v[90:93], v[150:153], v[110:113], v[90:93]\n" \
    "v_mfma_f32_16x16x32_bf16 v[30:33], v[138:141], v[110:113], v[30:33]\n" \
    "ds_read_b128 v[110:113], %[ap0] offset:59392\n" \
    "v_mov_b32 v161, 17\n" \
    "ds_write_b32 %[fr], v161\n" \
    "v_mfma_f32_16x16x32_bf16 v[54:57], v[142:145], v[118:121], v[54:57]\n" \
    "s_barrier\n" \
    "ds_read_b64_tr_b16 v[142:143], %[wp1] offset:34816\n" \
    "ds_read_b64_tr_b16 v[144:145], %[wp1] offset:36864\n" \
    "v_mfma_f32_16x16x32_bf16 v[74:77], v[118:121], v[146:149], v[74:77]\n" \
    "ds_read_b64_tr_b16 v[146:147], %[wp2] offset:34816\n" \
    "ds_read_b64_tr_b16 v[148:149], %[wp2] offset:36864\n" \
    "v_mfma_f32_16x16x32_bf16 v[94:97], v[150:153], v[118:121], v[94:97]\n" \
    "ds_read_b64_tr_b16 v[150:151], %[wp3] offset:34816\n" \
    "ds_read_b64_tr_b16 v[152:153], %[wp3] offset:36864\n" \
    "v_mfma_f32_16x16x32_bf16 v[34:37], v[138:141], v[118:121], v[34:37]\n" \
    "ds_read_b128 v[118:121], %[ap1] offset:61440\n" \
    "ds_read_b64_tr_b16 v[138:139], %[wp0] offset:34816\n" \
    "ds_read_b64_tr_b16 v[140:141], %[wp0] offset:36864\n" \
    "s_waitcnt lgkmcnt(11)\n" \
    "v_mfma_f32_16x16x32_bf16 v[54:57], v[126:129], v[114:117], v[54:57]\n" \
    "v_add_u32 %[wp0], %[tabc], %[l16]\n" \
    "v_cndmask_b32_e64 %[wp2], 0, -1, s[68:69]\n" \
    "v_mfma_f32_16x16x32_bf16 v[74:77], v[114:117], v[130:133], v[74:77]\n" \
    "v_add_u32 %[wp1], %[hxc], %[l16]\n" \
    "v_lshrrev_b32 %[wp3], 2, %[l16]\n" \
    "v_mfma_f32_16x16x32_bf16 v[94:97], v[134:137], v[114:117], v[94:97]\n" \
    "v_add_u32 %[wp3], %[hxd], %[wp3]\n" \
    "v_mfma_f32_16x16x32_bf16 v[34:37], v[122:125], v[114:117], v[34:37]\n" \
    "s_waitcnt lgkmcnt(10)\n" \
    "v_mfma_f32_16x16x32_bf16 v[50:53], v[126:129], v[110:113], v[50:53]\n" \
    "v_mfma_f32_16x16x32_bf16 v[70:73], v[110:113], v[130:133], v[70:73]\n" \
    "v_mfma_f32_16x16x32_bf16 v[90:93], v[134:137], v[110:113], v[90:93]\n" \
    "v_mfma_f32_16x16x32_bf16 v[30:33], v[122:125], v[110:113], v[30:33]\n" \
    "ds_read_b128 v[110:113], %[ap1] offset:59392\n" \
    "s_waitcnt lgkmcnt(3)\n" \
    "v_mfma_f32_16x16x32_bf16 v[54:57], v[142:145], v[118:121], v[54:57]\n" \
    "v_cndmask_b32_e64 v154, %[zr], %[vxb], s[60:61]\n" \
    "v_cndmask_b32_e64 v155, %[zr], %[vxb], s[62:63]\n" \
    "v_mfma_f32_16x16x32_bf16 v[74:77], v[118:121], v[146:149], v[74:77]\n" \
    "v_cndmask_b32_e64 v156, %[zr], %[vxb], s[64:65]\n" \
    "v_cndmask_b32_e64 v157, %[zr], %[vxb], s[66:67]\n" \
    "v_mfma_f32_16x16x32_bf16 v[94:97], v[150:153], v[118:121], v[94:97]\n" \
    "s_waitcnt lgkmcnt(1)\n" \
    "v_mfma_f32_16x16x32_bf16 v[34:37], v[138:141], v[118:121], v[34:37]\n" \
    "v_mfma_f32_16x16x32_bf16 v[46:49], v[126:129], v[106:109], v[46:49]\n" \
    "v_mfma_f32_16x16x32_bf16 v[66:69], v[106:109], v[130:133], v[66:69]\n" \
    "v_cvt_pk_bf16_f32 v54, v54, v55\n" \
    "v_cvt_pk_bf16_f32 v55, v56, v57\n" \
    "v_mfma_f32_16x16x32_bf16 v[86:89], v[134:137], v[106:109], v[86:89]\n" \
    "v_cvt_pk_bf16_f32 v120, v74, v75\n" \
    "v_cvt_pk_bf16_f32 v121, v76, v77\n" \
    "v_mfma_f32_16x16x32_bf16 v[26:29], v[122:125], v[106:109], v[26:29]\n" \
    "ds_read_b128 v[106:109], %[ap1] offset:57344\n" \
    "v_cvt_pk_bf16_f32 v94, v94, v95\n" \
    "v_cvt_pk_bf16_f32 v56, v96, v97\n" \
    "ds_write2_b32 %[scw], v94, v56 offset0:128 offset1:129\n" \
    "s_waitcnt lgkmcnt(2)\n" \
    "v_mfma_f32_16x16x32_bf16 v[50:53], v[142:145], v[110:113], v[50:53]\n" \
    "v_permlane16_swap_b32 v94, v56\n" \
    "v_mov_b32 v161, 1\n" \
    "ds_write_b64 %[wp1], v[54:55]\n" \
    "ds_write_b64 %[wp1], v[120:121] offset:8\n" \
    "ds_write_b32 %[wp3], v56\n" \
    "ds_write_b32 %[fr], v161 offset:144\n" \
    "v_mfma_f32_16x16x32_bf16 v[70:73], v[110:113], v[146:149], v[70:73]\n" \
    "v_cvt_pk_bf16_f32 v34, v34, v35\n" \
    "v_cvt_pk_bf16_f32 v35, v36, v37\n" \
    "v_mfma_f32_16x16x32_bf16 v[90:93], v[150:153], v[110:113], v[90:93]\n" \
    "v_and_b32 v36, v94, %[wp2]\n" \
    "v_mov_b32 v37, 0\n" \
    "v_mov_b32 v57, 0\n" \
    "v_mfma_f32_16x16x32_bf16 v[30:33], v[138:141], v[110:113], v[30:33]\n" \
    "v_mfma_f32_16x16x32_bf16 v[42:45], v[126:129], v[102:105], v[42:45]\n" \
    "v_mfma_f32_16x16x32_bf16 v[62:65], v[102:105], v[130:133], v[62:65]\n" \
    "v_cvt_pk_bf16_f32 v50, v50, v51\n" \
    "v_cvt_pk_bf16_f32 v51, v52, v53\n" \
    "v_mfma_f32_16x16x32_bf16 v[82:85], v[134:137], v[102:105], v[82:85]\n" \
    "v_cvt_pk_bf16_f32 v118, v70, v71\n" \
    "v_cvt_pk_bf16_f32 v119, v72, v73\n" \
    "v_mfma_f32_16x16x32_bf16 v[22:25], v[122:125], v[102:105], v[22:25]\n" \
    "ds_read_b128 v[102:105], %[ap1] offset:55296\n" \
    "v_cvt_pk_bf16_f32 v90, v90, v91\n" \
    "v_cvt_pk_bf16_f32 v52, v92, v93\n" \
    "ds_write2_b32 %[scw], v90, v52 offset0:96 offset1:97\n" \
    "s_waitcnt lgkmcnt(7)\n" \
    "v_mfma_f32_16x16x32_bf16 v[46:49], v[142:145], v[106:109], v[46:49]\n" \
    "v_permlane16_swap_b32 v90, v52\n" \
    "v_mfma_f32_16x16x32_bf16 v[66:69], v[106:109], v[146:149], v[66:69]\n" \
    "v_cvt_pk_bf16_f32 v30, v30, v31\n" \
    "v_cvt_pk_bf16_f32 v31, v32, v33\n" \
    "v_mfma_f32_16x16x32_bf16 v[86:89], v[150:153], v[106:109], v[86:89]\n" \
    "v_and_b32 v32, v90, %[wp2]\n" \
    "v_mov_b32 v33, 0\n" \
    "v_mov_b32 v53, 0\n" \
    "v_mfma_f32_16x16x32_bf16 v[26:29], v[138:141], v[106:109], v[26:29]\n" \
    "v_mfma_f32_16x16x32_bf16 v[38:41], v[126:129], v[98:101], v[38:41]\n" \
    "v_mfma_f32_16x16x32_bf16 v[58:61], v[98:101], v[130:133], v[58:61]\n" \
    "v_cvt_pk_bf16_f32 v46, v46, v47\n" \
    "v_cvt_pk_bf16_f32 v47, v48, v49\n" \
    "v_mfma_f32_16x16x32_bf16 v[78:81], v[134:137], v[98:101], v[78:81]\n" \
    "ds_read_b128 v[98:101], %[ap1] offset:53248\n" \
    "v_mov_b32 v161, 18\n" \
    "ds_write_b32 %[fr], v161\n" \
    "ds_read_b128 v[126:129], %[wp0] offset:1024\n" \
    "ds_read_b128 v[122:125], %[wp0]\n" \
    "ds_read_b128 v[130:133], %[wp0] offset:2048\n" \
    "v_cvt_pk_bf16_f32 v116, v66, v67\n" \
    "v_cvt_pk_bf16_f32 v117, v68, v69\n" \
    "s_waitcnt lgkmcnt(6)\n" \
    "v_mfma_f32_16x16x32_bf16 v[42:45], v[142:145], v[102:105], v[42:45]\n" \
    "v_cvt_pk_bf16_f32 v86, v86, v87\n" \
    "v_cvt_pk_bf16_f32 v48, v88, v89\n" \
    "ds_write2_b32 %[scw], v86, v48 offset0:64 offset1:65\n" \
    "v_mfma_f32_16x16x32_bf16 v[62:65], v[102:105], v[146:149], v[62:65]\n" \
    "v_permlane16_swap_b32 v86, v48\n" \
    "v_mfma_f32_16x16x32_bf16 v[82:85], v[150:153], v[102:105], v[82:85]\n" \
    "v_cvt_pk_bf16_f32 v26, v26, v27\n" \
    "v_cvt_pk_bf16_f32 v27, v28, v29\n" \
    "v_mfma_f32_16x16x32_bf16 v[22:25], v[138:141], v[102:105], v[22:25]\n" \
    "v_and_b32 v28, v86, %[wp2]\n" \
    "v_mov_b32 v29, 0\n" \
    "v_mov_b32 v49, 0\n" \
    "s_waitcnt lgkmcnt(5)\n" \
    "v_mfma_f32_16x16x32_bf16 v[38:41], v[142:145], v[98:101], v[38:41]\n" \
    "v_mfma_f32_16x16x32_bf16 v[58:61], v[98:101], v[146:149], v[58:61]\n" \
    "v_cvt_pk_bf16_f32 v42, v42, v43\n" \
    "v_cvt_pk_bf16_f32 v43, v44, v45\n" \
    "v_mfma_f32_16x16x32_bf16 v[78:81], v[150:153], v[98:101], v[78:81]\n" \
    "ds_read_b32 v159, %[zr] offset:1332\n" \
    "s_setprio 0\n" \
    "s_waitcnt lgkmcnt(4)\n" \
    "v_mfma_f32_16x16x32_bf16 v[74:77], v[54:57], v[34:37], v[126:129]\n" \
    "v_cvt_pk_bf16_f32 v82, v82, v83\n" \
    "v_cvt_pk_bf16_f32 v44, v84, v85\n" \
    "ds_write2_b32 %[scw], v82, v44 offset0:32 offset1:33\n" \
    "s_waitcnt lgkmcnt(4)\n" \
    "v_mfma_f32_16x16x32_bf16 v[94:97], v[50:53], v[34:37], v[122:125]\n" \
    "v_cvt_pk_bf16_f32 v114, v62, v63\n" \
    "v_cvt_pk_bf16_f32 v115, v64, v65\n" \
    "v_permlane16_swap_b32 v82, v44\n" \
    "v_mfma_f32_16x16x32_bf16 v[70:73], v[50:53], v[30:33], v[126:129]\n" \
    "v_cvt_pk_bf16_f32 v22, v22, v23\n" \
    "v_cvt_pk_bf16_f32 v23, v24, v25\n" \
    "v_and_b32 v24, v82, %[wp2]\n" \
    "v_mov_b32 v25, 0\n" \
    "v_mov_b32 v45, 0\n" \
    "v_mfma_f32_16x16x32_bf16 v[90:93], v[46:49], v[30:33], v[122:125]\n" \
    "v_cvt_pk_bf16_f32 v38, v38, v39\n" \
    "v_cvt_pk_bf16_f32 v39, v40, v41\n" \
    "v_cvt_pk_bf16_f32 v78, v78, v79\n" \
    "v_cvt_pk_bf16_f32 v40, v80, v81\n" \
    "ds_write2_b32 %[scw], v78, v40 offset0:0 offset1:1\n" \
    "v_mfma_f32_16x16x32_bf16 v[66:69], v[46:49], v[26:29], v[126:129]\n" \
    "v_mov_b32 v41, 0\n" \
    "v_permlane16_swap_b32 v78, v40\n" \
    "v_maximum3_f32 v144, v74, v75, v75\n" \
    "v_maximum3_f32 v145, v76, v77, v77\n" \
    "v_mfma_f32_16x16x32_bf16 v[86:89], v[42:45], v[26:29], v[122:125]\n" \
    "v_cvt_pk_bf16_f32 v112, v58, v59\n" \
    "v_cvt_pk_bf16_f32 v113, v60, v61\n" \
    "ds_read_b64_tr_b16 v[106:107], v157 offset:384\n" \
    "ds_read_b64_tr_b16 v[108:109], v157 offset:512\n" \
    "v_mfma_f32_16x16x32_bf16 v[62:65], v[42:45], v[22:25], v[126:129]\n" \
    "v_maximum3_f32 v144, v94, v95, v144\n" \
    "v_maximum3_f32 v145, v96, v97, v145\n" \
    "ds_read_b64_tr_b16 v[102:103], v156 offset:256\n" \
    "ds_read_b64_tr_b16 v[104:105], v156 offset:384\n" \
    "s_cmp_eq_u32 %[rb2], 0\n" \
    "s_cbranch_scc1 Lz_%=\n" \
    "v_mfma_f32_16x16x32_bf16 v[82:85], v[38:41], v[22:25], v[122:125]\n" \
    "s_branch Ly_%=\n" \
    "Lz_%=:\n" \
    "s_nop 7\n" \
    "v_mov_b32 v82, 0xff61b1e6\n" \
    "v_mov_b32 v83, 0xff61b1e6\n" \
    "v_mov_b32 v84, 0xff61b1e6\n" \
    "v_mov_b32 v85, 0xff61b1e6\n" \
    "Ly_%=:\n" \
    "v_maximum3_f32 v142, v70, v71, v71\n" \
    "v_maximum3_f32 v143, v72, v73, v73\n" \
    "ds_read_b64_tr_b16 v[98:99], v155 offset:128\n" \
    "ds_read_b64_tr_b16 v[100:101], v155 offset:256\n" \
    "v_maximum3_f32 v142, v90, v91, v142\n" \
    "v_maximum3_f32 v143, v92, v93, v143\n" \
    "ds_read_b64_tr_b16 v[134:135], v154 offset:0\n" \
    "ds_read_b64_tr_b16 v[136:137], v154 offset:128\n" \
    "v_maximum3_f32 v149, v144, v145, v145\n" \
    "v_maximum3_f32 v140, v66, v67, v67\n" \
    "v_maximum3_f32 v141, v68, v69, v69\n" \
    "v_maximum3_f32 v148, v142, v143, v143\n" \
    "v_maximum3_f32 v140, v86, v87, v140\n" \
    "v_maximum3_f32 v141, v88, v89, v141\n" \
    "v_maximum3_f32 v138, v62, v63, v63\n" \
    "v_maximum3_f32 v139, v64, v65, v65\n" \
    "v_maximum3_f32 v147, v140, v141, v141\n" \
    "v_maximum3_f32 v138, v82, v83, v138\n" \
    "v_maximum3_f32 v139, v84, v85, v139\n" \
    "v_maximum3_f32 v146, v138, v139, v139\n" \
    "s_waitcnt lgkmcnt(8)\n" \
    "s_branch Lend_%=\n" \
    "Llate_%=:\n" \
    "s_barrier\n" \
    "ds_read_b64_tr_b16 v[126:127], %[wp1] offset:0\n" \
    "ds_read_b64_tr_b16 v[128:129], %[wp1] offset:2048\n" \
    "ds_read_b128 v[102:105], %[ap0] offset:55296\n" \
    "ds_read_b64_tr_b16 v[130:131], %[wp2] offset:0\n" \
    "ds_read_b64_tr_b16 v[132:133], %[wp2] offset:2048\n" \
    "ds_read_b64_tr_b16 v[134:135], %[wp3] offset:0\n" \
    "ds_read_b64_tr_b16 v[136:137], %[wp3] offset:2048\n" \
    "ds_read_b64_tr_b16 v[122:123], %[wp0] offset:0\n" \
    "ds_read_b64_tr_b16 v[124:125], %[wp0] offset:2048\n" \
    "ds_read_b128 v[106:109], %[ap0] offset:57344\n" \
    "ds_read_b128 v[110:113], %[ap0] offset:59392\n" \
    "ds_read_b128 v[114:117], %[ap0] offset:61440\n" \
    "s_waitcnt lgkmcnt(9)\n" \
    "v_mfma_f32_16x16x32_bf16 v[42:45], v[126:129], v[102:105], 0\n" \
    "ds_read_b64_tr_b16 v[142:143], %[wp1] offset:34816\n" \
    "s_waitcnt lgkmcnt(8)\n" \
    "v_mfma_f32_16x16x32_bf16 v[62:65], v[102:105], v[130:133], 0\n" \
    "ds_read_b64_tr_b16 v[144:145], %[wp1] offset:36864\n" \
    "s_waitcnt lgkmcnt(7)\n" \
    "v_mfma_f32_16x16x32_bf16 v[82:85], v[134:137], v[102:105], 0\n" \
    "ds_read_b64_tr_b16 v[146:147], %[wp2] offset:34816\n" \
    "s_waitcnt lgkmcnt(6)\n" \
    "v_mfma_f32_16x16x32_bf16 v[22:25], v[122:125], v[102:105], 0\n" \
    "ds_read_b128 v[102:105], %[ap1] offset:55296\n" \
    "s_waitcnt lgkmcnt(6)\n" \
    "v_mfma_f32_16x16x32_bf16 v[46:49], v[126:129], v[106:109], 0\n" \
    "ds_read_b64_tr_b16 v[148:149], %[wp2] offset:36864\n" \
    "v_mfma_f32_16x16x32_bf16 v[66:69], v[106:109], v[130:133], 0\n" \
    "ds_read_b64_tr_b16 v[150:151], %[wp3] offset:34816\n" \
    "v_mfma_f32_16x16x32_bf16 v[86:89], v[134:137], v[106:109], 0\n" \
    "ds_read_b64_tr_b16 v[152:153], %[wp3] offset:36864\n" \
    "v_mfma_f32_16x16x32_bf16 v[26:29], v[122:125], v[106:109], 0\n" \
    "ds_read_b128 v[106:109], %[ap1] offset:57344\n" \
    "s_waitcnt lgkmcnt(9)\n" \
    "v_mfma_f32_16x16x32_bf16 v[50:53], v[126:129], v[110:113], 0\n" \
    "ds_read_b64_tr_b16 v[138:139], %[wp0] offset:34816\n" \
    "v_mfma_f32_16x16x32_bf16 v[70:73], v[110:113], v[130:133], 0\n" \
    "ds_read_b64_tr_b16 v[140:141], %[wp0] offset:36864\n" \
    "v_mfma_f32_16x16x32_bf16 v[90:93], v[134:137], v[110:113], 0\n" \
    "ds_read_b128 v[118:121], %[ap1] offset:61440\n" \
    "v_mfma_f32_16x16x32_bf16 v[30:33], v[122:125], v[110:113], 0\n" \
    "ds_read_b128 v[110:113], %[ap1] offset:59392\n" \
    "s_waitcnt lgkmcnt(12)\n" \
    "v_mfma_f32_16x16x32_bf16 v[54:57], v[126:129], v[114:117], 0\n" \
    "s_barrier\n" \
    "v_mfma_f32_16x16x32_bf16 v[74:77], v[114:117], v[130:133], 0\n" \
    "v_mfma_f32_16x16x32_bf16 v[94:97], v[134:137], v[114:117], 0\n" \
    "v_mfma_f32_16x16x32_bf16 v[34:37], v[122:125], v[114:117], 0\n" \
    "s_waitcnt lgkmcnt(8)\n" \
    "v_mfma_f32_16x16x32_bf16 v[42:45], v[142:145], v[102:105], v[42:45]\n" \
    "ds_read_b64_tr_b16 v[126:127], %[wp1] offset:51200\n" \
    "s_waitcnt lgkmcnt(8)\n" \
    "v_mfma_f32_16x16x32_bf16 v[62:65], v[102:105], v[146:149], v[62:65]\n" \
    "ds_read_b64_tr_b16 v[128:129], %[wp1] offset:53248\n" \
    "s_waitcnt lgkmcnt(7)\n" \
    "v_mfma_f32_16x16x32_bf16 v[82:85], v[150:153], v[102:105], v[82:85]\n" \
    "ds_read_b64_tr_b16 v[130:131], %[wp2] offset:51200\n" \
    "s_waitcnt lgkmcnt(5)\n" \
    "v_mfma_f32_16x16x32_bf16 v[22:25], v[138:141], v[102:105], v[22:25]\n" \
    "ds_read_b128 v[102:105], %[ap0] offset:2048\n" \
    "v_mfma_f32_16x16x32_bf16 v[46:49], v[142:145], v[106:109], v[46:49]\n" \
    "ds_read_b64_tr_b16 v[132:133], %[wp2] offset:53248\n" \
    "v_mfma_f32_16x16x32_bf16 v[66:69], v[106:109], v[146:149], v[66:69]\n" \
    "ds_read_b64_tr_b16 v[134:135], %[wp3] offset:51200\n" \
    "v_mfma_f32_16x16x32_bf16 v[86:89], v[150:153], v[106:109], v[86:89]\n" \
    "ds_read_b64_tr_b16 v[136:137], %[wp3] offset:53248\n" \
    "v_mfma_f32_16x16x32_bf16 v[26:29], v[138:141], v[106:109], v[26:29]\n" \
    "ds_read_b128 v[106:109], %[ap0] offset:4096\n" \
    "s_waitcnt lgkmcnt(8)\n" \
    "v_mfma_f32_16x16x32_bf16 v[50:53], v[142:145], v[110:113], v[50:53]\n" \
    "ds_read_b64_tr_b16 v[122:123], %[wp0] offset:51200\n" \
    "v_mfma_f32_16x16x32_bf16 v[70:73], v[110:113], v[146:149], v[70:73]\n" \
    "ds_read_b64_tr_b16 v[124:125], %[wp0] offset:53248\n" \
    "v_mfma_f32_16x16x32_bf16 v[90:93], v[150:153], v[110:113], v[90:93]\n" \
    "ds_read_b128 v[114:117], %[ap0] offset:8192\n" \
    "v_mfma_f32_16x16x32_bf16 v[30:33], v[138:141], v[110:113], v[30:33]\n" \
    "ds_read_b128 v[110:113], %[ap0] offset:6144\n" \
    "v_mfma_f32_16x16x32_bf16 v[54:57], v[142:145], v[118:121], v[54:57]\n" \
    "s_barrier\n" \
    "v_mfma_f32_16x16x32_bf16 v[74:77], v[118:121], v[146:149], v[74:77]\n" \
    "v_mfma_f32_16x16x32_bf16 v[94:97], v[150:153], v[118:121], v[94:97]\n" \
    "v_mfma_f32_16x16x32_bf16 v[34:37], v[138:141], v[118:121], v[34:37]\n" \
    "s_waitcnt lgkmcnt(8)\n" \
    "v_mfma_f32_16x16x32_bf16 v[42:45], v[126:129], v[102:105], v[42:45]\n" \
    "ds_read_b64_tr_b16 v[142:143], v155 offset:0\n" \
    "s_waitcnt lgkmcnt(8)\n" \
    "v_mfma_f32_16x16x32_bf16 v[62:65], v[102:105], v[130:133], v[62:65]\n" \
    "ds_read_b64_tr_b16 v[144:145], v155 offset:2048\n" \
    "s_waitcnt lgkmcnt(7)\n" \
    "v_mfma_f32_16x16x32_bf16 v[82:85], v[134:137], v[102:105], v[82:85]\n" \
    "ds_read_b64_tr_b16 v[146:147], v156 offset:0\n" \
    "s_waitcnt lgkmcnt(5)\n" \
    "v_mfma_f32_16x16x32_bf16 v[22:25], v[122:125], v[102:105], v[22:25]\n" \
    "ds_read_b128 v[102:105], %[ap1] offset:2048\n" \
    "v_mfma_f32_16x16x32_bf16 v[46:49], v[126:129], v[106:109], v[46:49]\n" \
    "ds_read_b64_tr_b16 v[148:149], v156 offset:2048\n" \
    "v_mfma_f32_16x16x32_bf16 v[66:69], v[106:109], v[130:133], v[66:69]\n" \
    "ds_read_b64_tr_b16 v[150:151], v157 offset:0\n" \
    "v_mfma_f32_16x16x32_bf16 v[86:89], v[134:137], v[106:109], v[86:89]\n" \
    "ds_read_b64_tr_b16 v[152:153], v157 offset:2048\n" \
    "v_mfma_f32_16x16x32_bf16 v[26:29], v[122:125], v[106:109], v[26:29]\n" \
    "ds_read_b128 v[106:109], %[ap1] offset:4096\n" \
    "s_waitcnt lgkmcnt(8)\n" \
    "v_mfma_f32_16x16x32_bf16 v[50:53], v[126:129], v[110:113], v[50:53]\n" \
    "ds_read_b64_tr_b16 v[138:139], v154 offset:0\n" \
    "v_mfma_f32_16x16x32_bf16 v[70:73], v[110:113], v[130:133], v[70:73]\n" \
    "ds_read_b64_tr_b16 v[140:141], v154 offset:2048\n" \
    "v_mfma_f32_16x16x32_bf16 v[90:93], v[134:137], v[110:113], v[90:93]\n" \
    "ds_read_b128 v[118:121], %[ap1] offset:8192\n" \
    "v_mfma_f32_16x16x32_bf16 v[30:33], v[122:125], v[110:113], v[30:33]\n" \
    "ds_read_b128 v[110:113], %[ap1] offset:6144\n" \
    "v_mfma_f32_16x16x32_bf16 v[54:57], v[126:129], v[114:117], v[54:57]\n" \
    "s_barrier\n" \
    "v_mfma_f32_16x16x32_bf16 v[74:77], v[114:117], v[130:133], v[74:77]\n" \
    "v_mfma_f32_16x16x32_bf16 v[94:97], v[134:137], v[114:117], v[94:97]\n" \
    "v_mfma_f32_16x16x32_bf16 v[34:37], v[122:125], v[114:117], v[34:37]\n" \
    "s_waitcnt lgkmcnt(8)\n" \
    "v_mfma_f32_16x16x32_bf16 v[42:45], v[142:145], v[102:105], v[42:45]\n" \
    "ds_read_b64_tr_b16 v[126:127], v155 offset:16384\n" \
    "s_waitcnt lgkmcnt(8)\n" \
    "v_mfma_f32_16x16x32_bf16 v[62:65], v[102:105], v[146:149], v[62:65]\n" \
    "ds_read_b64_tr_b16 v[128:129], v155 offset:18432\n" \
    "s_waitcnt lgkmcnt(7)\n" \
    "v_mfma_f32_16x16x32_bf16 v[82:85], v[150:153], v[102:105], v[82:85]\n" \
    "ds_read_b64_tr_b16 v[130:131], v156 offset:16384\n" \
    "s_waitcnt lgkmcnt(5)\n" \
    "v_mfma_f32_16x16x32_bf16 v[22:25], v[138:141], v[102:105], v[22:25]\n" \
    "ds_read_b128 v[102:105], %[ap0] offset:20480\n" \
    "v_mfma_f32_16x16x32_bf16 v[46:49], v[142:145], v[106:109], v[46:49]\n" \
    "ds_read_b64_tr_b16 v[132:133], v156 offset:18432\n" \
    "v_mfma_f32_16x16x32_bf16 v[66:69], v[106:109], v[146:149], v[66:69]\n" \
    "ds_read_b64_tr_b16 v[134:135], v157 offset:16384\n" \
    "v_mfma_f32_16x16x32_bf16 v[86:89], v[150:153], v[106:109], v[86:89]\n" \
    "ds_read_b64_tr_b16 v[136:137], v157 offset:18432\n" \
    "v_mfma_f32_16x16x32_bf16 v[26:29], v[138:141], v[106:109], v[26:29]\n" \
    "ds_read_b128 v[106:109], %[ap0] offset:22528\n" \
    "s_waitcnt lgkmcnt(8)\n" \
    "v_mfma_f32_16x16x32_bf16 v[50:53], v[142:145], v[110:113], v[50:53]\n" \
    "ds_read_b64_tr_b16 v[122:123], v154 offset:16384\n" \
    "v_mfma_f32_16x16x32_bf16 v[70:73], v[110:113], v[146:149], v[70:73]\n" \
    "ds_read_b64_tr_b16 v[124:125], v154 offset:18432\n" \
    "v_mfma_f32_16x16x32_bf16 v[90:93], v[150:153], v[110:113], v[90:93]\n" \
    "ds_read_b128 v[114:117], %[ap0] offset:26624\n" \
    "v_mfma_f32_16x16x32_bf16 v[30:33], v[138:141], v[110:113], v[30:33]\n" \
    "ds_read_b128 v[110:113], %[ap0] offset:24576\n" \
    "v_mfma_f32_16x16x32_bf16 v[54:57], v[142:145], v[118:121], v[54:57]\n" \
    "s_barrier\n" \
    "v_mfma_f32_16x16x32_bf16 v[74:77], v[118:121], v[146:149], v[74:77]\n" \
    "v_mfma_f32_16x16x32_bf16 v[94:97], v[150:153], v[118:121], v[94:97]\n" \
    "v_mfma_f32_16x16x32_bf16 v[34:37], v[138:141], v[118:121], v[34:37]\n" \
    "s_waitcnt lgkmcnt(8)\n" \
    "v_mfma_f32_16x16x32_bf16 v[42:45], v[126:129], v[102:105], v[42:45]\n" \
    "ds_read_b64_tr_b16 v[142:143], v155 offset:32768\n" \
    "s_waitcnt lgkmcnt(8)\n" \
    "v_mfma_f32_16x16x32_bf16 v[62:65], v[102:105], v[130:133], v[62:65]\n" \
    "ds_read_b64_tr_b16 v[144:145], v155 offset:34816\n" \
    "s_waitcnt lgkmcnt(7)\n" \
    "v_mfma_f32_16x16x32_bf16 v[82:85], v[134:137], v[102:105], v[82:85]\n" \
    "ds_read_b64_tr_b16 v[146:147], v156 offset:32768\n" \
    "s_waitcnt lgkmcnt(5)\n" \
    "v_mfma_f32_16x16x32_bf16 v[22:25], v[122:125], v[102:105], v[22:25]\n" \
    "ds_read_b128 v[102:105], %[ap1] offset:20480\n" \
    "v_mfma_f32_16x16x32_bf16 v[46:49], v[126:129], v[106:109], v[46:49]\n" \
    "ds_read_b64_tr_b16 v[148:149], v156 offset:34816\n" \
    "v_mfma_f32_16x16x32_bf16 v[66:69], v[106:109], v[130:133], v[66:69]\n" \
    "ds_read_b64_tr_b16 v[150:151], v157 offset:32768\n" \
    "v_mfma_f32_16x16x32_bf16 v[86:89], v[134:137], v[106:109], v[86:89]\n" \
    "ds_read_b64_tr_b16 v[152:153], v157 offset:34816\n" \
    "v_mfma_f32_16x16x32_bf16 v[26:29], v[122:125], v[106:109], v[26:29]\n" \
    "ds_read_b128 v[106:109], %[ap1] offset:22528\n" \
    "s_waitcnt lgkmcnt(8)\n" \
    "v_mfma_f32_16x16x32_bf16 v[50:53], v[126:129], v[110:113], v[50:53]\n" \
    "ds_read_b64_tr_b16 v[138:139], v154 offset:32768\n" \
    "v_mfma_f32_16x16x32_bf16 v[70:73], v[110:113], v[130:133], v[70:73]\n" \
    "ds_read_b64_tr_b16 v[140:141], v154 offset:34816\n" \
    "v_mfma_f32_16x16x32_bf16 v[90:93], v[134:137], v[110:113], v[90:93]\n" \
    "ds_read_b128 v[118:121], %[ap1] offset:26624\n" \
    "v_mfma_f32_16x16x32_bf16 v[30:33], v[122:125], v[110:113], v[30:33]\n" \
    "ds_read_b128 v[110:113], %[ap1] offset:24576\n" \
    "v_mfma_f32_16x16x32_bf16 v[54:57], v[126:129], v[114:117], v[54:57]\n" \
    "s_barrier\n" \
    "v_mfma_f32_16x16x32_bf16 v[74:77], v[114:117], v[130:133], v[74:77]\n" \
    "v_mfma_f32_16x16x32_bf16 v[94:97], v[134:137], v[114:117], v[94:97]\n" \
    "v_mfma_f32_16x16x32_bf16 v[34:37], v[122:125], v[114:117], v[34:37]\n" \
    "s_waitcnt lgkmcnt(8)\n" \
    "v_mfma_f32_16x16x32_bf16 v[42:45], v[142:145], v[102:105], v[42:45]\n" \
    "ds_read_b64_tr_b16 v[126:127], %[wp1] offset:0\n" \
    "s_waitcnt lgkmcnt(8)\n" \
    "v_mfma_f32_16x16x32_bf16 v[62:65], v[102:105], v[146:149], v[62:65]\n" \
    "ds_read_b64_tr_b16 v[128:129], %[wp1] offset:2048\n" \
    "s_waitcnt lgkmcnt(7)\n" \
    "v_mfma_f32_16x16x32_bf16 v[82:85], v[150:153], v[102:105], v[82:85]\n" \
    "ds_read_b64_tr_b16 v[130:131], %[wp2] offset:0\n" \
    "s_waitcnt lgkmcnt(5)\n" \
    "v_mfma_f32_16x16x32_bf16 v[22:25], v[138:141], v[102:105], v[22:25]\n" \
    "ds_read_b128 v[102:105], %[ap0] offset:55296\n" \
    "v_mfma_f32_16x16x32_bf16 v[46:49], v[142:145], v[106:109], v[46:49]\n" \
    "ds_read_b64_tr_b16 v[132:133], %[wp2] offset:2048\n" \
    "v_mfma_f32_16x16x32_bf16 v[66:69], v[106:109], v[146:149], v[66:69]\n" \
    "ds_read_b64_tr_b16 v[134:135], %[wp3] offset:0\n" \
    "v_mfma_f32_16x16x32_bf16 v[86:89], v[150:153], v[106:109], v[86:89]\n" \
    "ds_read_b64_tr_b16 v[136:137], %[wp3] offset:2048\n" \
    "v_mfma_f32_16x16x32_bf16 v[26:29], v[138:141], v[106:109], v[26:29]\n" \
    "ds_read_b128 v[106:109], %[ap0] offset:57344\n" \
    "s_waitcnt lgkmcnt(8)\n" \
    "v_mfma_f32_16x16x32_bf16 v[50:53], v[142:145], v[110:113], v[50:53]\n" \
    "ds_read_b64_tr_b16 v[122:123], %[wp0] offset:0\n" \
    "v_mfma_f32_16x16x32_bf16 v[70:73], v[110:113], v[146:149], v[70:73]\n" \
    "ds_read_b64_tr_b16 v[124:125], %[wp0] offset:2048\n" \
    "v_mfma_f32_16x16x32_bf16 v[90:93], v[150:153], v[110:113], v[90:93]\n" \
    "ds_read_b128 v[114:117], %[ap0] offset:61440\n" \
    "v_mfma_f32_16x16x32_bf16 v[30:33], v[138:141], v[110:113], v[30:33]\n" \
    "ds_read_b128 v[110:113], %[ap0] offset:59392\n" \
    "v_mfma_f32_16x16x32_bf16 v[54:57], v[142:145], v[118:121], v[54:57]\n" \
    "s_barrier\n" \
    "v_mfma_f32_16x16x32_bf16 v[74:77], v[118:121], v[146:149], v[74:77]\n" \
    "v_mfma_f32_16x16x32_bf16 v[94:97], v[150:153], v[118:121], v[94:97]\n" \
    "v_mfma_f32_16x16x32_bf16 v[34:37], v[138:141], v[118:121], v[34:37]\n" \
    "s_waitcnt lgkmcnt(8)\n" \
    "v_mfma_f32_16x16x32_bf16 v[42:45], v[126:129], v[102:105], v[42:45]\n" \
    "ds_read_b64_tr_b16 v[142:143], %[wp1] offset:34816\n" \
    "s_waitcnt lgkmcnt(8)\n" \
    "v_mfma_f32_16x16x32_bf16 v[62:65], v[102:105], v[130:133], v[62:65]\n" \
    "ds_read_b64_tr_b16 v[144:145], %[wp1] offset:36864\n" \
    "s_waitcnt lgkmcnt(7)\n" \
    "v_mfma_f32_16x16x32_bf16 v[82:85], v[134:137], v[102:105], v[82:85]\n" \
    "ds_read_b64_tr_b16 v[146:147], %[wp2] offset:34816\n" \
    "s_waitcnt lgkmcnt(5)\n" \
    "v_mfma_f32_16x16x32_bf16 v[22:25], v[122:125], v[102:105], v[22:25]\n" \
    "ds_read_b128 v[102:105], %[ap1] offset:55296\n" \
    "v_mfma_f32_16x16x32_bf16 v[46:49], v[126:129], v[106:109], v[46:49]\n" \
    "ds_read_b64_tr_b16 v[148:149], %[wp2] offset:36864\n" \
    "v_mfma_f32_16x16x32_bf16 v[66:69], v[106:109], v[130:133], v[66:69]\n" \
    "ds_read_b64_tr_b16 v[150:151], %[wp3] offset:34816\n" \
    "v_mfma_f32_16x16x32_bf16 v[86:89], v[134:137], v[106:109], v[86:89]\n" \
    "ds_read_b64_tr_b16 v[152:153], %[wp3] offset:36864\n" \
    "v_mfma_f32_16x16x32_bf16 v[26:29], v[122:125], v[106:109], v[26:29]\n" \
    "ds_read_b128 v[106:109], %[ap1] offset:57344\n" \
    "s_waitcnt lgkmcnt(8)\n" \
    "v_mfma_f32_16x16x32_bf16 v[50:53], v[126:129], v[110:113], v[50:53]\n" \
    "ds_read_b64_tr_b16 v[138:139], %[wp0] offset:34816\n" \
    "v_mfma_f32_16x16x32_bf16 v[70:73], v[110:113], v[130:133], v[70:73]\n" \
    "ds_read_b64_tr_b16 v[140:141], %[wp0] offset:36864\n" \
    "v_mfma_f32_16x16x32_bf16 v[90:93], v[134:137], v[110:113], v[90:93]\n" \
    "ds_read_b128 v[118:121], %[ap1] offset:61440\n" \
    "v_mfma_f32_16x16x32_bf16 v[30:33], v[122:125], v[110:113], v[30:33]\n" \
    "ds_read_b128 v[110:113], %[ap1] offset:59392\n" \
    "v_mfma_f32_16x16x32_bf16 v[54:57], v[126:129], v[114:117], v[54:57]\n" \
    "s_barrier\n" \
    "v_mfma_f32_16x16x32_bf16 v[74:77], v[114:117], v[130:133], v[74:77]\n" \
    "v_mfma_f32_16x16x32_bf16 v[94:97], v[134:137], v[114:117], v[94:97]\n" \
    "v_mfma_f32_16x16x32_bf16 v[34:37], v[122:125], v[114:117], v[34:37]\n" \
    "s_waitcnt lgkmcnt(8)\n" \
    "v_mfma_f32_16x16x32_bf16 v[42:45], v[142:145], v[102:105], v[42:45]\n" \
    "ds_read_b64_tr_b16 v[126:127], %[wp1] offset:51200\n" \
    "s_waitcnt lgkmcnt(8)\n" \
    "v_mfma_f32_16x16x32_bf16 v[62:65], v[102:105], v[146:149], v[62:65]\n" \
    "ds_read_b64_tr_b16 v[128:129], %[wp1] offset:53248\n" \
    "s_waitcnt lgkmcnt(7)\n" \
    "v_mfma_f32_16x16x32_bf16 v[82:85], v[150:153], v[102:105], v[82:85]\n" \
    "ds_read_b64_tr_b16 v[130:131], %[wp2] offset:51200\n" \
    "s_waitcnt lgkmcnt(5)\n" \
    "v_mfma_f32_16x16x32_bf16 v[22:25], v[138:141], v[102:105], v[22:25]\n" \
    "ds_read_b128 v[102:105], %[ap0] offset:2048\n" \
    "v_mfma_f32_16x16x32_bf16 v[46:49], v[142:145], v[106:109], v[46:49]\n" \
    "ds_read_b64_tr_b16 v[132:133], %[wp2] offset:53248\n" \
    "v_mfma_f32_16x16x32_bf16 v[66:69], v[106:109], v[146:149], v[66:69]\n" \
    "ds_read_b64_tr_b16 v[134:135], %[wp3] offset:51200\n" \
    "v_mfma_f32_16x16x32_bf16 v[86:89], v[150:153], v[106:109], v[86:89]\n" \
    "ds_read_b64_tr_b16 v[136:137], %[wp3] offset:53248\n" \
    "v_mfma_f32_16x16x32_bf16 v[26:29], v[138:141], v[106:109], v[26:29]\n" \
    "ds_read_b128 v[106:109], %[ap0] offset:4096\n" \
    "s_waitcnt lgkmcnt(8)\n" \
    "v_mfma_f32_16x16x32_bf16 v[50:53], v[142:145], v[110:113], v[50:53]\n" \
    "ds_read_b64_tr_b16 v[122:123], %[wp0] offset:51200\n" \
    "v_mfma_f32_16x16x32_bf16 v[70:73], v[110:113], v[146:149], v[70:73]\n" \
    "ds_read_b64_tr_b16 v[124:125], %[wp0] offset:53248\n" \
    "v_mfma_f32_16x16x32_bf16 v[90:93], v[150:153], v[110:113], v[90:93]\n" \
    "ds_read_b128 v[114:117], %[ap0] offset:8192\n" \
    "v_mfma_f32_16x16x32_bf16 v[30:33], v[138:141], v[110:113], v[30:33]\n" \
    "ds_read_b128 v[110:113], %[ap0] offset:6144\n" \
    "v_mfma_f32_16x16x32_bf16 v[54:57], v[142:145], v[118:121], v[54:57]\n" \
    "s_barrier\n" \
    "v_mfma_f32_16x16x32_bf16 v[74:77], v[118:121], v[146:149], v[74:77]\n" \
    "v_mfma_f32_16x16x32_bf16 v[94:97], v[150:153], v[118:121], v[94:97]\n" \
    "v_mfma_f32_16x16x32_bf16 v[34:37], v[138:141], v[118:121], v[34:37]\n" \
    "s_waitcnt lgkmcnt(8)\n" \
    "v_mfma_f32_16x16x32_bf16 v[42:45], v[126:129], v[102:105], v[42:45]\n" \
    "ds_read_b64_tr_b16 v[142:143], v155 offset:0\n" \
    "s_waitcnt lgkmcnt(8)\n" \
    "v_mfma_f32_16x16x32_bf16 v[62:65], v[102:105], v[130:133], v[62:65]\n" \
    "ds_read_b64_tr_b16 v[144:145], v155 offset:2048\n" \
    "s_waitcnt lgkmcnt(7)\n" \
    "v_mfma_f32_16x16x32_bf16 v[82:85], v[134:137], v[102:105], v[82:85]\n" \
    "ds_read_b64_tr_b16 v[146:147], v156 offset:0\n" \
    "s_waitcnt lgkmcnt(5)\n" \
    "v_mfma_f32_16x16x32_bf16 v[22:25], v[122:125], v[102:105], v[22:25]\n" \
    "ds_read_b128 v[102:105], %[ap1] offset:2048\n" \
    "v_mfma_f32_16x16x32_bf16 v[46:49], v[126:129], v[106:109], v[46:49]\n" \
    "ds_read_b64_tr_b16 v[148:149], v156 offset:2048\n" \
    "v_mfma_f32_16x16x32_bf16 v[66:69], v[106:109], v[130:133], v[66:69]\n" \
    "ds_read_b64_tr_b16 v[150:151], v157 offset:0\n" \
    "v_mfma_f32_16x16x32_bf16 v[86:89], v[134:137], v[106:109], v[86:89]\n" \
    "ds_read_b64_tr_b16 v[152:153], v157 offset:2048\n" \
    "v_mfma_f32_16x16x32_bf16 v[26:29], v[122:125], v[106:109], v[26:29]\n" \
    "ds_read_b128 v[106:109], %[ap1] offset:4096\n" \
    "s_waitcnt lgkmcnt(8)\n" \
    "v_mfma_f32_16x16x32_bf16 v[50:53], v[126:129], v[110:113], v[50:53]\n" \
    "ds_read_b64_tr_b16 v[138:139], v154 offset:0\n" \
    "v_mfma_f32_16x16x32_bf16 v[70:73], v[110:113], v[130:133], v[70:73]\n" \
    "ds_read_b64_tr_b16 v[140:141], v154 offset:2048\n" \
    "v_mfma_f32_16x16x32_bf16 v[90:93], v[134:137], v[110:113], v[90:93]\n" \
    "ds_read_b128 v[118:121], %[ap1] offset:8192\n" \
    "v_mfma_f32_16x16x32_bf16 v[30:33], v[122:125], v[110:113], v[30:33]\n" \
    "ds_read_b128 v[110:113], %[ap1] offset:6144\n" \
    "v_mfma_f32_16x16x32_bf16 v[54:57], v[126:129], v[114:117], v[54:57]\n" \
    "s_barrier\n" \
    "v_mfma_f32_16x16x32_bf16 v[74:77], v[114:117], v[130:133], v[74:77]\n" \
    "v_mfma_f32_16x16x32_bf16 v[94:97], v[134:137], v[114:117], v[94:97]\n" \
    "v_mfma_f32_16x16x32_bf16 v[34:37], v[122:125], v[114:117], v[34:37]\n" \
    "s_waitcnt lgkmcnt(8)\n" \
    "v_mfma_f32_16x16x32_bf16 v[42:45], v[142:145], v[102:105], v[42:45]\n" \
    "ds_read_b64_tr_b16 v[126:127], v155 offset:16384\n" \
    "s_waitcnt lgkmcnt(8)\n" \
    "v_mfma_f32_16x16x32_bf16 v[62:65], v[102:105], v[146:149], v[62:65]\n" \
    "ds_read_b64_tr_b16 v[128:129], v155 offset:18432\n" \
    "s_waitcnt lgkmcnt(7)\n" \
    "v_mfma_f32_16x16x32_bf16 v[82:85], v[150:153], v[102:105], v[82:85]\n" \
    "ds_read_b64_tr_b16 v[130:131], v156 offset:16384\n" \
    "s_waitcnt lgkmcnt(5)\n" \
    "v_mfma_f32_16x16x32_bf16 v[22:25], v[138:141], v[102:105], v[22:25]\n" \
    "ds_read_b128 v[102:105], %[ap0] offset:20480\n" \
    "v_mfma_f32_16x16x32_bf16 v[46:49], v[142:145], v[106:109], v[46:49]\n" \
    "ds_read_b64_tr_b16 v[132:133], v156 offset:18432\n" \
    "v_mfma_f32_16x16x32_bf16 v[66:69], v[106:109], v[146:149], v[66:69]\n" \
    "ds_read_b64_tr_b16 v[134:135], v157 offset:16384\n" \
    "v_mfma_f32_16x16x32_bf16 v[86:89], v[150:153], v[106:109], v[86:89]\n" \
    "ds_read_b64_tr_b16 v[136:137], v157 offset:18432\n" \
    "v_mfma_f32_16x16x32_bf16 v[26:29], v[138:141], v[106:109], v[26:29]\n" \
    "ds_read_b128 v[106:109], %[ap0] offset:22528\n" \
    "s_waitcnt lgkmcnt(8)\n" \
    "v_mfma_f32_16x16x32_bf16 v[50:53], v[142:145], v[110:113], v[50:53]\n" \
    "ds_read_b64_tr_b16 v[122:123], v154 offset:16384\n" \
    "v_mfma_f32_16x16x32_bf16 v[70:73], v[110:113], v[146:149], v[70:73]\n" \
    "ds_read_b64_tr_b16 v[124:125], v154 offset:18432\n" \
    "v_mfma_f32_16x16x32_bf16 v[90:93], v[150:153], v[110:113], v[90:93]\n" \
    "ds_read_b128 v[114:117], %[ap0] offset:26624\n" \
    "v_mfma_f32_16x16x32_bf16 v[30:33], v[138:141], v[110:113], v[30:33]\n" \
    "ds_read_b128 v[110:113], %[ap0] offset:24576\n" \
    "v_mfma_f32_16x16x32_bf16 v[54:57], v[142:145], v[118:121], v[54:57]\n" \
    "s_barrier\n" \
    "v_mfma_f32_16x16x32_bf16 v[74:77], v[118:121], v[146:149], v[74:77]\n" \
    "v_mfma_f32_16x16x32_bf16 v[94:97], v[150:153], v[118:121], v[94:97]\n" \
    "v_mfma_f32_16x16x32_bf16 v[34:37], v[138:141], v[118:121], v[34:37]\n" \
    "s_waitcnt lgkmcnt(8)\n" \
    "v_mfma_f32_16x16x32_bf16 v[42:45], v[126:129], v[102:105], v[42:45]\n" \
    "ds_read_b64_tr_b16 v[142:143], v155 offset:32768\n" \
    "s_waitcnt lgkmcnt(8)\n" \
    "v_mfma_f32_16x16x32_bf16 v[62:65], v[102:105], v[130:133], v[62:65]\n" \
    "ds_read_b64_tr_b16 v[144:145], v155 offset:34816\n" \
    "s_waitcnt lgkmcnt(7)\n" \
    "v_mfma_f32_16x16x32_bf16 v[82:85], v[134:137], v[102:105], v[82:85]\n" \
    "ds_read_b64_tr_b16 v[146:147], v156 offset:32768\n" \
    "s_waitcnt lgkmcnt(5)\n" \
    "v_mfma_f32_16x16x32_bf16 v[22:25], v[122:125], v[102:105], v[22:25]\n" \
    "ds_read_b128 v[102:105], %[ap1] offset:20480\n" \
    "v_mfma_f32_16x16x32_bf16 v[46:49], v[126:129], v[106:109], v[46:49]\n" \
    "ds_read_b64_tr_b16 v[148:149], v156 offset:34816\n" \
    "v_mfma_f32_16x16x32_bf16 v[66:69], v[106:109], v[130:133], v[66:69]\n" \
    "ds_read_b64_tr_b16 v[150:151], v157 offset:32768\n" \
    "v_mfma_f32_16x16x32_bf16 v[86:89], v[134:137], v[106:109], v[86:89]\n" \
    "ds_read_b64_tr_b16 v[152:153], v157 offset:34816\n" \
    "v_mfma_f32_16x16x32_bf16 v[26:29], v[122:125], v[106:109], v[26:29]\n" \
    "ds_read_b128 v[106:109], %[ap1] offset:22528\n" \
    "s_waitcnt lgkmcnt(8)\n" \
    "v_mfma_f32_16x16x32_bf16 v[50:53], v[126:129], v[110:113], v[50:53]\n" \
    "ds_read_b64_tr_b16 v[138:139], v154 offset:32768\n" \
    "v_mfma_f32_16x16x32_bf16 v[70:73], v[110:113], v[130:133], v[70:73]\n" \
    "ds_read_b64_tr_b16 v[140:141], v154 offset:34816\n" \
    "v_mfma_f32_16x16x32_bf16 v[90:93], v[134:137], v[110:113], v[90:93]\n" \
    "ds_read_b128 v[118:121], %[ap1] offset:26624\n" \
    "v_mfma_f32_16x16x32_bf16 v[30:33], v[122:125], v[110:113], v[30:33]\n" \
    "ds_read_b128 v[110:113], %[ap1] offset:24576\n" \
    "v_mfma_f32_16x16x32_bf16 v[54:57], v[126:129], v[114:117], v[54:57]\n" \
    "s_barrier\n" \
    "v_mfma_f32_16x16x32_bf16 v[74:77], v[114:117], v[130:133], v[74:77]\n" \
    "v_mfma_f32_16x16x32_bf16 v[94:97], v[134:137], v[114:117], v[94:97]\n" \
    "v_mfma_f32_16x16x32_bf16 v[34:37], v[122:125], v[114:117], v[34:37]\n" \
    "s_waitcnt lgkmcnt(8)\n" \
    "v_mfma_f32_16x16x32_bf16 v[42:45], v[142:145], v[102:105], v[42:45]\n" \
    "ds_read_b64_tr_b16 v[126:127], %[wp1] offset:0\n" \
    "s_waitcnt lgkmcnt(8)\n" \
    "v_mfma_f32_16x16x32_bf16 v[62:65], v[102:105], v[146:149], v[62:65]\n" \
    "ds_read_b64_tr_b16 v[128:129], %[wp1] offset:2048\n" \
    "s_waitcnt lgkmcnt(7)\n" \
    "v_mfma_f32_16x16x32_bf16 v[82:85], v[150:153], v[102:105], v[82:85]\n" \
    "ds_read_b64_tr_b16 v[130:131], %[wp2] offset:0\n" \
    "s_waitcnt lgkmcnt(5)\n" \
    "v_mfma_f32_16x16x32_bf16 v[22:25], v[138:141], v[102:105], v[22:25]\n" \
    "ds_read_b128 v[102:105], %[ap0] offset:55296\n" \
    "v_mfma_f32_16x16x32_bf16 v[46:49], v[142:145], v[106:109], v[46:49]\n" \
    "ds_read_b64_tr_b16 v[132:133], %[wp2] offset:2048\n" \
    "v_mfma_f32_16x16x32_bf16 v[66:69], v[106:109], v[146:149], v[66:69]\n" \
    "ds_read_b64_tr_b16 v[134:135], %[wp3] offset:0\n" \
    "v_mfma_f32_16x16x32_bf16 v[86:89], v[150:153], v[106:109], v[86:89]\n" \
    "ds_read_b64_tr_b16 v[136:137], %[wp3] offset:2048\n" \
    "v_mfma_f32_16x16x32_bf16 v[26:29], v[138:141], v[106:109], v[26:29]\n" \
    "ds_read_b128 v[106:109], %[ap0] offset:57344\n" \
    "s_waitcnt lgkmcnt(8)\n" \
    "v_mfma_f32_16x16x32_bf16 v[50:53], v[142:145], v[110:113], v[50:53]\n" \
    "ds_read_b64_tr_b16 v[122:123], %[wp0] offset:0\n" \
    "v_mfma_f32_16x16x32_bf16 v[70:73], v[110:113], v[146:149], v[70:73]\n" \
    "ds_read_b64_tr_b16 v[124:125], %[wp0] offset:2048\n" \
    "v_mfma_f32_16x16x32_bf16 v[90:93], v[150:153], v[110:113], v[90:93]\n" \
    "ds_read_b128 v[114:117], %[ap0] offset:61440\n" \
    "v_mfma_f32_16x16x32_bf16 v[30:33], v[138:141], v[110:113], v[30:33]\n" \
    "ds_read_b128 v[110:113], %[ap0] offset:59392\n" \
    "v_mfma_f32_16x16x32_bf16 v[54:57], v[142:145], v[118:121], v[54:57]\n" \
    "s_barrier\n" \
    "v_mfma_f32_16x16x32_bf16 v[74:77], v[118:121], v[146:149], v[74:77]\n" \
    "v_mfma_f32_16x16x32_bf16 v[94:97], v[150:153], v[118:121], v[94:97]\n" \
    "v_mfma_f32_16x16x32_bf16 v[34:37], v[138:141], v[118:121], v[34:37]\n" \
    "s_waitcnt lgkmcnt(8)\n" \
    "v_mfma_f32_16x16x32_bf16 v[42:45], v[126:129], v[102:105], v[42:45]\n" \
    "ds_read_b64_tr_b16 v[142:143], %[wp1] offset:34816\n" \
    "s_waitcnt lgkmcnt(8)\n" \
    "v_mfma_f32_16x16x32_bf16 v[62:65], v[102:105], v[130:133], v[62:65]\n" \
    "ds_read_b64_tr_b16 v[144:145], %[wp1] offset:36864\n" \
    "s_waitcnt lgkmcnt(7)\n" \
    "v_mfma_f32_16x16x32_bf16 v[82:85], v[134:137], v[102:105], v[82:85]\n" \
    "ds_read_b64_tr_b16 v[146:147], %[wp2] offset:34816\n" \
    "s_waitcnt lgkmcnt(5)\n" \
    "v_mfma_f32_16x16x32_bf16 v[22:25], v[122:125], v[102:105], v[22:25]\n" \
    "ds_read_b128 v[102:105], %[ap1] offset:55296\n" \
    "v_mfma_f32_16x16x32_bf16 v[46:49], v[126:129], v[106:109], v[46:49]\n" \
    "ds_read_b64_tr_b16 v[148:149], %[wp2] offset:36864\n" \
    "v_mfma_f32_16x16x32_bf16 v[66:69], v[106:109], v[130:133], v[66:69]\n" \
    "ds_read_b64_tr_b16 v[150:151], %[wp3] offset:34816\n" \
    "v_mfma_f32_16x16x32_bf16 v[86:89], v[134:137], v[106:109], v[86:89]\n" \
    "ds_read_b64_tr_b16 v[152:153], %[wp3] offset:36864\n" \
    "v_mfma_f32_16x16x32_bf16 v[26:29], v[122:125], v[106:109], v[26:29]\n" \
    "ds_read_b128 v[106:109], %[ap1] offset:57344\n" \
    "s_waitcnt lgkmcnt(8)\n" \
    "v_mfma_f32_16x16x32_bf16 v[50:53], v[126:129], v[110:113], v[50:53]\n" \
    "ds_read_b64_tr_b16 v[138:139], %[wp0] offset:34816\n" \
    "v_mfma_f32_16x16x32_bf16 v[70:73], v[110:113], v[130:133], v[70:73]\n" \
    "ds_read_b64_tr_b16 v[140:141], %[wp0] offset:36864\n" \
    "v_mfma_f32_16x16x32_bf16 v[90:93], v[134:137], v[110:113], v[90:93]\n" \
    "ds_read_b128 v[118:121], %[ap1] offset:61440\n" \
    "v_mfma_f32_16x16x32_bf16 v[30:33], v[122:125], v[110:113], v[30:33]\n" \
    "ds_read_b128 v[110:113], %[ap1] offset:59392\n" \
    "v_mfma_f32_16x16x32_bf16 v[54:57], v[126:129], v[114:117], v[54:57]\n" \
    "s_barrier\n" \
    "v_mfma_f32_16x16x32_bf16 v[74:77], v[114:117], v[130:133], v[74:77]\n" \
    "v_mfma_f32_16x16x32_bf16 v[94:97], v[134:137], v[114:117], v[94:97]\n" \
    "v_mfma_f32_16x16x32_bf16 v[34:37], v[122:125], v[114:117], v[34:37]\n" \
    "s_waitcnt lgkmcnt(8)\n" \
    "v_mfma_f32_16x16x32_bf16 v[42:45], v[142:145], v[102:105], v[42:45]\n" \
    "ds_read_b64_tr_b16 v[126:127], %[wp1] offset:51200\n" \
    "s_waitcnt lgkmcnt(8)\n" \
    "v_mfma_f32_16x16x32_bf16 v[62:65], v[102:105], v[146:149], v[62:65]\n" \
    "ds_read_b64_tr_b16 v[128:129], %[wp1] offset:53248\n" \
    "s_waitcnt lgkmcnt(7)\n" \
    "v_mfma_f32_16x16x32_bf16 v[82:85], v[150:153], v[102:105], v[82:85]\n" \
    "ds_read_b64_tr_b16 v[130:131], %[wp2] offset:51200\n" \
    "s_waitcnt lgkmcnt(5)\n" \
    "v_mfma_f32_16x16x32_bf16 v[22:25], v[138:141], v[102:105], v[22:25]\n" \
    "ds_read_b128 v[102:105], %[ap0] offset:2048\n" \
    "v_mfma_f32_16x16x32_bf16 v[46:49], v[142:145], v[106:109], v[46:49]\n" \
    "ds_read_b64_tr_b16 v[132:133], %[wp2] offset:53248\n" \
    "v_mfma_f32_16x16x32_bf16 v[66:69], v[106:109], v[146:149], v[66:69]\n" \
    "ds_read_b64_tr_b16 v[134:135], %[wp3] offset:51200\n" \
    "v_mfma_f32_16x16x32_bf16 v[86:89], v[150:153], v[106:109], v[86:89]\n" \
    "ds_read_b64_tr_b16 v[136:137], %[wp3] offset:53248\n" \
    "v_mfma_f32_16x16x32_bf16 v[26:29], v[138:141], v[106:109], v[26:29]\n" \
    "ds_read_b128 v[106:109], %[ap0] offset:4096\n" \
    "s_waitcnt lgkmcnt(8)\n" \
    "v_mfma_f32_16x16x32_bf16 v[50:53], v[142:145], v[110:113], v[50:53]\n" \
    "ds_read_b64_tr_b16 v[122:123], %[wp0] offset:51200\n" \
    "v_mfma_f32_16x16x32_bf16 v[70:73], v[110:113], v[146:149], v[70:73]\n" \
    "ds_read_b64_tr_b16 v[124:125], %[wp0] offset:53248\n" \
    "v_mfma_f32_16x16x32_bf16 v[90:93], v[150:153], v[110:113], v[90:93]\n" \
    "ds_read_b128 v[114:117], %[ap0] offset:8192\n" \
    "v_mfma_f32_16x16x32_bf16 v[30:33], v[138:141], v[110:113], v[30:33]\n" \
    "ds_read_b128 v[110:113], %[ap0] offset:6144\n" \
    "v_mfma_f32_16x16x32_bf16 v[54:57], v[142:145], v[118:121], v[54:57]\n" \
    "s_barrier\n" \
    "v_mfma_f32_16x16x32_bf16 v[74:77], v[118:121], v[146:149], v[74:77]\n" \
    "v_mfma_f32_16x16x32_bf16 v[94:97], v[150:153], v[118:121], v[94:97]\n" \
    "v_mfma_f32_16x16x32_bf16 v[34:37], v[138:141], v[118:121], v[34:37]\n" \
    "s_waitcnt lgkmcnt(8)\n" \
    "v_mfma_f32_16x16x32_bf16 v[42:45], v[126:129], v[102:105], v[42:45]\n" \
    "ds_read_b64_tr_b16 v[142:143], v155 offset:0\n" \
    "s_waitcnt lgkmcnt(8)\n" \
    "v_mfma_f32_16x16x32_bf16 v[62:65], v[102:105], v[130:133], v[62:65]\n" \
    "ds_read_b64_tr_b16 v[144:145], v155 offset:2048\n" \
    "s_waitcnt lgkmcnt(7)\n" \
    "v_mfma_f32_16x16x32_bf16 v[82:85], v[134:137], v[102:105], v[82:85]\n" \
    "ds_read_b64_tr_b16 v[146:147], v156 offset:0\n" \
    "s_waitcnt lgkmcnt(5)\n" \
    "v_mfma_f32_16x16x32_bf16 v[22:25], v[122:125], v[102:105], v[22:25]\n" \
    "ds_read_b128 v[102:105], %[ap1] offset:2048\n" \
    "v_mfma_f32_16x16x32_bf16 v[46:49], v[126:129], v[106:109], v[46:49]\n" \
    "ds_read_b64_tr_b16 v[148:149], v156 offset:2048\n" \
    "v_mfma_f32_16x16x32_bf16 v[66:69], v[106:109], v[130:133], v[66:69]\n" \
    "ds_read_b64_tr_b16 v[150:151], v157 offset:0\n" \
    "v_mfma_f32_16x16x32_bf16 v[86:89], v[134:137], v[106:109], v[86:89]\n" \
    "ds_read_b64_tr_b16 v[152:153], v157 offset:2048\n" \
    "v_mfma_f32_16x16x32_bf16 v[26:29], v[122:125], v[106:109], v[26:29]\n" \
    "ds_read_b128 v[106:109], %[ap1] offset:4096\n" \
    "s_waitcnt lgkmcnt(8)\n" \
    "v_mfma_f32_16x16x32_bf16 v[50:53], v[126:129], v[110:113], v[50:53]\n" \
    "ds_read_b64_tr_b16 v[138:139], v154 offset:0\n" \
    "v_mfma_f32_16x16x32_bf16 v[70:73], v[110:113], v[130:133], v[70:73]\n" \
    "ds_read_b64_tr_b16 v[140:141], v154 offset:2048\n" \
    "v_mfma_f32_16x16x32_bf16 v[90:93], v[134:137], v[110:113], v[90:93]\n" \
    "ds_read_b128 v[118:121], %[ap1] offset:8192\n" \
    "v_mfma_f32_16x16x32_bf16 v[30:33], v[122:125], v[110:113], v[30:33]\n" \
    "ds_read_b128 v[110:113], %[ap1] offset:6144\n" \
    "v_mfma_f32_16x16x32_bf16 v[54:57], v[126:129], v[114:117], v[54:57]\n" \
    "s_barrier\n" \
    "v_mfma_f32_16x16x32_bf16 v[74:77], v[114:117], v[130:133], v[74:77]\n" \
    "v_mfma_f32_16x16x32_bf16 v[94:97], v[134:137], v[114:117], v[94:97]\n" \
    "v_mfma_f32_16x16x32_bf16 v[34:37], v[122:125], v[114:117], v[34:37]\n" \
    "s_waitcnt lgkmcnt(8)\n" \
    "v_mfma_f32_16x16x32_bf16 v[42:45], v[142:145], v[102:105], v[42:45]\n" \
    "ds_read_b64_tr_b16 v[126:127], v155 offset:16384\n" \
    "s_waitcnt lgkmcnt(8)\n" \
    "v_mfma_f32_16x16x32_bf16 v[62:65], v[102:105], v[146:149], v[62:65]\n" \
    "ds_read_b64_tr_b16 v[128:129], v155 offset:18432\n" \
    "s_waitcnt lgkmcnt(7)\n" \
    "v_mfma_f32_16x16x32_bf16 v[82:85], v[150:153], v[102:105], v[82:85]\n" \
    "ds_read_b64_tr_b16 v[130:131], v156 offset:16384\n" \
    "s_waitcnt lgkmcnt(5)\n" \
    "v_mfma_f32_16x16x32_bf16 v[22:25], v[138:141], v[102:105], v[22:25]\n" \
    "ds_read_b128 v[102:105], %[ap0] offset:20480\n" \
    "v_mfma_f32_16x16x32_bf16 v[46:49], v[142:145], v[106:109], v[46:49]\n" \
    "ds_read_b64_tr_b16 v[132:133], v156 offset:18432\n" \
    "v_mfma_f32_16x16x32_bf16 v[66:69], v[106:109], v[146:149], v[66:69]\n" \
    "ds_read_b64_tr_b16 v[134:135], v157 offset:16384\n" \
    "v_mfma_f32_16x16x32_bf16 v[86:89], v[150:153], v[106:109], v[86:89]\n" \
    "ds_read_b64_tr_b16 v[136:137], v157 offset:18432\n" \
    "v_mfma_f32_16x16x32_bf16 v[26:29], v[138:141], v[106:109], v[26:29]\n" \
    "ds_read_b128 v[106:109], %[ap0] offset:22528\n" \
    "s_waitcnt lgkmcnt(8)\n" \
    "v_mfma_f32_16x16x32_bf16 v[50:53], v[142:145], v[110:113], v[50:53]\n" \
    "ds_read_b64_tr_b16 v[122:123], v154 offset:16384\n" \
    "v_mfma_f32_16x16x32_bf16 v[70:73], v[110:113], v[146:149], v[70:73]\n" \
    "ds_read_b64_tr_b16 v[124:125], v154 offset:18432\n" \
    "v_mfma_f32_16x16x32_bf16 v[90:93], v[150:153], v[110:113], v[90:93]\n" \
    "ds_read_b128 v[114:117], %[ap0] offset:26624\n" \
    "v_mfma_f32_16x16x32_bf16 v[30:33], v[138:141], v[110:113], v[30:33]\n" \
    "ds_read_b128 v[110:113], %[ap0] offset:24576\n" \
    "v_mfma_f32_16x16x32_bf16 v[54:57], v[142:145], v[118:121], v[54:57]\n" \
    "s_barrier\n" \
    "v_mfma_f32_16x16x32_bf16 v[74:77], v[118:121], v[146:149], v[74:77]\n" \
    "v_mfma_f32_16x16x32_bf16 v[94:97], v[150:153], v[118:121], v[94:97]\n" \
    "v_mfma_f32_16x16x32_bf16 v[34:37], v[138:141], v[118:121], v[34:37]\n" \
    "s_waitcnt lgkmcnt(8)\n" \
    "v_mfma_f32_16x16x32_bf16 v[42:45], v[126:129], v[102:105], v[42:45]\n" \
    "ds_read_b64_tr_b16 v[142:143], v155 offset:32768\n" \
    "s_waitcnt lgkmcnt(8)\n" \
    "v_mfma_f32_16x16x32_bf16 v[62:65], v[102:105], v[130:133], v[62:65]\n" \
    "ds_read_b64_tr_b16 v[144:145], v155 offset:34816\n" \
    "s_waitcnt lgkmcnt(7)\n" \
    "v_mfma_f32_16x16x32_bf16 v[82:85], v[134:137], v[102:105], v[82:85]\n" \
    "ds_read_b64_tr_b16 v[146:147], v156 offset:32768\n" \
    "s_waitcnt lgkmcnt(5)\n" \
    "v_mfma_f32_16x16x32_bf16 v[22:25], v[122:125], v[102:105], v[22:25]\n" \
    "ds_read_b128 v[102:105], %[ap1] offset:20480\n" \
    "v_mfma_f32_16x16x32_bf16 v[46:49], v[126:129], v[106:109], v[46:49]\n" \
    "ds_read_b64_tr_b16 v[148:149], v156 offset:34816\n" \
    "v_mfma_f32_16x16x32_bf16 v[66:69], v[106:109], v[130:133], v[66:69]\n" \
    "ds_read_b64_tr_b16 v[150:151], v157 offset:32768\n" \
    "v_mfma_f32_16x16x32_bf16 v[86:89], v[134:137], v[106:109], v[86:89]\n" \
    "ds_read_b64_tr_b16 v[152:153], v157 offset:34816\n" \
    "v_mfma_f32_16x16x32_bf16 v[26:29], v[122:125], v[106:109], v[26:29]\n" \
    "ds_read_b128 v[106:109], %[ap1] offset:22528\n" \
    "s_waitcnt lgkmcnt(8)\n" \
    "v_mfma_f32_16x16x32_bf16 v[50:53], v[126:129], v[110:113], v[50:53]\n" \
    "ds_read_b64_tr_b16 v[138:139], v154 offset:32768\n" \
    "v_mfma_f32_16x16x32_bf16 v[70:73], v[110:113], v[130:133], v[70:73]\n" \
    "ds_read_b64_tr_b16 v[140:141], v154 offset:34816\n" \
    "v_mfma_f32_16x16x32_bf16 v[90:93], v[134:137], v[110:113], v[90:93]\n" \
    "ds_read_b128 v[118:121], %[ap1] offset:26624\n" \
    "v_mfma_f32_16x16x32_bf16 v[30:33], v[122:125], v[110:113], v[30:33]\n" \
    "ds_read_b128 v[110:113], %[ap1] offset:24576\n" \
    "v_mov_b32 v161, 16\n" \
    "ds_write_b32 %[fr], v161\n" \
    "v_mfma_f32_16x16x32_bf16 v[54:57], v[126:129], v[114:117], v[54:57]\n" \
    "s_barrier\n" \
    "v_mfma_f32_16x16x32_bf16 v[74:77], v[114:117], v[130:133], v[74:77]\n" \
    "v_mfma_f32_16x16x32_bf16 v[94:97], v[134:137], v[114:117], v[94:97]\n" \
    "v_mfma_f32_16x16x32_bf16 v[34:37], v[122:125], v[114:117], v[34:37]\n" \
    "s_waitcnt lgkmcnt(9)\n" \
    "v_mfma_f32_16x16x32_bf16 v[42:45], v[142:145], v[102:105], v[42:45]\n" \
    "ds_read_b64_tr_b16 v[126:127], %[wp1] offset:0\n" \
    "s_waitcnt lgkmcnt(9)\n" \
    "v_mfma_f32_16x16x32_bf16 v[62:65], v[102:105], v[146:149], v[62:65]\n" \
    "ds_read_b64_tr_b16 v[128:129], %[wp1] offset:2048\n" \
    "s_waitcnt lgkmcnt(8)\n" \
    "v_mfma_f32_16x16x32_bf16 v[82:85], v[150:153], v[102:105], v[82:85]\n" \
    "ds_read_b64_tr_b16 v[130:131], %[wp2] offset:0\n" \
    "s_waitcnt lgkmcnt(6)\n" \
    "v_mfma_f32_16x16x32_bf16 v[22:25], v[138:141], v[102:105], v[22:25]\n" \
    "ds_read_b128 v[102:105], %[ap0] offset:55296\n" \
    "v_mfma_f32_16x16x32_bf16 v[46:49], v[142:145], v[106:109], v[46:49]\n" \
    "ds_read_b64_tr_b16 v[132:133], %[wp2] offset:2048\n" \
    "v_mfma_f32_16x16x32_bf16 v[66:69], v[106:109], v[146:149], v[66:69]\n" \
    "ds_read_b64_tr_b16 v[134:135], %[wp3] offset:0\n" \
    "v_mfma_f32_16x16x32_bf16 v[86:89], v[150:153], v[106:109], v[86:89]\n" \
    "ds_read_b64_tr_b16 v[136:137], %[wp3] offset:2048\n" \
    "v_mfma_f32_16x16x32_bf16 v[26:29], v[138:141], v[106:109], v[26:29]\n" \
    "ds_read_b128 v[106:109], %[ap0] offset:57344\n" \
    "s_waitcnt lgkmcnt(9)\n" \
    "v_mfma_f32_16x16x32_bf16 v[50:53], v[142:145], v[110:113], v[50:53]\n" \
    "ds_read_b64_tr_b16 v[122:123], %[wp0] offset:0\n" \
    "v_mfma_f32_16x16x32_bf16 v[70:73], v[110:113], v[146:149], v[70:73]\n" \
    "ds_read_b64_tr_b16 v[124:125], %[wp0] offset:2048\n" \
    "v_mfma_f32_16x16x32_bf16 v[90:93], v[150:153], v[110:113], v[90:93]\n" \
    "ds_read_b128 v[114:117], %[ap0] offset:61440\n" \
    "v_mfma_f32_16x16x32_bf16 v[30:33], v[138:141], v[110:113], v[30:33]\n" \
    "ds_read_b128 v[110:113], %[ap0] offset:59392\n" \
    "v_mov_b32 v161, 17\n" \
    "ds_write_b32 %[fr], v161\n" \
    "v_mfma_f32_16x16x32_bf16 v[54:57], v[142:145], v[118:121], v[54:57]\n" \
    "s_barrier\n" \
    "ds_read_b64_tr_b16 v[142:143], %[wp1] offset:34816\n" \
    "ds_read_b64_tr_b16 v[144:145], %[wp1] offset:36864\n" \
    "v_mfma_f32_16x16x32_bf16 v[74:77], v[118:121], v[146:149], v[74:77]\n" \
    "ds_read_b64_tr_b16 v[146:147], %[wp2] offset:34816\n" \
    "ds_read_b64_tr_b16 v[148:149], %[wp2] offset:36864\n" \
    "v_mfma_f32_16x16x32_bf16 v[94:97], v[150:153], v[118:121], v[94:97]\n" \
    "ds_read_b64_tr_b16 v[150:151], %[wp3] offset:34816\n" \
    "ds_read_b64_tr_b16 v[152:153], %[wp3] offset:36864\n" \
    "v_mfma_f32_16x16x32_bf16 v[34:37], v[138:141], v[118:121], v[34:37]\n" \
    "ds_read_b128 v[118:121], %[ap1] offset:61440\n" \
    "ds_read_b64_tr_b16 v[138:139], %[wp0] offset:34816\n" \
    "ds_read_b64_tr_b16 v[140:141], %[wp0] offset:36864\n" \
    "s_waitcnt lgkmcnt(11)\n" \
    "v_mfma_f32_16x16x32_bf16 v[54:57], v[126:129], v[114:117], v[54:57]\n" \
    "v_add_u32 %[wp0], %[tabc], %[l16]\n" \
    "v_cndmask_b32_e64 %[wp2], 0, -1, s[68:69]\n" \
    "v_mfma_f32_16x16x32_bf16 v[74:77], v[114:117], v[130:133], v[74:77]\n" \
    "v_add_u32 %[wp1], %[hxc], %[l16]\n" \
    "v_lshrrev_b32 %[wp3], 2, %[l16]\n" \
    "v_mfma_f32_16x16x32_bf16 v[94:97], v[134:137], v[114:117], v[94:97]\n" \
    "v_add_u32 %[wp3], %[hxd], %[wp3]\n" \
    "v_mfma_f32_16x16x32_bf16 v[34:37], v[122:125], v[114:117], v[34:37]\n" \
    "s_waitcnt lgkmcnt(10)\n" \
    "v_mfma_f32_16x16x32_bf16 v[50:53], v[126:129], v[110:113], v[50:53]\n" \
    "v_mfma_f32_16x16x32_bf16 v[70:73], v[110:113], v[130:133], v[70:73]\n" \
    "v_mfma_f32_16x16x32_bf16 v[90:93], v[134:137], v[110:113], v[90:93]\n" \
    "v_mfma_f32_16x16x32_bf16 v[30:33], v[122:125], v[110:113], v[30:33]\n" \
    "ds_read_b128 v[110:113], %[ap1] offset:59392\n" \
    "s_waitcnt lgkmcnt(3)\n" \
    "v_mfma_f32_16x16x32_bf16 v[54:57], v[142:145], v[118:121], v[54:57]\n" \
    "v_cndmask_b32_e64 v154, %[zr], %[vxb], s[60:61]\n" \
    "v_cndmask_b32_e64 v155, %[zr], %[vxb], s[62:63]\n" \
    "v_mfma_f32_16x16x32_bf16 v[74:77], v[118:121], v[146:149], v[74:77]\n" \
    "v_cndmask_b32_e64 v156, %[zr], %[vxb], s[64:65]\n" \
    "v_cndmask_b32_e64 v157, %[zr], %[vxb], s[66:67]\n" \
    "v_mfma_f32_16x16x32_bf16 v[94:97], v[150:153], v[118:121], v[94:97]\n" \
    "v_mov_b32 v41, 0\n" \
    "s_waitcnt lgkmcnt(1)\n" \
    "v_mfma_f32_16x16x32_bf16 v[34:37], v[138:141], v[118:121], v[34:37]\n" \
    "v_mfma_f32_16x16x32_bf16 v[46:49], v[126:129], v[106:109], v[46:49]\n" \
    "v_mfma_f32_16x16x32_bf16 v[66:69], v[106:109], v[130:133], v[66:69]\n" \
    "v_cvt_pk_bf16_f32 v54, v54, v55\n" \
    "v_cvt_pk_bf16_f32 v55, v56, v57\n" \
    "v_mfma_f32_16x16x32_bf16 v[86:89], v[134:137], v[106:109], v[86:89]\n" \
    "v_cvt_pk_bf16_f32 v120, v74, v75\n" \
    "v_cvt_pk_bf16_f32 v121, v76, v77\n" \
    "v_mfma_f32_16x16x32_bf16 v[26:29], v[122:125], v[106:109], v[26:29]\n" \
    "ds_read_b128 v[106:109], %[ap1] offset:57344\n" \
    "v_cvt_pk_bf16_f32 v94, v94, v95\n" \
    "v_cvt_pk_bf16_f32 v56, v96, v97\n" \
    "ds_write2_b32 %[scw], v94, v56 offset0:128 offset1:129\n" \
    "s_waitcnt lgkmcnt(2)\n" \
    "v_mfma_f32_16x16x32_bf16 v[50:53], v[142:145], v[110:113], v[50:53]\n" \
    "v_permlane16_swap_b32 v94, v56\n" \
    "v_mfma_f32_16x16x32_bf16 v[70:73], v[110:113], v[146:149], v[70:73]\n" \
    "v_cvt_pk_bf16_f32 v34, v34, v35\n" \
    "v_cvt_pk_bf16_f32 v35, v36, v37\n" \
    "v_mfma_f32_16x16x32_bf16 v[90:93], v[150:153], v[110:113], v[90:93]\n" \
    "v_and_b32 v36, v94, %[wp2]\n" \
    "v_mov_b32 v37, 0\n" \
    "v_mov_b32 v57, 0\n" \
    "v_mfma_f32_16x16x32_bf16 v[30:33], v[138:141], v[110:113], v[30:33]\n" \
    "v_mfma_f32_16x16x32_bf16 v[42:45], v[126:129], v[102:105], v[42:45]\n" \
    "v_mfma_f32_16x16x32_bf16 v[62:65], v[102:105], v[130:133], v[62:65]\n" \
    "v_cvt_pk_bf16_f32 v50, v50, v51\n" \
    "v_cvt_pk_bf16_f32 v51, v52, v53\n" \
    "v_mfma_f32_16x16x32_bf16 v[82:85], v[134:137], v[102:105], v[82:85]\n" \
    "v_cvt_pk_bf16_f32 v118, v70, v71\n" \
    "v_cvt_pk_bf16_f32 v119, v72, v73\n" \
    "v_mfma_f32_16x16x32_bf16 v[22:25], v[122:125], v[102:105], v[22:25]\n" \
    "ds_read_b128 v[102:105], %[ap1] offset:55296\n" \
    "v_mov_b32 v161, 18\n" \
    "ds_write_b32 %[fr], v161\n" \
    "ds_read_b128 v[126:129], %[wp0] offset:1024\n" \
    "ds_read_b128 v[122:125], %[wp0]\n" \
    "ds_read_b128 v[130:133], %[wp0] offset:2048\n" \
    "v_cvt_pk_bf16_f32 v90, v90, v91\n" \
    "v_cvt_pk_bf16_f32 v52, v92, v93\n" \
    "ds_write2_b32 %[scw], v90, v52 offset0:96 offset1:97\n" \
    "s_waitcnt lgkmcnt(7)\n" \
    "v_mfma_f32_16x16x32_bf16 v[46:49], v[142:145], v[106:109], v[46:49]\n" \
    "v_permlane16_swap_b32 v90, v52\n" \
    "ds_read_b32 v160, %[fr] offset:128\n" \
    "v_mfma_f32_16x16x32_bf16 v[66:69], v[106:109], v[146:149], v[66:69]\n" \
    "v_cvt_pk_bf16_f32 v30, v30, v31\n" \
    "v_cvt_pk_bf16_f32 v31, v32, v33\n" \
    "v_mfma_f32_16x16x32_bf16 v[86:89], v[150:153], v[106:109], v[86:89]\n" \
    "v_and_b32 v32, v90, %[wp2]\n" \
    "v_mov_b32 v33, 0\n" \
    "v_mov_b32 v53, 0\n" \
    "v_mfma_f32_16x16x32_bf16 v[26:29], v[138:141], v[106:109], v[26:29]\n" \
    "s_waitcnt lgkmcnt(6)\n" \
    "v_mfma_f32_16x16x32_bf16 v[42:45], v[142:145], v[102:105], v[42:45]\n" \
    "v_mfma_f32_16x16x32_bf16 v[62:65], v[102:105], v[146:149], v[62:65]\n" \
    "v_cvt_pk_bf16_f32 v46, v46, v47\n" \
    "v_cvt_pk_bf16_f32 v47, v48, v49\n" \
    "v_mfma_f32_16x16x32_bf16 v[82:85], v[150:153], v[102:105], v[82:85]\n" \
    "v_cvt_pk_bf16_f32 v116, v66, v67\n" \
    "v_cvt_pk_bf16_f32 v117, v68, v69\n" \
    "v_mfma_f32_16x16x32_bf16 v[22:25], v[138:141], v[102:105], v[22:25]\n" \
    "ds_read_b32 v159, %[zr] offset:1332\n" \
    "s_setprio 0\n" \
    "s_waitcnt lgkmcnt(1)\n" \
    "v_readfirstlane_b32 %[tmp], v160\n" \
    "s_cmp_lg_u32 %[tmp], 0\n" \
    "s_cbranch_scc1 Lhx_%=\n" \
    "s_mov_b32 %[cnt], 0\n" \
    "Lhxs_%=:\n" \
    "ds_read_b32 v160, %[fr] offset:128\n" \
    "s_add_u32 %[cnt], %[cnt], 1\n" \
    "s_waitcnt lgkmcnt(0)\n" \
    "v_readfirstlane_b32 %[tmp], v160\n" \
    "s_cmp_ge_u32 %[cnt], %[cap]\n" \
    "s_cbranch_scc1 Lhx_%=\n" \
    "s_cmp_eq_u32 %[tmp], 0\n" \
    "s_cbranch_scc1 Lhxs_%=\n" \
    "Lhx_%=:\n" \
    "ds_read_b64 v[38:39], %[wp1]\n" \
    "ds_read_b32 v40, %[wp3]\n" \
    "ds_read_b64 v[112:113], %[wp1] offset:8\n" \
    "v_mfma_f32_16x16x32_bf16 v[74:77], v[54:57], v[34:37], v[126:129]\n" \
    "v_cvt_pk_bf16_f32 v86, v86, v87\n" \
    "v_cvt_pk_bf16_f32 v48, v88, v89\n" \
    "ds_write2_b32 %[scw], v86, v48 offset0:64 offset1:65\n" \
    "v_mfma_f32_16x16x32_bf16 v[94:97], v[50:53], v[34:37], v[122:125]\n" \
    "v_cvt_pk_bf16_f32 v42, v42, v43\n" \
    "v_cvt_pk_bf16_f32 v43, v44, v45\n" \
    "v_permlane16_swap_b32 v86, v48\n" \
    "v_mfma_f32_16x16x32_bf16 v[70:73], v[50:53], v[30:33], v[126:129]\n" \
    "v_cvt_pk_bf16_f32 v26, v26, v27\n" \
    "v_cvt_pk_bf16_f32 v27, v28, v29\n" \
    "v_and_b32 v28, v86, %[wp2]\n" \
    "v_mov_b32 v29, 0\n" \
    "v_mov_b32 v49, 0\n" \
    "s_nop 1\n" \
    "v_mfma_f32_16x16x32_bf16 v[90:93], v[46:49], v[30:33], v[122:125]\n" \
    "v_cvt_pk_bf16_f32 v82, v82, v83\n" \
    "v_cvt_pk_bf16_f32 v44, v84, v85\n" \
    "ds_write2_b32 %[scw], v82, v44 offset0:32 offset1:33\n" \
    "v_cvt_pk_bf16_f32 v114, v62, v63\n" \
    "v_cvt_pk_bf16_f32 v115, v64, v65\n" \
    "v_mfma_f32_16x16x32_bf16 v[66:69], v[46:49], v[26:29], v[126:129]\n" \
    "v_mov_b32 v45, 0\n" \
    "v_permlane16_swap_b32 v82, v44\n" \
    "v_maximum3_f32 v144, v74, v75, v75\n" \
    "s_nop 0\n" \
    "v_mfma_f32_16x16x32_bf16 v[86:89], v[42:45], v[26:29], v[122:125]\n" \
    "v_cvt_pk_bf16_f32 v22, v22, v23\n" \
    "v_cvt_pk_bf16_f32 v23, v24, v25\n" \
    "v_and_b32 v24, v82, %[wp2]\n" \
    "v_mov_b32 v25, 0\n" \
    "v_maximum3_f32 v145, v76, v77, v77\n" \
    "ds_read_b64_tr_b16 v[106:107], v157 offset:384\n" \
    "ds_read_b64_tr_b16 v[108:109], v157 offset:512\n" \
    "v_mfma_f32_16x16x32_bf16 v[62:65], v[42:45], v[22:25], v[126:129]\n" \
    "v_maximum3_f32 v144, v94, v95, v144\n" \
    "v_maximum3_f32 v145, v96, v97, v145\n" \
    "s_waitcnt lgkmcnt(5)\n" \
    "v_mfma_f32_16x16x32_bf16 v[82:85], v[38:41], v[22:25], v[122:125]\n" \
    "ds_read_b64_tr_b16 v[102:103], v156 offset:256\n" \
    "ds_read_b64_tr_b16 v[104:105], v156 offset:384\n" \
    "v_maximum3_f32 v142, v70, v71, v71\n" \
    "v_maximum3_f32 v143, v72, v73, v73\n" \
    "ds_read_b64_tr_b16 v[98:99], v155 offset:128\n" \
    "ds_read_b64_tr_b16 v[100:101], v155 offset:256\n" \
    "v_maximum3_f32 v142, v90, v91, v142\n" \
    "v_maximum3_f32 v143, v92, v93, v143\n" \
    "ds_read_b64_tr_b16 v[134:135], v154 offset:0\n" \
    "ds_read_b64_tr_b16 v[136:137], v154 offset:128\n" \
    "v_maximum3_f32 v149, v144, v145, v145\n" \
    "v_maximum3_f32 v140, v66, v67, v67\n" \
    "v_maximum3_f32 v141, v68, v69, v69\n" \
    "v_maximum3_f32 v148, v142, v143, v143\n" \
    "v_maximum3_f32 v140, v86, v87, v140\n" \
    "v_maximum3_f32 v141, v88, v89, v141\n" \
    "v_maximum3_f32 v138, v62, v63, v63\n" \
    "v_maximum3_f32 v139, v64, v65, v65\n" \
    "v_maximum3_f32 v147, v140, v141, v141\n" \
    "v_maximum3_f32 v138, v82, v83, v138\n" \
    "v_maximum3_f32 v139, v84, v85, v139\n" \
    "v_maximum3_f32 v146, v138, v139, v139\n" \
    "s_waitcnt lgkmcnt(8)\n" \
    "Lend_%=:\n" \
    "s_nop 0\n" \
    "v_permlane16_swap_b32 v146, v147\n" \
    "v_permlane16_swap_b32 v148, v149\n" \
    "v_maximum3_f32 v146, v146, v147, v147\n" \
    "v_maximum3_f32 v148, v148, v149, v149\n" \
    "s_nop 1\n" \
    "v_permlane32_swap_b32 v146, v148\n" \
    "v_maximum3_f32 v146, v146, v148, v148\n" \
    "v_mul_f32 v146, 0x3ea52b5b, v146\n" \
    "v_mov_b32 v148, v146\n" \
    "s_nop 1\n" \
    "v_permlane32_swap_b32 v146, v148\n" \
    "v_mov_b32 v147, v146\n" \
    "v_mov_b32 v149, v148\n" \
    "s_nop 0\n" \
    "v_permlane16_swap_b32 v146, v147\n" \
    "v_permlane16_swap_b32 v148, v149\n" \
    "v_fma_f32 v138, v138, %[sc], -v146\n" \
    "v_fma_f32 v139, v139, %[sc], -v146\n" \
    "v_fma_f32 v140, v140, %[sc], -v147\n" \
    "v_fma_f32 v141, v141, %[sc], -v147\n" \
    "v_exp_f32 v138, v138\n" \
    "v_exp_f32 v139, v139\n" \
    "v_fma_f32 v142, v142, %[sc], -v148\n" \
    "v_fma_f32 v143, v143, %[sc], -v148\n" \
    "v_exp_f32 v140, v140\n" \
    "v_exp_f32 v141, v141\n" \
    "v_fma_f32 v144, v144, %[sc], -v149\n" \
    "v_fma_f32 v145, v145, %[sc], -v149\n" \
    "v_exp_f32 v142, v142\n" \
    "v_exp_f32 v143, v143\n" \
    "v_add_f32 v146, v138, v139\n" \
    "v_exp_f32 v144, v144\n" \
    "v_exp_f32 v145, v145\n" \
    "v_add_f32 v147, v140, v141\n" \
    "v_add_f32 v148, v142, v143\n" \
    "v_add_f32 v149, v144, v145\n" \
    "v_permlane16_swap_b32 v146, v147\n" \
    "s_nop 0\n" \
    "v_permlane16_swap_b32 v148, v149\n" \
    "v_add_f32 v146, v146, v147\n" \
    "v_add_f32 v148, v148, v149\n" \
    "s_nop 1\n" \
    "v_permlane32_swap_b32 v146, v148\n" \
    "v_add_f32 v146, v146, v148\n" \
    "v_rcp_f32 v146, v146\n" \
    "v_add_u32 v150, 40960, %[oo]\n" \
    "v_mov_b32 v148, v146\n" \
    "s_nop 1\n" \
    "v_permlane32_swap_b32 v146, v148\n" \
    "v_mov_b32 v147, v146\n" \
    "v_mov_b32 v149, v148\n" \
    "s_nop 0\n" \
    "v_permlane16_swap_b32 v146, v147\n" \
    "v_permlane16_swap_b32 v148, v149\n" \
    "v_mul_f32 v144, v144, v149\n" \
    "v_mul_f32 v145, v145, v149\n" \
    "v_cvt_pk_bf16_f32 v144, v144, v145\n" \
    "v_mul_f32 v142, v142, v148\n" \
    "v_mul_f32 v143, v143, v148\n" \
    "v_cvt_pk_bf16_f32 v142, v142, v143\n" \
    "v_perm_b32 v34, v144, v144, v130\n" \
    "v_perm_b32 v35, v144, v144, v131\n" \
    "v_perm_b32 v36, v144, v144, v132\n" \
    "v_perm_b32 v37, v144, v144, v133\n" \
    "v_mul_f32 v140, v140, v147\n" \
    "v_mul_f32 v141, v141, v147\n" \
    "v_cvt_pk_bf16_f32 v140, v140, v141\n" \
    "v_mfma_f32_16x16x32_bf16 v[74:77], v[118:121], v[34:37], 0\n" \
    "s_waitcnt lgkmcnt(6)\n" \
    "v_mfma_f32_16x16x32_bf16 v[94:97], v[106:109], v[34:37], 0\n" \
    "v_perm_b32 v30, v142, v142, v130\n" \
    "v_perm_b32 v31, v142, v142, v131\n" \
    "v_perm_b32 v32, v142, v142, v132\n" \
    "v_perm_b32 v33, v142, v142, v133\n" \
    "v_mul_f32 v138, v138, v146\n" \
    "v_mul_f32 v139, v139, v146\n" \
    "v_cvt_pk_bf16_f32 v138, v138, v139\n" \
    "v_mfma_f32_16x16x32_bf16 v[70:73], v[116:119], v[30:33], 0\n" \
    "s_waitcnt lgkmcnt(4)\n" \
    "v_mfma_f32_16x16x32_bf16 v[94:97], v[102:105], v[30:33], v[94:97]\n" \
    "v_perm_b32 v26, v140, v140, v130\n" \
    "v_perm_b32 v27, v140, v140, v131\n" \
    "v_perm_b32 v28, v140, v140, v132\n" \
    "v_perm_b32 v29, v140, v140, v133\n" \
    "s_nop 1\n" \
    "v_mfma_f32_16x16x32_bf16 v[66:69], v[114:117], v[26:29], 0\n" \
    "s_waitcnt lgkmcnt(2)\n" \
    "v_mfma_f32_16x16x32_bf16 v[94:97], v[98:101], v[26:29], v[94:97]\n" \
    "v_perm_b32 v22, v138, v138, v130\n" \
    "v_perm_b32 v23, v138, v138, v131\n" \
    "v_perm_b32 v24, v138, v138, v132\n" \
    "v_perm_b32 v25, v138, v138, v133\n" \
    "s_nop 1\n" \
    "v_mfma_f32_16x16x32_bf16 v[62:65], v[112:115], v[22:25], 0\n" \
    "s_waitcnt lgkmcnt(0)\n" \
    "v_mfma_f32_16x16x32_bf16 v[94:97], v[134:137], v[22:25], v[94:97]\n" \
    "v_readfirstlane_b32 %[tmp], v159\n" \
    "s_cmp_lg_u32 %[tmp], 0\n" \
    "s_cbranch_scc1 Lse_%=\n" \
    "s_mov_b32 %[cnt], 0\n" \
    "Lses_%=:\n" \
    "ds_read_b32 v159, %[zr] offset:1332\n" \
    "s_add_u32 %[cnt], %[cnt], 1\n" \
    "s_waitcnt lgkmcnt(0)\n" \
    "v_readfirstlane_b32 %[tmp], v159\n" \
    "s_cmp_ge_u32 %[cnt], %[cap]\n" \
    "s_cbranch_scc1 Lse_%=\n" \
    "s_cmp_eq_u32 %[tmp], 0\n" \
    "s_cbranch_scc1 Lses_%=\n" \
    "Lse_%=:\n" \
    "v_cvt_pk_bf16_f32 v52, v74, v75\n" \
    "v_cvt_pk_bf16_f32 v53, v76, v77\n" \
    "v_cvt_pk_bf16_f32 v50, v70, v71\n" \
    "v_cvt_pk_bf16_f32 v51, v72, v73\n" \
    "v_cvt_pk_bf16_f32 v44, v66, v67\n" \
    "v_cvt_pk_bf16_f32 v45, v68, v69\n" \
    "v_permlane16_swap_b32 v50, v52\n" \
    "v_permlane16_swap_b32 v51, v53\n" \
    "v_cvt_pk_bf16_f32 v42, v62, v63\n" \
    "v_cvt_pk_bf16_f32 v43, v64, v65\n" \
    "global_store_dwordx4 v150, v[50:53], %[wsp] sc1\n" \
    "v_cvt_pk_bf16_f32 v94, v94, v95\n" \
    "v_cvt_pk_bf16_f32 v95, v96, v97\n" \
    "v_permlane16_swap_b32 v42, v44\n" \
    "v_permlane16_swap_b32 v43, v45\n" \
    "global_store_dwordx4 %[oo], v[42:45], %[wsp] sc1\n" \
    "global_store_dwordx2 %[ox], v[94:95], %[wsp] sc1\n" \
    "s_nop 1\n"
#define G_CLOB "v22", "v23", "v24", "v25", "v26", "v27", "v28", "v29", "v30", "v31", "v32", "v33", "v34", "v35", "v36", "v37", "v38", "v39", "v40", "v41", "v42", "v43", "v44", "v45", "v46", "v47", "v48", "v49", "v50", "v51", "v52", "v53", "v54", "v55", "v56", "v57", "v58", "v59", "v60", "v61", "v62", "v63", "v64", "v65", "v66", "v67", "v68", "v69", "v70", "v71", "v72", "v73", "v74", "v75", "v76", "v77", "v78", "v79", "v80", "v81", "v82", "v83", "v84", "v85", "v86", "v87", "v88", "v89", "v90", "v91", "v92", "v93", "v94", "v95", "v96", "v97", "v98", "v99", "v100", "v101", "v102", "v103", "v104", "v105", "v106", "v107", "v108", "v109", "v110", "v111", "v112", "v113", "v114", "v115", "v116", "v117", "v118", "v119", "v120", "v121", "v122", "v123", "v124", "v125", "v126", "v127", "v128", "v129", "v130", "v131", "v132", "v133", "v134", "v135", "v136", "v137", "v138", "v139", "v140", "v141", "v142", "v143", "v144", "v145", "v146", "v147", "v148", "v149", "v150", "v151", "v152", "v153", "v154", "v155", "v156", "v157", "v158", "v159", "v160", "v161", "s60", "s61", "s62", "s63", "s64", "s65", "s66", "s67", "s68", "s69", "s70", "s71", "vcc", "scc", "memory"
#define P_ASM \
    "s_mov_b32 %[cnt], 0\n" \
    "La_%=:\n" \
    "ds_read_b32 v157, %[ctl] offset:56\n" \
    "s_add_u32 %[cnt], %[cnt], 1\n" \
    "s_waitcnt lgkmcnt(0)\n" \
    "v_readfirstlane_b32 %[tmp], v157\n" \
    "s_cmp_ge_u32 %[tmp], %[won]\n" \
    "s_cbranch_scc1 Lax_%=\n" \
    "s_sleep 2\n" \
    "s_cmp_lt_u32 %[cnt], %[cap]\n" \
    "s_cbranch_scc1 La_%=\n" \
    "Lax_%=:\n" \
    "ds_read_b64_tr_b16 v[16:17], %[pc] offset:0\n" \
    "ds_read_b64_tr_b16 v[18:19], %[pd] offset:0\n" \
    "ds_read_b64_tr_b16 v[20:21], %[pc] offset:32\n" \
    "ds_read_b64_tr_b16 v[22:23], %[pd] offset:32\n" \
    "ds_read_b64_tr_b16 v[24:25], %[pc] offset:64\n" \
    "ds_read_b64_tr_b16 v[26:27], %[pd] offset:64\n" \
    "ds_read_b64_tr_b16 v[28:29], %[pc] offset:96\n" \
    "ds_read_b64_tr_b16 v[30:31], %[pd] offset:96\n" \
    "ds_read_b64_tr_b16 v[32:33], %[pc] offset:128\n" \
    "ds_read_b64_tr_b16 v[34:35], %[pd] offset:128\n" \
    "ds_read_b64_tr_b16 v[36:37], %[pc] offset:5120\n" \
    "ds_read_b64_tr_b16 v[38:39], %[pd] offset:5120\n" \
    "ds_read_b64_tr_b16 v[40:41], %[pc] offset:5152\n" \
    "ds_read_b64_tr_b16 v[42:43], %[pd] offset:5152\n" \
    "ds_read_b64_tr_b16 v[44:45], %[pc] offset:5184\n" \
    "ds_read_b64_tr_b16 v[46:47], %[pd] offset:5184\n" \
    "ds_read_b64_tr_b16 v[48:49], %[pc] offset:5216\n" \
    "ds_read_b64_tr_b16 v[50:51], %[pd] offset:5216\n" \
    "ds_read_b64_tr_b16 v[52:53], %[pc] offset:5248\n" \
    "ds_read_b64_tr_b16 v[54:55], %[pd] offset:5248\n" \
    "ds_read_b64_tr_b16 v[56:57], %[pc] offset:10240\n" \
    "ds_read_b64_tr_b16 v[58:59], %[pd] offset:10240\n" \
    "ds_read_b64_tr_b16 v[60:61], %[pc] offset:10272\n" \
    "ds_read_b64_tr_b16 v[62:63], %[pd] offset:10272\n" \
    "ds_read_b64_tr_b16 v[64:65], %[pc] offset:10304\n" \
    "ds_read_b64_tr_b16 v[66:67], %[pd] offset:10304\n" \
    "ds_read_b64_tr_b16 v[68:69], %[pc] offset:10336\n" \
    "ds_read_b64_tr_b16 v[70:71], %[pd] offset:10336\n" \
    "ds_read_b64_tr_b16 v[72:73], %[pc] offset:10368\n" \
    "ds_read_b64_tr_b16 v[74:75], %[pd] offset:10368\n" \
    "ds_read_b64_tr_b16 v[76:77], %[pc] offset:15360\n" \
    "ds_read_b64_tr_b16 v[78:79], %[pd] offset:15360\n" \
    "ds_read_b64_tr_b16 v[80:81], %[pc] offset:15392\n" \
    "ds_read_b64_tr_b16 v[82:83], %[pd] offset:15392\n" \
    "ds_read_b64_tr_b16 v[84:85], %[pc] offset:15424\n" \
    "ds_read_b64_tr_b16 v[86:87], %[pd] offset:15424\n" \
    "ds_read_b64_tr_b16 v[88:89], %[pc] offset:15456\n" \
    "ds_read_b64_tr_b16 v[90:91], %[pd] offset:15456\n" \
    "ds_read_b64_tr_b16 v[92:93], %[pc] offset:15488\n" \
    "ds_read_b64_tr_b16 v[94:95], %[pd] offset:15488\n" \
    "ds_read_b64_tr_b16 v[96:97], %[pc] offset:20480\n" \
    "ds_read_b64_tr_b16 v[98:99], %[pd] offset:20480\n" \
    "ds_read_b64_tr_b16 v[100:101], %[pc] offset:20512\n" \
    "ds_read_b64_tr_b16 v[102:103], %[pd] offset:20512\n" \
    "ds_read_b64_tr_b16 v[104:105], %[pc] offset:20544\n" \
    "ds_read_b64_tr_b16 v[106:107], %[pd] offset:20544\n" \
    "ds_read_b64_tr_b16 v[108:109], %[pc] offset:20576\n" \
    "ds_read_b64_tr_b16 v[110:111], %[pd] offset:20576\n" \
    "ds_read_b64_tr_b16 v[112:113], %[pc] offset:20608\n" \
    "ds_read_b64_tr_b16 v[114:115], %[pd] offset:20608\n" \
    "v_mov_b32 v157, 1\n" \
    "s_mov_b64 exec, 1\n" \
    "ds_add_u32 %[ctl], v157 offset:96\n" \
    "s_mov_b64 exec, -1\n" \
    "s_mov_b32 %[cnt], 0\n" \
    "Lb_%=:\n" \
    "ds_read_b32 v157, %[ctl] offset:100\n" \
    "s_add_u32 %[cnt], %[cnt], 1\n" \
    "s_waitcnt lgkmcnt(0)\n" \
    "v_readfirstlane_b32 %[tmp], v157\n" \
    "s_cmp_ge_u32 %[tmp], 1\n" \
    "s_cbranch_scc1 Lbx_%=\n" \
    "s_sleep 1\n" \
    "s_cmp_lt_u32 %[cnt], %[cap]\n" \
    "s_cbranch_scc1 Lb_%=\n" \
    "Lbx_%=:\n" \
    "s_mov_b32 %[cnt], 0\n" \
    "Lc_%=:\n" \
    "ds_read_b32 v157, %[ctl] offset:48\n" \
    "s_add_u32 %[cnt], %[cnt], 1\n" \
    "s_waitcnt lgkmcnt(0)\n" \
    "v_readfirstlane_b32 %[tmp], v157\n" \
    "s_cmp_ge_u32 %[tmp], %[ost]\n" \
    "s_cbranch_scc1 Lcx_%=\n" \
    "s_sleep 1\n" \
    "s_cmp_lt_u32 %[cnt], %[cap]\n" \
    "s_cbranch_scc1 Lc_%=\n" \
    "Lcx_%=:\n" \
    "s_mov_b32 %[cnt], 0\n" \
    "Ld_%=:\n" \
    "global_load_dword v156, %[wat], %[wsp] sc1\n" \
    "s_add_u32 %[cnt], %[cnt], 1\n" \
    "s_waitcnt vmcnt(0)\n" \
    "v_cmp_eq_u32 vcc, %[so], v156\n" \
    "s_mov_b32 %[wm], vcc_lo\n" \
    "s_and_b32 %[tmp], %[wm], 3\n" \
    "s_cbranch_scc0 Ldx_%=\n" \
    "s_sleep 4\n" \
    "s_cmp_lt_u32 %[cnt], %[cap]\n" \
    "s_cbranch_scc1 Ld_%=\n" \
    "Ldx_%=:\n" \
    "s_setprio 1\n" \
    "global_load_dwordx4 v[116:119], %[ofo], %[wsp] offset:0 sc1\n" \
    "global_load_dwordx4 v[120:123], %[ofo], %[wsp] offset:1024 sc1\n" \
    "global_load_dwordx4 v[124:127], %[ofo], %[wsp] offset:2048 sc1\n" \
    "global_load_dwordx4 v[128:131], %[ofo], %[wsp] offset:3072 sc1\n" \
    "global_load_dwordx2 v[132:133], %[of4], %[wsp] sc1\n" \
    "global_load_dwordx2 v[134:135], %[of4], %[wsp] offset:128 sc1\n" \
    "ds_read_b32 v157, %[ctl] offset:96\n" \
    "s_mov_b32 %[ah], 1\n" \
    "s_and_b32 %[tmp], %[wm], 12\n" \
    "s_cbranch_scc1 Ln_%=\n" \
    "s_waitcnt vmcnt(5)\n" \
    "v_mfma_f32_16x16x32_bf16 v[136:139], v[16:19], v[116:119], 0\n" \
    "v_mfma_f32_16x16x32_bf16 v[140:143], v[20:23], v[116:119], 0\n" \
    "v_mfma_f32_16x16x32_bf16 v[144:147], v[24:27], v[116:119], 0\n" \
    "v_mfma_f32_16x16x32_bf16 v[148:151], v[28:31], v[116:119], 0\n" \
    "v_mfma_f32_16x16x32_bf16 v[152:155], v[32:35], v[116:119], 0\n" \
    "global_load_dwordx4 v[116:119], %[ofo], %[ws2] offset:0 sc1\n" \
    "s_waitcnt vmcnt(5)\n" \
    "v_mfma_f32_16x16x32_bf16 v[136:139], v[36:39], v[120:123], v[136:139]\n" \
    "v_mfma_f32_16x16x32_bf16 v[140:143], v[40:43], v[120:123], v[140:143]\n" \
    "v_mfma_f32_16x16x32_bf16 v[144:147], v[44:47], v[120:123], v[144:147]\n" \
    "v_mfma_f32_16x16x32_bf16 v[148:151], v[48:51], v[120:123], v[148:151]\n" \
    "v_mfma_f32_16x16x32_bf16 v[152:155], v[52:55], v[120:123], v[152:155]\n" \
    "global_load_dwordx4 v[120:123], %[ofo], %[ws2] offset:1024 sc1\n" \
    "s_waitcnt vmcnt(5)\n" \
    "v_mfma_f32_16x16x32_bf16 v[136:139], v[56:59], v[124:127], v[136:139]\n" \
    "v_mfma_f32_16x16x32_bf16 v[140:143], v[60:63], v[124:127], v[140:143]\n" \
    "v_mfma_f32_16x16x32_bf16 v[144:147], v[64:67], v[124:127], v[144:147]\n" \
    "v_mfma_f32_16x16x32_bf16 v[148:151], v[68:71], v[124:127], v[148:151]\n" \
    "v_mfma_f32_16x16x32_bf16 v[152:155], v[72:75], v[124:127], v[152:155]\n" \
    "global_load_dwordx4 v[124:127], %[ofo], %[ws2] offset:2048 sc1\n" \
    "s_waitcnt vmcnt(5)\n" \
    "v_mfma_f32_16x16x32_bf16 v[136:139], v[76:79], v[128:131], v[136:139]\n" \
    "v_mfma_f32_16x16x32_bf16 v[140:143], v[80:83], v[128:131], v[140:143]\n" \
    "v_mfma_f32_16x16x32_bf16 v[144:147], v[84:87], v[128:131], v[144:147]\n" \
    "v_mfma_f32_16x16x32_bf16 v[148:151], v[88:91], v[128:131], v[148:151]\n" \
    "v_mfma_f32_16x16x32_bf16 v[152:155], v[92:95], v[128:131], v[152:155]\n" \
    "global_load_dwordx4 v[128:131], %[ofo], %[ws2] offset:3072 sc1\n" \
    "s_waitcnt vmcnt(4)\n" \
    "v_mfma_f32_16x16x32_bf16 v[136:139], v[96:99], v[132:135], v[136:139]\n" \
    "v_mfma_f32_16x16x32_bf16 v[140:143], v[100:103], v[132:135], v[140:143]\n" \
    "v_mfma_f32_16x16x32_bf16 v[144:147], v[104:107], v[132:135], v[144:147]\n" \
    "v_mfma_f32_16x16x32_bf16 v[148:151], v[108:111], v[132:135], v[148:151]\n" \
    "v_mfma_f32_16x16x32_bf16 v[152:155], v[112:115], v[132:135], v[152:155]\n" \
    "global_load_dwordx2 v[132:133], %[of4], %[ws2] sc1\n" \
    "global_load_dwordx2 v[134:135], %[of4], %[ws2] offset:128 sc1\n" \
    "s_branch Lj_%=\n" \
    "Ln_%=:\n" \
    "s_waitcnt vmcnt(5)\n" \
    "global_load_dword v156, %[wat], %[wsp] sc1\n" \
    "s_waitcnt vmcnt(6)\n" \
    "v_mfma_f32_16x16x32_bf16 v[136:139], v[16:19], v[116:119], 0\n" \
    "v_mfma_f32_16x16x32_bf16 v[140:143], v[20:23], v[116:119], 0\n" \
    "v_mfma_f32_16x16x32_bf16 v[144:147], v[24:27], v[116:119], 0\n" \
    "v_mfma_f32_16x16x32_bf16 v[148:151], v[28:31], v[116:119], 0\n" \
    "v_mfma_f32_16x16x32_bf16 v[152:155], v[32:35], v[116:119], 0\n" \
    "s_waitcnt vmcnt(5)\n" \
    "v_mfma_f32_16x16x32_bf16 v[136:139], v[36:39], v[120:123], v[136:139]\n" \
    "v_mfma_f32_16x16x32_bf16 v[140:143], v[40:43], v[120:123], v[140:143]\n" \
    "v_mfma_f32_16x16x32_bf16 v[144:147], v[44:47], v[120:123], v[144:147]\n" \
    "v_mfma_f32_16x16x32_bf16 v[148:151], v[48:51], v[120:123], v[148:151]\n" \
    "v_mfma_f32_16x16x32_bf16 v[152:155], v[52:55], v[120:123], v[152:155]\n" \
    "s_waitcnt vmcnt(4)\n" \
    "v_mfma_f32_16x16x32_bf16 v[136:139], v[56:59], v[124:127], v[136:139]\n" \
    "v_mfma_f32_16x16x32_bf16 v[140:143], v[60:63], v[124:127], v[140:143]\n" \
    "v_mfma_f32_16x16x32_bf16 v[144:147], v[64:67], v[124:127], v[144:147]\n" \
    "v_mfma_f32_16x16x32_bf16 v[148:151], v[68:71], v[124:127], v[148:151]\n" \
    "v_mfma_f32_16x16x32_bf16 v[152:155], v[72:75], v[124:127], v[152:155]\n" \
    "s_waitcnt vmcnt(3)\n" \
    "v_mfma_f32_16x16x32_bf16 v[136:139], v[76:79], v[128:131], v[136:139]\n" \
    "v_mfma_f32_16x16x32_bf16 v[140:143], v[80:83], v[128:131], v[140:143]\n" \
    "v_mfma_f32_16x16x32_bf16 v[144:147], v[84:87], v[128:131], v[144:147]\n" \
    "v_mfma_f32_16x16x32_bf16 v[148:151], v[88:91], v[128:131], v[148:151]\n" \
    "v_mfma_f32_16x16x32_bf16 v[152:155], v[92:95], v[128:131], v[152:155]\n" \
    "s_waitcnt vmcnt(1)\n" \
    "v_mfma_f32_16x16x32_bf16 v[136:139], v[96:99], v[132:135], v[136:139]\n" \
    "v_mfma_f32_16x16x32_bf16 v[140:143], v[100:103], v[132:135], v[140:143]\n" \
    "v_mfma_f32_16x16x32_bf16 v[144:147], v[104:107], v[132:135], v[144:147]\n" \
    "v_mfma_f32_16x16x32_bf16 v[148:151], v[108:111], v[132:135], v[148:151]\n" \
    "v_mfma_f32_16x16x32_bf16 v[152:155], v[112:115], v[132:135], v[152:155]\n" \
    "s_waitcnt vmcnt(0)\n" \
    "v_cmp_eq_u32 vcc, %[so], v156\n" \
    "s_mov_b32 %[wm], vcc_lo\n" \
    "s_mov_b32 %[ah], 0\n" \
    "s_and_b32 %[tmp], %[wm], 12\n" \
    "s_cbranch_scc1 Lj_%=\n" \
    "s_mov_b32 %[ah], 1\n" \
    "global_load_dwordx4 v[116:119], %[ofo], %[ws2] offset:0 sc1\n" \
    "global_load_dwordx4 v[120:123], %[ofo], %[ws2] offset:1024 sc1\n" \
    "global_load_dwordx4 v[124:127], %[ofo], %[ws2] offset:2048 sc1\n" \
    "global_load_dwordx4 v[128:131], %[ofo], %[ws2] offset:3072 sc1\n" \
    "global_load_dwordx2 v[132:133], %[of4], %[ws2] sc1\n" \
    "global_load_dwordx2 v[134:135], %[of4], %[ws2] offset:128 sc1\n" \
    "Lj_%=:\n" \
    "s_waitcnt lgkmcnt(0)\n" \
    "v_readfirstlane_b32 %[tmp], v157\n" \
    "s_cmp_ge_u32 %[tmp], 8\n" \
    "s_cbranch_scc1 Lex_%=\n" \
    "s_mov_b32 %[cnt], 0\n" \
    "Le_%=:\n" \
    "ds_read_b32 v157, %[ctl] offset:96\n" \
    "s_add_u32 %[cnt], %[cnt], 1\n" \
    "s_waitcnt lgkmcnt(0)\n" \
    "v_readfirstlane_b32 %[tmp], v157\n" \
    "s_cmp_ge_u32 %[tmp], 8\n" \
    "s_cbranch_scc1 Lex_%=\n" \
    "s_sleep 1\n" \
    "s_cmp_lt_u32 %[cnt], %[cap]\n" \
    "s_cbranch_scc1 Le_%=\n" \
    "Lex_%=:\n" \
    "s_nop 3\n" \
    "v_cmp_u_f32 vcc, v136, v136\n" \
    "s_cbranch_vccz Lfx_%=\n" \
    "s_waitcnt vmcnt(0)\n" \
    "s_mov_b32 %[ah], 0\n" \
    "s_mov_b32 %[cnt], 0\n" \
    "Lf_%=:\n" \
    "global_load_dwordx4 v[116:119], %[ofo], %[wsp] offset:0 sc1\n" \
    "global_load_dwordx4 v[120:123], %[ofo], %[wsp] offset:1024 sc1\n" \
    "global_load_dwordx4 v[124:127], %[ofo], %[wsp] offset:2048 sc1\n" \
    "global_load_dwordx4 v[128:131], %[ofo], %[wsp] offset:3072 sc1\n" \
    "global_load_dwordx2 v[132:133], %[of4], %[wsp] sc1\n" \
    "global_load_dwordx2 v[134:135], %[of4], %[wsp] offset:128 sc1\n" \
    "s_add_u32 %[cnt], %[cnt], 1\n" \
    "s_waitcnt vmcnt(0)\n" \
    "v_xor_b32 v157, %[so], v116\n" \
    "v_xor_b32 v158, %[so], v117\n" \
    "v_min_u32 v157, v157, v158\n" \
    "v_xor_b32 v158, %[so], v118\n" \
    "v_min_u32 v157, v157, v158\n" \
    "v_xor_b32 v158, %[so], v119\n" \
    "v_min_u32 v157, v157, v158\n" \
    "v_xor_b32 v158, %[so], v120\n" \
    "v_min_u32 v157, v157, v158\n" \
    "v_xor_b32 v158, %[so], v121\n" \
    "v_min_u32 v157, v157, v158\n" \
    "v_xor_b32 v158, %[so], v122\n" \
    "v_min_u32 v157, v157, v158\n" \
    "v_xor_b32 v158, %[so], v123\n" \
    "v_min_u32 v157, v157, v158\n" \
    "v_xor_b32 v158, %[so], v124\n" \
    "v_min_u32 v157, v157, v158\n" \
    "v_xor_b32 v158, %[so], v125\n" \
    "v_min_u32 v157, v157, v158\n" \
    "v_xor_b32 v158, %[so], v126\n" \
    "v_min_u32 v157, v157, v158\n" \
    "v_xor_b32 v158, %[so], v127\n" \
    "v_min_u32 v157, v157, v158\n" \
    "v_xor_b32 v158, %[so], v128\n" \
    "v_min_u32 v157, v157, v158\n" \
    "v_xor_b32 v158, %[so], v129\n" \
    "v_min_u32 v157, v157, v158\n" \
    "v_xor_b32 v158, %[so], v130\n" \
    "v_min_u32 v157, v157, v158\n" \
    "v_xor_b32 v158, %[so], v131\n" \
    "v_min_u32 v157, v157, v158\n" \
    "v_xor_b32 v158, %[so], v132\n" \
    "v_min_u32 v157, v157, v158\n" \
    "v_xor_b32 v158, %[so], v133\n" \
    "v_min_u32 v157, v157, v158\n" \
    "v_xor_b32 v158, %[so], v134\n" \
    "v_min_u32 v157, v157, v158\n" \
    "v_xor_b32 v158, %[so], v135\n" \
    "v_min_u32 v157, v157, v158\n" \
    "v_cmp_eq_u32 vcc, 0, v157\n" \
    "s_cmp_lt_u32 %[cnt], %[cap]\n" \
    "s_cbranch_vccz Lfy_%=\n" \
    "s_cbranch_scc1 Lf_%=\n" \
    "Lfy_%=:\n" \
    "s_waitcnt vmcnt(0)\n" \
    "v_mfma_f32_16x16x32_bf16 v[136:139], v[16:19], v[116:119], 0\n" \
    "v_mfma_f32_16x16x32_bf16 v[140:143], v[20:23], v[116:119], 0\n" \
    "v_mfma_f32_16x16x32_bf16 v[144:147], v[24:27], v[116:119], 0\n" \
    "v_mfma_f32_16x16x32_bf16 v[148:151], v[28:31], v[116:119], 0\n" \
    "v_mfma_f32_16x16x32_bf16 v[152:155], v[32:35], v[116:119], 0\n" \
    "s_waitcnt vmcnt(0)\n" \
    "v_mfma_f32_16x16x32_bf16 v[136:139], v[36:39], v[120:123], v[136:139]\n" \
    "v_mfma_f32_16x16x32_bf16 v[140:143], v[40:43], v[120:123], v[140:143]\n" \
    "v_mfma_f32_16x16x32_bf16 v[144:147], v[44:47], v[120:123], v[144:147]\n" \
    "v_mfma_f32_16x16x32_bf16 v[148:151], v[48:51], v[120:123], v[148:151]\n" \
    "v_mfma_f32_16x16x32_bf16 v[152:155], v[52:55], v[120:123], v[152:155]\n" \
    "s_waitcnt vmcnt(0)\n" \
    "v_mfma_f32_16x16x32_bf16 v[136:139], v[56:59], v[124:127], v[136:139]\n" \
    "v_mfma_f32_16x16x32_bf16 v[140:143], v[60:63], v[124:127], v[140:143]\n" \
    "v_mfma_f32_16x16x32_bf16 v[144:147], v[64:67], v[124:127], v[144:147]\n" \
    "v_mfma_f32_16x16x32_bf16 v[148:151], v[68:71], v[124:127], v[148:151]\n" \
    "v_mfma_f32_16x16x32_bf16 v[152:155], v[72:75], v[124:127], v[152:155]\n" \
    "s_waitcnt vmcnt(0)\n" \
    "v_mfma_f32_16x16x32_bf16 v[136:139], v[76:79], v[128:131], v[136:139]\n" \
    "v_mfma_f32_16x16x32_bf16 v[140:143], v[80:83], v[128:131], v[140:143]\n" \
    "v_mfma_f32_16x16x32_bf16 v[144:147], v[84:87], v[128:131], v[144:147]\n" \
    "v_mfma_f32_16x16x32_bf16 v[148:151], v[88:91], v[128:131], v[148:151]\n" \
    "v_mfma_f32_16x16x32_bf16 v[152:155], v[92:95], v[128:131], v[152:155]\n" \
    "s_waitcnt vmcnt(0)\n" \
    "v_mfma_f32_16x16x32_bf16 v[136:139], v[96:99], v[132:135], v[136:139]\n" \
    "v_mfma_f32_16x16x32_bf16 v[140:143], v[100:103], v[132:135], v[140:143]\n" \
    "v_mfma_f32_16x16x32_bf16 v[144:147], v[104:107], v[132:135], v[144:147]\n" \
    "v_mfma_f32_16x16x32_bf16 v[148:151], v[108:111], v[132:135], v[148:151]\n" \
    "v_mfma_f32_16x16x32_bf16 v[152:155], v[112:115], v[132:135], v[152:155]\n" \
    "s_nop 3\n" \
    "Lfx_%=:\n" \
    "v_mov_b32 v157, 1\n" \
    "ds_write_b128 %[xa0], v[136:139] offset:0\n" \
    "ds_write_b128 %[xa0], v[140:143] offset:1024\n" \
    "ds_write_b128 %[xa0], v[144:147] offset:2048\n" \
    "ds_write_b128 %[xa0], v[148:151] offset:3072\n" \
    "ds_write_b128 %[xa0], v[152:155] offset:4096\n" \
    "ds_write_b32 %[pf], v157 offset:0\n" \
    "s_cmp_lg_u32 %[ah], 0\n" \
    "s_cbranch_scc1 Lk_%=\n" \
    "s_and_b32 %[tmp], %[wm], 12\n" \
    "s_cbranch_scc0 Lhx_%=\n" \
    "s_mov_b32 %[cnt], 0\n" \
    "Lh_%=:\n" \
    "global_load_dword v156, %[wat], %[wsp] sc1\n" \
    "s_add_u32 %[cnt], %[cnt], 1\n" \
    "s_waitcnt vmcnt(0)\n" \
    "v_cmp_eq_u32 vcc, %[so], v156\n" \
    "s_mov_b32 %[wm], vcc_lo\n" \
    "s_and_b32 %[tmp], %[wm], 12\n" \
    "s_cbranch_scc0 Lhx_%=\n" \
    "s_sleep 4\n" \
    "s_cmp_lt_u32 %[cnt], %[cap]\n" \
    "s_cbranch_scc1 Lh_%=\n" \
    "Lhx_%=:\n" \
    "global_load_dwordx4 v[116:119], %[ofo], %[ws2] offset:0 sc1\n" \
    "global_load_dwordx4 v[120:123], %[ofo], %[ws2] offset:1024 sc1\n" \
    "global_load_dwordx4 v[124:127], %[ofo], %[ws2] offset:2048 sc1\n" \
    "global_load_dwordx4 v[128:131], %[ofo], %[ws2] offset:3072 sc1\n" \
    "global_load_dwordx2 v[132:133], %[of4], %[ws2] sc1\n" \
    "global_load_dwordx2 v[134:135], %[of4], %[ws2] offset:128 sc1\n" \
    "Lk_%=:\n" \
    "s_waitcnt vmcnt(5)\n" \
    "v_mfma_f32_16x16x32_bf16 v[136:139], v[16:19], v[116:119], 0\n" \
    "v_mfma_f32_16x16x32_bf16 v[140:143], v[20:23], v[116:119], 0\n" \
    "s_waitcnt vmcnt(4)\n" \
    "v_mfma_f32_16x16x32_bf16 v[136:139], v[36:39], v[120:123], v[136:139]\n" \
    "v_mfma_f32_16x16x32_bf16 v[140:143], v[40:43], v[120:123], v[140:143]\n" \
    "s_waitcnt vmcnt(3)\n" \
    "v_mfma_f32_16x16x32_bf16 v[136:139], v[56:59], v[124:127], v[136:139]\n" \
    "v_mfma_f32_16x16x32_bf16 v[140:143], v[60:63], v[124:127], v[140:143]\n" \
    "s_waitcnt vmcnt(2)\n" \
    "v_mfma_f32_16x16x32_bf16 v[136:139], v[76:79], v[128:131], v[136:139]\n" \
    "v_mfma_f32_16x16x32_bf16 v[140:143], v[80:83], v[128:131], v[140:143]\n" \
    "s_waitcnt vmcnt(0)\n" \
    "v_mfma_f32_16x16x32_bf16 v[136:139], v[96:99], v[132:135], v[136:139]\n" \
    "v_mfma_f32_16x16x32_bf16 v[140:143], v[100:103], v[132:135], v[140:143]\n" \
    "v_mfma_f32_16x16x32_bf16 v[144:147], v[24:27], v[116:119], 0\n" \
    "v_mfma_f32_16x16x32_bf16 v[144:147], v[44:47], v[120:123], v[144:147]\n" \
    "v_mfma_f32_16x16x32_bf16 v[144:147], v[64:67], v[124:127], v[144:147]\n" \
    "v_mfma_f32_16x16x32_bf16 v[144:147], v[84:87], v[128:131], v[144:147]\n" \
    "v_mfma_f32_16x16x32_bf16 v[144:147], v[104:107], v[132:135], v[144:147]\n" \
    "v_mov_b32 v157, 1\n" \
    "v_mfma_f32_16x16x32_bf16 v[148:151], v[28:31], v[116:119], 0\n" \
    "v_mfma_f32_16x16x32_bf16 v[148:151], v[48:51], v[120:123], v[148:151]\n" \
    "ds_write_b128 %[xa1], v[136:139] offset:0\n" \
    "v_mfma_f32_16x16x32_bf16 v[148:151], v[68:71], v[124:127], v[148:151]\n" \
    "v_cmp_u_f32 vcc, v136, v136\n" \
    "v_mfma_f32_16x16x32_bf16 v[148:151], v[88:91], v[128:131], v[148:151]\n" \
    "ds_write_b128 %[xa1], v[140:143] offset:1024\n" \
    "v_mfma_f32_16x16x32_bf16 v[148:151], v[108:111], v[132:135], v[148:151]\n" \
    "v_mfma_f32_16x16x32_bf16 v[152:155], v[32:35], v[116:119], 0\n" \
    "v_mfma_f32_16x16x32_bf16 v[152:155], v[52:55], v[120:123], v[152:155]\n" \
    "v_mfma_f32_16x16x32_bf16 v[152:155], v[72:75], v[124:127], v[152:155]\n" \
    "ds_write_b128 %[xa1], v[144:147] offset:2048\n" \
    "v_mfma_f32_16x16x32_bf16 v[152:155], v[92:95], v[128:131], v[152:155]\n" \
    "v_mfma_f32_16x16x32_bf16 v[152:155], v[112:115], v[132:135], v[152:155]\n" \
    "s_cbranch_vccnz Li_%=\n" \
    "s_nop 3\n" \
    "ds_write_b128 %[xa1], v[148:151] offset:3072\n" \
    "s_nop 2\n" \
    "ds_write_b128 %[xa1], v[152:155] offset:4096\n" \
    "ds_write_b32 %[pf], v157 offset:32\n" \
    "s_branch Lz_%=\n" \
    "Li_%=:\n" \
    "s_waitcnt vmcnt(0)\n" \
    "s_mov_b32 %[ah], 0\n" \
    "s_mov_b32 %[cnt], 0\n" \
    "Li2_%=:\n" \
    "global_load_dwordx4 v[116:119], %[ofo], %[ws2] offset:0 sc1\n" \
    "global_load_dwordx4 v[120:123], %[ofo], %[ws2] offset:1024 sc1\n" \
    "global_load_dwordx4 v[124:127], %[ofo], %[ws2] offset:2048 sc1\n" \
    "global_load_dwordx4 v[128:131], %[ofo], %[ws2] offset:3072 sc1\n" \
    "global_load_dwordx2 v[132:133], %[of4], %[ws2] sc1\n" \
    "global_load_dwordx2 v[134:135], %[of4], %[ws2] offset:128 sc1\n" \
    "s_add_u32 %[cnt], %[cnt], 1\n" \
    "s_waitcnt vmcnt(0)\n" \
    "v_xor_b32 v157, %[so], v116\n" \
    "v_xor_b32 v158, %[so], v117\n" \
    "v_min_u32 v157, v157, v158\n" \
    "v_xor_b32 v158, %[so], v118\n" \
    "v_min_u32 v157, v157, v158\n" \
    "v_xor_b32 v158, %[so], v119\n" \
    "v_min_u32 v157, v157, v158\n" \
    "v_xor_b32 v158, %[so], v120\n" \
    "v_min_u32 v157, v157, v158\n" \
    "v_xor_b32 v158, %[so], v121\n" \
    "v_min_u32 v157, v157, v158\n" \
    "v_xor_b32 v158, %[so], v122\n" \
    "v_min_u32 v157, v157, v158\n" \
    "v_xor_b32 v158, %[so], v123\n" \
    "v_min_u32 v157, v157, v158\n" \
    "v_xor_b32 v158, %[so], v124\n" \
    "v_min_u32 v157, v157, v158\n" \
    "v_xor_b32 v158, %[so], v125\n" \
    "v_min_u32 v157, v157, v158\n" \
    "v_xor_b32 v158, %[so], v126\n" \
    "v_min_u32 v157, v157, v158\n" \
    "v_xor_b32 v158, %[so], v127\n" \
    "v_min_u32 v157, v157, v158\n" \
    "v_xor_b32 v158, %[so], v128\n" \
    "v_min_u32 v157, v157, v158\n" \
    "v_xor_b32 v158, %[so], v129\n" \
    "v_min_u32 v157, v157, v158\n" \
    "v_xor_b32 v158, %[so], v130\n" \
    "v_min_u32 v157, v157, v158\n" \
    "v_xor_b32 v158, %[so], v131\n" \
    "v_min_u32 v157, v157, v158\n" \
    "v_xor_b32 v158, %[so], v132\n" \
    "v_min_u32 v157, v157, v158\n" \
    "v_xor_b32 v158, %[so], v133\n" \
    "v_min_u32 v157, v157, v158\n" \
    "v_xor_b32 v158, %[so], v134\n" \
    "v_min_u32 v157, v157, v158\n" \
    "v_xor_b32 v158, %[so], v135\n" \
    "v_min_u32 v157, v157, v158\n" \
    "v_cmp_eq_u32 vcc, 0, v157\n" \
    "s_cmp_lt_u32 %[cnt], %[cap]\n" \
    "s_cbranch_vccz Li2y_%=\n" \
    "s_cbranch_scc1 Li2_%=\n" \
    "Li2y_%=:\n" \
    "s_waitcnt vmcnt(0)\n" \
    "v_mfma_f32_16x16x32_bf16 v[136:139], v[16:19], v[116:119], 0\n" \
    "v_mfma_f32_16x16x32_bf16 v[140:143], v[20:23], v[116:119], 0\n" \
    "v_mfma_f32_16x16x32_bf16 v[144:147], v[24:27], v[116:119], 0\n" \
    "v_mfma_f32_16x16x32_bf16 v[148:151], v[28:31], v[116:119], 0\n" \
    "v_mfma_f32_16x16x32_bf16 v[152:155], v[32:35], v[116:119], 0\n" \
    "s_waitcnt vmcnt(0)\n" \
    "v_mfma_f32_16x16x32_bf16 v[136:139], v[36:39], v[120:123], v[136:139]\n" \
    "v_mfma_f32_16x16x32_bf16 v[140:143], v[40:43], v[120:123], v[140:143]\n" \
    "v_mfma_f32_16x16x32_bf16 v[144:147], v[44:47], v[120:123], v[144:147]\n" \
    "v_mfma_f32_16x16x32_bf16 v[148:151], v[48:51], v[120:123], v[148:151]\n" \
    "v_mfma_f32_16x16x32_bf16 v[152:155], v[52:55], v[120:123], v[152:155]\n" \
    "s_waitcnt vmcnt(0)\n" \
    "v_mfma_f32_16x16x32_bf16 v[136:139], v[56:59], v[124:127], v[136:139]\n" \
    "v_mfma_f32_16x16x32_bf16 v[140:143], v[60:63], v[124:127], v[140:143]\n" \
    "v_mfma_f32_16x16x32_bf16 v[144:147], v[64:67], v[124:127], v[144:147]\n" \
    "v_mfma_f32_16x16x32_bf16 v[148:151], v[68:71], v[124:127], v[148:151]\n" \
    "v_mfma_f32_16x16x32_bf16 v[152:155], v[72:75], v[124:127], v[152:155]\n" \
    "s_waitcnt vmcnt(0)\n" \
    "v_mfma_f32_16x16x32_bf16 v[136:139], v[76:79], v[128:131], v[136:139]\n" \
    "v_mfma_f32_16x16x32_bf16 v[140:143], v[80:83], v[128:131], v[140:143]\n" \
    "v_mfma_f32_16x16x32_bf16 v[144:147], v[84:87], v[128:131], v[144:147]\n" \
    "v_mfma_f32_16x16x32_bf16 v[148:151], v[88:91], v[128:131], v[148:151]\n" \
    "v_mfma_f32_16x16x32_bf16 v[152:155], v[92:95], v[128:131], v[152:155]\n" \
    "s_waitcnt vmcnt(0)\n" \
    "v_mfma_f32_16x16x32_bf16 v[136:139], v[96:99], v[132:135], v[136:139]\n" \
    "v_mfma_f32_16x16x32_bf16 v[140:143], v[100:103], v[132:135], v[140:143]\n" \
    "v_mfma_f32_16x16x32_bf16 v[144:147], v[104:107], v[132:135], v[144:147]\n" \
    "v_mfma_f32_16x16x32_bf16 v[148:151], v[108:111], v[132:135], v[148:151]\n" \
    "v_mfma_f32_16x16x32_bf16 v[152:155], v[112:115], v[132:135], v[152:155]\n" \
    "s_nop 3\n" \
    "v_mov_b32 v157, 1\n" \
    "s_nop 1\n" \
    "ds_write_b128 %[xa1], v[136:139] offset:0\n" \
    "ds_write_b128 %[xa1], v[140:143] offset:1024\n" \
    "ds_write_b128 %[xa1], v[144:147] offset:2048\n" \
    "ds_write_b128 %[xa1], v[148:151] offset:3072\n" \
    "ds_write_b128 %[xa1], v[152:155] offset:4096\n" \
    "ds_write_b32 %[pf], v157 offset:32\n" \
    "Lz_%=:\n" \
    "s_nop 0\n"
#define P_CLOB "v16", "v17", "v18", "v19", "v20", "v21", "v22", "v23", "v24", "v25", "v26", "v27", "v28", "v29", "v30", "v31", "v32", "v33", "v34", "v35", "v36", "v37", "v38", "v39", "v40", "v41", "v42", "v43", "v44", "v45", "v46", "v47", "v48", "v49", "v50", "v51", "v52", "v53", "v54", "v55", "v56", "v57", "v58", "v59", "v60", "v61", "v62", "v63", "v64", "v65", "v66", "v67", "v68", "v69", "v70", "v71", "v72", "v73", "v74", "v75", "v76", "v77", "v78", "v79", "v80", "v81", "v82", "v83", "v84", "v85", "v86", "v87", "v88", "v89", "v90", "v91", "v92", "v93", "v94", "v95", "v96", "v97", "v98", "v99", "v100", "v101", "v102", "v103", "v104", "v105", "v106", "v107", "v108", "v109", "v110", "v111", "v112", "v113", "v114", "v115", "v116", "v117", "v118", "v119", "v120", "v121", "v122", "v123", "v124", "v125", "v126", "v127", "v128", "v129", "v130", "v131", "v132", "v133", "v134", "v135", "v136", "v137", "v138", "v139", "v140", "v141", "v142", "v143", "v144", "v145", "v146", "v147", "v148", "v149", "v150", "v151", "v152", "v153", "v154", "v155", "v156", "v157", "v158", "v159", "vcc", "scc", "memory"
static_assert(C_WO - L_CTRL == 56 && C_OST - L_CTRL == 48 && C_BP - L_CTRL == 96 && C_WO0 - L_CTRL == 184 && C_QR2 - L_CTRL == 188 && C_SENT2 - L_CTRL == 100, "offsets in the output stream");
static_assert(L_CTRL - G_ZR == 1280 && C_HX - C_FREE == 144 && C_SENT - L_CTRL == 52, "offsets in the projection stream");
static_assert(C_HR - L_CTRL == 144 && C_RED - L_CTRL == 84 && C_XD - L_CTRL == 80 && C_SENT - L_CTRL == 52, "offsets in the conv stream");
typedef __attribute__((ext_vector_type(16))) unsigned u32x16;
typedef __attribute__((address_space(3))) const unsigned lds_cu32;
typedef __attribute__((address_space(3))) u32x2 lds_u32x2;
typedef __attribute__((address_space(3))) u32x4 lds_u32x4;
typedef __attribute__((address_space(3))) f32x4 lds_f32x4;
#define GPTR(T, lo, hi) ((T*)(__attribute__((address_space(1))) T*)(((u64)(hi) << 32) | (u64)(lo)))
__device__ __forceinline__ float add1(float a, float b) { float r; asm("v_add_f32 %0, %1, %2" : "=v"(r) : "v"(a), "v"(b)); return r; }
__device__ __forceinline__ float mul1(float a, float b) { float r; asm("v_mul_f32 %0, %1, %2" : "=v"(r) : "v"(a), "v"(b)); return r; }
__device__ __forceinline__ f32x4 add4(f32x4 a, f32x4 b) { return (f32x4){add1(a[0], b[0]), add1(a[1], b[1]), add1(a[2], b[2]), add1(a[3], b[3])}; }
__device__ __forceinline__ f32x4 mul4(f32x4 a, f32x4 b) { return (f32x4){mul1(a[0], b[0]), mul1(a[1], b[1]), mul1(a[2], b[2]), mul1(a[3], b[3])}; }
#if defined(__HIP_DEVICE_COMPILE__) && defined(NO_PK)
#define NOPK __attribute__((target("no-packed-fp32-ops")))
#else
#define NOPK
#endif
__global__ NOPK __launch_bounds__(768, 3) void fused_kernel(const u16* x_, const u16* kin_, const u16* vin_, const u16* shift_k_,
                                                       const u16* ssm_k_, const u16* ssm_D_, const u16* wqkv_, const u16* wo_,
                                                       const u16* gw_, char* ws_, u16* out_) {
    u32x16 ka0;
    u32x4 ka1;
    u32x2 ka2;
    asm volatile("s_load_dwordx16 %0, %3, 0\n\ts_load_dwordx4 %1, %3, 64\n\ts_load_dwordx2 %2, %3, 80"
                 : "=&s"(ka0), "=&s"(ka1), "=&s"(ka2) : "s"(__builtin_amdgcn_kernarg_segment_ptr()));
#define x GPTR(const u16, ka0[0], ka0[1])
#define kin GPTR(const u16, ka0[2], ka0[3])
#define vin GPTR(const u16, ka0[4], ka0[5])
#define shift_k GPTR(const u16, ka0[6], ka0[7])
#define ssm_k GPTR(const u16, ka0[8], ka0[9])
#define ssm_D GPTR(const u16, ka0[10], ka0[11])
#define wqkv GPTR(const u16, ka0[12], ka0[13])
#define wo GPTR(const u16, ka0[14], ka0[15])
#define gw GPTR(const u16, ka1[0], ka1[1])
#define ws GPTR(char, ka1[2], ka1[3])
#define out GPTR(u16, ka2[0], ka2[1])
#define wsr __builtin_amdgcn_make_buffer_rsrc(ws, 0, 0x4000000, 0x00020000)
    __shared__ __attribute__((aligned(16))) char smem[LDS_TOTAL];
    const int bid = __builtin_amdgcn_workgroup_id_x();
    const int tid = __builtin_amdgcn_workitem_id_x();
    int lane = tid & 63;
    const int wave = __builtin_amdgcn_readfirstlane(tid >> 6);
    const int xcd = bid & 7, r5 = bid >> 3, r = r5 & 15;
    const bool isG = r5 >= 16;
    const int hg = (xcd & 1) * 4 + (r & 3);
    const int rb2 = (xcd >> 1) * 4 + (r >> 2);
    const int cq = (xcd & 1) * 2 + (r & 1), rbv = (xcd >> 1) * 8 + (r >> 1);
    const int vid = cq * 32 + rbv;
    const int sid = rbv * 4 + cq;
    const int rbo = xcd * 4 + (r >> 3) * 2 + ((r5 >> 4) ^ GH ^ 1), oc = r5 & 7;
    const int w = wave & 3;
    const int sl = (w & 1) * 2 + (w >> 1);
    const int rtq = (rbo >> 1) * 8 + (w & 1) * 4 + (rbo & 1) * 2 + (w >> 1);
    const int g = lane >> 4, n16 = lane & 15, q4 = n16 >> 2, p4 = lane & 3;
    const unsigned lb = (unsigned)(size_t)(lds_char*)smem;
    u64 tag;
    {
        u64 id = llvm_dispatch_id();
        u64 dp = (u64)(size_t)__builtin_amdgcn_dispatch_ptr();
        tag = ((u64)((unsigned)id ^ 0x5bd1e995u) << 32) | (u64)((unsigned)(dp >> 6) ^ (unsigned)(id >> 32) ^ 0x9e3779b9u);
    }
    const unsigned tlo = (unsigned)tag, thi = (unsigned)(tag >> 32);
    if (wave == 0) {
        u64 pc;
        asm volatile("s_getpc_b64 %0" : "=s"(pc));
        pc &= ~255ull;
        const unsigned cl = lane * 32 + r5;
        if (cl < CODE_LINES) GLT(cl * 128, pc);
    }

#define WO_PATS() unsigned wpm, wpo, wpm4, wpo4; { \
        const int c0_ = w * 64 + lane, r0_ = (c0_ * 6554) >> 16, k0_ = r0_ ^ ((r0_ & 8) >> 1), b0_ = oc * 160 + (c0_ - r0_ * 10) * 16; \
        const int c4_ = 256 + lane, r4_ = (c4_ * 6554) >> 16, k4_ = r4_ ^ ((r4_ & 8) >> 1), b4_ = oc * 160 + (c4_ - r4_ * 10) * 16; \
        wpm = opaque((k0_ + ((k0_ >> 4) << 3)) * (D_DIM * 2) + b0_); wpo = opaque((20 * (k0_ >> 2) + (k0_ & 3) + ((~k0_ & 4) << 2)) * (D_DIM * 2) + b0_); \
        wpm4 = opaque((k4_ + 8) * (D_DIM * 2) + b4_); wpo4 = opaque((20 * (k4_ >> 2) + (k4_ & 3) + ((~k4_ & 4) << 2)) * (D_DIM * 2) + b4_); }
#define WO_ROW(ks) (((ks) / 5) * 160 + ((ks) % 5 < 4 ? 40 * ((ks) % 5) : 0))
#define WO_K(ks) GLS((ks) % 5 == 4 ? wpo : wpm, (const char*)wo + WO_ROW(ks) * (D_DIM * 2), lb + w * 1024, WO_POS(ks));
#define SEL4(a, b, c, d) (w == 0 ? (a) : w == 1 ? (b) : w == 2 ? (c) : (d))
#define WO_X(a, b, c, d) GLS(SEL4((a) % 5 == 4, (b) % 5 == 4, (c) % 5 == 4, (d) % 5 == 4) ? wpo4 : wpm4, \
        (const char*)wo + SEL4(WO_ROW(a), WO_ROW(b), WO_ROW(c), WO_ROW(d)) * (D_DIM * 2), lb + SEL4(WO_POS(a), WO_POS(b), WO_POS(c), WO_POS(d)), 4096);
#define WO_4(a, b, c, d) { WO_K(a) WO_K(b) WO_K(c) WO_K(d) WO_X(a, b, c, d) }
#define RD_FLAGS() ((lane & 31) < 8 ? WS_HF + ((rbo >> 1) * 8 + (lane & 7)) * 128 : WS_SF + ((lane & 31) < 24 ? oc * 16 + (lane & 31) - 8 : (lane & 3) * 32 + (rbo & ~1) + ((lane >> 2) & 1)) * 128)
    const int kr = KROT;
#define T_C(i) __builtin_amdgcn_readfirstlane((kr + (i)) >= 10 ? kr + (i) - 10 : kr + (i))
#define T_WV() ({ const int j_ = min(n16, 14); (unsigned)((r * 4 + g) * (NQKV * 2) + j_ * 128 + ((j_ >= 5) + (j_ >= 10) + (xcd & 1)) * 640); })
#define T_XV() ((unsigned)(((xcd >> 1) * 512 + r * 32 + (lane & 31)) * (D_DIM * 2)))
#define T_W(wv, i) GLT(wv, (const char*)wqkv + (size_t)T_C(i) * (64 * NQKV * 2))
#define T_X(xv, i) { const int c0_ = T_C(i), c1_ = T_C((i) + 1); GLT((xv) + (lane < 32 ? c0_ : c1_) * 128, x); }
#define T_PAIR(i) { T_W(wv, i); T_X(xv_, i); T_W(wv, (i) + 1); }

    unsigned e0;
    if (isG) {
        const unsigned t0_ = T_WV() + (kr + (w >> 1)) * (64 * NQKV * 2);
        const unsigned t1_ = ((xcd >> 1) * 512 + (w == 1 ? r * 32 + (lane & 31) : r - ((xcd >> 1) ? 16 : 0))) * (D_DIM * 2) + (w == 1 ? kr + (lane >> 5) : min(lane, 9)) * 128;
        e0 = (w & 1) ? t1_ : t0_;
    } else {
        const unsigned c = (wave >= 5 ? wave - 5 : wave) * 64 + lane, row = (c * 3277) >> 16;
        e0 = rbv * (64 * D_DIM * 2) + cq * 320 + row * (D_DIM * 2 - 320) + c * 16;
    }
    asm volatile("" : "+v"(e0));
    asm volatile("s_waitcnt lgkmcnt(0)" : "+s"(ka0), "+s"(ka1), "+s"(ka2) :: "memory");
    if (isG) {
        if ((wave >> 2) == 1) GLT(e0, (w & 1) ? x : wqkv);
        BARRIER();
    }
    u32x2 skw;
    if (!isG && wave < 10) {
        const int tens = wave >= 5, i5 = wave - 5 * tens;
        const unsigned lw = lb + (tens ? V_VST : V_KST) + i5 * 1024;
        const char* kb = (const char*)(tens ? vin : kin);
        __builtin_amdgcn_s_sleep(V_DELAY);
        GLSP(e0, kb, lw, 0);
        GLSP(e0, kb + 16 * D_DIM * 2, lw, 5152);
        GLSP(e0, kb + 32 * D_DIM * 2, lw, 2 * 5152);
        GLSP(e0, kb + 48 * D_DIM * 2, lw, 3 * 5152);
        asm volatile("global_load_dwordx2 %0, %1, %2" : "=v"(skw) : "v"((cq * 160 + wave * 16 + n16) * 8), "s"(shift_k) : "memory");
        if (wave == 0) { if (lane < 60) GLS(e0, kb - (rbv ? 3 * D_DIM * 2 : 0), lb, V_KST - 32 - 960); }
    }
    if (wave == 11) {
        if (lane < 16) *(u32x4*)(smem + L_CTRL + lane * 16) = (u32x4){0u, 0u, 0u, 0u};
        asm volatile("s_waitcnt lgkmcnt(0)" ::: "memory");
    }
#define Y_OFF (WS_Y + rbo * (D_DIM * 128) + oc * 10240 + w * 1024)
#define G_LOAD() float g1 = __uint_as_float(__builtin_amdgcn_raw_buffer_load_b32(wsr, (lane & 15) * 4, WS_G + rtq * 64, 16));
#define Y_FIRST() u32x4 yv[3]; { yv[0] = __builtin_amdgcn_raw_buffer_load_b128(wsr, lane * 16, Y_OFF, 16); yv[1] = __builtin_amdgcn_raw_buffer_load_b128(wsr, lane * 16, Y_OFF + 4096, 16); \
        yv[2] = __builtin_amdgcn_raw_buffer_load_b128(wsr, lane * 16, Y_OFF + (w < 2 ? 8192 : 0), 16); }
#define RELANE() const int lane_ = opaque(lane); const int g = lane_ >> 4, n16 = lane_ & 15, q4 = n16 >> 2, p4 = lane_ & 3; \
        const unsigned qa = lb + (isG ? G_Q : V_Q) + w * 2560 + n16 * 160 + 8 * g
#define SENT4(v) __any(min(min((v)[0] ^ S_O, (v)[1] ^ S_O), min((v)[2] ^ S_O, (v)[3] ^ S_O)) == 0u)
#define Q_READ(base) f32x4 gq[5]; { const int l_ = opaque(lane); _Pragma("unroll") for (int ct = 0; ct < 5; ++ct) { \
            const u32x2 q2 = *(const lds_u32x2*)(size_t)(lb + (base) + w * 2560 + (l_ & 15) * 160 + 8 * (l_ >> 4) + ct * 32); \
            gq[ct] = (f32x4){lo_f(q2[0]), hi_f(q2[0]), lo_f(q2[1]), hi_f(q2[1])}; } \
        asm volatile("" : "+v"(gq[0]), "+v"(gq[1]), "+v"(gq[2]), "+v"(gq[3]), "+v"(gq[4]) :: "memory"); }
#define FINAL() _Pragma("unroll") for (int ct = 0; ct < 5; ++ct) { \
            const f32x4 sb = *(const lds_f32x4*)(size_t)(pa + (10240 - h1) + ct * 1024) + *(const lds_f32x4*)(size_t)(pa + (10240 - h1) + 5120 + ct * 1024); \
            const f32x4 fin = gq[ct] + g0 * (sa[ct] + sb); \
            pk[ct][0] = pack2(fin[0], fin[1]); \
            pk[ct][1] = pack2(fin[2], fin[3]); }
#define X4(v) min(min((v)[0] ^ S_O, (v)[1] ^ S_O), min((v)[2] ^ S_O, (v)[3] ^ S_O))
#define P_STAGE() { \
        RELANE(); \
        const unsigned l_y = lb + (isG ? G_Y : V_Y); \
        unsigned yw = l_y + (w * 8 + (lane_ >> 3)) * Y_PITCH + (lane_ & 7) * 16, yw2 = yw + (w < 2 ? 64 * Y_PITCH : 0); \
        unsigned yr = l_y + (4 * g + q4) * Y_PITCH + sl * 32 + p4 * 8; \
        float g0 = 1.f - g1; \
        unsigned pa = lb + PART(sl) + lane_ * 16; \
        const auto outr = __builtin_amdgcn_make_buffer_rsrc(out, 0, S_LEN * D_DIM * 2, 0x00020000); \
        unsigned ob = ((rtq * 16 + n16) * D_DIM + oc * 80) * 2, ob2 = ob + (64 + 4 * g) * 2; \
        ob += ((g & 1) * 16 + (g >> 1) * 8) * 2; \
_Pragma("unroll") \
        for (int ct = 0; ct < 5; ++ct) gq[ct] = mul4(gq[ct], (f32x4){g1, g1, g1, g1}); \
        asm volatile("" : "+v"(gq[0]), "+v"(gq[1]), "+v"(gq[2]), "+v"(gq[3]), "+v"(gq[4]), "+v"(ob), "+v"(ob2), "+v"(yw), "+v"(yw2), "+v"(yr), "+v"(pa), "+v"(g0)); \
        SB(); \
_Pragma("nounroll") \
        for (int it = 0; it < SPIN_CAP; ++it) { \
            *(lds_u32x4*)(size_t)yw = yv[0]; \
            *(lds_u32x4*)(size_t)(yw + 32 * Y_PITCH) = yv[1]; \
            *(lds_u32x4*)(size_t)yw2 = yv[2]; \
            if (!__any(min(X4(yv[0]), min(X4(yv[1]), X4(yv[2]))) == 0u)) break; \
            __builtin_amdgcn_s_sleep(Y_SLEEP); \
            asm volatile("" ::: "memory"); \
            if (SENT4(yv[0])) yv[0] = __builtin_amdgcn_raw_buffer_load_b128(wsr, lane * 16, Y_OFF, 16); \
            if (SENT4(yv[1])) yv[1] = __builtin_amdgcn_raw_buffer_load_b128(wsr, lane * 16, Y_OFF + 4096, 16); \
            if (SENT4(yv[2])) yv[2] = __builtin_amdgcn_raw_buffer_load_b128(wsr, lane * 16, Y_OFF + (w < 2 ? 8192 : 0), 16); \
        } \
        asm volatile("" ::: "memory"); \
        lds_add(lb + C_Y, lane); \
        STAMP(8); \
_Pragma("nounroll") \
        for (int it = 0; it < SPIN_CAP; ++it) if (__builtin_amdgcn_readfirstlane(lds_ld32(lb + C_Y)) >= 4) break; \
        u32x2 y2[5]; \
_Pragma("unroll") \
        for (int ct = 0; ct < 5; ++ct) y2[ct] = __builtin_bit_cast(u32x2, TRRD((lds_char*)(size_t)(yr + ct * 16 * Y_PITCH))); \
_Pragma("unroll") \
        for (int ct = 0; ct < 5; ++ct) gq[ct] = mul4(gq[ct], (f32x4){lo_f(y2[ct][0]), hi_f(y2[ct][0]), lo_f(y2[ct][1]), hi_f(y2[ct][1])}); \
        asm volatile("" : "+v"(gq[0]), "+v"(gq[1]), "+v"(gq[2]), "+v"(gq[3]), "+v"(gq[4])); \
        STAMP(9); \
          \
        u32x4 pf; \
_Pragma("nounroll") \
        for (int it = 0; it < SPIN_CAP; ++it) { \
            pf = lds_v128(lb + C_PF + sl * 16); \
            if (__builtin_amdgcn_readfirstlane((pf[0] & pf[1]) | (pf[2] & pf[3]))) break; \
            __builtin_amdgcn_s_sleep(1); \
        } \
        const unsigned h1 = __builtin_amdgcn_readfirstlane(pf[0] & pf[1]) ? 0 : 10240; \
        f32x4 sa[5]; \
_Pragma("unroll") \
        for (int ct = 0; ct < 5; ++ct) sa[ct] = add4(*(const lds_f32x4*)(size_t)(pa + h1 + ct * 1024), *(const lds_f32x4*)(size_t)(pa + h1 + 5120 + ct * 1024)); \
        asm volatile("" : "+v"(sa[0]), "+v"(sa[1]), "+v"(sa[2]), "+v"(sa[3]), "+v"(sa[4])); \
_Pragma("nounroll") \
        for (int it = 0; it < SPIN_CAP; ++it) { \
            if (__builtin_amdgcn_readfirstlane((pf[0] & pf[1]) & (pf[2] & pf[3]))) break; \
            pf = lds_v128(lb + C_PF + sl * 16); \
        } \
        asm volatile("" ::: "memory"); \
        STAMP(13); \
        unsigned pk[5][2]; \
        FINAL() \
_Pragma("unroll") \
        for (int p = 0; p < 2; ++p) { \
            const auto r0 = __builtin_amdgcn_permlane16_swap(pk[2 * p][0], pk[2 * p + 1][0], false, false); \
            const auto r1 = __builtin_amdgcn_permlane16_swap(pk[2 * p][1], pk[2 * p + 1][1], false, false); \
            __builtin_amdgcn_raw_buffer_store_b128((u32x4){r0[0], r1[0], r0[1], r1[1]}, outr, ob + p * 64, 0, OUT_AUX); \
        } \
        __builtin_amdgcn_raw_buffer_store_b64((u32x2){pk[4][0], pk[4][1]}, outr, ob2, 0, OUT_AUX); \
        FLUSH(); \
        return; }

    if (isG) {
      if (wave >= 4) {
        const int half = (wave >> 2) - 1;
        if (half) __builtin_amdgcn_s_setprio(C1_PRIO); else __builtin_amdgcn_s_setprio(C0_PRIO);
        const int rb = rb2 * 2 + half;
        unsigned ap[2], wp[4];
#pragma unroll
        for (int sub = 0; sub < 2; ++sub) ap[sub] = lb + half * 8192 + n16 * 128 + (((sub * 4 + g) ^ ((n16 >> 1) & 7)) * 16);
#pragma unroll
        for (int i = 0; i < 4; ++i) {
            const int part = i < 3 ? i : (p4 < 3 ? p4 : 2);
            const int Q = i < 3 ? 5 * w + (w & 1) + p4 : 5 * w + ((w & 1) ? 0 : 4);
            const int c2 = (Q >> 1) - 2 * (hg & 1);
            const int cc = ((unsigned)c2 < 8u ? part * 8 + c2 : 24 + part * 2 + (c2 & 1)) ^ (2 * ((g & 1) * 4 + q4));
            wp[i] = lb + W_OFF(0) + (8 * g + q4) * W_PITCH + cc * 16 + (Q & 1) * 8;
        }
        const int qi = n16, par = lane & 1;
        if (wave == 11) {
            f32x4 cm0, cm1;
            u32x4 psel;
#pragma unroll
            for (int e = 0; e < 4; ++e) {
                const bool own = 4 * g + e <= qi, pm = (e & 1) == par;
                cm0[e] = pm && !own ? 0.f : -3.0e38f;
                cm1[e] = pm && own ? 0.f : -3.0e38f;
                const bool ow = 4 * g + par + 2 * (e & 1) <= qi;
                const unsigned by = (e & 1) ? 0x0302u : 0x0100u;
                psel[e] = (ow == (e >= 2)) ? (par ? (by << 16) | 0x0c0cu : 0x0c0c0000u | by) : 0x0c0c0c0cu;
            }
            *(lds_f32x4*)(size_t)(lb + G_TAB + lane * 16) = cm0;
            *(lds_f32x4*)(size_t)(lb + G_TAB + 1024 + lane * 16) = cm1;
            *(lds_u32x4*)(size_t)(lb + G_TAB + 2048 + lane * 16) = psel;
            if (lane < 5) *(lds_u32x2*)(size_t)(lb + G_ZR + lane * 128) = (u32x2){0u, 0u};
        }
        const unsigned vxw = lb + G_VX + w * 1152;
        const unsigned scw = g == 2 ? vxw + half * 512 + qi * 8 : lb + G_VXD;
        const int hj = (hg & 1) * 4 + w;
        const unsigned oo = WS_O + ((rb * 4 + (g & 1)) * 20 + (hg >> 1) * 5 + (hj >> 1)) * 1024 + (((w & 1) * 2 + (g >> 1)) * 16 + qi) * 16;
        const unsigned ox = WS_O + ((rb * 4 + g) * 20 + (hg >> 1) * 5 + 4) * 1024 + hj * 128 + qi * 8;
        unsigned c0_, c1_, l16 = lane * 16;
        STAMP(6);
        CSTAMP(0);
        asm volatile(G_ASM
                     : [cnt] "=&s"(c0_), [tmp] "=&s"(c1_), [wp0] "+v"(wp[0]), [wp1] "+v"(wp[1]), [wp2] "+v"(wp[2]), [wp3] "+v"(wp[3]), [l16] "+v"(l16)
                     : [ap0] "v"(ap[0]), [ap1] "v"(ap[1]), [scw] "v"(scw), [oo] "v"(oo), [ox] "v"(ox),
                       [vxb] "v"(vxw + half * 512 + (4 * g + q4) * 8), [zr] "v"(lb + G_ZR), [fr] "v"(lb + C_FREE + (wave - 4) * 4),
                       [tabc] "s"(lb + G_TAB), [hxc] "s"(lb + G_HX + w * 1024), [hxd] "s"(lb + G_HXO + w * 256),
                       [wsp] "s"(ws), [half] "s"(half), [rb2] "s"(rb2), [cap] "s"(SPIN_CAP), [sc] "s"(0x3ea52b5bu),
                       [thi] "s"(thi), [hfo] "s"(w == HINT_W ? WS_H + ((rb2 * 8 + hg) * 2 + half) * 128 : 0)
                     : G_CLOB);
        lane = l16 >> 4;
        CSTAMP(1);
        __builtin_amdgcn_s_setprio(T_PRIO);
        if (half) {
            spin_ge(lb + C_SENT2, 1);
            G_LOAD();
            spin_ge(lb + C_WO, 4);
            spin_all(lb + C_FREE + (lane & 7) * 4, 18);
            Q_READ(G_Q);
            if (GY_DELAY > 0) __builtin_amdgcn_s_sleep(GY_DELAY);
            Y_FIRST();
            STAMP(4);
            P_STAGE();
        }
        lds_add(lb + C_OST, lane);
        STAMP(4);
      } else {
        __builtin_amdgcn_s_setprio(L_PRIO);
        unsigned ev[9];
        {
            const int hi = lane >> 5, pos = lane & 31;
#pragma unroll
            for (int v = 0; v < 2; ++v) {
                const int row = 2 * w + hi + 8 * v;
                int L = pos ^ (2 * (v * 4 + (row & 3)));
                L = L > 29 ? L - 2 : L;
                const int part = L < 24 ? L >> 3 : (L - 24) >> 1;
                const int chunk = L < 24 ? (L & 7) + 2 * (hg & 1) : (L & 1) + ((hg & 1) ? 0 : 8);
                ev[v] = row * (NQKV * 2) + chunk * 16 + part * 1280 + hg * 160;
                ev[v + 2] = ev[v] + 16 * NQKV * 2;
            }
            const int ch = (lane & 7) ^ ((4 * (w & 1) + g) & 7);
#pragma unroll
            for (int j = 0; j < 5; ++j) {
                int t = rb2 * 128 - 16 + 8 * w + (lane >> 3) + 32 * j;
                t = t < 0 ? 0 : t;
                t = t > S_LEN - 1 ? S_LEN - 1 : t;
                ev[4 + j] = t * (D_DIM * 2) + ch * 16;
            }
        }
        SB();
        if (L_DELAY > 0) __builtin_amdgcn_s_sleep(L_DELAY);
        STAMP(5);
        if (w < 2) ring_issue<5, 0, ring_need(-1, 5)>(ev, x, wqkv, kr, lb + w * 1024); else ring_issue<4, 0, ring_need(-1, 4)>(ev, x, wqkv, kr, lb + w * 1024);
        SB();
        unsigned qf[3];
        WO_PATS();
#pragma unroll
        for (int j = 0; j < 3; ++j) { const int c = min(j * 64 + lane, 159); const int row = c / 10, ch = c - row * 10; qf[j] = opaque(row * (D_DIM * 2) + ch * 16); }
        const unsigned sfo = opaque(RD_FLAGS());
        const unsigned sf1 = opaque(WS_HF + (rb2 * 8 + (lane & 7)) * 128);
        const unsigned fra = opaque(lb + C_FREE + (lane & 7) * 4);
        SB();
        if (w < 2) {
            ring_issue<5, ring_need(-1, 5), ring_cum(-1, 5)>(ev, x, wqkv, kr, lb + w * 1024);
            waitvm<ring_cum(-1, 5) - ring_need(-1, 5)>();
            STAMP(6);
            BARRIER();
            ring_run<5, 0>(ev, x, wqkv, kr, lb + w * 1024);
        } else {
            ring_issue<4, ring_need(-1, 4), ring_cum(-1, 4)>(ev, x, wqkv, kr, lb + w * 1024);
            waitvm<ring_cum(-1, 4) - ring_need(-1, 4)>();
            BARRIER();
            ring_run<4, 0>(ev, x, wqkv, kr, lb + w * 1024);
        }
        STAMP(2);
#define WO_POS(ks) ((ks) < 10 ? G_R0 + (ks) * 5120 : G_R1 + ((ks) - 10) * 5120)
        static_assert(JL == 17, "last barrier");
        WO_4(0, 1, 2, 3)
        WAIT_VM(9);
        BARRIER();
        WO_4(4, 5, 10, 11)
        WAIT_VM(10);
        BARRIER();
        __builtin_amdgcn_s_setprio(WO_PRIO);
        u32x2 f1 = {0u, 0u};
#define SF1_ASK() if (w == 2) asm volatile("global_load_dwordx2 %0, %1, %2 sc1" : "=v"(f1) : "v"(sf1), "s"(ws) : "memory");
#define SF1_SEE() if (w == 2) { asm volatile("s_waitcnt vmcnt(5)" : "+v"(f1) :: "memory"); if (__all(f1[0] == tlo && f1[1] == thi)) lds_st32(lb + C_SENT, 1); }
        if (SF1_AT == 0) SF1_ASK()
        WO_4(12, 6, 7, 8)
        if (SF1_AT == 0) SF1_SEE()
        spin_all(fra, 16);
        if (SF1_AT == 1) SF1_ASK()
        WO_4(9, 13, 14, 15)
        if (SF1_AT == 1) SF1_SEE()
        WO_K(16)
        spin_all(fra, 17);
        WO_K(17) WO_K(18) WO_K(19) WO_X(16, 17, 18, 19)
#undef WO_POS
#pragma unroll
        for (int j = 0; j < 3; ++j)
            if (j < 2 || lane < 32) GLS(qf[j], (const char*)x + (size_t)(rtq * 16) * (D_DIM * 2) + oc * 160, lb + w * 2560, G_Q + j * 1024);
        STAMP(3);
        if (w == 2 && !__all(f1[0] == tlo && f1[1] == thi)) {
            poll_flags(wsr, sf1, tlo, thi);
            lds_st32(lb + C_SENT, 1);
        }
        u32x2 fv = {0u, 0u};
        if (w == 3) {
            asm volatile("global_load_dwordx2 %0, %1, %2 sc1" : "=v"(fv) : "v"(sfo), "s"(ws) : "memory");
            WAIT_VM(1);
        } else {
            WAIT_VM(0);
        }
        lds_add(lb + C_WO, lane);
        if (w == 3) {
            asm volatile("s_waitcnt vmcnt(0)" : "+v"(fv) :: "memory");
            if (!__all(fv[0] == tlo && fv[1] == thi)) poll_flags(wsr, sfo, tlo, thi);
            lds_st32(lb + C_SENT2, 1);
        }
        STAMP(4);
      }
    } else {
        __builtin_amdgcn_s_setprio(3);
        const int c5 = wave < 4 ? wave : 4;
        const int d = vid * 5 + c5;
        unsigned Dw = *(const __attribute__((address_space(4))) unsigned*)(size_t)(ssm_D + (d & ~1));
        unsigned hp0 = *(const __attribute__((address_space(4))) unsigned*)(size_t)(ssm_k + (vid * 5 + 4) * S_LEN + w * 512 - 2);
        BARRIER();
        if (wave <= 4) hp0 = 0;
        asm volatile("" : "+s"(Dw), "+s"(hp0));
#define WO_POS(ks) (V_WO + (ks) * 5120)
#define KV_DONE() do { if (lds_add_rtn(lb + C_KVD, lane) == 9) { \
            if (lane == 0) __hip_atomic_store((u64*)(ws + WS_XF + vid * 128), tag, __ATOMIC_RELAXED, __HIP_MEMORY_SCOPE_AGENT); lds_add(lb + C_KVD, lane); } } while (0)
        if (wave == 10) {
            const unsigned wv = T_WV(), xv_ = T_XV();
            T_PAIR(2);
            T_PAIR(4);
            if (WO_MALL) GLT((unsigned)((vid * 50 + min(lane, 49)) * 128), wo);
        }
        if (wave < 10) {
            const int m0 = (g & 1) * 2 + (g >> 1);
            const unsigned kp = opaque(lb + V_KST + (8 * m0 + q4) * 320 + (m0 >> 1) * 32 + wave * 32 + p4 * 8);
            const unsigned km = opaque(kp - 1280 - ((m0 & 1) ? 0 : 32));
            const unsigned sw_ = opaque(lb + V_STG + wave * 2048 + n16 * 128 + ((m0 + n16) & 7) * 16);
            const unsigned sr_ = opaque(lb + V_STG + wave * 2048 + (lane >> 3) * 128 + ((lane + (lane >> 3)) & 7) * 16);
            const unsigned e1 = opaque(WS_KV + (cq * 160 + wave * 16 + (lane >> 3)) * KV_PITCH + rbv * 128 + (lane & 7) * 16), e2 = opaque(e1 + 8 * KV_PITCH);
            asm volatile("s_waitcnt vmcnt(0)" : "+v"(skw) :: "memory");
            lds_add(lb + C_KV, lane);
            const f32x2 s0 = {lo_f(skw[0]), lo_f(skw[0])}, s1 = {hi_f(skw[0]), hi_f(skw[0])}, s2 = {lo_f(skw[1]), lo_f(skw[1])}, s3 = {hi_f(skw[1]), hi_f(skw[1])};
            spin_ge(lb + C_KV, 10);
            STAMP(2);
            u32x2 kq[2][3], vq[2][2];
#pragma unroll
            for (int i = 0; i < 2; ++i) {
                kq[i][0] = __builtin_bit_cast(u32x2, TRRD((lds_char*)(size_t)(km + i * 10304)));
#pragma unroll
                for (int e = 0; e < 2; ++e) kq[i][e + 1] = __builtin_bit_cast(u32x2, TRRD((lds_char*)(size_t)(kp + i * 10304 + e * 1280)));
#pragma unroll
                for (int e = 0; e < 2; ++e) vq[i][e] = __builtin_bit_cast(u32x2, TRRD((lds_char*)(size_t)(kp + i * 10304 + (V_VST - V_KST) + e * 1280)));
            }
            if (rbv == 0 && m0 == 0) kq[0][0] = (u32x2){0u, 0u};
            f32x2 kf[12], res[8];
#pragma unroll
            for (int e = 1; e < 12; ++e) {
                const unsigned u0 = kq[0][e >> 2][(e >> 1) & 1], u1 = kq[1][e >> 2][(e >> 1) & 1];
                kf[e] = (e & 1) ? (f32x2){hi_f(u0), hi_f(u1)} : (f32x2){lo_f(u0), lo_f(u1)};
            }
#pragma unroll
            for (int e = 0; e < 8; ++e) {
                const unsigned u0 = vq[0][e >> 2][(e >> 1) & 1], u1 = vq[1][e >> 2][(e >> 1) & 1];
                f32x2 a = kf[e + 1] * s3;
                a = kf[e + 2] * s2 + a;
                a = kf[e + 3] * s1 + a;
                a = kf[e + 4] * s0 + a;
                res[e] = a * ((e & 1) ? (f32x2){hi_f(u0), hi_f(u1)} : (f32x2){lo_f(u0), lo_f(u1)});
            }
#pragma unroll
            for (int i = 0; i < 2; ++i) {
                u32x4 pk;
#pragma unroll
                for (int e = 0; e < 4; ++e) pk[e] = pack2(res[2 * e][i], res[2 * e + 1][i]);
                *(lds_vu32x4*)(size_t)(sw_ ^ (i * 64)) = pk;
            }
            const u32x4 pk0 = lds_v128(sr_), pk1 = lds_v128(sr_ + 1024);
            __builtin_amdgcn_raw_buffer_store_b128(pk0, wsr, e1, 0, 16);
            __builtin_amdgcn_raw_buffer_store_b128(pk1, wsr, e2, 0, 16);
            lds_add(lb + C_KVST, lane);
            STAMP(3);
        }
        if (wave >= 8) {
            const unsigned wv = T_WV(), xv_ = T_XV();
            WO_PATS();
            if (wave == 10) {
            } else if (wave == 11) {
                if (lane < 56) {
#pragma unroll
                    for (int c5 = 0; c5 < 5; ++c5)
                        *(lds_u32x4*)(size_t)(lb + (lane < 16 ? V_KV(c5) + lane * 16 : V_F + c5 * XC_BYTES + 15 * XC_ROW + 256 + (lane < 36 ? XC_COPY + (lane - 16) * 16 : XC_B + 4 + (lane - 36) * 16))) = (u32x4){0u, 0u, 0u, 0u};
                }
                const unsigned x4o = opaque(WS_KV + d * KV_PITCH + lane * 16);
                spin_ge(lb + C_KVD, 11);
                if (POLL_DELAY > 0) __builtin_amdgcn_s_sleep(POLL_DELAY);
                const unsigned xfo = WS_XF + (cq * 32 + (lane & 31)) * 128;
                const u32x2 fv = __builtin_amdgcn_raw_buffer_load_b64(wsr, xfo, 0, 16);
                if (!__all(fv[0] == tlo && fv[1] == thi)) {
                    lds_st32(lb + C_GO, 1);
                    poll_flags(wsr, xfo, tlo, thi);
                }
                asm volatile("s_mov_b32 m0, %2\n\ts_nop 0\n\tglobal_load_lds_dwordx4 %0, %1 sc1\n\tglobal_load_lds_dwordx4 %0, %1 offset:1024 sc1\n\t"
                             "global_load_lds_dwordx4 %0, %1 offset:2048 sc1\n\tglobal_load_lds_dwordx4 %0, %1 offset:3072 sc1" ::"v"(x4o), "s"(ws), "s"(lb + V_KV4 + 256) : "memory");
                lds_add(lb + C_KVR, lane);
                STAMP(2);
                WAIT_VM(0);
                lds_st32(lb + C_HR, 4);
                STAMP(3);
                spin_ge(lb + C_XD, 4);
                lds_st32(lb + C_GO, 1);
            } else {
                WAIT_VM(0);
                KV_DONE();
                STAMP(11);
                spin_ge(lb + OS_W, OS_N);
                const u32x4 sv = {S_O, S_O, S_O, S_O};
#pragma unroll
                for (int i = 0; i < 10; ++i) {
                    if (i >= SENT_PACE) waitvm<SENT_PACE - 1>();
                    __builtin_amdgcn_raw_buffer_store_b128(sv, wsr, WS_O + sid * 20480 + (wave & 1) * 10240 + lane * 16 + i * 1024, 0, 16);
                }
                WAIT_VM(0);
                if (lds_add_rtn(lb + C_YS, lane) == 1) {
                    if (lane == 0) __hip_atomic_store((u64*)(ws + WS_HF + sid * 128), tag, __ATOMIC_RELAXED, __HIP_MEMORY_SCOPE_AGENT);
                }
                STAMP(12);
            }
            if (wave == 10) {
                spin_ge(lb + GO_W, GO_N);
                T_PAIR(6);
                if (T4_EARLY) WAIT_VM(1); else spin_ge(lb + GO2_W, GO2_N);
                T_PAIR(8);
            }
            spin_mid(lb + C_XD, 4);
            if (wave == WT_WAVE) {
#pragma unroll
                for (int i = 0; i < 4; ++i) {
                    const int l_ = r * 250 + min(i * 64 + lane, 249);
                    GLT((unsigned)((l_ < 800 ? l_ : l_ + 2400) * 128), wo);
                }
            }
            STAMP(5);
            u32x2 fv = {0u, 0u};
            const unsigned sfo = RD_FLAGS();
            if (r < 8) WO_4(2, 3, 4, 5) else WO_4(6, 7, 8, 9)
            if (WO_PACE >= 0) waitvm<(WO_PACE < 0 ? 0 : WO_PACE)>();
            if (r < 8) WO_4(6, 7, 8, 9) else WO_4(2, 3, 4, 5)
            WO_K(10) WO_K(11) WO_K(12)
            if (w < 3) WO_X(10, 11, 12, 12)
            STAMP(4);
            if (wave == SF_WAVE) {
                spin_mid(lb + SF_W, SF_N);
                asm volatile("global_load_dwordx2 %0, %1, %2 sc1" : "=v"(fv) : "v"(sfo), "s"(ws) : "memory");
                WAIT_VM(1);
            } else {
                WAIT_VM(0);
            }
            lds_add(lb + C_WO, lane);
            if (wave == SF_WAVE) {
                asm volatile("s_waitcnt vmcnt(0)" : "+v"(fv) :: "memory");
                if (!__all(fv[0] == tlo && fv[1] == thi)) poll_flags(wsr, sfo, tlo, thi);
                lds_st32(lb + C_SENT2, 1);
                STAMP(3);
            }
            STAMP(6);
        } else {
            const unsigned xb = lb + V_F + c5 * XC_BYTES;
            const unsigned hb = lb + V_KV(c5) + 256;
            const float Dd = (d & 1) ? hi_f(Dw) : lo_f(Dw);
            u32x4 hv[4];
            const unsigned l16 = lane * 16;
            {
                const char* hsrc = (const char*)ssm_k + (size_t)d * (S_LEN * 2) + (wave < 4 ? 0 : w * 1024);
                spin_ge(lb + C_KVST, 10);
#define H_LD(i) asm volatile("global_load_dwordx4 %0, %1, %2 offset:%3" H_POL : "=v"(hv[i]) : "v"(l16), "s"(hsrc), "n"((i) * 1024) : "memory")
                H_LD(0);
                if (wave < 4) { H_LD(1); H_LD(2); H_LD(3); WAIT_VM(4); } else WAIT_VM(1);
                KV_DONE();
                STAMP(4);
            }
            {
                const unsigned da = xb + (3 - g) * XC_ROW + (15 - n16) * 16 + (wave < 4 ? 0 : -w * (4 * XC_ROW));
                u32x4 ra[4], rb[4];
                asm volatile("s_waitcnt vmcnt(0)" : "+v"(hv[0]), "+v"(hv[1]), "+v"(hv[2]), "+v"(hv[3]) :: "memory");
#define H_PREV(i) __builtin_amdgcn_update_dpp((i) == 0 ? hp0 : __builtin_amdgcn_readlane(hv[(i) == 0 ? 0 : (i) - 1][3], 63), hv[i][3], 0x138, 0xf, 0xf, false)
#define BFI16(lo, hi) (((lo) & 0xffffu) | ((hi) & 0xffff0000u))
#define H_PREP(i) { ra[i] = (u32x4){__builtin_amdgcn_alignbit(hv[i][3], hv[i][3], 16), __builtin_amdgcn_alignbit(hv[i][2], hv[i][2], 16), __builtin_amdgcn_alignbit(hv[i][1], hv[i][1], 16), __builtin_amdgcn_alignbit(hv[i][0], hv[i][0], 16)}; \
                    rb[i] = (u32x4){BFI16(hv[i][3], hv[i][2]), BFI16(hv[i][2], hv[i][1]), BFI16(hv[i][1], hv[i][0]), BFI16(hv[i][0], H_PREV(i))}; \
                    *(lds_u32x4*)(size_t)(da + (3 - (i)) * (4 * XC_ROW) + XC_COPY) = ra[i]; *(lds_u32x4*)(size_t)(da + (3 - (i)) * (4 * XC_ROW) + XC_B + 4) = rb[i]; }
#define H_DUP(i) { *(lds_u32x4*)(size_t)(da + (3 - (i)) * (4 * XC_ROW) + XC_COPY - 32) = ra[i]; *(lds_u32x4*)(size_t)(da + (3 - (i)) * (4 * XC_ROW) + XC_B + 4 - 32) = rb[i]; }
                H_PREP(0)
                if (wave < 4) {
                    H_PREP(1) H_PREP(2) H_PREP(3)
                    if (n16 == 15) { H_DUP(0) H_DUP(1) H_DUP(2) if (g < 3) H_DUP(3) }
                } else {
                    if (n16 == 15 && (w < 3 || g < 3)) H_DUP(0)
                }
            }
            STAMP(9);
#define Y_ADDR(j0) const int l2_ = opaque(lane), gg = l2_ >> 4, mm = (l2_ & 3) | ((l2_ & 4) << 1) | ((l2_ & 8) >> 1); \
            const unsigned kp = hb + 2 * (256 * (j0) + 128 * (gg >> 1) + 8 * mm + 4 * (gg & 1)); \
            const int ts = 256 * ((j0) + (gg & 1)) + 128 * (gg >> 1) + 8 * mm; \
            const unsigned yo = WS_Y + d * 128 + ((ts >> 7) * 2 + ((ts >> 5) & 1)) * (D_DIM * 128) + (((ts >> 6) & 1) * 32 + (ts & 31)) * 2;
            if (wave < 4) {
                Y_ADDR(0)
                const int b1 = n16 >> 3, phi = n16 & 7;
                const unsigned xp = xb + ((phi & 1) ? XC_COPY - 4 : XC_B) + (1 - b1) * XC_ROW + 2 * (8 - 2 * (phi >> 1) + 8 * g);
                const unsigned hp = hb + 2 * (8 * (mm + g) - 120);
                const int sx4 = (4 - w) * XC_BYTES + (w >> 1) * XC_ROW + (w & 1) * 128, sh4 = V_KV4 - w * KV_BYTES + w * 128;
                unsigned cs[10];
                unsigned c0_, c1_, scr = lb + V_SCR + w * 8192 + l16;
                __builtin_amdgcn_s_setprio(CONV_PRIO);
                spin_ge(lb + C_KVR, 5);
                STAMP(6);
                CSTAMP(0);
                asm volatile(CONV_ASM
                             : [cnt] "=&s"(c0_), [tmp] "=&s"(c1_), [scr] "+v"(scr)
                             : [xp] "v"(xp), [hp] "v"(hp), [sx4] "s"(sx4), [sh4] "s"(sh4), [kvl] "s"(hb), [kvr] "v"(kp),
                               [ctl] "v"(lb + L_CTRL), [dd] "s"(Dd), [cap] "s"(SPIN_CAP), [xo] "v"(WS_KV + d * KV_PITCH + l16), [wsp] "s"(ws), [yo] "v"(yo)
                             : CONV_CLOB);
                __builtin_amdgcn_s_setprio(3);
                lane = ((scr - lb - V_SCR) >> 4) & 63;
                CSTAMP(1);
                lds_add(lb + C_CONVN, lane);
                STAMP(12);
                unsigned qg[3];
#pragma unroll
                for (int j = 0; j < 3; ++j) { const int c = j * 64 + (j < 2 ? lane : lane & 31), row = (c * 6554) >> 16; qg[j] = row * (D_DIM * 2 - 160) + c * 16; }
                spin_ge(lb + C_CONVN, 4);
#pragma unroll
                for (int j = 0; j < 3; ++j)
                    if (j < 2 || lane < 32) GLS(qg[j], (const char*)x + (size_t)(rtq * 16) * (D_DIM * 2) + oc * 160, lb + w * 2560, V_Q + j * 1024);
                spin_ge(lb + C_SENT2, 1);
                G_LOAD();
                asm volatile("s_waitcnt vmcnt(0)" : "+v"(g1) :: "memory");
                Q_READ(V_Q);
                if (VY_DELAY > 0) __builtin_amdgcn_s_sleep(VY_DELAY);
                Y_FIRST();
                STAMP(7);
                P_STAGE();
            } else {
                lds_add(lb + C_KVR, lane);
                Y_ADDR(2 * w)
                WO_PATS();
                {
                    const int grow = (xcd >> 1) * 512 + r * 32 + (xcd & 1) * 16 + w * 4 + g;
                    const unsigned go = opaque(grow * (D_DIM * 2) + n16 * 16);
                    if (GATE_LATE) spin_mid(lb + C_XD, 4);
                    u32x4 gx[5], gv[5];
#pragma unroll
                    for (int i = 0; i < 5; ++i) {
                        gx[i] = *(const u32x4*)((const char*)x + go + i * 256);
                        gv[i] = *(const u32x4*)((const char*)gw + n16 * 16 + i * 256);
                    }
                    float sa[4] = {0.f, 0.f, 0.f, 0.f};
#pragma unroll
                    for (int i = 0; i < 5; ++i)
#pragma unroll
                        for (int e = 0; e < 4; ++e) sa[e] = __builtin_fmaf(hi_f(gx[i][e]), hi_f(gv[i][e]), __builtin_fmaf(lo_f(gx[i][e]), lo_f(gv[i][e]), sa[e]));
                    float sacc = (sa[0] + sa[1]) + (sa[2] + sa[3]);
                    sacc += __builtin_amdgcn_update_dpp(0.f, sacc, 0x128, 0xf, 0xf, true);
                    sacc += __builtin_amdgcn_update_dpp(0.f, sacc, 0x124, 0xf, 0xf, true);
                    sacc += __builtin_amdgcn_update_dpp(0.f, sacc, 0x122, 0xf, 0xf, true);
                    sacc += __builtin_amdgcn_update_dpp(0.f, sacc, 0x121, 0xf, 0xf, true);
                    const float gres = rbf(__builtin_amdgcn_rcpf(1.f + __expf(-rbf(sacc))));
                    if (n16 == 0) __builtin_amdgcn_raw_buffer_store_b32(__float_as_uint(gres), wsr, WS_G + grow * 4, 0, 16);
                    spin_mid(lb + SENT_W, SENT_N);
                    const u32x4 ones = {S_O, S_O, S_O, S_O};
#pragma unroll
                    for (int i = 0; i < 5; ++i) {
                        const int c_ = (w * 5 + i) * 64 + lane, b_ = (c_ * 1639) >> 16;
                        __builtin_amdgcn_raw_buffer_store_b128(ones, wsr, WS_Y + vid * 640 + b_ * (D_DIM * 128 - 640) + c_ * 16, 0, 16);
                    }
                    WAIT_VM(0);
                    if (lds_add_rtn(lb + C_SFC, lane) == 3) {
                        lds_st32(lb + C_SENT, 1);
                        if (lane == 0) __hip_atomic_store((u64*)(ws + WS_SF + vid * 128), tag, __ATOMIC_RELAXED, __HIP_MEMORY_SCOPE_AGENT);
                    }
                    STAMP(6);
                }
                spin_mid(lb + C_RED, 4);
                STAMP(5);
                f32x4 acc[2];
#pragma unroll
                for (int e = 0; e < 2; ++e) {
                    const u32x2 kk = *(const lds_u32x2*)(size_t)(kp + e * 512);
                    acc[e] = mul4((f32x4){lo_f(kk[0]), hi_f(kk[0]), lo_f(kk[1]), hi_f(kk[1])}, (f32x4){Dd, Dd, Dd, Dd});
#pragma unroll
                    for (int o = 0; o < 4; ++o) acc[e] = add4(acc[e], *(const lds_f32x4*)(size_t)(lb + V_SCR + o * 8192 + (2 * w + e) * 1024 + l16));
                }
                lds_add(lb + C_RED2, lane);
                const auto r0 = __builtin_amdgcn_permlane16_swap(pack2(acc[0][0], acc[0][1]), pack2(acc[1][0], acc[1][1]), false, false);
                const auto r1 = __builtin_amdgcn_permlane16_swap(pack2(acc[0][2], acc[0][3]), pack2(acc[1][2], acc[1][3]), false, false);
                STAMP(7);
                spin_ge(lb + C_RED2, 4);
                WO_4(0, 1, 13, 14) WO_4(15, 16, 17, 18) WO_K(19)
                if (w == 0) WO_X(19, 19, 19, 19)
                if (__builtin_amdgcn_readfirstlane(lds_ld32(lb + C_SENT))) {
                    __builtin_amdgcn_raw_buffer_store_b128((u32x4){r0[0], r1[0], r0[1], r1[1]}, wsr, yo, 0, 16);
                    WAIT_VM(1);
                    lds_add(lb + C_WO, lane);
                } else {
                    WAIT_VM(0);
                    lds_add(lb + C_WO, lane);
                    spin_mid(lb + C_SENT, 1);
                    __builtin_amdgcn_raw_buffer_store_b128((u32x4){r0[0], r1[0], r0[1], r1[1]}, wsr, yo, 0, 16);
                }
            }
            STAMP(12);
        }
    }

    __builtin_amdgcn_s_setprio(T_PRIO);
    {
        RELANE();
        const int kh = (wave >> 2) == (isG ? 0 : 1), kq = kh * 2 + (w & 1), pp = w >> 1;
        const int rte = (rbo >> 1) * 8 + (rbo & 1) * 2 + pp;
        const unsigned ofo = WS_O + (rte * 20 + kq * 5) * 1024 + lane_ * 16;
        const int hsw = (g & 1) * 4;
        const unsigned lofs = lb + (8 * g + hsw + q4) * 160 + p4 * 8 + (w & 1) * 25600;
        const unsigned pc = lofs + (isG ? (kh ? G_R1 : G_R0) : V_WO + kh * 51200);
        const unsigned pd = pc + (4 - 2 * hsw) * 160;
        const unsigned ob4 = WS_O + (rte * 20 + kq * 5 + 4) * 1024;
        const unsigned wo_ = ob4 + (lane_ & 2) * 40960 + (lane_ & 1) * 512 + WATCH_J * 128;
        const unsigned xa = lb + kq * 5120 + lane_ * 16;
        unsigned c0_, c1_, c2_, c3_;
        asm volatile(P_ASM
                     : [cnt] "=&s"(c0_), [tmp] "=&s"(c1_), [wm] "=&s"(c2_), [ah] "=&s"(c3_)
                     : [pc] "v"(pc), [pd] "v"(pd), [ofo] "v"(ofo), [of4] "v"(ob4 + g * 256 + n16 * 8), [wat] "v"(wo_), [xa0] "v"(xa + PART(pp)), [xa1] "v"(xa + PART(2 + pp)),
                       [pf] "v"(lb + C_PF + pp * 16 + kq * 4), [ctl] "v"(lb + L_CTRL), [wsp] "s"(ws), [ws2] "s"(ws + 4 * 20480), [won] "s"(isG ? 4 : 8),
                       [ost] "s"(isG ? G_WST : V_WST0), [cap] "s"(SPIN_CAP), [so] "s"(S_O), [thi] "s"(thi)
                     : P_CLOB);
        FLUSH();
    }
}

#undef x
#undef kin
#undef vin
#undef shift_k
#undef ssm_k
#undef ssm_D
#undef wqkv
#undef wo
#undef gw
#undef ws
#undef out
#undef wsr
extern "C" void kernel_launch(void* const* d_in, const int* in_sizes, int n_in, void* d_out, int out_size,
                              void* d_ws, size_t ws_size, hipStream_t stream) {
    const u16* q = (const u16*)d_in[0];
    const u16* k = (const u16*)d_in[1];
    const u16* v = (const u16*)d_in[2];
    const u16* shift_k = (const u16*)d_in[3];
    const u16* ssm_k = (const u16*)d_in[4];
    const u16* ssm_D = (const u16*)d_in[5];
    const u16* wqkv = (const u16*)d_in[7];
    const u16* wo = (const u16*)d_in[8];
    const u16* gw = (const u16*)d_in[9];
    fused_kernel<<<256, 768, 0, stream>>>(q, k, v, shift_k, ssm_k, ssm_D, wqkv, wo, gw, (char*)d_ws, (u16*)d_out);
}
```

```cpp
#define V_DELAY 6
#define MAP1 1
#define HINT_AT 9
#define G_OWN 0
#define OUT_AUX 2
#define L_DELAY 0
#define O_ROT 0
#define Y_SLEEP 3
#define CONV_PRIO 3
#define NO_PK 1
#define POLL_DELAY 16
#define T4_EARLY 1
#define VY_DELAY 10
#define SENT_PACE 3
#define GY_DELAY 12
#define CODE_LINES 402
#include <hip/hip_runtime.h>
#include <hip/hip_bf16.h>
#include <stdint.h>

#define S_LEN 2048
#define D_DIM 640
#define NQKV 1920

typedef unsigned short u16;
typedef unsigned long long u64;
typedef __attribute__((ext_vector_type(8))) short bf16x8;
typedef __attribute__((ext_vector_type(4))) short s16x4;
typedef __attribute__((ext_vector_type(4))) float f32x4;
typedef __attribute__((ext_vector_type(2))) float f32x2;
typedef __attribute__((ext_vector_type(4))) unsigned u32x4;
typedef __attribute__((ext_vector_type(2))) unsigned u32x2;
typedef __attribute__((ext_vector_type(2))) __bf16 bf16x2_t;
typedef __attribute__((address_space(3))) s16x4 lds_s16x4;
typedef __attribute__((address_space(3))) const s16x4 lds_cs16x4;
typedef __attribute__((address_space(3))) const bf16x8 lds_cbf16x8;
typedef __attribute__((address_space(3))) char lds_char;

#define __float_as_uint(f) __builtin_bit_cast(unsigned, (float)(f))
#define __uint_as_float(u) __builtin_bit_cast(float, (unsigned)(u))
#define __any(c) (__builtin_amdgcn_ballot_w64(c) != 0)
#define __all(c) (__builtin_amdgcn_ballot_w64(!(c)) == 0)
extern "C" __device__ unsigned long long llvm_dispatch_id() __asm("llvm.amdgcn.dispatch.id");

__device__ __forceinline__ float bf2f(u16 x) { return __uint_as_float(((unsigned)x) << 16); }
__device__ __forceinline__ u16 f2bf(float f) {
    __hip_bfloat16 b = __float2bfloat16(f);
    return *reinterpret_cast<u16*>(&b);
}
__device__ __forceinline__ float rbf(float f) { return bf2f(f2bf(f)); }
__device__ __forceinline__ float lo_f(unsigned u) { return __uint_as_float(u << 16); }
__device__ __forceinline__ float hi_f(unsigned u) { return __uint_as_float(u & 0xffff0000u); }
__device__ __forceinline__ unsigned pack2(float a, float b) {
    return __builtin_bit_cast(unsigned, __builtin_convertvector((f32x2){a, b}, bf16x2_t));
}
__device__ __forceinline__ float dot2(unsigned a, unsigned b, float c) {
    return __builtin_amdgcn_fdot2_f32_bf16(__builtin_bit_cast(bf16x2_t, a), __builtin_bit_cast(bf16x2_t, b), c, false);
}

#define SB() __builtin_amdgcn_sched_barrier(0)
#define WAIT_VM(n) asm volatile("s_waitcnt vmcnt(" #n ")" ::: "memory")
#define RAW_BARRIER() do { asm volatile("s_waitcnt lgkmcnt(0)" ::: "memory"); __builtin_amdgcn_s_barrier(); } while (0)

#define KV_PITCH 4224
#define WS_KV 4096
#define WS_O (WS_KV + D_DIM * KV_PITCH)
#define WS_Y (WS_O + D_DIM * S_LEN * 2)
#define WS_G (WS_Y + D_DIM * S_LEN * 2)
#define WS_XF (WS_G + S_LEN * 4)
#define WS_SF (WS_XF + 128 * 128)
#define WS_HF (WS_SF + 128 * 128)
#define WS_H (WS_HF + 128 * 128)
#define WS_END (WS_H + 256 * 128)
#define WS_DBG (WS_END + 4096)

#define A_SLOT 18432
#define W_SLOT 16384
#define W_PITCH 512
#define NW 6
#define A_OFF(s) ((s) == 0 ? 53248 : ((s) - 1) * A_SLOT)
#define W_OFF(s) ((s) == 0 ? 36864 : 71680 + ((s) - 1) * W_SLOT)
#define G_R1 0
#define G_R0 88064
#define G_XCH 53248
#define G_Y (G_XCH + 20480)
#define G_Q (G_R0 + 51200)
#define G_HX (G_Q + 10240)
#define G_VX 153600
#define G_TAB (G_VX + 4608)
#define G_HXO (G_TAB + 3072)
#define G_ZR (G_HXO + 1024)
#define G_VXD (G_ZR + 64)
#define KV_BYTES 4352
#define V_F (4 * KV_BYTES)
#define XC_ROW 288
#define XC_B 12
#define XC_COPY 4944
#define XC_BYTES (XC_COPY + 17 * XC_ROW)
#define V_KV4 (V_F + 5 * XC_BYTES)
#define V_KV(c) ((c) < 4 ? (c) * KV_BYTES : V_KV4)
#define V_WO (V_KV4 + KV_BYTES - 10240)
#define V_KST (V_WO + 10240 + 1312)
#define V_VST (V_KST + 20608)
#define V_STG 113536
#define V_QS (V_WO + 10240)
#define V_SCR 130560
#define V_XCH 0
#define V_Q 20480
#define V_Y (V_Q + 10240)
#define Y_PITCH 160
#define L_TOUCH 163328
#define L_CTRL 163584
#define LDS_TOTAL 163840
static_assert(V_VST + 20608 <= V_STG && V_STG % 128 == 0 && V_STG + 20480 <= L_TOUCH && V_WO >= V_F + 4 * XC_BYTES && V_WO + 10 * 10240 <= L_TOUCH &&
              V_WO + 6 * 10240 <= V_SCR && V_SCR + 32768 <= L_TOUCH && V_Y + 80 * Y_PITCH <= V_WO && V_WO % 16 == 0, "lds");
static_assert(W_OFF(5) + W_SLOT == G_VX && G_HX + 4096 == G_VX && G_ZR + 656 <= L_TOUCH && G_Y + 80 * Y_PITCH <= G_R0 && G_TAB % 256 == 0, "lds");
#define C_FREE (L_CTRL + 16)
#define C_OST (L_CTRL + 48)
#define C_SENT (L_CTRL + 52)
#define C_WO (L_CTRL + 56)
#define C_KV (L_CTRL + 60)
#define C_KVST (L_CTRL + 64)
#define C_CONVN (L_CTRL + 68)
#define C_Y (L_CTRL + 72)
#define C_KVR (L_CTRL + 76)
#define C_XD (L_CTRL + 80)
#define C_RED (L_CTRL + 84)
#define C_ORDY (L_CTRL + 88)
#define C_QR (L_CTRL + 92)
#define C_XCH (L_CTRL + 96)
#define C_PRE (L_CTRL + 112)
#define C_SFC (L_CTRL + 136)
#define C_GO (L_CTRL + 140)
#define C_HR (L_CTRL + 144)
#define C_RED2 (L_CTRL + 148)
#define C_KVD (L_CTRL + 152)
#define C_WT (L_CTRL + 156)
#define C_HX (L_CTRL + 160)
#define C_OST1 (L_CTRL + 176)
#define C_HS (L_CTRL + 180)
#define C_WO0 (L_CTRL + 184)
#define C_QR2 (L_CTRL + 188)
#define V_Q2 (V_WO + 81920)
#define C_SENT2 (L_CTRL + 100)
#define C_YS (L_CTRL + 104)
#define C_BP C_XCH
#define C_PF (L_CTRL + 192)
#define PART(slot) (isG ? ((slot) < 2 ? G_R1 + (slot) * 20480 : G_R0 + ((slot) - 2) * 20480) : V_WO + (slot) * 20480)

#define CH(p)
#define SPIN_CAP 4000000
#define GH 0
#define WO_PRIO 0
#define T_PRIO 3
#define C0_PRIO 2
#define C1_PRIO 1
#define L_PRIO 3
#define O_SLEEP 4
#define O_DELAY 0
#define POLL_SLEEP 2
#define W_SLEEP 4
#define X_SLEEP 2
#define X_DELAY 0
#define NBUF 4
#define WATCH_RT 2
#define JL 17
#define RING_E0 70
#define RING_F 105
#define WOT_SPLIT 0
#define GO_W C_KVD
#define GO_N 11
#define GATE_LATE 0
#define WOT_PACE 3
#define SENT_W C_XD
#define SENT_N 4
#define NSENT 20
#define T0_PRIO T_PRIO
#define G_WST 4
#define V_WST 4
#define V_WST0 0
#define WR_X 2
#define HALO_AT 0
#define H_SLEEP 2
#define HS_WAIT 1
#define H_DELAY 0
#define LAST_PRIO 3
#define WATCH_C 16
#define GO2_W C_XD
#define GO2_N 4
#define H_POL ""
#define KV_POL ""
#define WATCH_J 0
#define SF_EARLY 2
#define HINT_W 3
#define WT_WAVE 11
#define WO_PACE 1
#define SF_WAVE 11
#define YS_LATE 1
#define SF_W C_RED2
#define SF_N 4
#define SO_AT 17
#define OS_W C_KVD
#define OS_N 11
#define SF1_AT 0
#define PG_MASK 15
#define FILT_LATE 0
#define GW_DELAY 0
#define O_AHEAD 4
#define PASS_PRIO 1
#define G_OST 8
#define V_ODELAY 0
#define W_LATE 1
#define Y_SL0 0
#define SF_DELAY 8
#define WO_MALL 1
#define LATE_AT C_LOOP
#define LATE_N 4
#define KROT ((0x27057250u >> (xcd * 4)) & 15)

#define STAMP(id)
#define FLUSH()
#define CSTAMP(i)

__device__ __forceinline__ unsigned lds_ld32(unsigned a) {
    unsigned v;
    asm volatile("ds_read_b32 %0, %1\n\ts_waitcnt lgkmcnt(0)" : "=v"(v) : "v"(a) : "memory");
    return v;
}
__device__ __forceinline__ unsigned lds_min4(unsigned a) {
    u32x4 v;
    asm volatile("ds_read_b128 %0, %1\n\ts_waitcnt lgkmcnt(0)" : "=v"(v) : "v"(a) : "memory");
    return min(min(v[0], v[1]), min(v[2], v[3]));
}
__device__ __forceinline__ unsigned lds_min8(unsigned a) {
    u32x4 v, u;
    asm volatile("ds_read_b128 %0, %2\n\tds_read_b128 %1, %2 offset:16\n\ts_waitcnt lgkmcnt(0)" : "=&v"(v), "=&v"(u) : "v"(a) : "memory");
    return min(min(min(v[0], v[1]), min(v[2], v[3])), min(min(u[0], u[1]), min(u[2], u[3])));
}
__device__ __forceinline__ void lds_st32(unsigned a, unsigned v) {
    asm volatile("ds_write_b32 %0, %1" ::"v"(a), "v"(v) : "memory");
}
__device__ __forceinline__ unsigned lds_add_rtn(unsigned a, int lane) {
    unsigned old = 0;
    if (lane == 0) {
        unsigned one = 1;
        asm volatile("ds_add_rtn_u32 %0, %1, %2\n\ts_waitcnt lgkmcnt(0)" : "=v"(old) : "v"(a), "v"(one) : "memory");
    }
    return __builtin_amdgcn_readfirstlane(old);
}
__device__ __forceinline__ void lds_add(unsigned a, int lane, unsigned n = 1) {
    if (lane == 0) asm volatile("ds_add_u32 %0, %1" ::"v"(a), "v"(n) : "memory");
}
__device__ __forceinline__ void spin_min4(unsigned a, unsigned target) {
#pragma nounroll
    for (int i = 0; i < SPIN_CAP; ++i) {
        if (__builtin_amdgcn_readfirstlane(lds_min4(a)) >= target) break;
        __builtin_amdgcn_s_sleep(1);
    }
}
__device__ __forceinline__ void spin_min8(unsigned a, unsigned target) {
#pragma nounroll
    for (int i = 0; i < SPIN_CAP; ++i) {
        if (__builtin_amdgcn_readfirstlane(lds_min8(a)) >= target) break;
        __builtin_amdgcn_s_sleep(1);
    }
}
__device__ __forceinline__ void spin_ge(unsigned a, unsigned target) {
#pragma nounroll
    for (int i = 0; i < SPIN_CAP; ++i) {
        if (__builtin_amdgcn_readfirstlane(lds_ld32(a)) >= target) break;
        __builtin_amdgcn_s_sleep(1);
    }
}
typedef __attribute__((address_space(3))) volatile u32x4 lds_vu32x4;
typedef __attribute__((address_space(3))) volatile unsigned lds_vu32_t;
__device__ __forceinline__ u32x4 lds_v128(unsigned a) { return *(lds_vu32x4*)(size_t)a; }
__device__ __forceinline__ unsigned lds_v32(unsigned a) { return *(lds_vu32_t*)(size_t)a; }
__device__ __forceinline__ float fmax_raw(float a, float b) { return __builtin_elementwise_maximum(a, b); }
__device__ __forceinline__ float fmax3_raw(float a, float b, float c) { return __builtin_elementwise_maximum(__builtin_elementwise_maximum(a, b), c); }
__device__ __forceinline__ unsigned opaque(unsigned v) { asm volatile("" : "+v"(v)); return v; }
__device__ __forceinline__ bf16x8 cat8(s16x4 a, s16x4 b) { return __builtin_shufflevector(a, b, 0, 1, 2, 3, 4, 5, 6, 7); }

__device__ __forceinline__ void poll_flags(__amdgpu_buffer_rsrc_t rs, unsigned off, unsigned tlo, unsigned thi) {
#pragma nounroll
    for (int i = 0; i < SPIN_CAP; ++i) {
        asm volatile("" ::: "memory");
        const u32x2 v = __builtin_amdgcn_raw_buffer_load_b64(rs, off, 0, 16);
        if (__all(v[0] == tlo && v[1] == thi)) break;
        __builtin_amdgcn_s_sleep(POLL_SLEEP);
    }
    __builtin_amdgcn_fence(__ATOMIC_ACQUIRE, "wavefront");
}

__device__ __forceinline__ void spin_slow(unsigned a, unsigned target) {
#pragma nounroll
    for (int i = 0; i < SPIN_CAP; ++i) {
        if (__builtin_amdgcn_readfirstlane(lds_ld32(a)) >= target) break;
        __builtin_amdgcn_s_sleep(6);
    }
}
__device__ __forceinline__ void spin_mid(unsigned a, unsigned target) {
#pragma nounroll
    for (int i = 0; i < SPIN_CAP; ++i) {
        if (__builtin_amdgcn_readfirstlane(lds_ld32(a)) >= target) break;
        __builtin_amdgcn_s_sleep(3);
    }
}
__device__ __forceinline__ void spin_all(unsigned a, unsigned target) {
#pragma nounroll
    for (int i = 0; i < SPIN_CAP; ++i) {
        if (__all(lds_ld32(a) >= target)) break;
        __builtin_amdgcn_s_sleep(1);
    }
}

template <int N> __device__ __forceinline__ void waitvm() { asm volatile("s_waitcnt vmcnt(%0)" ::"n"(N) : "memory"); }
#define BARRIER() asm volatile("s_barrier" ::: "memory")
#define GLS(voff, sbase, lw, ldsoff) asm volatile("s_add_u32 m0, %2, %3\n\ts_nop 0\n\tglobal_load_lds_dwordx4 %0, %1" ::"v"(voff), "s"(sbase), "s"(lw), "n"(ldsoff) : "memory", "scc")
#define GLSP(voff, sbase, lw, ldsoff) asm volatile("s_add_u32 m0, %2, %3\n\ts_nop 0\n\tglobal_load_lds_dwordx4 %0, %1" KV_POL ::"v"(voff), "s"(sbase), "s"(lw), "n"(ldsoff) : "memory", "scc")
#define GLT(voff, sbase) asm volatile("s_mov_b32 m0, %2\n\ts_nop 0\n\tglobal_load_lds_dword %0, %1" ::"v"(voff), "s"(sbase), "s"(lb + L_TOUCH) : "memory")
#define TRRD(p) __builtin_amdgcn_ds_read_tr16_b64_v4i16((lds_s16x4*)(p))

constexpr int ring_total(int NA) { return 10 * (8 + NA); }
constexpr int ring_need(int J, int NA) { return J < 0 ? NA + 8 : J >= JL ? 10 * (8 + NA) : (J & 1) ? (NA + 8) * ((J + 3) / 2) : (NA + 8) * ((J + 2) / 2) + 4 + NA; }
constexpr int ring_isA(int p, int NA) { return p < NA ? 1 : p < NA + 8 ? 0 : (p % (8 + NA) >= 4 && p % (8 + NA) < 4 + NA); }
constexpr int ring_tile(int p, int NA) { return p < NA ? 0 : p < NA + 8 ? (p - NA) / 4 : ring_isA(p, NA) ? p / (8 + NA) : 2 * (p / (8 + NA)) + (p % (8 + NA) >= 4); }
constexpr int ring_sub(int p, int NA) { return p < NA ? p : p < NA + 8 ? (p - NA) % 4 : p % (8 + NA) < 4 ? p % (8 + NA) : p % (8 + NA) < 4 + NA ? p % (8 + NA) - 4 : p % (8 + NA) - 4 - NA; }
constexpr int ring_cum(int J, int NA) {
    int c = J >= (JL < 16 ? JL : 16) ? ring_total(NA) : NA + 8 + (8 + NA) * (2 * RING_E0 + RING_F * (J + 1)) / 200;
    if (c > ring_total(NA)) c = ring_total(NA);
    if (c < ring_need(J, NA)) c = ring_need(J, NA);
    while (ring_isA(c - 1, NA) ? 2 * (ring_tile(c - 1, NA) - 3) + 1 > J - 1 : ring_tile(c - 1, NA) - NW > J - 1) --c;
    return c;
}
static_assert(ring_cum(16, 5) == ring_total(5) && ring_cum(16, 4) == ring_total(4), "ring");
template <int NA, int P0, int P1>
__device__ __forceinline__ void ring_issue(const unsigned (&ev)[9], const void* xp, const void* wp, int kr, unsigned lw) {
    if constexpr (P0 < P1) {
        constexpr int t = ring_tile(P0, NA), sub = ring_sub(P0, NA);
        if constexpr (ring_isA(P0, NA)) {
            const int c = __builtin_amdgcn_readfirstlane(kr + t >= 10 ? kr + t - 10 : kr + t);
            GLS(ev[4 + sub], (const char*)xp + c * 128, lw, A_OFF(t % 3) + sub * 4096);
        } else {
            const int c = __builtin_amdgcn_readfirstlane(2 * kr + t >= 20 ? 2 * kr + t - 20 : 2 * kr + t);
            GLS(ev[sub], (const char*)wp + (size_t)c * (32 * NQKV * 2), lw, W_OFF(t % NW) + sub * 4096);
        }
        ring_issue<NA, P0 + 1, P1>(ev, xp, wp, kr, lw);
    }
}
#define S_O 0xff81ff81u
template <int NA, int J>
__device__ __forceinline__ void ring_run(const unsigned (&ev)[9], const void* xp, const void* wp, int kr, unsigned lw) {
    ring_issue<NA, ring_cum(J - 1, NA), ring_cum(J, NA)>(ev, xp, wp, kr, lw);
    if constexpr (J < 16) {
        waitvm<ring_cum(J, NA) - ring_need(J, NA)>();
        BARRIER();
        ring_run<NA, J + 1>(ev, xp, wp, kr, lw);
    }
}

#define CONV_ASM \
    "s_mov_b32 m0, %[kvl]\n" \
    "v_mov_b32 v50, 1\n" \
    "global_load_lds_dwordx4 %[xo], %[wsp] offset:0 sc1\n" \
    "global_load_lds_dwordx4 %[xo], %[wsp] offset:1024 sc1\n" \
    "global_load_lds_dwordx4 %[xo], %[wsp] offset:2048 sc1\n" \
    "global_load_lds_dwordx4 %[xo], %[wsp] offset:3072 sc1\n" \
    "s_mov_b64 exec, 1\n" \
    "ds_add_u32 %[ctl], v50 offset:80\n" \
    "s_mov_b64 exec, -1\n" \
    "v_add_u32 v84, %[sx4], %[xp]\n" \
    "v_add_u32 v51, %[sh4], %[hp]\n" \
    "v_add_u32 v160, 0, %[xp]\n" \
    "v_add_u32 v161, 1152, %[xp]\n" \
    "v_add_u32 v85, 1152, v84\n" \
    "v_add_u32 v162, 2304, %[xp]\n" \
    "v_add_u32 v86, 2304, v84\n" \
    "v_add_u32 v163, 3456, %[xp]\n" \
    "v_add_u32 v87, 3456, v84\n" \
    "ds_read2_b32 v[88:89], v87 offset0:144 offset1:145\n" \
    "ds_read2_b32 v[90:91], v87 offset0:146 offset1:147\n" \
    "ds_read2_b32 v[92:93], v87 offset0:0 offset1:1\n" \
    "ds_read2_b32 v[94:95], v87 offset0:2 offset1:3\n" \
    "ds_read2_b32 v[96:97], v86 offset0:144 offset1:145\n" \
    "ds_read2_b32 v[98:99], v86 offset0:146 offset1:147\n" \
    "ds_read2_b32 v[100:101], v86 offset0:0 offset1:1\n" \
    "ds_read2_b32 v[102:103], v86 offset0:2 offset1:3\n" \
    "ds_read2_b32 v[104:105], v85 offset0:144 offset1:145\n" \
    "ds_read2_b32 v[106:107], v85 offset0:146 offset1:147\n" \
    "ds_read2_b32 v[108:109], v85 offset0:0 offset1:1\n" \
    "ds_read2_b32 v[110:111], v85 offset0:2 offset1:3\n" \
    "ds_read2_b32 v[112:113], v84 offset0:144 offset1:145\n" \
    "ds_read2_b32 v[114:115], v84 offset0:146 offset1:147\n" \
    "ds_read2_b32 v[116:117], v84 offset0:0 offset1:1\n" \
    "ds_read2_b32 v[118:119], v84 offset0:2 offset1:3\n" \
    "s_mov_b32 %[cnt], 0\n" \
    "Lsp_%=:\n" \
    "ds_read_b32 v50, %[ctl] offset:144\n" \
    "s_add_u32 %[cnt], %[cnt], 1\n" \
    "s_waitcnt lgkmcnt(0)\n" \
    "v_readfirstlane_b32 %[tmp], v50\n" \
    "s_cmp_ge_u32 %[cnt], %[cap]\n" \
    "s_cbranch_scc1 Lgo_%=\n" \
    "s_cmp_lt_u32 %[tmp], 4\n" \
    "s_cbranch_scc1 Lsp_%=\n" \
    "Lgo_%=:\n" \
    "ds_read_b128 v[148:151], v51 offset:3584\n" \
    "ds_read_b128 v[144:147], v51 offset:3072\n" \
    "ds_read_b128 v[140:143], v51 offset:2560\n" \
    "ds_read_b128 v[136:139], v51 offset:2048\n" \
    "ds_read_b128 v[132:135], v51 offset:1536\n" \
    "ds_read_b128 v[128:131], v51 offset:1024\n" \
    "ds_read_b128 v[124:127], v51 offset:512\n" \
    "ds_read_b128 v[120:123], v51 offset:0\n" \
    "s_waitcnt lgkmcnt(7)\n" \
    "v_mfma_f32_16x16x32_bf16 v[80:83], v[88:91], v[148:151], 0\n" \
    "ds_read_b128 v[148:151], v51 offset:3648\n" \
    "s_waitcnt lgkmcnt(7)\n" \
    "v_mfma_f32_16x16x32_bf16 v[76:79], v[88:91], v[144:147], 0\n" \
    "ds_read_b128 v[156:159], v51 offset:576\n" \
    "s_waitcnt lgkmcnt(7)\n" \
    "v_mfma_f32_16x16x32_bf16 v[72:75], v[88:91], v[140:143], 0\n" \
    "ds_read_b128 v[152:155], v51 offset:64\n" \
    "s_waitcnt lgkmcnt(7)\n" \
    "v_mfma_f32_16x16x32_bf16 v[68:71], v[88:91], v[136:139], 0\n" \
    "s_waitcnt lgkmcnt(6)\n" \
    "v_mfma_f32_16x16x32_bf16 v[64:67], v[88:91], v[132:135], 0\n" \
    "s_waitcnt lgkmcnt(5)\n" \
    "v_mfma_f32_16x16x32_bf16 v[60:63], v[88:91], v[128:131], 0\n" \
    "s_waitcnt lgkmcnt(4)\n" \
    "v_mfma_f32_16x16x32_bf16 v[56:59], v[88:91], v[124:127], 0\n" \
    "s_waitcnt lgkmcnt(3)\n" \
    "v_mfma_f32_16x16x32_bf16 v[52:55], v[88:91], v[120:123], 0\n" \
    "ds_read2_b32 v[88:89], v87 offset0:160 offset1:161\n" \
    "v_mfma_f32_16x16x32_bf16 v[80:83], v[92:95], v[144:147], v[80:83]\n" \
    "ds_read2_b32 v[90:91], v87 offset0:162 offset1:163\n" \
    "v_mfma_f32_16x16x32_bf16 v[76:79], v[92:95], v[140:143], v[76:79]\n" \
    "ds_read_b128 v[144:147], v51 offset:3136\n" \
    "v_mfma_f32_16x16x32_bf16 v[72:75], v[92:95], v[136:139], v[72:75]\n" \
    "v_mfma_f32_16x16x32_bf16 v[68:71], v[92:95], v[132:135], v[68:71]\n" \
    "v_mfma_f32_16x16x32_bf16 v[64:67], v[92:95], v[128:131], v[64:67]\n" \
    "v_mfma_f32_16x16x32_bf16 v[60:63], v[92:95], v[124:127], v[60:63]\n" \
    "v_mfma_f32_16x16x32_bf16 v[56:59], v[92:95], v[120:123], v[56:59]\n" \
    "ds_read2_b32 v[92:93], v87 offset0:16 offset1:17\n" \
    "v_mfma_f32_16x16x32_bf16 v[80:83], v[96:99], v[140:143], v[80:83]\n" \
    "ds_read_b128 v[140:143], v51 offset:2624\n" \
    "v_mfma_f32_16x16x32_bf16 v[76:79], v[96:99], v[136:139], v[76:79]\n" \
    "ds_read2_b32 v[94:95], v87 offset0:18 offset1:19\n" \
    "v_mfma_f32_16x16x32_bf16 v[72:75], v[96:99], v[132:135], v[72:75]\n" \
    "v_mfma_f32_16x16x32_bf16 v[68:71], v[96:99], v[128:131], v[68:71]\n" \
    "v_mfma_f32_16x16x32_bf16 v[64:67], v[96:99], v[124:127], v[64:67]\n" \
    "v_mfma_f32_16x16x32_bf16 v[60:63], v[96:99], v[120:123], v[60:63]\n" \
    "ds_read2_b32 v[96:97], v86 offset0:160 offset1:161\n" \
    "v_mfma_f32_16x16x32_bf16 v[80:83], v[100:103], v[136:139], v[80:83]\n" \
    "ds_read_b128 v[136:139], v51 offset:2112\n" \
    "v_mfma_f32_16x16x32_bf16 v[76:79], v[100:103], v[132:135], v[76:79]\n" \
    "ds_read2_b32 v[98:99], v86 offset0:162 offset1:163\n" \
    "v_mfma_f32_16x16x32_bf16 v[72:75], v[100:103], v[128:131], v[72:75]\n" \
    "v_mfma_f32_16x16x32_bf16 v[68:71], v[100:103], v[124:127], v[68:71]\n" \
    "v_mfma_f32_16x16x32_bf16 v[64:67], v[100:103], v[120:123], v[64:67]\n" \
    "ds_read2_b32 v[100:101], v86 offset0:16 offset1:17\n" \
    "v_mfma_f32_16x16x32_bf16 v[80:83], v[104:107], v[132:135], v[80:83]\n" \
    "ds_read_b128 v[132:135], v51 offset:1600\n" \
    "v_mfma_f32_16x16x32_bf16 v[76:79], v[104:107], v[128:131], v[76:79]\n" \
    "ds_read2_b32 v[102:103], v86 offset0:18 offset1:19\n" \
    "v_mfma_f32_16x16x32_bf16 v[72:75], v[104:107], v[124:127], v[72:75]\n" \
    "v_mfma_f32_16x16x32_bf16 v[68:71], v[104:107], v[120:123], v[68:71]\n" \
    "ds_read2_b32 v[104:105], v85 offset0:160 offset1:161\n" \
    "v_mfma_f32_16x16x32_bf16 v[80:83], v[108:111], v[128:131], v[80:83]\n" \
    "ds_read_b128 v[128:131], v51 offset:1088\n" \
    "v_mfma_f32_16x16x32_bf16 v[76:79], v[108:111], v[124:127], v[76:79]\n" \
    "ds_read2_b32 v[106:107], v85 offset0:162 offset1:163\n" \
    "v_mfma_f32_16x16x32_bf16 v[72:75], v[108:111], v[120:123], v[72:75]\n" \
    "ds_read2_b32 v[108:109], v85 offset0:16 offset1:17\n" \
    "v_mfma_f32_16x16x32_bf16 v[80:83], v[112:115], v[124:127], v[80:83]\n" \
    "ds_read2_b32 v[110:111], v85 offset0:18 offset1:19\n" \
    "v_mfma_f32_16x16x32_bf16 v[76:79], v[112:115], v[120:123], v[76:79]\n" \
    "ds_read2_b32 v[112:113], v84 offset0:160 offset1:161\n" \
    "v_mfma_f32_16x16x32_bf16 v[80:83], v[116:119], v[120:123], v[80:83]\n" \
    "ds_read2_b32 v[114:115], v84 offset0:162 offset1:163\n" \
    "s_waitcnt lgkmcnt(15)\n" \
    "v_mfma_f32_16x16x32_bf16 v[80:83], v[88:91], v[148:151], v[80:83]\n" \
    "ds_read2_b32 v[116:117], v84 offset0:16 offset1:17\n" \
    "v_mfma_f32_16x16x32_bf16 v[76:79], v[88:91], v[144:147], v[76:79]\n" \
    "ds_read2_b32 v[118:119], v84 offset0:18 offset1:19\n" \
    "s_waitcnt lgkmcnt(15)\n" \
    "v_mfma_f32_16x16x32_bf16 v[72:75], v[88:91], v[140:143], v[72:75]\n" \
    "s_waitcnt vmcnt(0)\n" \
    "ds_read_b128 v[148:151], %[hp] offset:3584\n" \
    "s_waitcnt lgkmcnt(14)\n" \
    "v_mfma_f32_16x16x32_bf16 v[68:71], v[88:91], v[136:139], v[68:71]\n" \
    "ds_read_b128 v[124:127], %[hp] offset:512\n" \
    "s_waitcnt lgkmcnt(12)\n" \
    "v_mfma_f32_16x16x32_bf16 v[64:67], v[88:91], v[132:135], v[64:67]\n" \
    "ds_read_b128 v[120:123], %[hp] offset:0\n" \
    "s_waitcnt lgkmcnt(10)\n" \
    "v_mfma_f32_16x16x32_bf16 v[60:63], v[88:91], v[128:131], v[60:63]\n" \
    "v_mfma_f32_16x16x32_bf16 v[56:59], v[88:91], v[156:159], v[56:59]\n" \
    "v_mfma_f32_16x16x32_bf16 v[52:55], v[88:91], v[152:155], v[52:55]\n" \
    "ds_read2_b32 v[88:89], v163 offset0:144 offset1:145\n" \
    "v_mfma_f32_16x16x32_bf16 v[80:83], v[92:95], v[144:147], v[80:83]\n" \
    "ds_read2_b32 v[90:91], v163 offset0:146 offset1:147\n" \
    "v_mfma_f32_16x16x32_bf16 v[76:79], v[92:95], v[140:143], v[76:79]\n" \
    "ds_read_b128 v[144:147], %[hp] offset:3072\n" \
    "v_mfma_f32_16x16x32_bf16 v[72:75], v[92:95], v[136:139], v[72:75]\n" \
    "v_mfma_f32_16x16x32_bf16 v[68:71], v[92:95], v[132:135], v[68:71]\n" \
    "ds_write_b128 %[scr], v[52:55] offset:0\n" \
    "v_mfma_f32_16x16x32_bf16 v[64:67], v[92:95], v[128:131], v[64:67]\n" \
    "ds_read_b64 v[86:87], %[kvr] offset:0\n" \
    "v_mfma_f32_16x16x32_bf16 v[60:63], v[92:95], v[156:159], v[60:63]\n" \
    "v_mfma_f32_16x16x32_bf16 v[56:59], v[92:95], v[152:155], v[56:59]\n" \
    "ds_read2_b32 v[92:93], v163 offset0:0 offset1:1\n" \
    "v_mfma_f32_16x16x32_bf16 v[80:83], v[96:99], v[140:143], v[80:83]\n" \
    "ds_read_b128 v[140:143], %[hp] offset:2560\n" \
    "v_mfma_f32_16x16x32_bf16 v[76:79], v[96:99], v[136:139], v[76:79]\n" \
    "ds_read2_b32 v[94:95], v163 offset0:2 offset1:3\n" \
    "v_mfma_f32_16x16x32_bf16 v[72:75], v[96:99], v[132:135], v[72:75]\n" \
    "v_mfma_f32_16x16x32_bf16 v[68:71], v[96:99], v[128:131], v[68:71]\n" \
    "ds_write_b128 %[scr], v[56:59] offset:1024\n" \
    "v_mfma_f32_16x16x32_bf16 v[64:67], v[96:99], v[156:159], v[64:67]\n" \
    "v_mfma_f32_16x16x32_bf16 v[60:63], v[96:99], v[152:155], v[60:63]\n" \
    "ds_read2_b32 v[96:97], v162 offset0:144 offset1:145\n" \
    "v_mfma_f32_16x16x32_bf16 v[80:83], v[100:103], v[136:139], v[80:83]\n" \
    "ds_read_b128 v[136:139], %[hp] offset:2048\n" \
    "v_mfma_f32_16x16x32_bf16 v[76:79], v[100:103], v[132:135], v[76:79]\n" \
    "ds_read2_b32 v[98:99], v162 offset0:146 offset1:147\n" \
    "v_mfma_f32_16x16x32_bf16 v[72:75], v[100:103], v[128:131], v[72:75]\n" \
    "v_mfma_f32_16x16x32_bf16 v[68:71], v[100:103], v[156:159], v[68:71]\n" \
    "s_waitcnt lgkmcnt(7)\n" \
    "v_lshlrev_b32 v84, 16, v86\n" \
    "ds_write_b128 %[scr], v[60:63] offset:2048\n" \
    "v_mfma_f32_16x16x32_bf16 v[64:67], v[100:103], v[152:155], v[64:67]\n" \
    "ds_read2_b32 v[100:101], v162 offset0:0 offset1:1\n" \
    "v_mfma_f32_16x16x32_bf16 v[80:83], v[104:107], v[132:135], v[80:83]\n" \
    "ds_read_b128 v[132:135], %[hp] offset:1536\n" \
    "v_and_b32 v85, 0xffff0000, v86\n" \
    "v_mfma_f32_16x16x32_bf16 v[76:79], v[104:107], v[128:131], v[76:79]\n" \
    "ds_read2_b32 v[102:103], v162 offset0:2 offset1:3\n" \
    "v_mfma_f32_16x16x32_bf16 v[72:75], v[104:107], v[156:159], v[72:75]\n" \
    "v_lshlrev_b32 v86, 16, v87\n" \
    "v_mfma_f32_16x16x32_bf16 v[68:71], v[104:107], v[152:155], v[68:71]\n" \
    "ds_read2_b32 v[104:105], v161 offset0:144 offset1:145\n" \
    "ds_write_b128 %[scr], v[64:67] offset:3072\n" \
    "v_mfma_f32_16x16x32_bf16 v[80:83], v[108:111], v[128:131], v[80:83]\n" \
    "ds_read_b128 v[128:131], %[hp] offset:1024\n" \
    "v_and_b32 v87, 0xffff0000, v87\n" \
    "v_mfma_f32_16x16x32_bf16 v[76:79], v[108:111], v[156:159], v[76:79]\n" \
    "ds_read2_b32 v[106:107], v161 offset0:146 offset1:147\n" \
    "v_mfma_f32_16x16x32_bf16 v[72:75], v[108:111], v[152:155], v[72:75]\n" \
    "ds_read2_b32 v[108:109], v161 offset0:0 offset1:1\n" \
    "v_mfma_f32_16x16x32_bf16 v[80:83], v[112:115], v[156:159], v[80:83]\n" \
    "ds_read2_b32 v[110:111], v161 offset0:2 offset1:3\n" \
    "ds_write_b128 %[scr], v[68:71] offset:4096\n" \
    "v_mfma_f32_16x16x32_bf16 v[76:79], v[112:115], v[152:155], v[76:79]\n" \
    "ds_read2_b32 v[112:113], v160 offset0:144 offset1:145\n" \
    "v_mfma_f32_16x16x32_bf16 v[80:83], v[116:119], v[152:155], v[80:83]\n" \
    "ds_read2_b32 v[114:115], v160 offset0:146 offset1:147\n" \
    "v_mfma_f32_16x16x32_bf16 v[44:47], v[88:91], v[148:151], 0\n" \
    "ds_read2_b32 v[116:117], v160 offset0:0 offset1:1\n" \
    "ds_write_b128 %[scr], v[72:75] offset:5120\n" \
    "v_mfma_f32_16x16x32_bf16 v[40:43], v[88:91], v[144:147], 0\n" \
    "ds_read2_b32 v[118:119], v160 offset0:2 offset1:3\n" \
    "s_waitcnt lgkmcnt(15)\n" \
    "v_mfma_f32_16x16x32_bf16 v[36:39], v[88:91], v[140:143], 0\n" \
    "ds_read_b128 v[148:151], %[hp] offset:3648\n" \
    "ds_write_b128 %[scr], v[76:79] offset:6144\n" \
    "v_mfma_f32_16x16x32_bf16 v[32:35], v[88:91], v[136:139], 0\n" \
    "ds_read_b128 v[156:159], %[hp] offset:576\n" \
    "s_waitcnt lgkmcnt(15)\n" \
    "v_mfma_f32_16x16x32_bf16 v[28:31], v[88:91], v[132:135], 0\n" \
    "ds_read_b128 v[152:155], %[hp] offset:64\n" \
    "ds_write_b128 %[scr], v[80:83] offset:7168\n" \
    "v_mov_b32 v50, 1\n" \
    "s_mov_b64 exec, 1\n" \
    "ds_add_u32 %[ctl], v50 offset:84\n" \
    "s_mov_b64 exec, -1\n" \
    "s_setprio 0\n" \
    "s_waitcnt lgkmcnt(15)\n" \
    "v_mfma_f32_16x16x32_bf16 v[24:27], v[88:91], v[128:131], 0\n" \
    "v_mfma_f32_16x16x32_bf16 v[20:23], v[88:91], v[124:127], 0\n" \
    "v_mfma_f32_16x16x32_bf16 v[16:19], v[88:91], v[120:123], 0\n" \
    "ds_read2_b32 v[88:89], v163 offset0:160 offset1:161\n" \
    "v_mfma_f32_16x16x32_bf16 v[44:47], v[92:95], v[144:147], v[44:47]\n" \
    "ds_read2_b32 v[90:91], v163 offset0:162 offset1:163\n" \
    "v_mfma_f32_16x16x32_bf16 v[40:43], v[92:95], v[140:143], v[40:43]\n" \
    "ds_read_b128 v[144:147], %[hp] offset:3136\n" \
    "v_mfma_f32_16x16x32_bf16 v[36:39], v[92:95], v[136:139], v[36:39]\n" \
    "v_fmac_f32 v16, %[dd], v84\n" \
    "v_fmac_f32 v17, %[dd], v85\n" \
    "v_mfma_f32_16x16x32_bf16 v[32:35], v[92:95], v[132:135], v[32:35]\n" \
    "v_fmac_f32 v18, %[dd], v86\n" \
    "v_fmac_f32 v19, %[dd], v87\n" \
    "v_mfma_f32_16x16x32_bf16 v[28:31], v[92:95], v[128:131], v[28:31]\n" \
    "ds_read_b64 v[86:87], %[kvr] offset:512\n" \
    "v_mfma_f32_16x16x32_bf16 v[24:27], v[92:95], v[124:127], v[24:27]\n" \
    "v_mfma_f32_16x16x32_bf16 v[20:23], v[92:95], v[120:123], v[20:23]\n" \
    "ds_read2_b32 v[92:93], v163 offset0:16 offset1:17\n" \
    "v_mfma_f32_16x16x32_bf16 v[44:47], v[96:99], v[140:143], v[44:47]\n" \
    "ds_read_b128 v[140:143], %[hp] offset:2624\n" \
    "v_mfma_f32_16x16x32_bf16 v[40:43], v[96:99], v[136:139], v[40:43]\n" \
    "ds_read2_b32 v[94:95], v163 offset0:18 offset1:19\n" \
    "v_mfma_f32_16x16x32_bf16 v[36:39], v[96:99], v[132:135], v[36:39]\n" \
    "v_mfma_f32_16x16x32_bf16 v[32:35], v[96:99], v[128:131], v[32:35]\n" \
    "v_mfma_f32_16x16x32_bf16 v[28:31], v[96:99], v[124:127], v[28:31]\n" \
    "v_mfma_f32_16x16x32_bf16 v[24:27], v[96:99], v[120:123], v[24:27]\n" \
    "ds_read2_b32 v[96:97], v162 offset0:160 offset1:161\n" \
    "v_mfma_f32_16x16x32_bf16 v[44:47], v[100:103], v[136:139], v[44:47]\n" \
    "ds_read_b128 v[136:139], %[hp] offset:2112\n" \
    "v_mfma_f32_16x16x32_bf16 v[40:43], v[100:103], v[132:135], v[40:43]\n" \
    "ds_read2_b32 v[98:99], v162 offset0:162 offset1:163\n" \
    "v_mfma_f32_16x16x32_bf16 v[36:39], v[100:103], v[128:131], v[36:39]\n" \
    "v_mfma_f32_16x16x32_bf16 v[32:35], v[100:103], v[124:127], v[32:35]\n" \
    "s_waitcnt lgkmcnt(6)\n" \
    "v_lshlrev_b32 v84, 16, v86\n" \
    "s_setprio 3\n" \
    "v_mfma_f32_16x16x32_bf16 v[28:31], v[100:103], v[120:123], v[28:31]\n" \
    "ds_read2_b32 v[100:101], v162 offset0:16 offset1:17\n" \
    "v_mfma_f32_16x16x32_bf16 v[44:47], v[104:107], v[132:135], v[44:47]\n" \
    "ds_read_b128 v[132:135], %[hp] offset:1600\n" \
    "v_and_b32 v85, 0xffff0000, v86\n" \
    "v_mfma_f32_16x16x32_bf16 v[40:43], v[104:107], v[128:131], v[40:43]\n" \
    "ds_read2_b32 v[102:103], v162 offset0:18 offset1:19\n" \
    "v_mfma_f32_16x16x32_bf16 v[36:39], v[104:107], v[124:127], v[36:39]\n" \
    "v_lshlrev_b32 v86, 16, v87\n" \
    "v_mfma_f32_16x16x32_bf16 v[32:35], v[104:107], v[120:123], v[32:35]\n" \
    "ds_read2_b32 v[104:105], v161 offset0:160 offset1:161\n" \
    "v_mfma_f32_16x16x32_bf16 v[44:47], v[108:111], v[128:131], v[44:47]\n" \
    "ds_read_b128 v[128:131], %[hp] offset:1088\n" \
    "v_and_b32 v87, 0xffff0000, v87\n" \
    "v_mfma_f32_16x16x32_bf16 v[40:43], v[108:111], v[124:127], v[40:43]\n" \
    "ds_read2_b32 v[106:107], v161 offset0:162 offset1:163\n" \
    "v_mfma_f32_16x16x32_bf16 v[36:39], v[108:111], v[120:123], v[36:39]\n" \
    "ds_read2_b32 v[108:109], v161 offset0:16 offset1:17\n" \
    "v_mfma_f32_16x16x32_bf16 v[44:47], v[112:115], v[124:127], v[44:47]\n" \
    "ds_read2_b32 v[110:111], v161 offset0:18 offset1:19\n" \
    "v_mfma_f32_16x16x32_bf16 v[40:43], v[112:115], v[120:123], v[40:43]\n" \
    "ds_read2_b32 v[112:113], v160 offset0:160 offset1:161\n" \
    "v_mfma_f32_16x16x32_bf16 v[44:47], v[116:119], v[120:123], v[44:47]\n" \
    "ds_read2_b32 v[114:115], v160 offset0:162 offset1:163\n" \
    "v_mfma_f32_16x16x32_bf16 v[44:47], v[88:91], v[148:151], v[44:47]\n" \
    "ds_read2_b32 v[116:117], v160 offset0:16 offset1:17\n" \
    "v_mfma_f32_16x16x32_bf16 v[40:43], v[88:91], v[144:147], v[40:43]\n" \
    "ds_read2_b32 v[118:119], v160 offset0:18 offset1:19\n" \
    "s_waitcnt lgkmcnt(15)\n" \
    "v_mfma_f32_16x16x32_bf16 v[36:39], v[88:91], v[140:143], v[36:39]\n" \
    "ds_read_b128 v[148:151], %[hp] offset:3712\n" \
    "s_waitcnt lgkmcnt(14)\n" \
    "v_mfma_f32_16x16x32_bf16 v[32:35], v[88:91], v[136:139], v[32:35]\n" \
    "ds_read_b128 v[124:127], %[hp] offset:640\n" \
    "s_waitcnt lgkmcnt(12)\n" \
    "v_mfma_f32_16x16x32_bf16 v[28:31], v[88:91], v[132:135], v[28:31]\n" \
    "ds_read_b128 v[120:123], %[hp] offset:128\n" \
    "s_waitcnt lgkmcnt(10)\n" \
    "v_mfma_f32_16x16x32_bf16 v[24:27], v[88:91], v[128:131], v[24:27]\n" \
    "v_mfma_f32_16x16x32_bf16 v[20:23], v[88:91], v[156:159], v[20:23]\n" \
    "v_mfma_f32_16x16x32_bf16 v[16:19], v[88:91], v[152:155], v[16:19]\n" \
    "ds_read2_b32 v[88:89], v163 offset0:176 offset1:177\n" \
    "v_mfma_f32_16x16x32_bf16 v[44:47], v[92:95], v[144:147], v[44:47]\n" \
    "ds_read2_b32 v[90:91], v163 offset0:178 offset1:179\n" \
    "v_mfma_f32_16x16x32_bf16 v[40:43], v[92:95], v[140:143], v[40:43]\n" \
    "ds_read_b128 v[144:147], %[hp] offset:3200\n" \
    "v_fmac_f32 v20, %[dd], v84\n" \
    "v_fmac_f32 v21, %[dd], v85\n" \
    "v_mfma_f32_16x16x32_bf16 v[36:39], v[92:95], v[136:139], v[36:39]\n" \
    "v_fmac_f32 v22, %[dd], v86\n" \
    "v_fmac_f32 v23, %[dd], v87\n" \
    "v_mfma_f32_16x16x32_bf16 v[32:35], v[92:95], v[132:135], v[32:35]\n" \
    "v_mfma_f32_16x16x32_bf16 v[28:31], v[92:95], v[128:131], v[28:31]\n" \
    "ds_read_b64 v[86:87], %[kvr] offset:1024\n" \
    "v_mfma_f32_16x16x32_bf16 v[24:27], v[92:95], v[156:159], v[24:27]\n" \
    "v_mfma_f32_16x16x32_bf16 v[20:23], v[92:95], v[152:155], v[20:23]\n" \
    "ds_read2_b32 v[92:93], v163 offset0:32 offset1:33\n" \
    "v_mfma_f32_16x16x32_bf16 v[44:47], v[96:99], v[140:143], v[44:47]\n" \
    "ds_read_b128 v[140:143], %[hp] offset:2688\n" \
    "v_mfma_f32_16x16x32_bf16 v[40:43], v[96:99], v[136:139], v[40:43]\n" \
    "ds_read2_b32 v[94:95], v163 offset0:34 offset1:35\n" \
    "v_mfma_f32_16x16x32_bf16 v[36:39], v[96:99], v[132:135], v[36:39]\n" \
    "v_mfma_f32_16x16x32_bf16 v[32:35], v[96:99], v[128:131], v[32:35]\n" \
    "v_mfma_f32_16x16x32_bf16 v[28:31], v[96:99], v[156:159], v[28:31]\n" \
    "v_mfma_f32_16x16x32_bf16 v[24:27], v[96:99], v[152:155], v[24:27]\n" \
    "ds_read2_b32 v[96:97], v162 offset0:176 offset1:177\n" \
    "v_mfma_f32_16x16x32_bf16 v[44:47], v[100:103], v[136:139], v[44:47]\n" \
    "ds_read_b128 v[136:139], %[hp] offset:2176\n" \
    "v_mfma_f32_16x16x32_bf16 v[40:43], v[100:103], v[132:135], v[40:43]\n" \
    "ds_read2_b32 v[98:99], v162 offset0:178 offset1:179\n" \
    "v_mfma_f32_16x16x32_bf16 v[36:39], v[100:103], v[128:131], v[36:39]\n" \
    "v_mfma_f32_16x16x32_bf16 v[32:35], v[100:103], v[156:159], v[32:35]\n" \
    "s_waitcnt lgkmcnt(6)\n" \
    "v_lshlrev_b32 v84, 16, v86\n" \
    "v_mfma_f32_16x16x32_bf16 v[28:31], v[100:103], v[152:155], v[28:31]\n" \
    "ds_read2_b32 v[100:101], v162 offset0:32 offset1:33\n" \
    "v_mfma_f32_16x16x32_bf16 v[44:47], v[104:107], v[132:135], v[44:47]\n" \
    "ds_read_b128 v[132:135], %[hp] offset:1664\n" \
    "v_and_b32 v85, 0xffff0000, v86\n" \
    "v_mfma_f32_16x16x32_bf16 v[40:43], v[104:107], v[128:131], v[40:43]\n" \
    "ds_read2_b32 v[102:103], v162 offset0:34 offset1:35\n" \
    "v_mfma_f32_16x16x32_bf16 v[36:39], v[104:107], v[156:159], v[36:39]\n" \
    "v_lshlrev_b32 v86, 16, v87\n" \
    "v_mfma_f32_16x16x32_bf16 v[32:35], v[104:107], v[152:155], v[32:35]\n" \
    "ds_read2_b32 v[104:105], v161 offset0:176 offset1:177\n" \
    "v_mfma_f32_16x16x32_bf16 v[44:47], v[108:111], v[128:131], v[44:47]\n" \
    "ds_read_b128 v[128:131], %[hp] offset:1152\n" \
    "v_and_b32 v87, 0xffff0000, v87\n" \
    "v_mfma_f32_16x16x32_bf16 v[40:43], v[108:111], v[156:159], v[40:43]\n" \
    "ds_read2_b32 v[106:107], v161 offset0:178 offset1:179\n" \
    "v_mfma_f32_16x16x32_bf16 v[36:39], v[108:111], v[152:155], v[36:39]\n" \
    "ds_read2_b32 v[108:109], v161 offset0:32 offset1:33\n" \
    "v_mfma_f32_16x16x32_bf16 v[44:47], v[112:115], v[156:159], v[44:47]\n" \
    "ds_read2_b32 v[110:111], v161 offset0:34 offset1:35\n" \
    "v_mfma_f32_16x16x32_bf16 v[40:43], v[112:115], v[152:155], v[40:43]\n" \
    "ds_read2_b32 v[112:113], v160 offset0:176 offset1:177\n" \
    "v_mfma_f32_16x16x32_bf16 v[44:47], v[116:119], v[152:155], v[44:47]\n" \
    "ds_read2_b32 v[114:115], v160 offset0:178 offset1:179\n" \
    "v_mfma_f32_16x16x32_bf16 v[44:47], v[88:91], v[148:151], v[44:47]\n" \
    "ds_read2_b32 v[116:117], v160 offset0:32 offset1:33\n" \
    "v_mfma_f32_16x16x32_bf16 v[40:43], v[88:91], v[144:147], v[40:43]\n" \
    "ds_read2_b32 v[118:119], v160 offset0:34 offset1:35\n" \
    "s_waitcnt lgkmcnt(15)\n" \
    "v_mfma_f32_16x16x32_bf16 v[36:39], v[88:91], v[140:143], v[36:39]\n" \
    "ds_read_b128 v[148:151], %[hp] offset:3776\n" \
    "s_waitcnt lgkmcnt(14)\n" \
    "v_mfma_f32_16x16x32_bf16 v[32:35], v[88:91], v[136:139], v[32:35]\n" \
    "ds_read_b128 v[156:159], %[hp] offset:704\n" \
    "s_waitcnt lgkmcnt(12)\n" \
    "v_mfma_f32_16x16x32_bf16 v[28:31], v[88:91], v[132:135], v[28:31]\n" \
    "ds_read_b128 v[152:155], %[hp] offset:192\n" \
    "s_waitcnt lgkmcnt(10)\n" \
    "v_mfma_f32_16x16x32_bf16 v[24:27], v[88:91], v[128:131], v[24:27]\n" \
    "v_mfma_f32_16x16x32_bf16 v[20:23], v[88:91], v[124:127], v[20:23]\n" \
    "v_mfma_f32_16x16x32_bf16 v[16:19], v[88:91], v[120:123], v[16:19]\n" \
    "ds_read2_b32 v[88:89], v163 offset0:192 offset1:193\n" \
    "v_mfma_f32_16x16x32_bf16 v[44:47], v[92:95], v[144:147], v[44:47]\n" \
    "ds_read2_b32 v[90:91], v163 offset0:194 offset1:195\n" \
    "v_fmac_f32 v24, %[dd], v84\n" \
    "v_fmac_f32 v25, %[dd], v85\n" \
    "v_mfma_f32_16x16x32_bf16 v[40:43], v[92:95], v[140:143], v[40:43]\n" \
    "ds_read_b128 v[144:147], %[hp] offset:3264\n" \
    "v_fmac_f32 v26, %[dd], v86\n" \
    "v_fmac_f32 v27, %[dd], v87\n" \
    "v_mfma_f32_16x16x32_bf16 v[36:39], v[92:95], v[136:139], v[36:39]\n" \
    "v_mfma_f32_16x16x32_bf16 v[32:35], v[92:95], v[132:135], v[32:35]\n" \
    "v_mfma_f32_16x16x32_bf16 v[28:31], v[92:95], v[128:131], v[28:31]\n" \
    "ds_read_b64 v[86:87], %[kvr] offset:1536\n" \
    "v_mfma_f32_16x16x32_bf16 v[24:27], v[92:95], v[124:127], v[24:27]\n" \
    "v_mfma_f32_16x16x32_bf16 v[20:23], v[92:95], v[120:123], v[20:23]\n" \
    "ds_read2_b32 v[92:93], v163 offset0:48 offset1:49\n" \
    "v_mfma_f32_16x16x32_bf16 v[44:47], v[96:99], v[140:143], v[44:47]\n" \
    "ds_read_b128 v[140:143], %[hp] offset:2752\n" \
    "v_mfma_f32_16x16x32_bf16 v[40:43], v[96:99], v[136:139], v[40:43]\n" \
    "ds_read2_b32 v[94:95], v163 offset0:50 offset1:51\n" \
    "v_mfma_f32_16x16x32_bf16 v[36:39], v[96:99], v[132:135], v[36:39]\n" \
    "v_mfma_f32_16x16x32_bf16 v[32:35], v[96:99], v[128:131], v[32:35]\n" \
    "v_mfma_f32_16x16x32_bf16 v[28:31], v[96:99], v[124:127], v[28:31]\n" \
    "v_mfma_f32_16x16x32_bf16 v[24:27], v[96:99], v[120:123], v[24:27]\n" \
    "ds_read2_b32 v[96:97], v162 offset0:192 offset1:193\n" \
    "v_mfma_f32_16x16x32_bf16 v[44:47], v[100:103], v[136:139], v[44:47]\n" \
    "ds_read_b128 v[136:139], %[hp] offset:2240\n" \
    "v_mfma_f32_16x16x32_bf16 v[40:43], v[100:103], v[132:135], v[40:43]\n" \
    "ds_read2_b32 v[98:99], v162 offset0:194 offset1:195\n" \
    "v_mfma_f32_16x16x32_bf16 v[36:39], v[100:103], v[128:131], v[36:39]\n" \
    "v_mfma_f32_16x16x32_bf16 v[32:35], v[100:103], v[124:127], v[32:35]\n" \
    "s_waitcnt lgkmcnt(6)\n" \
    "v_lshlrev_b32 v84, 16, v86\n" \
    "v_mfma_f32_16x16x32_bf16 v[28:31], v[100:103], v[120:123], v[28:31]\n" \
    "ds_read2_b32 v[100:101], v162 offset0:48 offset1:49\n" \
    "v_mfma_f32_16x16x32_bf16 v[44:47], v[104:107], v[132:135], v[44:47]\n" \
    "ds_read_b128 v[132:135], %[hp] offset:1728\n" \
    "v_and_b32 v85, 0xffff0000, v86\n" \
    "v_mfma_f32_16x16x32_bf16 v[40:43], v[104:107], v[128:131], v[40:43]\n" \
    "ds_read2_b32 v[102:103], v162 offset0:50 offset1:51\n" \
    "v_mfma_f32_16x16x32_bf16 v[36:39], v[104:107], v[124:127], v[36:39]\n" \
    "v_lshlrev_b32 v86, 16, v87\n" \
    "v_mfma_f32_16x16x32_bf16 v[32:35], v[104:107], v[120:123], v[32:35]\n" \
    "ds_read2_b32 v[104:105], v161 offset0:192 offset1:193\n" \
    "v_mfma_f32_16x16x32_bf16 v[44:47], v[108:111], v[128:131], v[44:47]\n" \
    "ds_read_b128 v[128:131], %[hp] offset:1216\n" \
    "v_and_b32 v87, 0xffff0000, v87\n" \
    "v_mfma_f32_16x16x32_bf16 v[40:43], v[108:111], v[124:127], v[40:43]\n" \
    "ds_read2_b32 v[106:107], v161 offset0:194 offset1:195\n" \
    "v_mfma_f32_16x16x32_bf16 v[36:39], v[108:111], v[120:123], v[36:39]\n" \
    "ds_read2_b32 v[108:109], v161 offset0:48 offset1:49\n" \
    "v_mfma_f32_16x16x32_bf16 v[44:47], v[112:115], v[124:127], v[44:47]\n" \
    "ds_read2_b32 v[110:111], v161 offset0:50 offset1:51\n" \
    "v_mfma_f32_16x16x32_bf16 v[40:43], v[112:115], v[120:123], v[40:43]\n" \
    "ds_read2_b32 v[112:113], v160 offset0:192 offset1:193\n" \
    "v_mfma_f32_16x16x32_bf16 v[44:47], v[116:119], v[120:123], v[44:47]\n" \
    "ds_read2_b32 v[114:115], v160 offset0:194 offset1:195\n" \
    "v_mfma_f32_16x16x32_bf16 v[44:47], v[88:91], v[148:151], v[44:47]\n" \
    "ds_read2_b32 v[116:117], v160 offset0:48 offset1:49\n" \
    "v_mfma_f32_16x16x32_bf16 v[40:43], v[88:91], v[144:147], v[40:43]\n" \
    "ds_read2_b32 v[118:119], v160 offset0:50 offset1:51\n" \
    "s_waitcnt lgkmcnt(15)\n" \
    "v_mfma_f32_16x16x32_bf16 v[36:39], v[88:91], v[140:143], v[36:39]\n" \
    "ds_read_b128 v[148:151], %[hp] offset:3840\n" \
    "s_waitcnt lgkmcnt(14)\n" \
    "v_mfma_f32_16x16x32_bf16 v[32:35], v[88:91], v[136:139], v[32:35]\n" \
    "ds_read_b128 v[124:127], %[hp] offset:768\n" \
    "s_waitcnt lgkmcnt(12)\n" \
    "v_mfma_f32_16x16x32_bf16 v[28:31], v[88:91], v[132:135], v[28:31]\n" \
    "ds_read_b128 v[120:123], %[hp] offset:256\n" \
    "s_waitcnt lgkmcnt(10)\n" \
    "v_mfma_f32_16x16x32_bf16 v[24:27], v[88:91], v[128:131], v[24:27]\n" \
    "v_mfma_f32_16x16x32_bf16 v[20:23], v[88:91], v[156:159], v[20:23]\n" \
    "v_mfma_f32_16x16x32_bf16 v[16:19], v[88:91], v[152:155], v[16:19]\n" \
    "ds_read2_b32 v[88:89], v163 offset0:216 offset1:217\n" \
    "v_fmac_f32 v28, %[dd], v84\n" \
    "v_fmac_f32 v29, %[dd], v85\n" \
    "v_mfma_f32_16x16x32_bf16 v[44:47], v[92:95], v[144:147], v[44:47]\n" \
    "ds_read2_b32 v[90:91], v163 offset0:218 offset1:219\n" \
    "v_fmac_f32 v30, %[dd], v86\n" \
    "v_fmac_f32 v31, %[dd], v87\n" \
    "v_mfma_f32_16x16x32_bf16 v[40:43], v[92:95], v[140:143], v[40:43]\n" \
    "ds_read_b128 v[144:147], %[hp] offset:3328\n" \
    "v_mfma_f32_16x16x32_bf16 v[36:39], v[92:95], v[136:139], v[36:39]\n" \
    "v_mfma_f32_16x16x32_bf16 v[32:35], v[92:95], v[132:135], v[32:35]\n" \
    "v_mfma_f32_16x16x32_bf16 v[28:31], v[92:95], v[128:131], v[28:31]\n" \
    "ds_read_b64 v[86:87], %[kvr] offset:2048\n" \
    "v_mfma_f32_16x16x32_bf16 v[24:27], v[92:95], v[156:159], v[24:27]\n" \
    "v_mfma_f32_16x16x32_bf16 v[20:23], v[92:95], v[152:155], v[20:23]\n" \
    "ds_read2_b32 v[92:93], v163 offset0:72 offset1:73\n" \
    "v_mfma_f32_16x16x32_bf16 v[44:47], v[96:99], v[140:143], v[44:47]\n" \
    "ds_read_b128 v[140:143], %[hp] offset:2816\n" \
    "v_mfma_f32_16x16x32_bf16 v[40:43], v[96:99], v[136:139], v[40:43]\n" \
    "ds_read2_b32 v[94:95], v163 offset0:74 offset1:75\n" \
    "v_mfma_f32_16x16x32_bf16 v[36:39], v[96:99], v[132:135], v[36:39]\n" \
    "v_mfma_f32_16x16x32_bf16 v[32:35], v[96:99], v[128:131], v[32:35]\n" \
    "v_mfma_f32_16x16x32_bf16 v[28:31], v[96:99], v[156:159], v[28:31]\n" \
    "v_mfma_f32_16x16x32_bf16 v[24:27], v[96:99], v[152:155], v[24:27]\n" \
    "ds_read2_b32 v[96:97], v162 offset0:216 offset1:217\n" \
    "v_mfma_f32_16x16x32_bf16 v[44:47], v[100:103], v[136:139], v[44:47]\n" \
    "ds_read_b128 v[136:139], %[hp] offset:2304\n" \
    "v_mfma_f32_16x16x32_bf16 v[40:43], v[100:103], v[132:135], v[40:43]\n" \
    "ds_read2_b32 v[98:99], v162 offset0:218 offset1:219\n" \
    "v_mfma_f32_16x16x32_bf16 v[36:39], v[100:103], v[128:131], v[36:39]\n" \
    "v_mfma_f32_16x16x32_bf16 v[32:35], v[100:103], v[156:159], v[32:35]\n" \
    "s_waitcnt lgkmcnt(6)\n" \
    "v_lshlrev_b32 v84, 16, v86\n" \
    "v_mfma_f32_16x16x32_bf16 v[28:31], v[100:103], v[152:155], v[28:31]\n" \
    "ds_read2_b32 v[100:101], v162 offset0:72 offset1:73\n" \
    "v_mfma_f32_16x16x32_bf16 v[44:47], v[104:107], v[132:135], v[44:47]\n" \
    "ds_read_b128 v[132:135], %[hp] offset:1792\n" \
    "v_and_b32 v85, 0xffff0000, v86\n" \
    "v_mfma_f32_16x16x32_bf16 v[40:43], v[104:107], v[128:131], v[40:43]\n" \
    "ds_read2_b32 v[102:103], v162 offset0:74 offset1:75\n" \
    "v_mfma_f32_16x16x32_bf16 v[36:39], v[104:107], v[156:159], v[36:39]\n" \
    "v_lshlrev_b32 v86, 16, v87\n" \
    "v_mfma_f32_16x16x32_bf16 v[32:35], v[104:107], v[152:155], v[32:35]\n" \
    "ds_read2_b32 v[104:105], v161 offset0:216 offset1:217\n" \
    "v_mfma_f32_16x16x32_bf16 v[44:47], v[108:111], v[128:131], v[44:47]\n" \
    "ds_read_b128 v[128:131], %[hp] offset:1280\n" \
    "v_and_b32 v87, 0xffff0000, v87\n" \
    "v_mfma_f32_16x16x32_bf16 v[40:43], v[108:111], v[156:159], v[40:43]\n" \
    "ds_read2_b32 v[106:107], v161 offset0:218 offset1:219\n" \
    "v_mfma_f32_16x16x32_bf16 v[36:39], v[108:111], v[152:155], v[36:39]\n" \
    "ds_read2_b32 v[108:109], v161 offset0:72 offset1:73\n" \
    "v_mfma_f32_16x16x32_bf16 v[44:47], v[112:115], v[156:159], v[44:47]\n" \
    "ds_read2_b32 v[110:111], v161 offset0:74 offset1:75\n" \
    "v_mfma_f32_16x16x32_bf16 v[40:43], v[112:115], v[152:155], v[40:43]\n" \
    "ds_read2_b32 v[112:113], v160 offset0:216 offset1:217\n" \
    "v_mfma_f32_16x16x32_bf16 v[44:47], v[116:119], v[152:155], v[44:47]\n" \
    "ds_read2_b32 v[114:115], v160 offset0:218 offset1:219\n" \
    "v_add_u32 v160, 288, v160\n" \
    "v_add_u32 v161, 288, v161\n" \
    "v_add_u32 v162, 288, v162\n" \
    "v_add_u32 v163, 288, v163\n" \
    "v_mfma_f32_16x16x32_bf16 v[44:47], v[88:91], v[148:151], v[44:47]\n" \
    "ds_read2_b32 v[116:117], v160 offset0:0 offset1:1\n" \
    "v_mfma_f32_16x16x32_bf16 v[40:43], v[88:91], v[144:147], v[40:43]\n" \
    "ds_read2_b32 v[118:119], v160 offset0:2 offset1:3\n" \
    "s_waitcnt lgkmcnt(15)\n" \
    "v_mfma_f32_16x16x32_bf16 v[36:39], v[88:91], v[140:143], v[36:39]\n" \
    "ds_read_b128 v[148:151], %[hp] offset:3904\n" \
    "s_waitcnt lgkmcnt(14)\n" \
    "v_mfma_f32_16x16x32_bf16 v[32:35], v[88:91], v[136:139], v[32:35]\n" \
    "ds_read_b128 v[156:159], %[hp] offset:832\n" \
    "s_waitcnt lgkmcnt(12)\n" \
    "v_mfma_f32_16x16x32_bf16 v[28:31], v[88:91], v[132:135], v[28:31]\n" \
    "ds_read_b128 v[152:155], %[hp] offset:320\n" \
    "s_waitcnt lgkmcnt(10)\n" \
    "v_mfma_f32_16x16x32_bf16 v[24:27], v[88:91], v[128:131], v[24:27]\n" \
    "v_mfma_f32_16x16x32_bf16 v[20:23], v[88:91], v[124:127], v[20:23]\n" \
    "v_fmac_f32 v32, %[dd], v84\n" \
    "v_fmac_f32 v33, %[dd], v85\n" \
    "v_mfma_f32_16x16x32_bf16 v[16:19], v[88:91], v[120:123], v[16:19]\n" \
    "ds_read2_b32 v[88:89], v163 offset0:160 offset1:161\n" \
    "v_fmac_f32 v34, %[dd], v86\n" \
    "v_fmac_f32 v35, %[dd], v87\n" \
    "v_mfma_f32_16x16x32_bf16 v[44:47], v[92:95], v[144:147], v[44:47]\n" \
    "ds_read2_b32 v[90:91], v163 offset0:162 offset1:163\n" \
    "v_mfma_f32_16x16x32_bf16 v[40:43], v[92:95], v[140:143], v[40:43]\n" \
    "ds_read_b128 v[144:147], %[hp] offset:3392\n" \
    "v_mfma_f32_16x16x32_bf16 v[36:39], v[92:95], v[136:139], v[36:39]\n" \
    "v_mfma_f32_16x16x32_bf16 v[32:35], v[92:95], v[132:135], v[32:35]\n" \
    "v_mfma_f32_16x16x32_bf16 v[28:31], v[92:95], v[128:131], v[28:31]\n" \
    "ds_read_b64 v[86:87], %[kvr] offset:2560\n" \
    "v_mfma_f32_16x16x32_bf16 v[24:27], v[92:95], v[124:127], v[24:27]\n" \
    "v_mfma_f32_16x16x32_bf16 v[20:23], v[92:95], v[120:123], v[20:23]\n" \
    "ds_read2_b32 v[92:93], v163 offset0:16 offset1:17\n" \
    "v_mfma_f32_16x16x32_bf16 v[44:47], v[96:99], v[140:143], v[44:47]\n" \
    "ds_read_b128 v[140:143], %[hp] offset:2880\n" \
    "v_mfma_f32_16x16x32_bf16 v[40:43], v[96:99], v[136:139], v[40:43]\n" \
    "ds_read2_b32 v[94:95], v163 offset0:18 offset1:19\n" \
    "v_mfma_f32_16x16x32_bf16 v[36:39], v[96:99], v[132:135], v[36:39]\n" \
    "v_mfma_f32_16x16x32_bf16 v[32:35], v[96:99], v[128:131], v[32:35]\n" \
    "v_mfma_f32_16x16x32_bf16 v[28:31], v[96:99], v[124:127], v[28:31]\n" \
    "v_mfma_f32_16x16x32_bf16 v[24:27], v[96:99], v[120:123], v[24:27]\n" \
    "ds_read2_b32 v[96:97], v162 offset0:160 offset1:161\n" \
    "v_mfma_f32_16x16x32_bf16 v[44:47], v[100:103], v[136:139], v[44:47]\n" \
    "ds_read_b128 v[136:139], %[hp] offset:2368\n" \
    "v_mfma_f32_16x16x32_bf16 v[40:43], v[100:103], v[132:135], v[40:43]\n" \
    "ds_read2_b32 v[98:99], v162 offset0:162 offset1:163\n" \
    "v_mfma_f32_16x16x32_bf16 v[36:39], v[100:103], v[128:131], v[36:39]\n" \
    "v_mfma_f32_16x16x32_bf16 v[32:35], v[100:103], v[124:127], v[32:35]\n" \
    "s_waitcnt lgkmcnt(6)\n" \
    "v_lshlrev_b32 v84, 16, v86\n" \
    "v_mfma_f32_16x16x32_bf16 v[28:31], v[100:103], v[120:123], v[28:31]\n" \
    "ds_read2_b32 v[100:101], v162 offset0:16 offset1:17\n" \
    "v_mfma_f32_16x16x32_bf16 v[44:47], v[104:107], v[132:135], v[44:47]\n" \
    "ds_read_b128 v[132:135], %[hp] offset:1856\n" \
    "v_and_b32 v85, 0xffff0000, v86\n" \
    "v_mfma_f32_16x16x32_bf16 v[40:43], v[104:107], v[128:131], v[40:43]\n" \
    "ds_read2_b32 v[102:103], v162 offset0:18 offset1:19\n" \
    "v_mfma_f32_16x16x32_bf16 v[36:39], v[104:107], v[124:127], v[36:39]\n" \
    "v_lshlrev_b32 v86, 16, v87\n" \
    "v_mfma_f32_16x16x32_bf16 v[32:35], v[104:107], v[120:123], v[32:35]\n" \
    "ds_read2_b32 v[104:105], v161 offset0:160 offset1:161\n" \
    "v_mfma_f32_16x16x32_bf16 v[44:47], v[108:111], v[128:131], v[44:47]\n" \
    "ds_read_b128 v[128:131], %[hp] offset:1344\n" \
    "v_and_b32 v87, 0xffff0000, v87\n" \
    "v_mfma_f32_16x16x32_bf16 v[40:43], v[108:111], v[124:127], v[40:43]\n" \
    "ds_read2_b32 v[106:107], v161 offset0:162 offset1:163\n" \
    "v_mfma_f32_16x16x32_bf16 v[36:39], v[108:111], v[120:123], v[36:39]\n" \
    "ds_read2_b32 v[108:109], v161 offset0:16 offset1:17\n" \
    "v_mfma_f32_16x16x32_bf16 v[44:47], v[112:115], v[124:127], v[44:47]\n" \
    "ds_read2_b32 v[110:111], v161 offset0:18 offset1:19\n" \
    "v_mfma_f32_16x16x32_bf16 v[40:43], v[112:115], v[120:123], v[40:43]\n" \
    "ds_read2_b32 v[112:113], v160 offset0:160 offset1:161\n" \
    "v_mfma_f32_16x16x32_bf16 v[44:47], v[116:119], v[120:123], v[44:47]\n" \
    "ds_read2_b32 v[114:115], v160 offset0:162 offset1:163\n" \
    "v_mfma_f32_16x16x32_bf16 v[44:47], v[88:91], v[148:151], v[44:47]\n" \
    "ds_read2_b32 v[116:117], v160 offset0:16 offset1:17\n" \
    "v_mfma_f32_16x16x32_bf16 v[40:43], v[88:91], v[144:147], v[40:43]\n" \
    "ds_read2_b32 v[118:119], v160 offset0:18 offset1:19\n" \
    "s_waitcnt lgkmcnt(15)\n" \
    "v_mfma_f32_16x16x32_bf16 v[36:39], v[88:91], v[140:143], v[36:39]\n" \
    "ds_read_b128 v[148:151], %[hp] offset:3968\n" \
    "s_waitcnt lgkmcnt(14)\n" \
    "v_mfma_f32_16x16x32_bf16 v[32:35], v[88:91], v[136:139], v[32:35]\n" \
    "ds_read_b128 v[124:127], %[hp] offset:896\n" \
    "s_waitcnt lgkmcnt(12)\n" \
    "v_mfma_f32_16x16x32_bf16 v[28:31], v[88:91], v[132:135], v[28:31]\n" \
    "ds_read_b128 v[120:123], %[hp] offset:384\n" \
    "s_waitcnt lgkmcnt(10)\n" \
    "v_mfma_f32_16x16x32_bf16 v[24:27], v[88:91], v[128:131], v[24:27]\n" \
    "v_fmac_f32 v36, %[dd], v84\n" \
    "v_fmac_f32 v37, %[dd], v85\n" \
    "v_mfma_f32_16x16x32_bf16 v[20:23], v[88:91], v[156:159], v[20:23]\n" \
    "v_fmac_f32 v38, %[dd], v86\n" \
    "v_fmac_f32 v39, %[dd], v87\n" \
    "v_mfma_f32_16x16x32_bf16 v[16:19], v[88:91], v[152:155], v[16:19]\n" \
    "ds_read2_b32 v[88:89], v163 offset0:176 offset1:177\n" \
    "v_mfma_f32_16x16x32_bf16 v[44:47], v[92:95], v[144:147], v[44:47]\n" \
    "ds_read2_b32 v[90:91], v163 offset0:178 offset1:179\n" \
    "v_mfma_f32_16x16x32_bf16 v[40:43], v[92:95], v[140:143], v[40:43]\n" \
    "ds_read_b128 v[144:147], %[hp] offset:3456\n" \
    "v_mfma_f32_16x16x32_bf16 v[36:39], v[92:95], v[136:139], v[36:39]\n" \
    "v_mfma_f32_16x16x32_bf16 v[32:35], v[92:95], v[132:135], v[32:35]\n" \
    "v_mfma_f32_16x16x32_bf16 v[28:31], v[92:95], v[128:131], v[28:31]\n" \
    "ds_read_b64 v[86:87], %[kvr] offset:3072\n" \
    "v_mfma_f32_16x16x32_bf16 v[24:27], v[92:95], v[156:159], v[24:27]\n" \
    "v_mfma_f32_16x16x32_bf16 v[20:23], v[92:95], v[152:155], v[20:23]\n" \
    "ds_read2_b32 v[92:93], v163 offset0:32 offset1:33\n" \
    "v_mfma_f32_16x16x32_bf16 v[44:47], v[96:99], v[140:143], v[44:47]\n" \
    "ds_read_b128 v[140:143], %[hp] offset:2944\n" \
    "v_mfma_f32_16x16x32_bf16 v[40:43], v[96:99], v[136:139], v[40:43]\n" \
    "ds_read2_b32 v[94:95], v163 offset0:34 offset1:35\n" \
    "v_mfma_f32_16x16x32_bf16 v[36:39], v[96:99], v[132:135], v[36:39]\n" \
    "v_mfma_f32_16x16x32_bf16 v[32:35], v[96:99], v[128:131], v[32:35]\n" \
    "v_mfma_f32_16x16x32_bf16 v[28:31], v[96:99], v[156:159], v[28:31]\n" \
    "v_mfma_f32_16x16x32_bf16 v[24:27], v[96:99], v[152:155], v[24:27]\n" \
    "ds_read2_b32 v[96:97], v162 offset0:176 offset1:177\n" \
    "v_mfma_f32_16x16x32_bf16 v[44:47], v[100:103], v[136:139], v[44:47]\n" \
    "ds_read_b128 v[136:139], %[hp] offset:2432\n" \
    "v_mfma_f32_16x16x32_bf16 v[40:43], v[100:103], v[132:135], v[40:43]\n" \
    "ds_read2_b32 v[98:99], v162 offset0:178 offset1:179\n" \
    "v_mfma_f32_16x16x32_bf16 v[36:39], v[100:103], v[128:131], v[36:39]\n" \
    "v_mfma_f32_16x16x32_bf16 v[32:35], v[100:103], v[156:159], v[32:35]\n" \
    "s_waitcnt lgkmcnt(6)\n" \
    "v_lshlrev_b32 v84, 16, v86\n" \
    "v_mfma_f32_16x16x32_bf16 v[28:31], v[100:103], v[152:155], v[28:31]\n" \
    "ds_read2_b32 v[100:101], v162 offset0:32 offset1:33\n" \
    "v_mfma_f32_16x16x32_bf16 v[44:47], v[104:107], v[132:135], v[44:47]\n" \
    "ds_read_b128 v[132:135], %[hp] offset:1920\n" \
    "v_and_b32 v85, 0xffff0000, v86\n" \
    "v_mfma_f32_16x16x32_bf16 v[40:43], v[104:107], v[128:131], v[40:43]\n" \
    "ds_read2_b32 v[102:103], v162 offset0:34 offset1:35\n" \
    "v_mfma_f32_16x16x32_bf16 v[36:39], v[104:107], v[156:159], v[36:39]\n" \
    "v_lshlrev_b32 v86, 16, v87\n" \
    "v_mfma_f32_16x16x32_bf16 v[32:35], v[104:107], v[152:155], v[32:35]\n" \
    "ds_read2_b32 v[104:105], v161 offset0:176 offset1:177\n" \
    "v_mfma_f32_16x16x32_bf16 v[44:47], v[108:111], v[128:131], v[44:47]\n" \
    "ds_read_b128 v[128:131], %[hp] offset:1408\n" \
    "v_and_b32 v87, 0xffff0000, v87\n" \
    "v_mfma_f32_16x16x32_bf16 v[40:43], v[108:111], v[156:159], v[40:43]\n" \
    "ds_read2_b32 v[106:107], v161 offset0:178 offset1:179\n" \
    "v_mfma_f32_16x16x32_bf16 v[36:39], v[108:111], v[152:155], v[36:39]\n" \
    "ds_read2_b32 v[108:109], v161 offset0:32 offset1:33\n" \
    "v_mfma_f32_16x16x32_bf16 v[44:47], v[112:115], v[156:159], v[44:47]\n" \
    "ds_read2_b32 v[110:111], v161 offset0:34 offset1:35\n" \
    "v_mfma_f32_16x16x32_bf16 v[40:43], v[112:115], v[152:155], v[40:43]\n" \
    "ds_read2_b32 v[112:113], v160 offset0:176 offset1:177\n" \
    "v_mfma_f32_16x16x32_bf16 v[44:47], v[116:119], v[152:155], v[44:47]\n" \
    "ds_read2_b32 v[114:115], v160 offset0:178 offset1:179\n" \
    "v_mfma_f32_16x16x32_bf16 v[44:47], v[88:91], v[148:151], v[44:47]\n" \
    "ds_read2_b32 v[116:117], v160 offset0:32 offset1:33\n" \
    "v_mfma_f32_16x16x32_bf16 v[40:43], v[88:91], v[144:147], v[40:43]\n" \
    "ds_read2_b32 v[118:119], v160 offset0:34 offset1:35\n" \
    "s_waitcnt lgkmcnt(15)\n" \
    "v_mfma_f32_16x16x32_bf16 v[36:39], v[88:91], v[140:143], v[36:39]\n" \
    "ds_read_b128 v[152:155], %[hp] offset:448\n" \
    "s_waitcnt lgkmcnt(14)\n" \
    "v_mfma_f32_16x16x32_bf16 v[32:35], v[88:91], v[136:139], v[32:35]\n" \
    "ds_read_b128 v[156:159], %[hp] offset:960\n" \
    "s_waitcnt lgkmcnt(12)\n" \
    "v_mfma_f32_16x16x32_bf16 v[28:31], v[88:91], v[132:135], v[28:31]\n" \
    "ds_read_b128 v[148:151], %[hp] offset:4032\n" \
    "v_fmac_f32 v40, %[dd], v84\n" \
    "v_fmac_f32 v41, %[dd], v85\n" \
    "s_waitcnt lgkmcnt(10)\n" \
    "v_mfma_f32_16x16x32_bf16 v[24:27], v[88:91], v[128:131], v[24:27]\n" \
    "v_fmac_f32 v42, %[dd], v86\n" \
    "v_fmac_f32 v43, %[dd], v87\n" \
    "v_mfma_f32_16x16x32_bf16 v[20:23], v[88:91], v[124:127], v[20:23]\n" \
    "v_mfma_f32_16x16x32_bf16 v[16:19], v[88:91], v[120:123], v[16:19]\n" \
    "ds_read2_b32 v[88:89], v163 offset0:192 offset1:193\n" \
    "v_mfma_f32_16x16x32_bf16 v[44:47], v[92:95], v[144:147], v[44:47]\n" \
    "ds_read2_b32 v[90:91], v163 offset0:194 offset1:195\n" \
    "v_mfma_f32_16x16x32_bf16 v[40:43], v[92:95], v[140:143], v[40:43]\n" \
    "ds_read_b128 v[144:147], %[hp] offset:3520\n" \
    "v_mfma_f32_16x16x32_bf16 v[36:39], v[92:95], v[136:139], v[36:39]\n" \
    "v_mfma_f32_16x16x32_bf16 v[32:35], v[92:95], v[132:135], v[32:35]\n" \
    "v_mfma_f32_16x16x32_bf16 v[28:31], v[92:95], v[128:131], v[28:31]\n" \
    "ds_read_b64 v[86:87], %[kvr] offset:3584\n" \
    "v_mfma_f32_16x16x32_bf16 v[24:27], v[92:95], v[124:127], v[24:27]\n" \
    "v_mfma_f32_16x16x32_bf16 v[20:23], v[92:95], v[120:123], v[20:23]\n" \
    "ds_read2_b32 v[92:93], v163 offset0:48 offset1:49\n" \
    "v_mfma_f32_16x16x32_bf16 v[44:47], v[96:99], v[140:143], v[44:47]\n" \
    "ds_read2_b32 v[94:95], v163 offset0:50 offset1:51\n" \
    "v_mfma_f32_16x16x32_bf16 v[40:43], v[96:99], v[136:139], v[40:43]\n" \
    "ds_read_b128 v[140:143], %[hp] offset:3008\n" \
    "v_mfma_f32_16x16x32_bf16 v[36:39], v[96:99], v[132:135], v[36:39]\n" \
    "v_mfma_f32_16x16x32_bf16 v[32:35], v[96:99], v[128:131], v[32:35]\n" \
    "v_mfma_f32_16x16x32_bf16 v[28:31], v[96:99], v[124:127], v[28:31]\n" \
    "v_mfma_f32_16x16x32_bf16 v[24:27], v[96:99], v[120:123], v[24:27]\n" \
    "ds_read2_b32 v[96:97], v162 offset0:192 offset1:193\n" \
    "v_mfma_f32_16x16x32_bf16 v[44:47], v[100:103], v[136:139], v[44:47]\n" \
    "ds_read2_b32 v[98:99], v162 offset0:194 offset1:195\n" \
    "v_mfma_f32_16x16x32_bf16 v[40:43], v[100:103], v[132:135], v[40:43]\n" \
    "ds_read_b128 v[136:139], %[hp] offset:2496\n" \
    "v_mfma_f32_16x16x32_bf16 v[36:39], v[100:103], v[128:131], v[36:39]\n" \
    "v_mfma_f32_16x16x32_bf16 v[32:35], v[100:103], v[124:127], v[32:35]\n" \
    "s_waitcnt lgkmcnt(6)\n" \
    "v_lshlrev_b32 v84, 16, v86\n" \
    "v_mfma_f32_16x16x32_bf16 v[28:31], v[100:103], v[120:123], v[28:31]\n" \
    "ds_read2_b32 v[100:101], v162 offset0:48 offset1:49\n" \
    "v_mfma_f32_16x16x32_bf16 v[44:47], v[104:107], v[132:135], v[44:47]\n" \
    "ds_read_b128 v[132:135], %[hp] offset:1984\n" \
    "v_and_b32 v85, 0xffff0000, v86\n" \
    "v_mfma_f32_16x16x32_bf16 v[40:43], v[104:107], v[128:131], v[40:43]\n" \
    "ds_read2_b32 v[102:103], v162 offset0:50 offset1:51\n" \
    "v_mfma_f32_16x16x32_bf16 v[36:39], v[104:107], v[124:127], v[36:39]\n" \
    "v_lshlrev_b32 v86, 16, v87\n" \
    "v_mfma_f32_16x16x32_bf16 v[32:35], v[104:107], v[120:123], v[32:35]\n" \
    "ds_read2_b32 v[104:105], v161 offset0:192 offset1:193\n" \
    "v_mfma_f32_16x16x32_bf16 v[44:47], v[108:111], v[128:131], v[44:47]\n" \
    "ds_read_b128 v[128:131], %[hp] offset:1472\n" \
    "v_and_b32 v87, 0xffff0000, v87\n" \
    "v_mfma_f32_16x16x32_bf16 v[40:43], v[108:111], v[124:127], v[40:43]\n" \
    "ds_read2_b32 v[106:107], v161 offset0:194 offset1:195\n" \
    "v_mfma_f32_16x16x32_bf16 v[36:39], v[108:111], v[120:123], v[36:39]\n" \
    "ds_read2_b32 v[108:109], v161 offset0:48 offset1:49\n" \
    "v_mfma_f32_16x16x32_bf16 v[44:47], v[112:115], v[124:127], v[44:47]\n" \
    "ds_read2_b32 v[110:111], v161 offset0:50 offset1:51\n" \
    "v_mfma_f32_16x16x32_bf16 v[40:43], v[112:115], v[120:123], v[40:43]\n" \
    "ds_read2_b32 v[112:113], v160 offset0:192 offset1:193\n" \
    "v_mfma_f32_16x16x32_bf16 v[44:47], v[116:119], v[120:123], v[44:47]\n" \
    "ds_read2_b32 v[114:115], v160 offset0:194 offset1:195\n" \
    "v_mfma_f32_16x16x32_bf16 v[16:19], v[88:91], v[152:155], v[16:19]\n" \
    "ds_read2_b32 v[116:117], v160 offset0:48 offset1:49\n" \
    "ds_read_b32 v50, %[ctl] offset:52\n" \
    "v_mfma_f32_16x16x32_bf16 v[20:23], v[88:91], v[156:159], v[20:23]\n" \
    "ds_read2_b32 v[118:119], v160 offset0:50 offset1:51\n" \
    "s_waitcnt lgkmcnt(15)\n" \
    "v_mfma_f32_16x16x32_bf16 v[20:23], v[92:95], v[152:155], v[20:23]\n" \
    "s_waitcnt lgkmcnt(8)\n" \
    "v_mfma_f32_16x16x32_bf16 v[24:27], v[88:91], v[128:131], v[24:27]\n" \
    "v_fmac_f32 v44, %[dd], v84\n" \
    "v_fmac_f32 v45, %[dd], v85\n" \
    "v_mfma_f32_16x16x32_bf16 v[24:27], v[92:95], v[156:159], v[24:27]\n" \
    "v_fmac_f32 v46, %[dd], v86\n" \
    "v_fmac_f32 v47, %[dd], v87\n" \
    "v_mfma_f32_16x16x32_bf16 v[24:27], v[96:99], v[152:155], v[24:27]\n" \
    "v_mfma_f32_16x16x32_bf16 v[28:31], v[88:91], v[132:135], v[28:31]\n" \
    "v_cvt_pk_bf16_f32 v52, v16, v17\n" \
    "v_cvt_pk_bf16_f32 v54, v20, v21\n" \
    "v_mfma_f32_16x16x32_bf16 v[28:31], v[92:95], v[128:131], v[28:31]\n" \
    "v_cvt_pk_bf16_f32 v53, v18, v19\n" \
    "v_cvt_pk_bf16_f32 v55, v22, v23\n" \
    "v_mfma_f32_16x16x32_bf16 v[28:31], v[96:99], v[156:159], v[28:31]\n" \
    "v_add_u32 v68, 0, %[yo]\n" \
    "v_mfma_f32_16x16x32_bf16 v[28:31], v[100:103], v[152:155], v[28:31]\n" \
    "s_waitcnt lgkmcnt(1)\n" \
    "v_readfirstlane_b32 %[tmp], v50\n" \
    "s_mov_b32 %[cnt], 0\n" \
    "s_cmp_lg_u32 %[tmp], 0\n" \
    "s_cbranch_scc1 Lok_%=\n" \
    "Ls2_%=:\n" \
    "ds_read_b32 v50, %[ctl] offset:52\n" \
    "s_add_u32 %[cnt], %[cnt], 1\n" \
    "s_waitcnt lgkmcnt(0)\n" \
    "v_readfirstlane_b32 %[tmp], v50\n" \
    "s_cmp_ge_u32 %[cnt], %[cap]\n" \
    "s_cbranch_scc1 Lok_%=\n" \
    "s_cmp_eq_u32 %[tmp], 0\n" \
    "s_cbranch_scc1 Ls2_%=\n" \
    "Lok_%=:\n" \
    "v_permlane16_swap_b32 v52, v54\n" \
    "v_mfma_f32_16x16x32_bf16 v[32:35], v[88:91], v[136:139], v[32:35]\n" \
    "v_permlane16_swap_b32 v53, v55\n" \
    "v_mfma_f32_16x16x32_bf16 v[32:35], v[92:95], v[132:135], v[32:35]\n" \
    "global_store_dwordx4 v68, v[52:55], %[wsp] sc1\n" \
    "v_mfma_f32_16x16x32_bf16 v[32:35], v[96:99], v[128:131], v[32:35]\n" \
    "v_mfma_f32_16x16x32_bf16 v[32:35], v[100:103], v[156:159], v[32:35]\n" \
    "v_cvt_pk_bf16_f32 v56, v24, v25\n" \
    "v_cvt_pk_bf16_f32 v58, v28, v29\n" \
    "v_mfma_f32_16x16x32_bf16 v[32:35], v[104:107], v[152:155], v[32:35]\n" \
    "v_cvt_pk_bf16_f32 v57, v26, v27\n" \
    "v_cvt_pk_bf16_f32 v59, v30, v31\n" \
    "v_mfma_f32_16x16x32_bf16 v[36:39], v[88:91], v[140:143], v[36:39]\n" \
    "v_add_u32 v69, 655360, %[yo]\n" \
    "v_mfma_f32_16x16x32_bf16 v[36:39], v[92:95], v[136:139], v[36:39]\n" \
    "v_permlane16_swap_b32 v56, v58\n" \
    "v_mfma_f32_16x16x32_bf16 v[36:39], v[96:99], v[132:135], v[36:39]\n" \
    "v_permlane16_swap_b32 v57, v59\n" \
    "v_mfma_f32_16x16x32_bf16 v[36:39], v[100:103], v[128:131], v[36:39]\n" \
    "global_store_dwordx4 v69, v[56:59], %[wsp] sc1\n" \
    "v_mfma_f32_16x16x32_bf16 v[36:39], v[104:107], v[156:159], v[36:39]\n" \
    "v_mfma_f32_16x16x32_bf16 v[36:39], v[108:111], v[152:155], v[36:39]\n" \
    "v_mfma_f32_16x16x32_bf16 v[40:43], v[88:91], v[144:147], v[40:43]\n" \
    "v_mfma_f32_16x16x32_bf16 v[40:43], v[92:95], v[140:143], v[40:43]\n" \
    "v_mfma_f32_16x16x32_bf16 v[40:43], v[96:99], v[136:139], v[40:43]\n" \
    "v_mfma_f32_16x16x32_bf16 v[40:43], v[100:103], v[132:135], v[40:43]\n" \
    "v_cvt_pk_bf16_f32 v60, v32, v33\n" \
    "v_cvt_pk_bf16_f32 v62, v36, v37\n" \
    "v_mfma_f32_16x16x32_bf16 v[40:43], v[104:107], v[128:131], v[40:43]\n" \
    "v_cvt_pk_bf16_f32 v61, v34, v35\n" \
    "v_cvt_pk_bf16_f32 v63, v38, v39\n" \
    "v_mfma_f32_16x16x32_bf16 v[40:43], v[108:111], v[156:159], v[40:43]\n" \
    "v_add_u32 v70, 1310720, %[yo]\n" \
    "v_mfma_f32_16x16x32_bf16 v[40:43], v[112:115], v[152:155], v[40:43]\n" \
    "v_permlane16_swap_b32 v60, v62\n" \
    "v_mfma_f32_16x16x32_bf16 v[44:47], v[88:91], v[148:151], v[44:47]\n" \
    "v_permlane16_swap_b32 v61, v63\n" \
    "v_mfma_f32_16x16x32_bf16 v[44:47], v[92:95], v[144:147], v[44:47]\n" \
    "global_store_dwordx4 v70, v[60:63], %[wsp] sc1\n" \
    "v_mfma_f32_16x16x32_bf16 v[44:47], v[96:99], v[140:143], v[44:47]\n" \
    "v_mfma_f32_16x16x32_bf16 v[44:47], v[100:103], v[136:139], v[44:47]\n" \
    "v_mfma_f32_16x16x32_bf16 v[44:47], v[104:107], v[132:135], v[44:47]\n" \
    "v_mfma_f32_16x16x32_bf16 v[44:47], v[108:111], v[128:131], v[44:47]\n" \
    "v_mfma_f32_16x16x32_bf16 v[44:47], v[112:115], v[156:159], v[44:47]\n" \
    "s_waitcnt lgkmcnt(0)\n" \
    "v_mfma_f32_16x16x32_bf16 v[44:47], v[116:119], v[152:155], v[44:47]\n" \
    "s_nop 7\n" \
    "v_cvt_pk_bf16_f32 v64, v40, v41\n" \
    "v_cvt_pk_bf16_f32 v66, v44, v45\n" \
    "v_cvt_pk_bf16_f32 v65, v42, v43\n" \
    "v_cvt_pk_bf16_f32 v67, v46, v47\n" \
    "v_add_u32 v71, 1966080, %[yo]\n" \
    "s_nop 0\n" \
    "v_permlane16_swap_b32 v64, v66\n" \
    "v_permlane16_swap_b32 v65, v67\n" \
    "global_store_dwordx4 v71, v[64:67], %[wsp] sc1\n" \
    "s_nop 1\n"
#define CONV_CLOB "v16", "v17", "v18", "v19", "v20", "v21", "v22", "v23", "v24", "v25", "v26", "v27", "v28", "v29", "v30", "v31", "v32", "v33", "v34", "v35", "v36", "v37", "v38", "v39", "v40", "v41", "v42", "v43", "v44", "v45", "v46", "v47", "v50", "v51", "v52", "v53", "v54", "v55", "v56", "v57", "v58", "v59", "v60", "v61", "v62", "v63", "v64", "v65", "v66", "v67", "v68", "v69", "v70", "v71", "v72", "v73", "v74", "v75", "v76", "v77", "v78", "v79", "v80", "v81", "v82", "v83", "v84", "v85", "v86", "v87", "v88", "v89", "v90", "v91", "v92", "v93", "v94", "v95", "v96", "v97", "v98", "v99", "v100", "v101", "v102", "v103", "v104", "v105", "v106", "v107", "v108", "v109", "v110", "v111", "v112", "v113", "v114", "v115", "v116", "v117", "v118", "v119", "v120", "v121", "v122", "v123", "v124", "v125", "v126", "v127", "v128", "v129", "v130", "v131", "v132", "v133", "v134", "v135", "v136", "v137", "v138", "v139", "v140", "v141", "v142", "v143", "v144", "v145", "v146", "v147", "v148", "v149", "v150", "v151", "v152", "v153", "v154", "v155", "v156", "v157", "v158", "v159", "v160", "v161", "v162", "v163", "scc", "memory"
#define G_ASM \
    "v_add_u32 v154, 67584, %[wp0]\n" \
    "v_add_u32 v155, 67584, %[wp1]\n" \
    "v_add_u32 v156, 67584, %[wp2]\n" \
    "v_add_u32 v157, 67584, %[wp3]\n" \
    "s_mov_b32 s60, 0x11111111\n" \
    "s_mov_b32 s61, 0x11111111\n" \
    "s_mov_b32 s62, 0x22222222\n" \
    "s_mov_b32 s63, 0x22222222\n" \
    "s_mov_b32 s64, 0x44444444\n" \
    "s_mov_b32 s65, 0x44444444\n" \
    "s_mov_b32 s66, 0x88888888\n" \
    "s_mov_b32 s67, 0x88888888\n" \
    "s_mov_b32 s68, -1\n" \
    "s_mov_b32 s69, 0\n" \
    "s_cmp_eq_u32 %[half], 0\n" \
    "s_waitcnt lgkmcnt(0)\n" \
    "s_cbranch_scc0 Llate_%=\n" \
    "s_barrier\n" \
    "ds_read_b64_tr_b16 v[126:127], %[wp1] offset:0\n" \
    "ds_read_b64_tr_b16 v[128:129], %[wp1] offset:2048\n" \
    "ds_read_b128 v[98:101], %[ap0] offset:53248\n" \
    "ds_read_b64_tr_b16 v[130:131], %[wp2] offset:0\n" \
    "ds_read_b64_tr_b16 v[132:133], %[wp2] offset:2048\n" \
    "ds_read_b64_tr_b16 v[134:135], %[wp3] offset:0\n" \
    "ds_read_b64_tr_b16 v[136:137], %[wp3] offset:2048\n" \
    "ds_read_b128 v[102:105], %[ap0] offset:55296\n" \
    "ds_read_b64_tr_b16 v[122:123], %[wp0] offset:0\n" \
    "ds_read_b64_tr_b16 v[124:125], %[wp0] offset:2048\n" \
    "ds_read_b128 v[106:109], %[ap0] offset:57344\n" \
    "ds_read_b128 v[110:113], %[ap0] offset:59392\n" \
    "ds_read_b128 v[114:117], %[ap0] offset:61440\n" \
    "s_waitcnt lgkmcnt(10)\n" \
    "v_mfma_f32_16x16x32_bf16 v[38:41], v[126:129], v[98:101], 0\n" \
    "ds_read_b64_tr_b16 v[142:143], %[wp1] offset:34816\n" \
    "s_waitcnt lgkmcnt(9)\n" \
    "v_mfma_f32_16x16x32_bf16 v[58:61], v[98:101], v[130:133], 0\n" \
    "ds_read_b64_tr_b16 v[144:145], %[wp1] offset:36864\n" \
    "s_waitcnt lgkmcnt(8)\n" \
    "v_mfma_f32_16x16x32_bf16 v[78:81], v[134:137], v[98:101], 0\n" \
    "ds_read_b128 v[98:101], %[ap1] offset:53248\n" \
    "s_waitcnt lgkmcnt(8)\n" \
    "v_mfma_f32_16x16x32_bf16 v[42:45], v[126:129], v[102:105], 0\n" \
    "ds_read_b64_tr_b16 v[146:147], %[wp2] offset:34816\n" \
    "v_mfma_f32_16x16x32_bf16 v[62:65], v[102:105], v[130:133], 0\n" \
    "ds_read_b64_tr_b16 v[148:149], %[wp2] offset:36864\n" \
    "v_mfma_f32_16x16x32_bf16 v[82:85], v[134:137], v[102:105], 0\n" \
    "ds_read_b64_tr_b16 v[150:151], %[wp3] offset:34816\n" \
    "s_waitcnt lgkmcnt(9)\n" \
    "v_mfma_f32_16x16x32_bf16 v[22:25], v[122:125], v[102:105], 0\n" \
    "ds_read_b128 v[102:105], %[ap1] offset:55296\n" \
    "s_waitcnt lgkmcnt(9)\n" \
    "v_mfma_f32_16x16x32_bf16 v[46:49], v[126:129], v[106:109], 0\n" \
    "ds_read_b64_tr_b16 v[152:153], %[wp3] offset:36864\n" \
    "v_mfma_f32_16x16x32_bf16 v[66:69], v[106:109], v[130:133], 0\n" \
    "ds_read_b64_tr_b16 v[138:139], %[wp0] offset:34816\n" \
    "v_mfma_f32_16x16x32_bf16 v[86:89], v[134:137], v[106:109], 0\n" \
    "ds_read_b64_tr_b16 v[140:141], %[wp0] offset:36864\n" \
    "v_mfma_f32_16x16x32_bf16 v[26:29], v[122:125], v[106:109], 0\n" \
    "ds_read_b128 v[106:109], %[ap1] offset:57344\n" \
    "s_waitcnt lgkmcnt(12)\n" \
    "v_mfma_f32_16x16x32_bf16 v[50:53], v[126:129], v[110:113], 0\n" \
    "ds_read_b128 v[118:121], %[ap1] offset:61440\n" \
    "v_mfma_f32_16x16x32_bf16 v[70:73], v[110:113], v[130:133], 0\n" \
    "v_mfma_f32_16x16x32_bf16 v[90:93], v[134:137], v[110:113], 0\n" \
    "v_mfma_f32_16x16x32_bf16 v[30:33], v[122:125], v[110:113], 0\n" \
    "ds_read_b128 v[110:113], %[ap1] offset:59392\n" \
    "s_waitcnt lgkmcnt(13)\n" \
    "v_mfma_f32_16x16x32_bf16 v[54:57], v[126:129], v[114:117], 0\n" \
    "s_barrier\n" \
    "v_mfma_f32_16x16x32_bf16 v[74:77], v[114:117], v[130:133], 0\n" \
    "v_mfma_f32_16x16x32_bf16 v[94:97], v[134:137], v[114:117], 0\n" \
    "v_mfma_f32_16x16x32_bf16 v[34:37], v[122:125], v[114:117], 0\n" \
    "s_waitcnt lgkmcnt(10)\n" \
    "v_mfma_f32_16x16x32_bf16 v[38:41], v[142:145], v[98:101], v[38:41]\n" \
    "ds_read_b64_tr_b16 v[126:127], %[wp1] offset:51200\n" \
    "s_waitcnt lgkmcnt(9)\n" \
    "v_mfma_f32_16x16x32_bf16 v[58:61], v[98:101], v[146:149], v[58:61]\n" \
    "ds_read_b64_tr_b16 v[128:129], %[wp1] offset:53248\n" \
    "s_waitcnt lgkmcnt(7)\n" \
    "v_mfma_f32_16x16x32_bf16 v[78:81], v[150:153], v[98:101], v[78:81]\n" \
    "ds_read_b128 v[98:101], %[ap0] offset:0\n" \
    "v_mfma_f32_16x16x32_bf16 v[42:45], v[142:145], v[102:105], v[42:45]\n" \
    "ds_read_b64_tr_b16 v[130:131], %[wp2] offset:51200\n" \
    "v_mfma_f32_16x16x32_bf16 v[62:65], v[102:105], v[146:149], v[62:65]\n" \
    "ds_read_b64_tr_b16 v[132:133], %[wp2] offset:53248\n" \
    "v_mfma_f32_16x16x32_bf16 v[82:85], v[150:153], v[102:105], v[82:85]\n" \
    "ds_read_b64_tr_b16 v[134:135], %[wp3] offset:51200\n" \
    "s_waitcnt lgkmcnt(9)\n" \
    "v_mfma_f32_16x16x32_bf16 v[22:25], v[138:141], v[102:105], v[22:25]\n" \
    "ds_read_b128 v[102:105], %[ap0] offset:2048\n" \
    "s_waitcnt lgkmcnt(9)\n" \
    "v_mfma_f32_16x16x32_bf16 v[46:49], v[142:145], v[106:109], v[46:49]\n" \
    "ds_read_b64_tr_b16 v[136:137], %[wp3] offset:53248\n" \
    "v_mfma_f32_16x16x32_bf16 v[66:69], v[106:109], v[146:149], v[66:69]\n" \
    "ds_read_b64_tr_b16 v[122:123], %[wp0] offset:51200\n" \
    "v_mfma_f32_16x16x32_bf16 v[86:89], v[150:153], v[106:109], v[86:89]\n" \
    "ds_read_b64_tr_b16 v[124:125], %[wp0] offset:53248\n" \
    "v_mfma_f32_16x16x32_bf16 v[26:29], v[138:141], v[106:109], v[26:29]\n" \
    "ds_read_b128 v[106:109], %[ap0] offset:4096\n" \
    "s_waitcnt lgkmcnt(11)\n" \
    "v_mfma_f32_16x16x32_bf16 v[50:53], v[142:145], v[110:113], v[50:53]\n" \
    "ds_read_b128 v[114:117], %[ap0] offset:8192\n" \
    "v_mfma_f32_16x16x32_bf16 v[70:73], v[110:113], v[146:149], v[70:73]\n" \
    "v_mfma_f32_16x16x32_bf16 v[90:93], v[150:153], v[110:113], v[90:93]\n" \
    "v_mfma_f32_16x16x32_bf16 v[30:33], v[138:141], v[110:113], v[30:33]\n" \
    "ds_read_b128 v[110:113], %[ap0] offset:6144\n" \
    "v_mfma_f32_16x16x32_bf16 v[54:57], v[142:145], v[118:121], v[54:57]\n" \
    "s_barrier\n" \
    "v_mfma_f32_16x16x32_bf16 v[74:77], v[118:121], v[146:149], v[74:77]\n" \
    "v_mfma_f32_16x16x32_bf16 v[94:97], v[150:153], v[118:121], v[94:97]\n" \
    "v_mfma_f32_16x16x32_bf16 v[34:37], v[138:141], v[118:121], v[34:37]\n" \
    "s_waitcnt lgkmcnt(10)\n" \
    "v_mfma_f32_16x16x32_bf16 v[38:41], v[126:129], v[98:101], v[38:41]\n" \
    "ds_read_b64_tr_b16 v[142:143], v155 offset:0\n" \
    "s_waitcnt lgkmcnt(9)\n" \
    "v_mfma_f32_16x16x32_bf16 v[58:61], v[98:101], v[130:133], v[58:61]\n" \
    "ds_read_b64_tr_b16 v[144:145], v155 offset:2048\n" \
    "s_waitcnt lgkmcnt(7)\n" \
    "v_mfma_f32_16x16x32_bf16 v[78:81], v[134:137], v[98:101], v[78:81]\n" \
    "ds_read_b128 v[98:101], %[ap1] offset:0\n" \
    "v_mfma_f32_16x16x32_bf16 v[42:45], v[126:129], v[102:105], v[42:45]\n" \
    "ds_read_b64_tr_b16 v[146:147], v156 offset:0\n" \
    "v_mfma_f32_16x16x32_bf16 v[62:65], v[102:105], v[130:133], v[62:65]\n" \
    "ds_read_b64_tr_b16 v[148:149], v156 offset:2048\n" \
    "v_mfma_f32_16x16x32_bf16 v[82:85], v[134:137], v[102:105], v[82:85]\n" \
    "ds_read_b64_tr_b16 v[150:151], v157 offset:0\n" \
    "s_waitcnt lgkmcnt(9)\n" \
    "v_mfma_f32_16x16x32_bf16 v[22:25], v[122:125], v[102:105], v[22:25]\n" \
    "ds_read_b128 v[102:105], %[ap1] offset:2048\n" \
    "s_waitcnt lgkmcnt(9)\n" \
    "v_mfma_f32_16x16x32_bf16 v[46:49], v[126:129], v[106:109], v[46:49]\n" \
    "ds_read_b64_tr_b16 v[152:153], v157 offset:2048\n" \
    "v_mfma_f32_16x16x32_bf16 v[66:69], v[106:109], v[130:133], v[66:69]\n" \
    "ds_read_b64_tr_b16 v[138:139], v154 offset:0\n" \
    "v_mfma_f32_16x16x32_bf16 v[86:89], v[134:137], v[106:109], v[86:89]\n" \
    "ds_read_b64_tr_b16 v[140:141], v154 offset:2048\n" \
    "v_mfma_f32_16x16x32_bf16 v[26:29], v[122:125], v[106:109], v[26:29]\n" \
    "ds_read_b128 v[106:109], %[ap1] offset:4096\n" \
    "s_waitcnt lgkmcnt(11)\n" \
    "v_mfma_f32_16x16x32_bf16 v[50:53], v[126:129], v[110:113], v[50:53]\n" \
    "ds_read_b128 v[118:121], %[ap1] offset:8192\n" \
    "v_mfma_f32_16x16x32_bf16 v[70:73], v[110:113], v[130:133], v[70:73]\n" \
    "v_mfma_f32_16x16x32_bf16 v[90:93], v[134:137], v[110:113], v[90:93]\n" \
    "v_mfma_f32_16x16x32_bf16 v[30:33], v[122:125], v[110:113], v[30:33]\n" \
    "ds_read_b128 v[110:113], %[ap1] offset:6144\n" \
    "v_mfma_f32_16x16x32_bf16 v[54:57], v[126:129], v[114:117], v[54:57]\n" \
    "s_barrier\n" \
    "v_mfma_f32_16x16x32_bf16 v[74:77], v[114:117], v[130:133], v[74:77]\n" \
    "v_mfma_f32_16x16x32_bf16 v[94:97], v[134:137], v[114:117], v[94:97]\n" \
    "v_mfma_f32_16x16x32_bf16 v[34:37], v[122:125], v[114:117], v[34:37]\n" \
    "s_waitcnt lgkmcnt(10)\n" \
    "v_mfma_f32_16x16x32_bf16 v[38:41], v[142:145], v[98:101], v[38:41]\n" \
    "ds_read_b64_tr_b16 v[126:127], v155 offset:16384\n" \
    "s_waitcnt lgkmcnt(9)\n" \
    "v_mfma_f32_16x16x32_bf16 v[58:61], v[98:101], v[146:149], v[58:61]\n" \
    "ds_read_b64_tr_b16 v[128:129], v155 offset:18432\n" \
    "s_waitcnt lgkmcnt(7)\n" \
    "v_mfma_f32_16x16x32_bf16 v[78:81], v[150:153], v[98:101], v[78:81]\n" \
    "ds_read_b128 v[98:101], %[ap0] offset:18432\n" \
    "v_mfma_f32_16x16x32_bf16 v[42:45], v[142:145], v[102:105], v[42:45]\n" \
    "ds_read_b64_tr_b16 v[130:131], v156 offset:16384\n" \
    "v_mfma_f32_16x16x32_bf16 v[62:65], v[102:105], v[146:149], v[62:65]\n" \
    "ds_read_b64_tr_b16 v[132:133], v156 offset:18432\n" \
    "v_mfma_f32_16x16x32_bf16 v[82:85], v[150:153], v[102:105], v[82:85]\n" \
    "ds_read_b64_tr_b16 v[134:135], v157 offset:16384\n" \
    "s_waitcnt lgkmcnt(9)\n" \
    "v_mfma_f32_16x16x32_bf16 v[22:25], v[138:141], v[102:105], v[22:25]\n" \
    "ds_read_b128 v[102:105], %[ap0] offset:20480\n" \
    "s_waitcnt lgkmcnt(9)\n" \
    "v_mfma_f32_16x16x32_bf16 v[46:49], v[142:145], v[106:109], v[46:49]\n" \
    "ds_read_b64_tr_b16 v[136:137], v157 offset:18432\n" \
    "v_mfma_f32_16x16x32_bf16 v[66:69], v[106:109], v[146:149], v[66:69]\n" \
    "ds_read_b64_tr_b16 v[122:123], v154 offset:16384\n" \
    "v_mfma_f32_16x16x32_bf16 v[86:89], v[150:153], v[106:109], v[86:89]\n" \
    "ds_read_b64_tr_b16 v[124:125], v154 offset:18432\n" \
    "v_mfma_f32_16x16x32_bf16 v[26:29], v[138:141], v[106:109], v[26:29]\n" \
    "ds_read_b128 v[106:109], %[ap0] offset:22528\n" \
    "s_waitcnt lgkmcnt(11)\n" \
    "v_mfma_f32_16x16x32_bf16 v[50:53], v[142:145], v[110:113], v[50:53]\n" \
    "ds_read_b128 v[114:117], %[ap0] offset:26624\n" \
    "v_mfma_f32_16x16x32_bf16 v[70:73], v[110:113], v[146:149], v[70:73]\n" \
    "v_mfma_f32_16x16x32_bf16 v[90:93], v[150:153], v[110:113], v[90:93]\n" \
    "v_mfma_f32_16x16x32_bf16 v[30:33], v[138:141], v[110:113], v[30:33]\n" \
    "ds_read_b128 v[110:113], %[ap0] offset:24576\n" \
    "v_mfma_f32_16x16x32_bf16 v[54:57], v[142:145], v[118:121], v[54:57]\n" \
    "s_barrier\n" \
    "v_mfma_f32_16x16x32_bf16 v[74:77], v[118:121], v[146:149], v[74:77]\n" \
    "v_mfma_f32_16x16x32_bf16 v[94:97], v[150:153], v[118:121], v[94:97]\n" \
    "v_mfma_f32_16x16x32_bf16 v[34:37], v[138:141], v[118:121], v[34:37]\n" \
    "s_waitcnt lgkmcnt(10)\n" \
    "v_mfma_f32_16x16x32_bf16 v[38:41], v[126:129], v[98:101], v[38:41]\n" \
    "ds_read_b64_tr_b16 v[142:143], v155 offset:32768\n" \
    "s_waitcnt lgkmcnt(9)\n" \
    "v_mfma_f32_16x16x32_bf16 v[58:61], v[98:101], v[130:133], v[58:61]\n" \
    "ds_read_b64_tr_b16 v[144:145], v155 offset:34816\n" \
    "s_waitcnt lgkmcnt(7)\n" \
    "v_mfma_f32_16x16x32_bf16 v[78:81], v[134:137], v[98:101], v[78:81]\n" \
    "ds_read_b128 v[98:101], %[ap1] offset:18432\n" \
    "v_mfma_f32_16x16x32_bf16 v[42:45], v[126:129], v[102:105], v[42:45]\n" \
    "ds_read_b64_tr_b16 v[146:147], v156 offset:32768\n" \
    "v_mfma_f32_16x16x32_bf16 v[62:65], v[102:105], v[130:133], v[62:65]\n" \
    "ds_read_b64_tr_b16 v[148:149], v156 offset:34816\n" \
    "v_mfma_f32_16x16x32_bf16 v[82:85], v[134:137], v[102:105], v[82:85]\n" \
    "ds_read_b64_tr_b16 v[150:151], v157 offset:32768\n" \
    "s_waitcnt lgkmcnt(9)\n" \
    "v_mfma_f32_16x16x32_bf16 v[22:25], v[122:125], v[102:105], v[22:25]\n" \
    "ds_read_b128 v[102:105], %[ap1] offset:20480\n" \
    "s_waitcnt lgkmcnt(9)\n" \
    "v_mfma_f32_16x16x32_bf16 v[46:49], v[126:129], v[106:109], v[46:49]\n" \
    "ds_read_b64_tr_b16 v[152:153], v157 offset:34816\n" \
    "v_mfma_f32_16x16x32_bf16 v[66:69], v[106:109], v[130:133], v[66:69]\n" \
    "ds_read_b64_tr_b16 v[138:139], v154 offset:32768\n" \
    "v_mfma_f32_16x16x32_bf16 v[86:89], v[134:137], v[106:109], v[86:89]\n" \
    "ds_read_b64_tr_b16 v[140:141], v154 offset:34816\n" \
    "v_mfma_f32_16x16x32_bf16 v[26:29], v[122:125], v[106:109], v[26:29]\n" \
    "ds_read_b128 v[106:109], %[ap1] offset:22528\n" \
    "s_waitcnt lgkmcnt(11)\n" \
    "v_mfma_f32_16x16x32_bf16 v[50:53], v[126:129], v[110:113], v[50:53]\n" \
    "ds_read_b128 v[118:121], %[ap1] offset:26624\n" \
    "v_mfma_f32_16x16x32_bf16 v[70:73], v[110:113], v[130:133], v[70:73]\n" \
    "v_mfma_f32_16x16x32_bf16 v[90:93], v[134:137], v[110:113], v[90:93]\n" \
    "v_mfma_f32_16x16x32_bf16 v[30:33], v[122:125], v[110:113], v[30:33]\n" \
    "ds_read_b128 v[110:113], %[ap1] offset:24576\n" \
    "v_mfma_f32_16x16x32_bf16 v[54:57], v[126:129], v[114:117], v[54:57]\n" \
    "s_barrier\n" \
    "v_mfma_f32_16x16x32_bf16 v[74:77], v[114:117], v[130:133], v[74:77]\n" \
    "v_mfma_f32_16x16x32_bf16 v[94:97], v[134:137], v[114:117], v[94:97]\n" \
    "v_mfma_f32_16x16x32_bf16 v[34:37], v[122:125], v[114:117], v[34:37]\n" \
    "s_waitcnt lgkmcnt(10)\n" \
    "v_mfma_f32_16x16x32_bf16 v[38:41], v[142:145], v[98:101], v[38:41]\n" \
    "ds_read_b64_tr_b16 v[126:127], %[wp1] offset:0\n" \
    "s_waitcnt lgkmcnt(9)\n" \
    "v_mfma_f32_16x16x32_bf16 v[58:61], v[98:101], v[146:149], v[58:61]\n" \
    "ds_read_b64_tr_b16 v[128:129], %[wp1] offset:2048\n" \
    "s_waitcnt lgkmcnt(7)\n" \
    "v_mfma_f32_16x16x32_bf16 v[78:81], v[150:153], v[98:101], v[78:81]\n" \
    "ds_read_b128 v[98:101], %[ap0] offset:53248\n" \
    "v_mfma_f32_16x16x32_bf16 v[42:45], v[142:145], v[102:105], v[42:45]\n" \
    "ds_read_b64_tr_b16 v[130:131], %[wp2] offset:0\n" \
    "v_mfma_f32_16x16x32_bf16 v[62:65], v[102:105], v[146:149], v[62:65]\n" \
    "ds_read_b64_tr_b16 v[132:133], %[wp2] offset:2048\n" \
    "v_mfma_f32_16x16x32_bf16 v[82:85], v[150:153], v[102:105], v[82:85]\n" \
    "ds_read_b64_tr_b16 v[134:135], %[wp3] offset:0\n" \
    "s_waitcnt lgkmcnt(9)\n" \
    "v_mfma_f32_16x16x32_bf16 v[22:25], v[138:141], v[102:105], v[22:25]\n" \
    "ds_read_b128 v[102:105], %[ap0] offset:55296\n" \
    "s_waitcnt lgkmcnt(9)\n" \
    "v_mfma_f32_16x16x32_bf16 v[46:49], v[142:145], v[106:109], v[46:49]\n" \
    "ds_read_b64_tr_b16 v[136:137], %[wp3] offset:2048\n" \
    "v_mfma_f32_16x16x32_bf16 v[66:69], v[106:109], v[146:149], v[66:69]\n" \
    "ds_read_b64_tr_b16 v[122:123], %[wp0] offset:0\n" \
    "v_mfma_f32_16x16x32_bf16 v[86:89], v[150:153], v[106:109], v[86:89]\n" \
    "ds_read_b64_tr_b16 v[124:125], %[wp0] offset:2048\n" \
    "v_mfma_f32_16x16x32_bf16 v[26:29], v[138:141], v[106:109], v[26:29]\n" \
    "ds_read_b128 v[106:109], %[ap0] offset:57344\n" \
    "s_waitcnt lgkmcnt(11)\n" \
    "v_mfma_f32_16x16x32_bf16 v[50:53], v[142:145], v[110:113], v[50:53]\n" \
    "ds_read_b128 v[114:117], %[ap0] offset:61440\n" \
    "v_mfma_f32_16x16x32_bf16 v[70:73], v[110:113], v[146:149], v[70:73]\n" \
    "v_mfma_f32_16x16x32_bf16 v[90:93], v[150:153], v[110:113], v[90:93]\n" \
    "v_mfma_f32_16x16x32_bf16 v[30:33], v[138:141], v[110:113], v[30:33]\n" \
    "ds_read_b128 v[110:113], %[ap0] offset:59392\n" \
    "v_mfma_f32_16x16x32_bf16 v[54:57], v[142:145], v[118:121], v[54:57]\n" \
    "s_barrier\n" \
    "v_mfma_f32_16x16x32_bf16 v[74:77], v[118:121], v[146:149], v[74:77]\n" \
    "v_mfma_f32_16x16x32_bf16 v[94:97], v[150:153], v[118:121], v[94:97]\n" \
    "v_mfma_f32_16x16x32_bf16 v[34:37], v[138:141], v[118:121], v[34:37]\n" \
    "s_waitcnt lgkmcnt(10)\n" \
    "v_mfma_f32_16x16x32_bf16 v[38:41], v[126:129], v[98:101], v[38:41]\n" \
    "ds_read_b64_tr_b16 v[142:143], %[wp1] offset:34816\n" \
    "s_waitcnt lgkmcnt(9)\n" \
    "v_mfma_f32_16x16x32_bf16 v[58:61], v[98:101], v[130:133], v[58:61]\n" \
    "ds_read_b64_tr_b16 v[144:145], %[wp1] offset:36864\n" \
    "s_waitcnt lgkmcnt(7)\n" \
    "v_mfma_f32_16x16x32_bf16 v[78:81], v[134:137], v[98:101], v[78:81]\n" \
    "ds_read_b128 v[98:101], %[ap1] offset:53248\n" \
    "v_mfma_f32_16x16x32_bf16 v[42:45], v[126:129], v[102:105], v[42:45]\n" \
    "ds_read_b64_tr_b16 v[146:147], %[wp2] offset:34816\n" \
    "v_mfma_f32_16x16x32_bf16 v[62:65], v[102:105], v[130:133], v[62:65]\n" \
    "ds_read_b64_tr_b16 v[148:149], %[wp2] offset:36864\n" \
    "v_mfma_f32_16x16x32_bf16 v[82:85], v[134:137], v[102:105], v[82:85]\n" \
    "ds_read_b64_tr_b16 v[150:151], %[wp3] offset:34816\n" \
    "s_waitcnt lgkmcnt(9)\n" \
    "v_mfma_f32_16x16x32_bf16 v[22:25], v[122:125], v[102:105], v[22:25]\n" \
    "ds_read_b128 v[102:105], %[ap1] offset:55296\n" \
    "s_waitcnt lgkmcnt(9)\n" \
    "v_mfma_f32_16x16x32_bf16 v[46:49], v[126:129], v[106:109], v[46:49]\n" \
    "ds_read_b64_tr_b16 v[152:153], %[wp3] offset:36864\n" \
    "v_mfma_f32_16x16x32_bf16 v[66:69], v[106:109], v[130:133], v[66:69]\n" \
    "ds_read_b64_tr_b16 v[138:139], %[wp0] offset:34816\n" \
    "v_mfma_f32_16x16x32_bf16 v[86:89], v[134:137], v[106:109], v[86:89]\n" \
    "ds_read_b64_tr_b16 v[140:141], %[wp0] offset:36864\n" \
    "v_mfma_f32_16x16x32_bf16 v[26:29], v[122:125], v[106:109], v[26:29]\n" \
    "ds_read_b128 v[106:109], %[ap1] offset:57344\n" \
    "s_waitcnt lgkmcnt(11)\n" \
    "v_mfma_f32_16x16x32_bf16 v[50:53], v[126:129], v[110:113], v[50:53]\n" \
    "ds_read_b128 v[118:121], %[ap1] offset:61440\n" \
    "v_mfma_f32_16x16x32_bf16 v[70:73], v[110:113], v[130:133], v[70:73]\n" \
    "v_mfma_f32_16x16x32_bf16 v[90:93], v[134:137], v[110:113], v[90:93]\n" \
    "v_mfma_f32_16x16x32_bf16 v[30:33], v[122:125], v[110:113], v[30:33]\n" \
    "ds_read_b128 v[110:113], %[ap1] offset:59392\n" \
    "v_mfma_f32_16x16x32_bf16 v[54:57], v[126:129], v[114:117], v[54:57]\n" \
    "s_barrier\n" \
    "v_mfma_f32_16x16x32_bf16 v[74:77], v[114:117], v[130:133], v[74:77]\n" \
    "v_mfma_f32_16x16x32_bf16 v[94:97], v[134:137], v[114:117], v[94:97]\n" \
    "v_mfma_f32_16x16x32_bf16 v[34:37], v[122:125], v[114:117], v[34:37]\n" \
    "s_waitcnt lgkmcnt(10)\n" \
    "v_mfma_f32_16x16x32_bf16 v[38:41], v[142:145], v[98:101], v[38:41]\n" \
    "ds_read_b64_tr_b16 v[126:127], %[wp1] offset:51200\n" \
    "s_waitcnt lgkmcnt(9)\n" \
    "v_mfma_f32_16x16x32_bf16 v[58:61], v[98:101], v[146:149], v[58:61]\n" \
    "ds_read_b64_tr_b16 v[128:129], %[wp1] offset:53248\n" \
    "s_waitcnt lgkmcnt(7)\n" \
    "v_mfma_f32_16x16x32_bf16 v[78:81], v[150:153], v[98:101], v[78:81]\n" \
    "ds_read_b128 v[98:101], %[ap0] offset:0\n" \
    "v_mfma_f32_16x16x32_bf16 v[42:45], v[142:145], v[102:105], v[42:45]\n" \
    "ds_read_b64_tr_b16 v[130:131], %[wp2] offset:51200\n" \
    "v_mfma_f32_16x16x32_bf16 v[62:65], v[102:105], v[146:149], v[62:65]\n" \
    "ds_read_b64_tr_b16 v[132:133], %[wp2] offset:53248\n" \
    "v_mfma_f32_16x16x32_bf16 v[82:85], v[150:153], v[102:105], v[82:85]\n" \
    "ds_read_b64_tr_b16 v[134:135], %[wp3] offset:51200\n" \
    "s_waitcnt lgkmcnt(9)\n" \
    "v_mfma_f32_16x16x32_bf16 v[22:25], v[138:141], v[102:105], v[22:25]\n" \
    "ds_read_b128 v[102:105], %[ap0] offset:2048\n" \
    "s_waitcnt lgkmcnt(9)\n" \
    "v_mfma_f32_16x16x32_bf16 v[46:49], v[142:145], v[106:109], v[46:49]\n" \
    "ds_read_b64_tr_b16 v[136:137], %[wp3] offset:53248\n" \
    "v_mfma_f32_16x16x32_bf16 v[66:69], v[106:109], v[146:149], v[66:69]\n" \
    "ds_read_b64_tr_b16 v[122:123], %[wp0] offset:51200\n" \
    "v_mfma_f32_16x16x32_bf16 v[86:89], v[150:153], v[106:109], v[86:89]\n" \
    "ds_read_b64_tr_b16 v[124:125], %[wp0] offset:53248\n" \
    "v_mfma_f32_16x16x32_bf16 v[26:29], v[138:141], v[106:109], v[26:29]\n" \
    "ds_read_b128 v[106:109], %[ap0] offset:4096\n" \
    "s_waitcnt lgkmcnt(11)\n" \
    "v_mfma_f32_16x16x32_bf16 v[50:53], v[142:145], v[110:113], v[50:53]\n" \
    "ds_read_b128 v[114:117], %[ap0] offset:8192\n" \
    "v_mfma_f32_16x16x32_bf16 v[70:73], v[110:113], v[146:149], v[70:73]\n" \
    "v_mfma_f32_16x16x32_bf16 v[90:93], v[150:153], v[110:113], v[90:93]\n" \
    "v_mfma_f32_16x16x32_bf16 v[30:33], v[138:141], v[110:113], v[30:33]\n" \
    "ds_read_b128 v[110:113], %[ap0] offset:6144\n" \
    "v_mfma_f32_16x16x32_bf16 v[54:57], v[142:145], v[118:121], v[54:57]\n" \
    "s_barrier\n" \
    "v_mfma_f32_16x16x32_bf16 v[74:77], v[118:121], v[146:149], v[74:77]\n" \
    "v_mfma_f32_16x16x32_bf16 v[94:97], v[150:153], v[118:121], v[94:97]\n" \
    "v_mfma_f32_16x16x32_bf16 v[34:37], v[138:141], v[118:121], v[34:37]\n" \
    "s_waitcnt lgkmcnt(10)\n" \
    "v_mfma_f32_16x16x32_bf16 v[38:41], v[126:129], v[98:101], v[38:41]\n" \
    "ds_read_b64_tr_b16 v[142:143], v155 offset:0\n" \
    "s_waitcnt lgkmcnt(9)\n" \
    "v_mfma_f32_16x16x32_bf16 v[58:61], v[98:101], v[130:133], v[58:61]\n" \
    "ds_read_b64_tr_b16 v[144:145], v155 offset:2048\n" \
    "s_waitcnt lgkmcnt(7)\n" \
    "v_mfma_f32_16x16x32_bf16 v[78:81], v[134:137], v[98:101], v[78:81]\n" \
    "ds_read_b128 v[98:101], %[ap1] offset:0\n" \
    "v_mfma_f32_16x16x32_bf16 v[42:45], v[126:129], v[102:105], v[42:45]\n" \
    "ds_read_b64_tr_b16 v[146:147], v156 offset:0\n" \
    "v_mfma_f32_16x16x32_bf16 v[62:65], v[102:105], v[130:133], v[62:65]\n" \
    "ds_read_b64_tr_b16 v[148:149], v156 offset:2048\n" \
    "v_mfma_f32_16x16x32_bf16 v[82:85], v[134:137], v[102:105], v[82:85]\n" \
    "ds_read_b64_tr_b16 v[150:151], v157 offset:0\n" \
    "s_waitcnt lgkmcnt(9)\n" \
    "v_mfma_f32_16x16x32_bf16 v[22:25], v[122:125], v[102:105], v[22:25]\n" \
    "ds_read_b128 v[102:105], %[ap1] offset:2048\n" \
    "s_waitcnt lgkmcnt(9)\n" \
    "v_mfma_f32_16x16x32_bf16 v[46:49], v[126:129], v[106:109], v[46:49]\n" \
    "ds_read_b64_tr_b16 v[152:153], v157 offset:2048\n" \
    "v_mfma_f32_16x16x32_bf16 v[66:69], v[106:109], v[130:133], v[66:69]\n" \
    "ds_read_b64_tr_b16 v[138:139], v154 offset:0\n" \
    "v_mfma_f32_16x16x32_bf16 v[86:89], v[134:137], v[106:109], v[86:89]\n" \
    "ds_read_b64_tr_b16 v[140:141], v154 offset:2048\n" \
    "v_mfma_f32_16x16x32_bf16 v[26:29], v[122:125], v[106:109], v[26:29]\n" \
    "ds_read_b128 v[106:109], %[ap1] offset:4096\n" \
    "s_waitcnt lgkmcnt(11)\n" \
    "v_mfma_f32_16x16x32_bf16 v[50:53], v[126:129], v[110:113], v[50:53]\n" \
    "ds_read_b128 v[118:121], %[ap1] offset:8192\n" \
    "v_mfma_f32_16x16x32_bf16 v[70:73], v[110:113], v[130:133], v[70:73]\n" \
    "v_mfma_f32_16x16x32_bf16 v[90:93], v[134:137], v[110:113], v[90:93]\n" \
    "v_mfma_f32_16x16x32_bf16 v[30:33], v[122:125], v[110:113], v[30:33]\n" \
    "ds_read_b128 v[110:113], %[ap1] offset:6144\n" \
    "v_mfma_f32_16x16x32_bf16 v[54:57], v[126:129], v[114:117], v[54:57]\n" \
    "s_barrier\n" \
    "v_mfma_f32_16x16x32_bf16 v[74:77], v[114:117], v[130:133], v[74:77]\n" \
    "v_mfma_f32_16x16x32_bf16 v[94:97], v[134:137], v[114:117], v[94:97]\n" \
    "v_mfma_f32_16x16x32_bf16 v[34:37], v[122:125], v[114:117], v[34:37]\n" \
    "s_waitcnt lgkmcnt(10)\n" \
    "v_mfma_f32_16x16x32_bf16 v[38:41], v[142:145], v[98:101], v[38:41]\n" \
    "ds_read_b64_tr_b16 v[126:127], v155 offset:16384\n" \
    "s_waitcnt lgkmcnt(9)\n" \
    "v_mfma_f32_16x16x32_bf16 v[58:61], v[98:101], v[146:149], v[58:61]\n" \
    "ds_read_b64_tr_b16 v[128:129], v155 offset:18432\n" \
    "s_waitcnt lgkmcnt(7)\n" \
    "v_mfma_f32_16x16x32_bf16 v[78:81], v[150:153], v[98:101], v[78:81]\n" \
    "ds_read_b128 v[98:101], %[ap0] offset:18432\n" \
    "v_mfma_f32_16x16x32_bf16 v[42:45], v[142:145], v[102:105], v[42:45]\n" \
    "ds_read_b64_tr_b16 v[130:131], v156 offset:16384\n" \
    "v_mfma_f32_16x16x32_bf16 v[62:65], v[102:105], v[146:149], v[62:65]\n" \
    "ds_read_b64_tr_b16 v[132:133], v156 offset:18432\n" \
    "v_mfma_f32_16x16x32_bf16 v[82:85], v[150:153], v[102:105], v[82:85]\n" \
    "ds_read_b64_tr_b16 v[134:135], v157 offset:16384\n" \
    "s_waitcnt lgkmcnt(9)\n" \
    "v_mfma_f32_16x16x32_bf16 v[22:25], v[138:141], v[102:105], v[22:25]\n" \
    "ds_read_b128 v[102:105], %[ap0] offset:20480\n" \
    "s_waitcnt lgkmcnt(9)\n" \
    "v_mfma_f32_16x16x32_bf16 v[46:49], v[142:145], v[106:109], v[46:49]\n" \
    "ds_read_b64_tr_b16 v[136:137], v157 offset:18432\n" \
    "v_mfma_f32_16x16x32_bf16 v[66:69], v[106:109], v[146:149], v[66:69]\n" \
    "ds_read_b64_tr_b16 v[122:123], v154 offset:16384\n" \
    "v_mfma_f32_16x16x32_bf16 v[86:89], v[150:153], v[106:109], v[86:89]\n" \
    "ds_read_b64_tr_b16 v[124:125], v154 offset:18432\n" \
    "v_mfma_f32_16x16x32_bf16 v[26:29], v[138:141], v[106:109], v[26:29]\n" \
    "ds_read_b128 v[106:109], %[ap0] offset:22528\n" \
    "s_waitcnt lgkmcnt(11)\n" \
    "v_mfma_f32_16x16x32_bf16 v[50:53], v[142:145], v[110:113], v[50:53]\n" \
    "ds_read_b128 v[114:117], %[ap0] offset:26624\n" \
    "v_mfma_f32_16x16x32_bf16 v[70:73], v[110:113], v[146:149], v[70:73]\n" \
    "v_mfma_f32_16x16x32_bf16 v[90:93], v[150:153], v[110:113], v[90:93]\n" \
    "v_mfma_f32_16x16x32_bf16 v[30:33], v[138:141], v[110:113], v[30:33]\n" \
    "ds_read_b128 v[110:113], %[ap0] offset:24576\n" \
    "v_mfma_f32_16x16x32_bf16 v[54:57], v[142:145], v[118:121], v[54:57]\n" \
    "s_barrier\n" \
    "v_mfma_f32_16x16x32_bf16 v[74:77], v[118:121], v[146:149], v[74:77]\n" \
    "v_mfma_f32_16x16x32_bf16 v[94:97], v[150:153], v[118:121], v[94:97]\n" \
    "v_mfma_f32_16x16x32_bf16 v[34:37], v[138:141], v[118:121], v[34:37]\n" \
    "s_waitcnt lgkmcnt(10)\n" \
    "v_mfma_f32_16x16x32_bf16 v[38:41], v[126:129], v[98:101], v[38:41]\n" \
    "ds_read_b64_tr_b16 v[142:143], v155 offset:32768\n" \
    "s_waitcnt lgkmcnt(9)\n" \
    "v_mfma_f32_16x16x32_bf16 v[58:61], v[98:101], v[130:133], v[58:61]\n" \
    "ds_read_b64_tr_b16 v[144:145], v155 offset:34816\n" \
    "s_waitcnt lgkmcnt(7)\n" \
    "v_mfma_f32_16x16x32_bf16 v[78:81], v[134:137], v[98:101], v[78:81]\n" \
    "ds_read_b128 v[98:101], %[ap1] offset:18432\n" \
    "v_mfma_f32_16x16x32_bf16 v[42:45], v[126:129], v[102:105], v[42:45]\n" \
    "ds_read_b64_tr_b16 v[146:147], v156 offset:32768\n" \
    "v_mfma_f32_16x16x32_bf16 v[62:65], v[102:105], v[130:133], v[62:65]\n" \
    "ds_read_b64_tr_b16 v[148:149], v156 offset:34816\n" \
    "v_mfma_f32_16x16x32_bf16 v[82:85], v[134:137], v[102:105], v[82:85]\n" \
    "ds_read_b64_tr_b16 v[150:151], v157 offset:32768\n" \
    "s_waitcnt lgkmcnt(9)\n" \
    "v_mfma_f32_16x16x32_bf16 v[22:25], v[122:125], v[102:105], v[22:25]\n" \
    "ds_read_b128 v[102:105], %[ap1] offset:20480\n" \
    "s_waitcnt lgkmcnt(9)\n" \
    "v_mfma_f32_16x16x32_bf16 v[46:49], v[126:129], v[106:109], v[46:49]\n" \
    "ds_read_b64_tr_b16 v[152:153], v157 offset:34816\n" \
    "v_mfma_f32_16x16x32_bf16 v[66:69], v[106:109], v[130:133], v[66:69]\n" \
    "ds_read_b64_tr_b16 v[138:139], v154 offset:32768\n" \
    "v_mfma_f32_16x16x32_bf16 v[86:89], v[134:137], v[106:109], v[86:89]\n" \
    "ds_read_b64_tr_b16 v[140:141], v154 offset:34816\n" \
    "v_mfma_f32_16x16x32_bf16 v[26:29], v[122:125], v[106:109], v[26:29]\n" \
    "ds_read_b128 v[106:109], %[ap1] offset:22528\n" \
    "s_waitcnt lgkmcnt(11)\n" \
    "v_mfma_f32_16x16x32_bf16 v[50:53], v[126:129], v[110:113], v[50:53]\n" \
    "ds_read_b128 v[118:121], %[ap1] offset:26624\n" \
    "v_mfma_f32_16x16x32_bf16 v[70:73], v[110:113], v[130:133], v[70:73]\n" \
    "v_mfma_f32_16x16x32_bf16 v[90:93], v[134:137], v[110:113], v[90:93]\n" \
    "v_mfma_f32_16x16x32_bf16 v[30:33], v[122:125], v[110:113], v[30:33]\n" \
    "ds_read_b128 v[110:113], %[ap1] offset:24576\n" \
    "v_mfma_f32_16x16x32_bf16 v[54:57], v[126:129], v[114:117], v[54:57]\n" \
    "s_barrier\n" \
    "v_mfma_f32_16x16x32_bf16 v[74:77], v[114:117], v[130:133], v[74:77]\n" \
    "v_mfma_f32_16x16x32_bf16 v[94:97], v[134:137], v[114:117], v[94:97]\n" \
    "v_mfma_f32_16x16x32_bf16 v[34:37], v[122:125], v[114:117], v[34:37]\n" \
    "s_waitcnt lgkmcnt(10)\n" \
    "v_mfma_f32_16x16x32_bf16 v[38:41], v[142:145], v[98:101], v[38:41]\n" \
    "ds_read_b64_tr_b16 v[126:127], %[wp1] offset:0\n" \
    "s_waitcnt lgkmcnt(9)\n" \
    "v_mfma_f32_16x16x32_bf16 v[58:61], v[98:101], v[146:149], v[58:61]\n" \
    "ds_read_b64_tr_b16 v[128:129], %[wp1] offset:2048\n" \
    "s_waitcnt lgkmcnt(7)\n" \
    "v_mfma_f32_16x16x32_bf16 v[78:81], v[150:153], v[98:101], v[78:81]\n" \
    "ds_read_b128 v[98:101], %[ap0] offset:53248\n" \
    "v_mfma_f32_16x16x32_bf16 v[42:45], v[142:145], v[102:105], v[42:45]\n" \
    "ds_read_b64_tr_b16 v[130:131], %[wp2] offset:0\n" \
    "v_mfma_f32_16x16x32_bf16 v[62:65], v[102:105], v[146:149], v[62:65]\n" \
    "ds_read_b64_tr_b16 v[132:133], %[wp2] offset:2048\n" \
    "v_mfma_f32_16x16x32_bf16 v[82:85], v[150:153], v[102:105], v[82:85]\n" \
    "ds_read_b64_tr_b16 v[134:135], %[wp3] offset:0\n" \
    "s_waitcnt lgkmcnt(9)\n" \
    "v_mfma_f32_16x16x32_bf16 v[22:25], v[138:141], v[102:105], v[22:25]\n" \
    "ds_read_b128 v[102:105], %[ap0] offset:55296\n" \
    "s_waitcnt lgkmcnt(9)\n" \
    "v_mfma_f32_16x16x32_bf16 v[46:49], v[142:145], v[106:109], v[46:49]\n" \
    "ds_read_b64_tr_b16 v[136:137], %[wp3] offset:2048\n" \
    "v_mfma_f32_16x16x32_bf16 v[66:69], v[106:109], v[146:149], v[66:69]\n" \
    "ds_read_b64_tr_b16 v[122:123], %[wp0] offset:0\n" \
    "v_mfma_f32_16x16x32_bf16 v[86:89], v[150:153], v[106:109], v[86:89]\n" \
    "ds_read_b64_tr_b16 v[124:125], %[wp0] offset:2048\n" \
    "v_mfma_f32_16x16x32_bf16 v[26:29], v[138:141], v[106:109], v[26:29]\n" \
    "ds_read_b128 v[106:109], %[ap0] offset:57344\n" \
    "s_waitcnt lgkmcnt(11)\n" \
    "v_mfma_f32_16x16x32_bf16 v[50:53], v[142:145], v[110:113], v[50:53]\n" \
    "ds_read_b128 v[114:117], %[ap0] offset:61440\n" \
    "v_mfma_f32_16x16x32_bf16 v[70:73], v[110:113], v[146:149], v[70:73]\n" \
    "v_mfma_f32_16x16x32_bf16 v[90:93], v[150:153], v[110:113], v[90:93]\n" \
    "v_mfma_f32_16x16x32_bf16 v[30:33], v[138:141], v[110:113], v[30:33]\n" \
    "ds_read_b128 v[110:113], %[ap0] offset:59392\n" \
    "v_mfma_f32_16x16x32_bf16 v[54:57], v[142:145], v[118:121], v[54:57]\n" \
    "s_barrier\n" \
    "v_mfma_f32_16x16x32_bf16 v[74:77], v[118:121], v[146:149], v[74:77]\n" \
    "v_mfma_f32_16x16x32_bf16 v[94:97], v[150:153], v[118:121], v[94:97]\n" \
    "v_mfma_f32_16x16x32_bf16 v[34:37], v[138:141], v[118:121], v[34:37]\n" \
    "s_waitcnt lgkmcnt(10)\n" \
    "v_mfma_f32_16x16x32_bf16 v[38:41], v[126:129], v[98:101], v[38:41]\n" \
    "ds_read_b64_tr_b16 v[142:143], %[wp1] offset:34816\n" \
    "s_waitcnt lgkmcnt(9)\n" \
    "v_mfma_f32_16x16x32_bf16 v[58:61], v[98:101], v[130:133], v[58:61]\n" \
    "ds_read_b64_tr_b16 v[144:145], %[wp1] offset:36864\n" \
    "s_waitcnt lgkmcnt(7)\n" \
    "v_mfma_f32_16x16x32_bf16 v[78:81], v[134:137], v[98:101], v[78:81]\n" \
    "ds_read_b128 v[98:101], %[ap1] offset:53248\n" \
    "v_mfma_f32_16x16x32_bf16 v[42:45], v[126:129], v[102:105], v[42:45]\n" \
    "ds_read_b64_tr_b16 v[146:147], %[wp2] offset:34816\n" \
    "v_mfma_f32_16x16x32_bf16 v[62:65], v[102:105], v[130:133], v[62:65]\n" \
    "ds_read_b64_tr_b16 v[148:149], %[wp2] offset:36864\n" \
    "v_mfma_f32_16x16x32_bf16 v[82:85], v[134:137], v[102:105], v[82:85]\n" \
    "ds_read_b64_tr_b16 v[150:151], %[wp3] offset:34816\n" \
    "s_waitcnt lgkmcnt(9)\n" \
    "v_mfma_f32_16x16x32_bf16 v[22:25], v[122:125], v[102:105], v[22:25]\n" \
    "ds_read_b128 v[102:105], %[ap1] offset:55296\n" \
    "s_waitcnt lgkmcnt(9)\n" \
    "v_mfma_f32_16x16x32_bf16 v[46:49], v[126:129], v[106:109], v[46:49]\n" \
    "ds_read_b64_tr_b16 v[152:153], %[wp3] offset:36864\n" \
    "v_mfma_f32_16x16x32_bf16 v[66:69], v[106:109], v[130:133], v[66:69]\n" \
    "ds_read_b64_tr_b16 v[138:139], %[wp0] offset:34816\n" \
    "v_mfma_f32_16x16x32_bf16 v[86:89], v[134:137], v[106:109], v[86:89]\n" \
    "ds_read_b64_tr_b16 v[140:141], %[wp0] offset:36864\n" \
    "v_mfma_f32_16x16x32_bf16 v[26:29], v[122:125], v[106:109], v[26:29]\n" \
    "ds_read_b128 v[106:109], %[ap1] offset:57344\n" \
    "s_waitcnt lgkmcnt(11)\n" \
    "v_mfma_f32_16x16x32_bf16 v[50:53], v[126:129], v[110:113], v[50:53]\n" \
    "ds_read_b128 v[118:121], %[ap1] offset:61440\n" \
    "v_mfma_f32_16x16x32_bf16 v[70:73], v[110:113], v[130:133], v[70:73]\n" \
    "v_mfma_f32_16x16x32_bf16 v[90:93], v[134:137], v[110:113], v[90:93]\n" \
    "v_mfma_f32_16x16x32_bf16 v[30:33], v[122:125], v[110:113], v[30:33]\n" \
    "ds_read_b128 v[110:113], %[ap1] offset:59392\n" \
    "v_mfma_f32_16x16x32_bf16 v[54:57], v[126:129], v[114:117], v[54:57]\n" \
    "s_barrier\n" \
    "v_mfma_f32_16x16x32_bf16 v[74:77], v[114:117], v[130:133], v[74:77]\n" \
    "v_mfma_f32_16x16x32_bf16 v[94:97], v[134:137], v[114:117], v[94:97]\n" \
    "v_mfma_f32_16x16x32_bf16 v[34:37], v[122:125], v[114:117], v[34:37]\n" \
    "s_waitcnt lgkmcnt(10)\n" \
    "v_mfma_f32_16x16x32_bf16 v[38:41], v[142:145], v[98:101], v[38:41]\n" \
    "ds_read_b64_tr_b16 v[126:127], %[wp1] offset:51200\n" \
    "s_waitcnt lgkmcnt(9)\n" \
    "v_mfma_f32_16x16x32_bf16 v[58:61], v[98:101], v[146:149], v[58:61]\n" \
    "ds_read_b64_tr_b16 v[128:129], %[wp1] offset:53248\n" \
    "s_waitcnt lgkmcnt(7)\n" \
    "v_mfma_f32_16x16x32_bf16 v[78:81], v[150:153], v[98:101], v[78:81]\n" \
    "ds_read_b128 v[98:101], %[ap0] offset:0\n" \
    "v_mfma_f32_16x16x32_bf16 v[42:45], v[142:145], v[102:105], v[42:45]\n" \
    "ds_read_b64_tr_b16 v[130:131], %[wp2] offset:51200\n" \
    "v_mfma_f32_16x16x32_bf16 v[62:65], v[102:105], v[146:149], v[62:65]\n" \
    "ds_read_b64_tr_b16 v[132:133], %[wp2] offset:53248\n" \
    "v_mfma_f32_16x16x32_bf16 v[82:85], v[150:153], v[102:105], v[82:85]\n" \
    "ds_read_b64_tr_b16 v[134:135], %[wp3] offset:51200\n" \
    "s_waitcnt lgkmcnt(9)\n" \
    "v_mfma_f32_16x16x32_bf16 v[22:25], v[138:141], v[102:105], v[22:25]\n" \
    "ds_read_b128 v[102:105], %[ap0] offset:2048\n" \
    "s_waitcnt lgkmcnt(9)\n" \
    "v_mfma_f32_16x16x32_bf16 v[46:49], v[142:145], v[106:109], v[46:49]\n" \
    "ds_read_b64_tr_b16 v[136:137], %[wp3] offset:53248\n" \
    "v_mfma_f32_16x16x32_bf16 v[66:69], v[106:109], v[146:149], v[66:69]\n" \
    "ds_read_b64_tr_b16 v[122:123], %[wp0] offset:51200\n" \
    "v_mfma_f32_16x16x32_bf16 v[86:89], v[150:153], v[106:109], v[86:89]\n" \
    "ds_read_b64_tr_b16 v[124:125], %[wp0] offset:53248\n" \
    "v_mfma_f32_16x16x32_bf16 v[26:29], v[138:141], v[106:109], v[26:29]\n" \
    "ds_read_b128 v[106:109], %[ap0] offset:4096\n" \
    "s_waitcnt lgkmcnt(11)\n" \
    "v_mfma_f32_16x16x32_bf16 v[50:53], v[142:145], v[110:113], v[50:53]\n" \
    "ds_read_b128 v[114:117], %[ap0] offset:8192\n" \
    "v_mfma_f32_16x16x32_bf16 v[70:73], v[110:113], v[146:149], v[70:73]\n" \
    "v_mfma_f32_16x16x32_bf16 v[90:93], v[150:153], v[110:113], v[90:93]\n" \
    "v_mfma_f32_16x16x32_bf16 v[30:33], v[138:141], v[110:113], v[30:33]\n" \
    "ds_read_b128 v[110:113], %[ap0] offset:6144\n" \
    "v_mfma_f32_16x16x32_bf16 v[54:57], v[142:145], v[118:121], v[54:57]\n" \
    "s_barrier\n" \
    "v_mfma_f32_16x16x32_bf16 v[74:77], v[118:121], v[146:149], v[74:77]\n" \
    "v_mfma_f32_16x16x32_bf16 v[94:97], v[150:153], v[118:121], v[94:97]\n" \
    "v_mfma_f32_16x16x32_bf16 v[34:37], v[138:141], v[118:121], v[34:37]\n" \
    "s_waitcnt lgkmcnt(10)\n" \
    "v_mfma_f32_16x16x32_bf16 v[38:41], v[126:129], v[98:101], v[38:41]\n" \
    "ds_read_b64_tr_b16 v[142:143], v155 offset:0\n" \
    "s_waitcnt lgkmcnt(9)\n" \
    "v_mfma_f32_16x16x32_bf16 v[58:61], v[98:101], v[130:133], v[58:61]\n" \
    "ds_read_b64_tr_b16 v[144:145], v155 offset:2048\n" \
    "s_waitcnt lgkmcnt(7)\n" \
    "v_mfma_f32_16x16x32_bf16 v[78:81], v[134:137], v[98:101], v[78:81]\n" \
    "ds_read_b128 v[98:101], %[ap1] offset:0\n" \
    "v_mfma_f32_16x16x32_bf16 v[42:45], v[126:129], v[102:105], v[42:45]\n" \
    "ds_read_b64_tr_b16 v[146:147], v156 offset:0\n" \
    "v_mfma_f32_16x16x32_bf16 v[62:65], v[102:105], v[130:133], v[62:65]\n" \
    "ds_read_b64_tr_b16 v[148:149], v156 offset:2048\n" \
    "v_mfma_f32_16x16x32_bf16 v[82:85], v[134:137], v[102:105], v[82:85]\n" \
    "ds_read_b64_tr_b16 v[150:151], v157 offset:0\n" \
    "s_waitcnt lgkmcnt(9)\n" \
    "v_mfma_f32_16x16x32_bf16 v[22:25], v[122:125], v[102:105], v[22:25]\n" \
    "ds_read_b128 v[102:105], %[ap1] offset:2048\n" \
    "s_waitcnt lgkmcnt(9)\n" \
    "v_mfma_f32_16x16x32_bf16 v[46:49], v[126:129], v[106:109], v[46:49]\n" \
    "ds_read_b64_tr_b16 v[152:153], v157 offset:2048\n" \
    "v_mfma_f32_16x16x32_bf16 v[66:69], v[106:109], v[130:133], v[66:69]\n" \
    "ds_read_b64_tr_b16 v[138:139], v154 offset:0\n" \
    "v_mfma_f32_16x16x32_bf16 v[86:89], v[134:137], v[106:109], v[86:89]\n" \
    "ds_read_b64_tr_b16 v[140:141], v154 offset:2048\n" \
    "v_mfma_f32_16x16x32_bf16 v[26:29], v[122:125], v[106:109], v[26:29]\n" \
    "ds_read_b128 v[106:109], %[ap1] offset:4096\n" \
    "s_waitcnt lgkmcnt(11)\n" \
    "v_mfma_f32_16x16x32_bf16 v[50:53], v[126:129], v[110:113], v[50:53]\n" \
    "ds_read_b128 v[118:121], %[ap1] offset:8192\n" \
    "v_mfma_f32_16x16x32_bf16 v[70:73], v[110:113], v[130:133], v[70:73]\n" \
    "v_mfma_f32_16x16x32_bf16 v[90:93], v[134:137], v[110:113], v[90:93]\n" \
    "v_mfma_f32_16x16x32_bf16 v[30:33], v[122:125], v[110:113], v[30:33]\n" \
    "ds_read_b128 v[110:113], %[ap1] offset:6144\n" \
    "v_mfma_f32_16x16x32_bf16 v[54:57], v[126:129], v[114:117], v[54:57]\n" \
    "s_barrier\n" \
    "v_mfma_f32_16x16x32_bf16 v[74:77], v[114:117], v[130:133], v[74:77]\n" \
    "v_mfma_f32_16x16x32_bf16 v[94:97], v[134:137], v[114:117], v[94:97]\n" \
    "v_mfma_f32_16x16x32_bf16 v[34:37], v[122:125], v[114:117], v[34:37]\n" \
    "s_waitcnt lgkmcnt(10)\n" \
    "v_mfma_f32_16x16x32_bf16 v[38:41], v[142:145], v[98:101], v[38:41]\n" \
    "ds_read_b64_tr_b16 v[126:127], v155 offset:16384\n" \
    "s_waitcnt lgkmcnt(9)\n" \
    "v_mfma_f32_16x16x32_bf16 v[58:61], v[98:101], v[146:149], v[58:61]\n" \
    "ds_read_b64_tr_b16 v[128:129], v155 offset:18432\n" \
    "s_waitcnt lgkmcnt(7)\n" \
    "v_mfma_f32_16x16x32_bf16 v[78:81], v[150:153], v[98:101], v[78:81]\n" \
    "ds_read_b128 v[98:101], %[ap0] offset:18432\n" \
    "v_mfma_f32_16x16x32_bf16 v[42:45], v[142:145], v[102:105], v[42:45]\n" \
    "ds_read_b64_tr_b16 v[130:131], v156 offset:16384\n" \
    "v_mfma_f32_16x16x32_bf16 v[62:65], v[102:105], v[146:149], v[62:65]\n" \
    "ds_read_b64_tr_b16 v[132:133], v156 offset:18432\n" \
    "v_mfma_f32_16x16x32_bf16 v[82:85], v[150:153], v[102:105], v[82:85]\n" \
    "ds_read_b64_tr_b16 v[134:135], v157 offset:16384\n" \
    "s_waitcnt lgkmcnt(9)\n" \
    "v_mfma_f32_16x16x32_bf16 v[22:25], v[138:141], v[102:105], v[22:25]\n" \
    "ds_read_b128 v[102:105], %[ap0] offset:20480\n" \
    "s_waitcnt lgkmcnt(9)\n" \
    "v_mfma_f32_16x16x32_bf16 v[46:49], v[142:145], v[106:109], v[46:49]\n" \
    "ds_read_b64_tr_b16 v[136:137], v157 offset:18432\n" \
    "v_mfma_f32_16x16x32_bf16 v[66:69], v[106:109], v[146:149], v[66:69]\n" \
    "ds_read_b64_tr_b16 v[122:123], v154 offset:16384\n" \
    "v_mfma_f32_16x16x32_bf16 v[86:89], v[150:153], v[106:109], v[86:89]\n" \
    "ds_read_b64_tr_b16 v[124:125], v154 offset:18432\n" \
    "v_mfma_f32_16x16x32_bf16 v[26:29], v[138:141], v[106:109], v[26:29]\n" \
    "ds_read_b128 v[106:109], %[ap0] offset:22528\n" \
    "s_waitcnt lgkmcnt(11)\n" \
    "v_mfma_f32_16x16x32_bf16 v[50:53], v[142:145], v[110:113], v[50:53]\n" \
    "ds_read_b128 v[114:117], %[ap0] offset:26624\n" \
    "v_mfma_f32_16x16x32_bf16 v[70:73], v[110:113], v[146:149], v[70:73]\n" \
    "v_mfma_f32_16x16x32_bf16 v[90:93], v[150:153], v[110:113], v[90:93]\n" \
    "v_mfma_f32_16x16x32_bf16 v[30:33], v[138:141], v[110:113], v[30:33]\n" \
    "ds_read_b128 v[110:113], %[ap0] offset:24576\n" \
    "v_mfma_f32_16x16x32_bf16 v[54:57], v[142:145], v[118:121], v[54:57]\n" \
    "s_barrier\n" \
    "v_mfma_f32_16x16x32_bf16 v[74:77], v[118:121], v[146:149], v[74:77]\n" \
    "v_mfma_f32_16x16x32_bf16 v[94:97], v[150:153], v[118:121], v[94:97]\n" \
    "v_mfma_f32_16x16x32_bf16 v[34:37], v[138:141], v[118:121], v[34:37]\n" \
    "s_waitcnt lgkmcnt(10)\n" \
    "v_mfma_f32_16x16x32_bf16 v[38:41], v[126:129], v[98:101], v[38:41]\n" \
    "ds_read_b64_tr_b16 v[142:143], v155 offset:32768\n" \
    "s_waitcnt lgkmcnt(9)\n" \
    "v_mfma_f32_16x16x32_bf16 v[58:61], v[98:101], v[130:133], v[58:61]\n" \
    "ds_read_b64_tr_b16 v[144:145], v155 offset:34816\n" \
    "s_waitcnt lgkmcnt(7)\n" \
    "v_mfma_f32_16x16x32_bf16 v[78:81], v[134:137], v[98:101], v[78:81]\n" \
    "ds_read_b128 v[98:101], %[ap1] offset:18432\n" \
    "v_mfma_f32_16x16x32_bf16 v[42:45], v[126:129], v[102:105], v[42:45]\n" \
    "ds_read_b64_tr_b16 v[146:147], v156 offset:32768\n" \
    "v_mfma_f32_16x16x32_bf16 v[62:65], v[102:105], v[130:133], v[62:65]\n" \
    "ds_read_b64_tr_b16 v[148:149], v156 offset:34816\n" \
    "v_mfma_f32_16x16x32_bf16 v[82:85], v[134:137], v[102:105], v[82:85]\n" \
    "ds_read_b64_tr_b16 v[150:151], v157 offset:32768\n" \
    "s_waitcnt lgkmcnt(9)\n" \
    "v_mfma_f32_16x16x32_bf16 v[22:25], v[122:125], v[102:105], v[22:25]\n" \
    "ds_read_b128 v[102:105], %[ap1] offset:20480\n" \
    "s_waitcnt lgkmcnt(9)\n" \
    "v_mfma_f32_16x16x32_bf16 v[46:49], v[126:129], v[106:109], v[46:49]\n" \
    "ds_read_b64_tr_b16 v[152:153], v157 offset:34816\n" \
    "v_mfma_f32_16x16x32_bf16 v[66:69], v[106:109], v[130:133], v[66:69]\n" \
    "ds_read_b64_tr_b16 v[138:139], v154 offset:32768\n" \
    "v_mfma_f32_16x16x32_bf16 v[86:89], v[134:137], v[106:109], v[86:89]\n" \
    "ds_read_b64_tr_b16 v[140:141], v154 offset:34816\n" \
    "v_mfma_f32_16x16x32_bf16 v[26:29], v[122:125], v[106:109], v[26:29]\n" \
    "ds_read_b128 v[106:109], %[ap1] offset:22528\n" \
    "s_waitcnt lgkmcnt(11)\n" \
    "v_mfma_f32_16x16x32_bf16 v[50:53], v[126:129], v[110:113], v[50:53]\n" \
    "ds_read_b128 v[118:121], %[ap1] offset:26624\n" \
    "v_mfma_f32_16x16x32_bf16 v[70:73], v[110:113], v[130:133], v[70:73]\n" \
    "v_mfma_f32_16x16x32_bf16 v[90:93], v[134:137], v[110:113], v[90:93]\n" \
    "v_mfma_f32_16x16x32_bf16 v[30:33], v[122:125], v[110:113], v[30:33]\n" \
    "ds_read_b128 v[110:113], %[ap1] offset:24576\n" \
    "v_mov_b32 v161, 16\n" \
    "ds_write_b32 %[fr], v161\n" \
    "v_mfma_f32_16x16x32_bf16 v[54:57], v[126:129], v[114:117], v[54:57]\n" \
    "s_barrier\n" \
    "v_mfma_f32_16x16x32_bf16 v[74:77], v[114:117], v[130:133], v[74:77]\n" \
    "v_mfma_f32_16x16x32_bf16 v[94:97], v[134:137], v[114:117], v[94:97]\n" \
    "v_mfma_f32_16x16x32_bf16 v[34:37], v[122:125], v[114:117], v[34:37]\n" \
    "s_waitcnt lgkmcnt(11)\n" \
    "v_mfma_f32_16x16x32_bf16 v[38:41], v[142:145], v[98:101], v[38:41]\n" \
    "ds_read_b64_tr_b16 v[126:127], %[wp1] offset:0\n" \
    "s_waitcnt lgkmcnt(10)\n" \
    "v_mfma_f32_16x16x32_bf16 v[58:61], v[98:101], v[146:149], v[58:61]\n" \
    "ds_read_b64_tr_b16 v[128:129], %[wp1] offset:2048\n" \
    "s_waitcnt lgkmcnt(8)\n" \
    "v_mfma_f32_16x16x32_bf16 v[78:81], v[150:153], v[98:101], v[78:81]\n" \
    "ds_read_b128 v[98:101], %[ap0] offset:53248\n" \
    "v_mfma_f32_16x16x32_bf16 v[42:45], v[142:145], v[102:105], v[42:45]\n" \
    "ds_read_b64_tr_b16 v[130:131], %[wp2] offset:0\n" \
    "v_mfma_f32_16x16x32_bf16 v[62:65], v[102:105], v[146:149], v[62:65]\n" \
    "ds_read_b64_tr_b16 v[132:133], %[wp2] offset:2048\n" \
    "v_mfma_f32_16x16x32_bf16 v[82:85], v[150:153], v[102:105], v[82:85]\n" \
    "ds_read_b64_tr_b16 v[134:135], %[wp3] offset:0\n" \
    "s_waitcnt lgkmcnt(10)\n" \
    "v_mfma_f32_16x16x32_bf16 v[22:25], v[138:141], v[102:105], v[22:25]\n" \
    "ds_read_b128 v[102:105], %[ap0] offset:55296\n" \
    "s_waitcnt lgkmcnt(10)\n" \
    "v_mfma_f32_16x16x32_bf16 v[46:49], v[142:145], v[106:109], v[46:49]\n" \
    "ds_read_b64_tr_b16 v[136:137], %[wp3] offset:2048\n" \
    "v_mfma_f32_16x16x32_bf16 v[66:69], v[106:109], v[146:149], v[66:69]\n" \
    "ds_read_b64_tr_b16 v[122:123], %[wp0] offset:0\n" \
    "v_mfma_f32_16x16x32_bf16 v[86:89], v[150:153], v[106:109], v[86:89]\n" \
    "ds_read_b64_tr_b16 v[124:125], %[wp0] offset:2048\n" \
    "v_mfma_f32_16x16x32_bf16 v[26:29], v[138:141], v[106:109], v[26:29]\n" \
    "ds_read_b128 v[106:109], %[ap0] offset:57344\n" \
    "s_waitcnt lgkmcnt(12)\n" \
    "v_mfma_f32_16x16x32_bf16 v[50:53], v[142:145], v[110:113], v[50:53]\n" \
    "ds_read_b128 v[114:117], %[ap0] offset:61440\n" \
    "v_mfma_f32_16x16x32_bf16 v[70:73], v[110:113], v[146:149], v[70:73]\n" \
    "v_mfma_f32_16x16x32_bf16 v[90:93], v[150:153], v[110:113], v[90:93]\n" \
    "v_mfma_f32_16x16x32_bf16 v[30:33], v[138:141], v[110:113], v[30:33]\n" \
    "ds_read_b128 v[110:113], %[ap0] offset:59392\n" \
    "v_mov_b32 v161, 17\n" \
    "ds_write_b32 %[fr], v161\n" \
    "v_mfma_f32_16x16x32_bf16 v[54:57], v[142:145], v[118:121], v[54:57]\n" \
    "s_barrier\n" \
    "ds_read_b64_tr_b16 v[142:143], %[wp1] offset:34816\n" \
    "ds_read_b64_tr_b16 v[144:145], %[wp1] offset:36864\n" \
    "v_mfma_f32_16x16x32_bf16 v[74:77], v[118:121], v[146:149], v[74:77]\n" \
    "ds_read_b64_tr_b16 v[146:147], %[wp2] offset:34816\n" \
    "ds_read_b64_tr_b16 v[148:149], %[wp2] offset:36864\n" \
    "v_mfma_f32_16x16x32_bf16 v[94:97], v[150:153], v[118:121], v[94:97]\n" \
    "ds_read_b64_tr_b16 v[150:151], %[wp3] offset:34816\n" \
    "ds_read_b64_tr_b16 v[152:153], %[wp3] offset:36864\n" \
    "v_mfma_f32_16x16x32_bf16 v[34:37], v[138:141], v[118:121], v[34:37]\n" \
    "ds_read_b128 v[118:121], %[ap1] offset:61440\n" \
    "ds_read_b64_tr_b16 v[138:139], %[wp0] offset:34816\n" \
    "ds_read_b64_tr_b16 v[140:141], %[wp0] offset:36864\n" \
    "s_waitcnt lgkmcnt(11)\n" \
    "v_mfma_f32_16x16x32_bf16 v[54:57], v[126:129], v[114:117], v[54:57]\n" \
    "v_add_u32 %[wp0], %[tabc], %[l16]\n" \
    "v_cndmask_b32_e64 %[wp2], 0, -1, s[68:69]\n" \
    "v_mfma_f32_16x16x32_bf16 v[74:77], v[114:117], v[130:133], v[74:77]\n" \
    "v_add_u32 %[wp1], %[hxc], %[l16]\n" \
    "v_lshrrev_b32 %[wp3], 2, %[l16]\n" \
    "v_mfma_f32_16x16x32_bf16 v[94:97], v[134:137], v[114:117], v[94:97]\n" \
    "v_add_u32 %[wp3], %[hxd], %[wp3]\n" \
    "v_xor_b32 v158, 128, %[wp0]\n" \
    "v_mfma_f32_16x16x32_bf16 v[34:37], v[122:125], v[114:117], v[34:37]\n" \
    "s_waitcnt lgkmcnt(10)\n" \
    "v_mfma_f32_16x16x32_bf16 v[50:53], v[126:129], v[110:113], v[50:53]\n" \
    "v_mfma_f32_16x16x32_bf16 v[70:73], v[110:113], v[130:133], v[70:73]\n" \
    "v_mfma_f32_16x16x32_bf16 v[90:93], v[134:137], v[110:113], v[90:93]\n" \
    "v_mfma_f32_16x16x32_bf16 v[30:33], v[122:125], v[110:113], v[30:33]\n" \
    "ds_read_b128 v[110:113], %[ap1] offset:59392\n" \
    "s_waitcnt lgkmcnt(3)\n" \
    "v_mfma_f32_16x16x32_bf16 v[54:57], v[142:145], v[118:121], v[54:57]\n" \
    "v_cndmask_b32_e64 v154, %[zr], %[vxb], s[60:61]\n" \
    "v_cndmask_b32_e64 v155, %[zr], %[vxb], s[62:63]\n" \
    "v_mfma_f32_16x16x32_bf16 v[74:77], v[118:121], v[146:149], v[74:77]\n" \
    "v_cndmask_b32_e64 v156, %[zr], %[vxb], s[64:65]\n" \
    "v_cndmask_b32_e64 v157, %[zr], %[vxb], s[66:67]\n" \
    "v_mfma_f32_16x16x32_bf16 v[94:97], v[150:153], v[118:121], v[94:97]\n" \
    "s_waitcnt lgkmcnt(1)\n" \
    "v_mfma_f32_16x16x32_bf16 v[34:37], v[138:141], v[118:121], v[34:37]\n" \
    "v_mfma_f32_16x16x32_bf16 v[46:49], v[126:129], v[106:109], v[46:49]\n" \
    "v_mfma_f32_16x16x32_bf16 v[66:69], v[106:109], v[130:133], v[66:69]\n" \
    "v_cvt_pk_bf16_f32 v54, v54, v55\n" \
    "v_cvt_pk_bf16_f32 v55, v56, v57\n" \
    "v_mfma_f32_16x16x32_bf16 v[86:89], v[134:137], v[106:109], v[86:89]\n" \
    "v_cvt_pk_bf16_f32 v120, v74, v75\n" \
    "v_cvt_pk_bf16_f32 v121, v76, v77\n" \
    "v_mfma_f32_16x16x32_bf16 v[26:29], v[122:125], v[106:109], v[26:29]\n" \
    "ds_read_b128 v[106:109], %[ap1] offset:57344\n" \
    "v_cvt_pk_bf16_f32 v94, v94, v95\n" \
    "v_cvt_pk_bf16_f32 v56, v96, v97\n" \
    "ds_write2_b32 %[scw], v94, v56 offset0:128 offset1:129\n" \
    "s_waitcnt lgkmcnt(2)\n" \
    "v_mfma_f32_16x16x32_bf16 v[50:53], v[142:145], v[110:113], v[50:53]\n" \
    "v_permlane16_swap_b32 v94, v56\n" \
    "v_mov_b32 v161, 1\n" \
    "ds_write_b64 %[wp1], v[54:55]\n" \
    "ds_write_b64 %[wp1], v[120:121] offset:8\n" \
    "ds_write_b32 %[wp3], v56\n" \
    "ds_write_b32 %[fr], v161 offset:144\n" \
    "v_mfma_f32_16x16x32_bf16 v[70:73], v[110:113], v[146:149], v[70:73]\n" \
    "v_cvt_pk_bf16_f32 v34, v34, v35\n" \
    "v_cvt_pk_bf16_f32 v35, v36, v37\n" \
    "v_mfma_f32_16x16x32_bf16 v[90:93], v[150:153], v[110:113], v[90:93]\n" \
    "v_and_b32 v36, v94, %[wp2]\n" \
    "v_mov_b32 v37, 0\n" \
    "v_mov_b32 v57, 0\n" \
    "v_mfma_f32_16x16x32_bf16 v[30:33], v[138:141], v[110:113], v[30:33]\n" \
    "v_mfma_f32_16x16x32_bf16 v[42:45], v[126:129], v[102:105], v[42:45]\n" \
    "v_mfma_f32_16x16x32_bf16 v[62:65], v[102:105], v[130:133], v[62:65]\n" \
    "v_cvt_pk_bf16_f32 v50, v50, v51\n" \
    "v_cvt_pk_bf16_f32 v51, v52, v53\n" \
    "v_mfma_f32_16x16x32_bf16 v[82:85], v[134:137], v[102:105], v[82:85]\n" \
    "v_cvt_pk_bf16_f32 v118, v70, v71\n" \
    "v_cvt_pk_bf16_f32 v119, v72, v73\n" \
    "v_mfma_f32_16x16x32_bf16 v[22:25], v[122:125], v[102:105], v[22:25]\n" \
    "ds_read_b128 v[102:105], %[ap1] offset:55296\n" \
    "v_cvt_pk_bf16_f32 v90, v90, v91\n" \
    "v_cvt_pk_bf16_f32 v52, v92, v93\n" \
    "ds_write2_b32 %[scw], v90, v52 offset0:96 offset1:97\n" \
    "s_waitcnt lgkmcnt(7)\n" \
    "v_mfma_f32_16x16x32_bf16 v[46:49], v[142:145], v[106:109], v[46:49]\n" \
    "v_permlane16_swap_b32 v90, v52\n" \
    "v_mfma_f32_16x16x32_bf16 v[66:69], v[106:109], v[146:149], v[66:69]\n" \
    "v_cvt_pk_bf16_f32 v30, v30, v31\n" \
    "v_cvt_pk_bf16_f32 v31, v32, v33\n" \
    "v_mfma_f32_16x16x32_bf16 v[86:89], v[150:153], v[106:109], v[86:89]\n" \
    "v_and_b32_dpp v32, v90, %[wp2] row_ror:8 row_mask:0xf bank_mask:0xf\n" \
    "v_mov_b32 v33, 0\n" \
    "v_mov_b32 v53, 0\n" \
    "v_mfma_f32_16x16x32_bf16 v[26:29], v[138:141], v[106:109], v[26:29]\n" \
    "v_mov_b32_dpp v30, v30 row_ror:8 row_mask:0xf bank_mask:0xf\n" \
    "v_mov_b32_dpp v31, v31 row_ror:8 row_mask:0xf bank_mask:0xf\n" \
    "v_mfma_f32_16x16x32_bf16 v[38:41], v[126:129], v[98:101], v[38:41]\n" \
    "v_mfma_f32_16x16x32_bf16 v[58:61], v[98:101], v[130:133], v[58:61]\n" \
    "v_cvt_pk_bf16_f32 v46, v46, v47\n" \
    "v_cvt_pk_bf16_f32 v47, v48, v49\n" \
    "v_mfma_f32_16x16x32_bf16 v[78:81], v[134:137], v[98:101], v[78:81]\n" \
    "ds_read_b128 v[98:101], %[ap1] offset:53248\n" \
    "v_mov_b32 v161, 18\n" \
    "ds_write_b32 %[fr], v161\n" \
    "ds_read_b128 v[126:129], %[wp0] offset:1024\n" \
    "ds_read_b128 v[122:125], %[wp0]\n" \
    "ds_read_b128 v[134:137], v158 offset:1024\n" \
    "ds_read_b128 v[130:133], v158\n" \
    "v_cvt_pk_bf16_f32 v116, v66, v67\n" \
    "v_cvt_pk_bf16_f32 v117, v68, v69\n" \
    "s_waitcnt lgkmcnt(7)\n" \
    "v_mfma_f32_16x16x32_bf16 v[42:45], v[142:145], v[102:105], v[42:45]\n" \
    "v_cvt_pk_bf16_f32 v86, v86, v87\n" \
    "v_cvt_pk_bf16_f32 v48, v88, v89\n" \
    "ds_write2_b32 %[scw], v86, v48 offset0:64 offset1:65\n" \
    "v_mfma_f32_16x16x32_bf16 v[62:65], v[102:105], v[146:149], v[62:65]\n" \
    "v_permlane16_swap_b32 v86, v48\n" \
    "v_mfma_f32_16x16x32_bf16 v[82:85], v[150:153], v[102:105], v[82:85]\n" \
    "v_cvt_pk_bf16_f32 v26, v26, v27\n" \
    "v_cvt_pk_bf16_f32 v27, v28, v29\n" \
    "v_mfma_f32_16x16x32_bf16 v[22:25], v[138:141], v[102:105], v[22:25]\n" \
    "v_and_b32 v28, v86, %[wp2]\n" \
    "v_mov_b32 v29, 0\n" \
    "v_mov_b32 v49, 0\n" \
    "s_waitcnt lgkmcnt(6)\n" \
    "v_mfma_f32_16x16x32_bf16 v[38:41], v[142:145], v[98:101], v[38:41]\n" \
    "v_mfma_f32_16x16x32_bf16 v[58:61], v[98:101], v[146:149], v[58:61]\n" \
    "v_cvt_pk_bf16_f32 v42, v42, v43\n" \
    "v_cvt_pk_bf16_f32 v43, v44, v45\n" \
    "v_mfma_f32_16x16x32_bf16 v[78:81], v[150:153], v[98:101], v[78:81]\n" \
    "ds_read_b32 v159, %[zr] offset:1332\n" \
    "s_setprio 0\n" \
    "s_waitcnt lgkmcnt(5)\n" \
    "v_mfma_f32_16x16x32_bf16 v[74:77], v[54:57], v[34:37], v[126:129]\n" \
    "v_cvt_pk_bf16_f32 v82, v82, v83\n" \
    "v_cvt_pk_bf16_f32 v44, v84, v85\n" \
    "ds_write2_b32 %[scw], v82, v44 offset0:32 offset1:33\n" \
    "s_waitcnt lgkmcnt(5)\n" \
    "v_mfma_f32_16x16x32_bf16 v[94:97], v[50:53], v[34:37], v[122:125]\n" \
    "v_cvt_pk_bf16_f32 v114, v62, v63\n" \
    "v_cvt_pk_bf16_f32 v115, v64, v65\n" \
    "v_permlane16_swap_b32 v82, v44\n" \
    "s_waitcnt lgkmcnt(4)\n" \
    "v_mfma_f32_16x16x32_bf16 v[70:73], v[50:53], v[30:33], v[134:137]\n" \
    "v_cvt_pk_bf16_f32 v22, v22, v23\n" \
    "v_cvt_pk_bf16_f32 v23, v24, v25\n" \
    "v_cvt_pk_bf16_f32 v38, v38, v39\n" \
    "s_waitcnt lgkmcnt(3)\n" \
    "v_mfma_f32_16x16x32_bf16 v[90:93], v[46:49], v[30:33], v[130:133]\n" \
    "v_cvt_pk_bf16_f32 v39, v40, v41\n" \
    "v_and_b32_dpp v24, v82, %[wp2] row_ror:8 row_mask:0xf bank_mask:0xf\n" \
    "v_mov_b32_dpp v22, v22 row_ror:8 row_mask:0xf bank_mask:0xf\n" \
    "v_mov_b32_dpp v23, v23 row_ror:8 row_mask:0xf bank_mask:0xf\n" \
    "v_mov_b32 v25, 0\n" \
    "v_mov_b32 v45, 0\n" \
    "v_mfma_f32_16x16x32_bf16 v[66:69], v[46:49], v[26:29], v[126:129]\n" \
    "v_cvt_pk_bf16_f32 v78, v78, v79\n" \
    "v_cvt_pk_bf16_f32 v40, v80, v81\n" \
    "ds_write2_b32 %[scw], v78, v40 offset0:0 offset1:1\n" \
    "v_mov_b32 v41, 0\n" \
    "v_mfma_f32_16x16x32_bf16 v[86:89], v[42:45], v[26:29], v[122:125]\n" \
    "v_permlane16_swap_b32 v78, v40\n" \
    "v_cvt_pk_bf16_f32 v112, v58, v59\n" \
    "v_cvt_pk_bf16_f32 v113, v60, v61\n" \
    "v_mfma_f32_16x16x32_bf16 v[62:65], v[42:45], v[22:25], v[134:137]\n" \
    "ds_read_b128 v[122:125], %[wp0] offset:2048\n" \
    "v_maximum3_f32 v142, v70, v90, v90\n" \
    "v_maximum3_f32 v143, v71, v91, v91\n" \
    "s_cmp_eq_u32 %[rb2], 0\n" \
    "s_cbranch_scc1 Lz_%=\n" \
    "v_mfma_f32_16x16x32_bf16 v[82:85], v[38:41], v[22:25], v[130:133]\n" \
    "s_branch Ly_%=\n" \
    "Lz_%=:\n" \
    "s_nop 7\n" \
    "v_mov_b32 v82, 0xff61b1e6\n" \
    "v_mov_b32 v83, 0xff61b1e6\n" \
    "v_mov_b32 v84, 0xff61b1e6\n" \
    "v_mov_b32 v85, 0xff61b1e6\n" \
    "Ly_%=:\n" \
    "v_maximum3_f32 v144, v72, v92, v92\n" \
    "v_maximum3_f32 v145, v73, v93, v93\n" \
    "ds_read_b64_tr_b16 v[106:107], v157 offset:384\n" \
    "ds_read_b64_tr_b16 v[108:109], v157 offset:512\n" \
    "v_maximum3_f32 v142, v74, v94, v142\n" \
    "v_maximum3_f32 v143, v75, v95, v143\n" \
    "v_maximum3_f32 v144, v76, v96, v144\n" \
    "v_maximum3_f32 v145, v77, v97, v145\n" \
    "ds_read_b64_tr_b16 v[102:103], v156 offset:256\n" \
    "ds_read_b64_tr_b16 v[104:105], v156 offset:384\n" \
    "ds_read_b64_tr_b16 v[98:99], v155 offset:128\n" \
    "ds_read_b64_tr_b16 v[100:101], v155 offset:256\n" \
    "ds_read_b64_tr_b16 v[150:151], v154 offset:0\n" \
    "ds_read_b64_tr_b16 v[152:153], v154 offset:128\n" \
    "s_waitcnt lgkmcnt(8)\n" \
    "s_branch Lend_%=\n" \
    "Llate_%=:\n" \
    "s_barrier\n" \
    "ds_read_b64_tr_b16 v[126:127], %[wp1] offset:0\n" \
    "ds_read_b64_tr_b16 v[128:129], %[wp1] offset:2048\n" \
    "ds_read_b128 v[102:105], %[ap0] offset:55296\n" \
    "ds_read_b64_tr_b16 v[130:131], %[wp2] offset:0\n" \
    "ds_read_b64_tr_b16 v[132:133], %[wp2] offset:2048\n" \
    "ds_read_b64_tr_b16 v[134:135], %[wp3] offset:0\n" \
    "ds_read_b64_tr_b16 v[136:137], %[wp3] offset:2048\n" \
    "ds_read_b64_tr_b16 v[122:123], %[wp0] offset:0\n" \
    "ds_read_b64_tr_b16 v[124:125], %[wp0] offset:2048\n" \
    "ds_read_b128 v[106:109], %[ap0] offset:57344\n" \
    "ds_read_b128 v[110:113], %[ap0] offset:59392\n" \
    "ds_read_b128 v[114:117], %[ap0] offset:61440\n" \
    "s_waitcnt lgkmcnt(9)\n" \
    "v_mfma_f32_16x16x32_bf16 v[42:45], v[126:129], v[102:105], 0\n" \
    "ds_read_b64_tr_b16 v[142:143], %[wp1] offset:34816\n" \
    "s_waitcnt lgkmcnt(8)\n" \
    "v_mfma_f32_16x16x32_bf16 v[62:65], v[102:105], v[130:133], 0\n" \
    "ds_read_b64_tr_b16 v[144:145], %[wp1] offset:36864\n" \
    "s_waitcnt lgkmcnt(7)\n" \
    "v_mfma_f32_16x16x32_bf16 v[82:85], v[134:137], v[102:105], 0\n" \
    "ds_read_b64_tr_b16 v[146:147], %[wp2] offset:34816\n" \
    "s_waitcnt lgkmcnt(6)\n" \
    "v_mfma_f32_16x16x32_bf16 v[22:25], v[122:125], v[102:105], 0\n" \
    "ds_read_b128 v[102:105], %[ap1] offset:55296\n" \
    "s_waitcnt lgkmcnt(6)\n" \
    "v_mfma_f32_16x16x32_bf16 v[46:49], v[126:129], v[106:109], 0\n" \
    "ds_read_b64_tr_b16 v[148:149], %[wp2] offset:36864\n" \
    "v_mfma_f32_16x16x32_bf16 v[66:69], v[106:109], v[130:133], 0\n" \
    "ds_read_b64_tr_b16 v[150:151], %[wp3] offset:34816\n" \
    "v_mfma_f32_16x16x32_bf16 v[86:89], v[134:137], v[106:109], 0\n" \
    "ds_read_b64_tr_b16 v[152:153], %[wp3] offset:36864\n" \
    "v_mfma_f32_16x16x32_bf16 v[26:29], v[122:125], v[106:109], 0\n" \
    "ds_read_b128 v[106:109], %[ap1] offset:57344\n" \
    "s_waitcnt lgkmcnt(9)\n" \
    "v_mfma_f32_16x16x32_bf16 v[50:53], v[126:129], v[110:113], 0\n" \
    "ds_read_b64_tr_b16 v[138:139], %[wp0] offset:34816\n" \
    "v_mfma_f32_16x16x32_bf16 v[70:73], v[110:113], v[130:133], 0\n" \
    "ds_read_b64_tr_b16 v[140:141], %[wp0] offset:36864\n" \
    "v_mfma_f32_16x16x32_bf16 v[90:93], v[134:137], v[110:113], 0\n" \
    "ds_read_b128 v[118:121], %[ap1] offset:61440\n" \
    "v_mfma_f32_16x16x32_bf16 v[30:33], v[122:125], v[110:113], 0\n" \
    "ds_read_b128 v[110:113], %[ap1] offset:59392\n" \
    "s_waitcnt lgkmcnt(12)\n" \
    "v_mfma_f32_16x16x32_bf16 v[54:57], v[126:129], v[114:117], 0\n" \
    "s_barrier\n" \
    "v_mfma_f32_16x16x32_bf16 v[74:77], v[114:117], v[130:133], 0\n" \
    "v_mfma_f32_16x16x32_bf16 v[94:97], v[134:137], v[114:117], 0\n" \
    "v_mfma_f32_16x16x32_bf16 v[34:37], v[122:125], v[114:117], 0\n" \
    "s_waitcnt lgkmcnt(8)\n" \
    "v_mfma_f32_16x16x32_bf16 v[42:45], v[142:145], v[102:105], v[42:45]\n" \
    "ds_read_b64_tr_b16 v[126:127], %[wp1] offset:51200\n" \
    "s_waitcnt lgkmcnt(8)\n" \
    "v_mfma_f32_16x16x32_bf16 v[62:65], v[102:105], v[146:149], v[62:65]\n" \
    "ds_read_b64_tr_b16 v[128:129], %[wp1] offset:53248\n" \
    "s_waitcnt lgkmcnt(7)\n" \
    "v_mfma_f32_16x16x32_bf16 v[82:85], v[150:153], v[102:105], v[82:85]\n" \
    "ds_read_b64_tr_b16 v[130:131], %[wp2] offset:51200\n" \
    "s_waitcnt lgkmcnt(5)\n" \
    "v_mfma_f32_16x16x32_bf16 v[22:25], v[138:141], v[102:105], v[22:25]\n" \
    "ds_read_b128 v[102:105], %[ap0] offset:2048\n" \
    "v_mfma_f32_16x16x32_bf16 v[46:49], v[142:145], v[106:109], v[46:49]\n" \
    "ds_read_b64_tr_b16 v[132:133], %[wp2] offset:53248\n" \
    "v_mfma_f32_16x16x32_bf16 v[66:69], v[106:109], v[146:149], v[66:69]\n" \
    "ds_read_b64_tr_b16 v[134:135], %[wp3] offset:51200\n" \
    "v_mfma_f32_16x16x32_bf16 v[86:89], v[150:153], v[106:109], v[86:89]\n" \
    "ds_read_b64_tr_b16 v[136:137], %[wp3] offset:53248\n" \
    "v_mfma_f32_16x16x32_bf16 v[26:29], v[138:141], v[106:109], v[26:29]\n" \
    "ds_read_b128 v[106:109], %[ap0] offset:4096\n" \
    "s_waitcnt lgkmcnt(8)\n" \
    "v_mfma_f32_16x16x32_bf16 v[50:53], v[142:145], v[110:113], v[50:53]\n" \
    "ds_read_b64_tr_b16 v[122:123], %[wp0] offset:51200\n" \
    "v_mfma_f32_16x16x32_bf16 v[70:73], v[110:113], v[146:149], v[70:73]\n" \
    "ds_read_b64_tr_b16 v[124:125], %[wp0] offset:53248\n" \
    "v_mfma_f32_16x16x32_bf16 v[90:93], v[150:153], v[110:113], v[90:93]\n" \
    "ds_read_b128 v[114:117], %[ap0] offset:8192\n" \
    "v_mfma_f32_16x16x32_bf16 v[30:33], v[138:141], v[110:113], v[30:33]\n" \
    "ds_read_b128 v[110:113], %[ap0] offset:6144\n" \
    "v_mfma_f32_16x16x32_bf16 v[54:57], v[142:145], v[118:121], v[54:57]\n" \
    "s_barrier\n" \
    "v_mfma_f32_16x16x32_bf16 v[74:77], v[118:121], v[146:149], v[74:77]\n" \
    "v_mfma_f32_16x16x32_bf16 v[94:97], v[150:153], v[118:121], v[94:97]\n" \
    "v_mfma_f32_16x16x32_bf16 v[34:37], v[138:141], v[118:121], v[34:37]\n" \
    "s_waitcnt lgkmcnt(8)\n" \
    "v_mfma_f32_16x16x32_bf16 v[42:45], v[126:129], v[102:105], v[42:45]\n" \
    "ds_read_b64_tr_b16 v[142:143], v155 offset:0\n" \
    "s_waitcnt lgkmcnt(8)\n" \
    "v_mfma_f32_16x16x32_bf16 v[62:65], v[102:105], v[130:133], v[62:65]\n" \
    "ds_read_b64_tr_b16 v[144:145], v155 offset:2048\n" \
    "s_waitcnt lgkmcnt(7)\n" \
    "v_mfma_f32_16x16x32_bf16 v[82:85], v[134:137], v[102:105], v[82:85]\n" \
    "ds_read_b64_tr_b16 v[146:147], v156 offset:0\n" \
    "s_waitcnt lgkmcnt(5)\n" \
    "v_mfma_f32_16x16x32_bf16 v[22:25], v[122:125], v[102:105], v[22:25]\n" \
    "ds_read_b128 v[102:105], %[ap1] offset:2048\n" \
    "v_mfma_f32_16x16x32_bf16 v[46:49], v[126:129], v[106:109], v[46:49]\n" \
    "ds_read_b64_tr_b16 v[148:149], v156 offset:2048\n" \
    "v_mfma_f32_16x16x32_bf16 v[66:69], v[106:109], v[130:133], v[66:69]\n" \
    "ds_read_b64_tr_b16 v[150:151], v157 offset:0\n" \
    "v_mfma_f32_16x16x32_bf16 v[86:89], v[134:137], v[106:109], v[86:89]\n" \
    "ds_read_b64_tr_b16 v[152:153], v157 offset:2048\n" \
    "v_mfma_f32_16x16x32_bf16 v[26:29], v[122:125], v[106:109], v[26:29]\n" \
    "ds_read_b128 v[106:109], %[ap1] offset:4096\n" \
    "s_waitcnt lgkmcnt(8)\n" \
    "v_mfma_f32_16x16x32_bf16 v[50:53], v[126:129], v[110:113], v[50:53]\n" \
    "ds_read_b64_tr_b16 v[138:139], v154 offset:0\n" \
    "v_mfma_f32_16x16x32_bf16 v[70:73], v[110:113], v[130:133], v[70:73]\n" \
    "ds_read_b64_tr_b16 v[140:141], v154 offset:2048\n" \
    "v_mfma_f32_16x16x32_bf16 v[90:93], v[134:137], v[110:113], v[90:93]\n" \
    "ds_read_b128 v[118:121], %[ap1] offset:8192\n" \
    "v_mfma_f32_16x16x32_bf16 v[30:33], v[122:125], v[110:113], v[30:33]\n" \
    "ds_read_b128 v[110:113], %[ap1] offset:6144\n" \
    "v_mfma_f32_16x16x32_bf16 v[54:57], v[126:129], v[114:117], v[54:57]\n" \
    "s_barrier\n" \
    "v_mfma_f32_16x16x32_bf16 v[74:77], v[114:117], v[130:133], v[74:77]\n" \
    "v_mfma_f32_16x16x32_bf16 v[94:97], v[134:137], v[114:117], v[94:97]\n" \
    "v_mfma_f32_16x16x32_bf16 v[34:37], v[122:125], v[114:117], v[34:37]\n" \
    "s_waitcnt lgkmcnt(8)\n" \
    "v_mfma_f32_16x16x32_bf16 v[42:45], v[142:145], v[102:105], v[42:45]\n" \
    "ds_read_b64_tr_b16 v[126:127], v155 offset:16384\n" \
    "s_waitcnt lgkmcnt(8)\n" \
    "v_mfma_f32_16x16x32_bf16 v[62:65], v[102:105], v[146:149], v[62:65]\n" \
    "ds_read_b64_tr_b16 v[128:129], v155 offset:18432\n" \
    "s_waitcnt lgkmcnt(7)\n" \
    "v_mfma_f32_16x16x32_bf16 v[82:85], v[150:153], v[102:105], v[82:85]\n" \
    "ds_read_b64_tr_b16 v[130:131], v156 offset:16384\n" \
    "s_waitcnt lgkmcnt(5)\n" \
    "v_mfma_f32_16x16x32_bf16 v[22:25], v[138:141], v[102:105], v[22:25]\n" \
    "ds_read_b128 v[102:105], %[ap0] offset:20480\n" \
    "v_mfma_f32_16x16x32_bf16 v[46:49], v[142:145], v[106:109], v[46:49]\n" \
    "ds_read_b64_tr_b16 v[132:133], v156 offset:18432\n" \
    "v_mfma_f32_16x16x32_bf16 v[66:69], v[106:109], v[146:149], v[66:69]\n" \
    "ds_read_b64_tr_b16 v[134:135], v157 offset:16384\n" \
    "v_mfma_f32_16x16x32_bf16 v[86:89], v[150:153], v[106:109], v[86:89]\n" \
    "ds_read_b64_tr_b16 v[136:137], v157 offset:18432\n" \
    "v_mfma_f32_16x16x32_bf16 v[26:29], v[138:141], v[106:109], v[26:29]\n" \
    "ds_read_b128 v[106:109], %[ap0] offset:22528\n" \
    "s_waitcnt lgkmcnt(8)\n" \
    "v_mfma_f32_16x16x32_bf16 v[50:53], v[142:145], v[110:113], v[50:53]\n" \
    "ds_read_b64_tr_b16 v[122:123], v154 offset:16384\n" \
    "v_mfma_f32_16x16x32_bf16 v[70:73], v[110:113], v[146:149], v[70:73]\n" \
    "ds_read_b64_tr_b16 v[124:125], v154 offset:18432\n" \
    "v_mfma_f32_16x16x32_bf16 v[90:93], v[150:153], v[110:113], v[90:93]\n" \
    "ds_read_b128 v[114:117], %[ap0] offset:26624\n" \
    "v_mfma_f32_16x16x32_bf16 v[30:33], v[138:141], v[110:113], v[30:33]\n" \
    "ds_read_b128 v[110:113], %[ap0] offset:24576\n" \
    "v_mfma_f32_16x16x32_bf16 v[54:57], v[142:145], v[118:121], v[54:57]\n" \
    "s_barrier\n" \
    "v_mfma_f32_16x16x32_bf16 v[74:77], v[118:121], v[146:149], v[74:77]\n" \
    "v_mfma_f32_16x16x32_bf16 v[94:97], v[150:153], v[118:121], v[94:97]\n" \
    "v_mfma_f32_16x16x32_bf16 v[34:37], v[138:141], v[118:121], v[34:37]\n" \
    "s_waitcnt lgkmcnt(8)\n" \
    "v_mfma_f32_16x16x32_bf16 v[42:45], v[126:129], v[102:105], v[42:45]\n" \
    "ds_read_b64_tr_b16 v[142:143], v155 offset:32768\n" \
    "s_waitcnt lgkmcnt(8)\n" \
    "v_mfma_f32_16x16x32_bf16 v[62:65], v[102:105], v[130:133], v[62:65]\n" \
    "ds_read_b64_tr_b16 v[144:145], v155 offset:34816\n" \
    "s_waitcnt lgkmcnt(7)\n" \
    "v_mfma_f32_16x16x32_bf16 v[82:85], v[134:137], v[102:105], v[82:85]\n" \
    "ds_read_b64_tr_b16 v[146:147], v156 offset:32768\n" \
    "s_waitcnt lgkmcnt(5)\n" \
    "v_mfma_f32_16x16x32_bf16 v[22:25], v[122:125], v[102:105], v[22:25]\n" \
    "ds_read_b128 v[102:105], %[ap1] offset:20480\n" \
    "v_mfma_f32_16x16x32_bf16 v[46:49], v[126:129], v[106:109], v[46:49]\n" \
    "ds_read_b64_tr_b16 v[148:149], v156 offset:34816\n" \
    "v_mfma_f32_16x16x32_bf16 v[66:69], v[106:109], v[130:133], v[66:69]\n" \
    "ds_read_b64_tr_b16 v[150:151], v157 offset:32768\n" \
    "v_mfma_f32_16x16x32_bf16 v[86:89], v[134:137], v[106:109], v[86:89]\n" \
    "ds_read_b64_tr_b16 v[152:153], v157 offset:34816\n" \
    "v_mfma_f32_16x16x32_bf16 v[26:29], v[122:125], v[106:109], v[26:29]\n" \
    "ds_read_b128 v[106:109], %[ap1] offset:22528\n" \
    "s_waitcnt lgkmcnt(8)\n" \
    "v_mfma_f32_16x16x32_bf16 v[50:53], v[126:129], v[110:113], v[50:53]\n" \
    "ds_read_b64_tr_b16 v[138:139], v154 offset:32768\n" \
    "v_mfma_f32_16x16x32_bf16 v[70:73], v[110:113], v[130:133], v[70:73]\n" \
    "ds_read_b64_tr_b16 v[140:141], v154 offset:34816\n" \
    "v_mfma_f32_16x16x32_bf16 v[90:93], v[134:137], v[110:113], v[90:93]\n" \
    "ds_read_b128 v[118:121], %[ap1] offset:26624\n" \
    "v_mfma_f32_16x16x32_bf16 v[30:33], v[122:125], v[110:113], v[30:33]\n" \
    "ds_read_b128 v[110:113], %[ap1] offset:24576\n" \
    "v_mfma_f32_16x16x32_bf16 v[54:57], v[126:129], v[114:117], v[54:57]\n" \
    "s_barrier\n" \
    "v_mfma_f32_16x16x32_bf16 v[74:77], v[114:117], v[130:133], v[74:77]\n" \
    "v_mfma_f32_16x16x32_bf16 v[94:97], v[134:137], v[114:117], v[94:97]\n" \
    "v_mfma_f32_16x16x32_bf16 v[34:37], v[122:125], v[114:117], v[34:37]\n" \
    "s_waitcnt lgkmcnt(8)\n" \
    "v_mfma_f32_16x16x32_bf16 v[42:45], v[142:145], v[102:105], v[42:45]\n" \
    "ds_read_b64_tr_b16 v[126:127], %[wp1] offset:0\n" \
    "s_waitcnt lgkmcnt(8)\n" \
    "v_mfma_f32_16x16x32_bf16 v[62:65], v[102:105], v[146:149], v[62:65]\n" \
    "ds_read_b64_tr_b16 v[128:129], %[wp1] offset:2048\n" \
    "s_waitcnt lgkmcnt(7)\n" \
    "v_mfma_f32_16x16x32_bf16 v[82:85], v[150:153], v[102:105], v[82:85]\n" \
    "ds_read_b64_tr_b16 v[130:131], %[wp2] offset:0\n" \
    "s_waitcnt lgkmcnt(5)\n" \
    "v_mfma_f32_16x16x32_bf16 v[22:25], v[138:141], v[102:105], v[22:25]\n" \
    "ds_read_b128 v[102:105], %[ap0] offset:55296\n" \
    "v_mfma_f32_16x16x32_bf16 v[46:49], v[142:145], v[106:109], v[46:49]\n" \
    "ds_read_b64_tr_b16 v[132:133], %[wp2] offset:2048\n" \
    "v_mfma_f32_16x16x32_bf16 v[66:69], v[106:109], v[146:149], v[66:69]\n" \
    "ds_read_b64_tr_b16 v[134:135], %[wp3] offset:0\n" \
    "v_mfma_f32_16x16x32_bf16 v[86:89], v[150:153], v[106:109], v[86:89]\n" \
    "ds_read_b64_tr_b16 v[136:137], %[wp3] offset:2048\n" \
    "v_mfma_f32_16x16x32_bf16 v[26:29], v[138:141], v[106:109], v[26:29]\n" \
    "ds_read_b128 v[106:109], %[ap0] offset:57344\n" \
    "s_waitcnt lgkmcnt(8)\n" \
    "v_mfma_f32_16x16x32_bf16 v[50:53], v[142:145], v[110:113], v[50:53]\n" \
    "ds_read_b64_tr_b16 v[122:123], %[wp0] offset:0\n" \
    "v_mfma_f32_16x16x32_bf16 v[70:73], v[110:113], v[146:149], v[70:73]\n" \
    "ds_read_b64_tr_b16 v[124:125], %[wp0] offset:2048\n" \
    "v_mfma_f32_16x16x32_bf16 v[90:93], v[150:153], v[110:113], v[90:93]\n" \
    "ds_read_b128 v[114:117], %[ap0] offset:61440\n" \
    "v_mfma_f32_16x16x32_bf16 v[30:33], v[138:141], v[110:113], v[30:33]\n" \
    "ds_read_b128 v[110:113], %[ap0] offset:59392\n" \
    "v_mfma_f32_16x16x32_bf16 v[54:57], v[142:145], v[118:121], v[54:57]\n" \
    "s_barrier\n" \
    "v_mfma_f32_16x16x32_bf16 v[74:77], v[118:121], v[146:149], v[74:77]\n" \
    "v_mfma_f32_16x16x32_bf16 v[94:97], v[150:153], v[118:121], v[94:97]\n" \
    "v_mfma_f32_16x16x32_bf16 v[34:37], v[138:141], v[118:121], v[34:37]\n" \
    "s_waitcnt lgkmcnt(8)\n" \
    "v_mfma_f32_16x16x32_bf16 v[42:45], v[126:129], v[102:105], v[42:45]\n" \
    "ds_read_b64_tr_b16 v[142:143], %[wp1] offset:34816\n" \
    "s_waitcnt lgkmcnt(8)\n" \
    "v_mfma_f32_16x16x32_bf16 v[62:65], v[102:105], v[130:133], v[62:65]\n" \
    "ds_read_b64_tr_b16 v[144:145], %[wp1] offset:36864\n" \
    "s_waitcnt lgkmcnt(7)\n" \
    "v_mfma_f32_16x16x32_bf16 v[82:85], v[134:137], v[102:105], v[82:85]\n" \
    "ds_read_b64_tr_b16 v[146:147], %[wp2] offset:34816\n" \
    "s_waitcnt lgkmcnt(5)\n" \
    "v_mfma_f32_16x16x32_bf16 v[22:25], v[122:125], v[102:105], v[22:25]\n" \
    "ds_read_b128 v[102:105], %[ap1] offset:55296\n" \
    "v_mfma_f32_16x16x32_bf16 v[46:49], v[126:129], v[106:109], v[46:49]\n" \
    "ds_read_b64_tr_b16 v[148:149], %[wp2] offset:36864\n" \
    "v_mfma_f32_16x16x32_bf16 v[66:69], v[106:109], v[130:133], v[66:69]\n" \
    "ds_read_b64_tr_b16 v[150:151], %[wp3] offset:34816\n" \
    "v_mfma_f32_16x16x32_bf16 v[86:89], v[134:137], v[106:109], v[86:89]\n" \
    "ds_read_b64_tr_b16 v[152:153], %[wp3] offset:36864\n" \
    "v_mfma_f32_16x16x32_bf16 v[26:29], v[122:125], v[106:109], v[26:29]\n" \
    "ds_read_b128 v[106:109], %[ap1] offset:57344\n" \
    "s_waitcnt lgkmcnt(8)\n" \
    "v_mfma_f32_16x16x32_bf16 v[50:53], v[126:129], v[110:113], v[50:53]\n" \
    "ds_read_b64_tr_b16 v[138:139], %[wp0] offset:34816\n" \
    "v_mfma_f32_16x16x32_bf16 v[70:73], v[110:113], v[130:133], v[70:73]\n" \
    "ds_read_b64_tr_b16 v[140:141], %[wp0] offset:36864\n" \
    "v_mfma_f32_16x16x32_bf16 v[90:93], v[134:137], v[110:113], v[90:93]\n" \
    "ds_read_b128 v[118:121], %[ap1] offset:61440\n" \
    "v_mfma_f32_16x16x32_bf16 v[30:33], v[122:125], v[110:113], v[30:33]\n" \
    "ds_read_b128 v[110:113], %[ap1] offset:59392\n" \
    "v_mfma_f32_16x16x32_bf16 v[54:57], v[126:129], v[114:117], v[54:57]\n" \
    "s_barrier\n" \
    "v_mfma_f32_16x16x32_bf16 v[74:77], v[114:117], v[130:133], v[74:77]\n" \
    "v_mfma_f32_16x16x32_bf16 v[94:97], v[134:137], v[114:117], v[94:97]\n" \
    "v_mfma_f32_16x16x32_bf16 v[34:37], v[122:125], v[114:117], v[34:37]\n" \
    "s_waitcnt lgkmcnt(8)\n" \
    "v_mfma_f32_16x16x32_bf16 v[42:45], v[142:145], v[102:105], v[42:45]\n" \
    "ds_read_b64_tr_b16 v[126:127], %[wp1] offset:51200\n" \
    "s_waitcnt lgkmcnt(8)\n" \
    "v_mfma_f32_16x16x32_bf16 v[62:65], v[102:105], v[146:149], v[62:65]\n" \
    "ds_read_b64_tr_b16 v[128:129], %[wp1] offset:53248\n" \
    "s_waitcnt lgkmcnt(7)\n" \
    "v_mfma_f32_16x16x32_bf16 v[82:85], v[150:153], v[102:105], v[82:85]\n" \
    "ds_read_b64_tr_b16 v[130:131], %[wp2] offset:51200\n" \
    "s_waitcnt lgkmcnt(5)\n" \
    "v_mfma_f32_16x16x32_bf16 v[22:25], v[138:141], v[102:105], v[22:25]\n" \
    "ds_read_b128 v[102:105], %[ap0] offset:2048\n" \
    "v_mfma_f32_16x16x32_bf16 v[46:49], v[142:145], v[106:109], v[46:49]\n" \
    "ds_read_b64_tr_b16 v[132:133], %[wp2] offset:53248\n" \
    "v_mfma_f32_16x16x32_bf16 v[66:69], v[106:109], v[146:149], v[66:69]\n" \
    "ds_read_b64_tr_b16 v[134:135], %[wp3] offset:51200\n" \
    "v_mfma_f32_16x16x32_bf16 v[86:89], v[150:153], v[106:109], v[86:89]\n" \
    "ds_read_b64_tr_b16 v[136:137], %[wp3] offset:53248\n" \
    "v_mfma_f32_16x16x32_bf16 v[26:29], v[138:141], v[106:109], v[26:29]\n" \
    "ds_read_b128 v[106:109], %[ap0] offset:4096\n" \
    "s_waitcnt lgkmcnt(8)\n" \
    "v_mfma_f32_16x16x32_bf16 v[50:53], v[142:145], v[110:113], v[50:53]\n" \
    "ds_read_b64_tr_b16 v[122:123], %[wp0] offset:51200\n" \
    "v_mfma_f32_16x16x32_bf16 v[70:73], v[110:113], v[146:149], v[70:73]\n" \
    "ds_read_b64_tr_b16 v[124:125], %[wp0] offset:53248\n" \
    "v_mfma_f32_16x16x32_bf16 v[90:93], v[150:153], v[110:113], v[90:93]\n" \
    "ds_read_b128 v[114:117], %[ap0] offset:8192\n" \
    "v_mfma_f32_16x16x32_bf16 v[30:33], v[138:141], v[110:113], v[30:33]\n" \
    "ds_read_b128 v[110:113], %[ap0] offset:6144\n" \
    "v_mfma_f32_16x16x32_bf16 v[54:57], v[142:145], v[118:121], v[54:57]\n" \
    "s_barrier\n" \
    "v_mfma_f32_16x16x32_bf16 v[74:77], v[118:121], v[146:149], v[74:77]\n" \
    "v_mfma_f32_16x16x32_bf16 v[94:97], v[150:153], v[118:121], v[94:97]\n" \
    "v_mfma_f32_16x16x32_bf16 v[34:37], v[138:141], v[118:121], v[34:37]\n" \
    "s_waitcnt lgkmcnt(8)\n" \
    "v_mfma_f32_16x16x32_bf16 v[42:45], v[126:129], v[102:105], v[42:45]\n" \
    "ds_read_b64_tr_b16 v[142:143], v155 offset:0\n" \
    "s_waitcnt lgkmcnt(8)\n" \
    "v_mfma_f32_16x16x32_bf16 v[62:65], v[102:105], v[130:133], v[62:65]\n" \
    "ds_read_b64_tr_b16 v[144:145], v155 offset:2048\n" \
    "s_waitcnt lgkmcnt(7)\n" \
    "v_mfma_f32_16x16x32_bf16 v[82:85], v[134:137], v[102:105], v[82:85]\n" \
    "ds_read_b64_tr_b16 v[146:147], v156 offset:0\n" \
    "s_waitcnt lgkmcnt(5)\n" \
    "v_mfma_f32_16x16x32_bf16 v[22:25], v[122:125], v[102:105], v[22:25]\n" \
    "ds_read_b128 v[102:105], %[ap1] offset:2048\n" \
    "v_mfma_f32_16x16x32_bf16 v[46:49], v[126:129], v[106:109], v[46:49]\n" \
    "ds_read_b64_tr_b16 v[148:149], v156 offset:2048\n" \
    "v_mfma_f32_16x16x32_bf16 v[66:69], v[106:109], v[130:133], v[66:69]\n" \
    "ds_read_b64_tr_b16 v[150:151], v157 offset:0\n" \
    "v_mfma_f32_16x16x32_bf16 v[86:89], v[134:137], v[106:109], v[86:89]\n" \
    "ds_read_b64_tr_b16 v[152:153], v157 offset:2048\n" \
    "v_mfma_f32_16x16x32_bf16 v[26:29], v[122:125], v[106:109], v[26:29]\n" \
    "ds_read_b128 v[106:109], %[ap1] offset:4096\n" \
    "s_waitcnt lgkmcnt(8)\n" \
    "v_mfma_f32_16x16x32_bf16 v[50:53], v[126:129], v[110:113], v[50:53]\n" \
    "ds_read_b64_tr_b16 v[138:139], v154 offset:0\n" \
    "v_mfma_f32_16x16x32_bf16 v[70:73], v[110:113], v[130:133], v[70:73]\n" \
    "ds_read_b64_tr_b16 v[140:141], v154 offset:2048\n" \
    "v_mfma_f32_16x16x32_bf16 v[90:93], v[134:137], v[110:113], v[90:93]\n" \
    "ds_read_b128 v[118:121], %[ap1] offset:8192\n" \
    "v_mfma_f32_16x16x32_bf16 v[30:33], v[122:125], v[110:113], v[30:33]\n" \
    "ds_read_b128 v[110:113], %[ap1] offset:6144\n" \
    "v_mfma_f32_16x16x32_bf16 v[54:57], v[126:129], v[114:117], v[54:57]\n" \
    "s_barrier\n" \
    "v_mfma_f32_16x16x32_bf16 v[74:77], v[114:117], v[130:133], v[74:77]\n" \
    "v_mfma_f32_16x16x32_bf16 v[94:97], v[134:137], v[114:117], v[94:97]\n" \
    "v_mfma_f32_16x16x32_bf16 v[34:37], v[122:125], v[114:117], v[34:37]\n" \
    "s_waitcnt lgkmcnt(8)\n" \
    "v_mfma_f32_16x16x32_bf16 v[42:45], v[142:145], v[102:105], v[42:45]\n" \
    "ds_read_b64_tr_b16 v[126:127], v155 offset:16384\n" \
    "s_waitcnt lgkmcnt(8)\n" \
    "v_mfma_f32_16x16x32_bf16 v[62:65], v[102:105], v[146:149], v[62:65]\n" \
    "ds_read_b64_tr_b16 v[128:129], v155 offset:18432\n" \
    "s_waitcnt lgkmcnt(7)\n" \
    "v_mfma_f32_16x16x32_bf16 v[82:85], v[150:153], v[102:105], v[82:85]\n" \
    "ds_read_b64_tr_b16 v[130:131], v156 offset:16384\n" \
    "s_waitcnt lgkmcnt(5)\n" \
    "v_mfma_f32_16x16x32_bf16 v[22:25], v[138:141], v[102:105], v[22:25]\n" \
    "ds_read_b128 v[102:105], %[ap0] offset:20480\n" \
    "v_mfma_f32_16x16x32_bf16 v[46:49], v[142:145], v[106:109], v[46:49]\n" \
    "ds_read_b64_tr_b16 v[132:133], v156 offset:18432\n" \
    "v_mfma_f32_16x16x32_bf16 v[66:69], v[106:109], v[146:149], v[66:69]\n" \
    "ds_read_b64_tr_b16 v[134:135], v157 offset:16384\n" \
    "v_mfma_f32_16x16x32_bf16 v[86:89], v[150:153], v[106:109], v[86:89]\n" \
    "ds_read_b64_tr_b16 v[136:137], v157 offset:18432\n" \
    "v_mfma_f32_16x16x32_bf16 v[26:29], v[138:141], v[106:109], v[26:29]\n" \
    "ds_read_b128 v[106:109], %[ap0] offset:22528\n" \
    "s_waitcnt lgkmcnt(8)\n" \
    "v_mfma_f32_16x16x32_bf16 v[50:53], v[142:145], v[110:113], v[50:53]\n" \
    "ds_read_b64_tr_b16 v[122:123], v154 offset:16384\n" \
    "v_mfma_f32_16x16x32_bf16 v[70:73], v[110:113], v[146:149], v[70:73]\n" \
    "ds_read_b64_tr_b16 v[124:125], v154 offset:18432\n" \
    "v_mfma_f32_16x16x32_bf16 v[90:93], v[150:153], v[110:113], v[90:93]\n" \
    "ds_read_b128 v[114:117], %[ap0] offset:26624\n" \
    "v_mfma_f32_16x16x32_bf16 v[30:33], v[138:141], v[110:113], v[30:33]\n" \
    "ds_read_b128 v[110:113], %[ap0] offset:24576\n" \
    "v_mfma_f32_16x16x32_bf16 v[54:57], v[142:145], v[118:121], v[54:57]\n" \
    "s_barrier\n" \
    "v_mfma_f32_16x16x32_bf16 v[74:77], v[118:121], v[146:149], v[74:77]\n" \
    "v_mfma_f32_16x16x32_bf16 v[94:97], v[150:153], v[118:121], v[94:97]\n" \
    "v_mfma_f32_16x16x32_bf16 v[34:37], v[138:141], v[118:121], v[34:37]\n" \
    "s_waitcnt lgkmcnt(8)\n" \
    "v_mfma_f32_16x16x32_bf16 v[42:45], v[126:129], v[102:105], v[42:45]\n" \
    "ds_read_b64_tr_b16 v[142:143], v155 offset:32768\n" \
    "s_waitcnt lgkmcnt(8)\n" \
    "v_mfma_f32_16x16x32_bf16 v[62:65], v[102:105], v[130:133], v[62:65]\n" \
    "ds_read_b64_tr_b16 v[144:145], v155 offset:34816\n" \
    "s_waitcnt lgkmcnt(7)\n" \
    "v_mfma_f32_16x16x32_bf16 v[82:85], v[134:137], v[102:105], v[82:85]\n" \
    "ds_read_b64_tr_b16 v[146:147], v156 offset:32768\n" \
    "s_waitcnt lgkmcnt(5)\n" \
    "v_mfma_f32_16x16x32_bf16 v[22:25], v[122:125], v[102:105], v[22:25]\n" \
    "ds_read_b128 v[102:105], %[ap1] offset:20480\n" \
    "v_mfma_f32_16x16x32_bf16 v[46:49], v[126:129], v[106:109], v[46:49]\n" \
    "ds_read_b64_tr_b16 v[148:149], v156 offset:34816\n" \
    "v_mfma_f32_16x16x32_bf16 v[66:69], v[106:109], v[130:133], v[66:69]\n" \
    "ds_read_b64_tr_b16 v[150:151], v157 offset:32768\n" \
    "v_mfma_f32_16x16x32_bf16 v[86:89], v[134:137], v[106:109], v[86:89]\n" \
    "ds_read_b64_tr_b16 v[152:153], v157 offset:34816\n" \
    "v_mfma_f32_16x16x32_bf16 v[26:29], v[122:125], v[106:109], v[26:29]\n" \
    "ds_read_b128 v[106:109], %[ap1] offset:22528\n" \
    "s_waitcnt lgkmcnt(8)\n" \
    "v_mfma_f32_16x16x32_bf16 v[50:53], v[126:129], v[110:113], v[50:53]\n" \
    "ds_read_b64_tr_b16 v[138:139], v154 offset:32768\n" \
    "v_mfma_f32_16x16x32_bf16 v[70:73], v[110:113], v[130:133], v[70:73]\n" \
    "ds_read_b64_tr_b16 v[140:141], v154 offset:34816\n" \
    "v_mfma_f32_16x16x32_bf16 v[90:93], v[134:137], v[110:113], v[90:93]\n" \
    "ds_read_b128 v[118:121], %[ap1] offset:26624\n" \
    "v_mfma_f32_16x16x32_bf16 v[30:33], v[122:125], v[110:113], v[30:33]\n" \
    "ds_read_b128 v[110:113], %[ap1] offset:24576\n" \
    "v_mfma_f32_16x16x32_bf16 v[54:57], v[126:129], v[114:117], v[54:57]\n" \
    "s_barrier\n" \
    "v_mfma_f32_16x16x32_bf16 v[74:77], v[114:117], v[130:133], v[74:77]\n" \
    "v_mfma_f32_16x16x32_bf16 v[94:97], v[134:137], v[114:117], v[94:97]\n" \
    "v_mfma_f32_16x16x32_bf16 v[34:37], v[122:125], v[114:117], v[34:37]\n" \
    "s_waitcnt lgkmcnt(8)\n" \
    "v_mfma_f32_16x16x32_bf16 v[42:45], v[142:145], v[102:105], v[42:45]\n" \
    "ds_read_b64_tr_b16 v[126:127], %[wp1] offset:0\n" \
    "s_waitcnt lgkmcnt(8)\n" \
    "v_mfma_f32_16x16x32_bf16 v[62:65], v[102:105], v[146:149], v[62:65]\n" \
    "ds_read_b64_tr_b16 v[128:129], %[wp1] offset:2048\n" \
    "s_waitcnt lgkmcnt(7)\n" \
    "v_mfma_f32_16x16x32_bf16 v[82:85], v[150:153], v[102:105], v[82:85]\n" \
    "ds_read_b64_tr_b16 v[130:131], %[wp2] offset:0\n" \
    "s_waitcnt lgkmcnt(5)\n" \
    "v_mfma_f32_16x16x32_bf16 v[22:25], v[138:141], v[102:105], v[22:25]\n" \
    "ds_read_b128 v[102:105], %[ap0] offset:55296\n" \
    "v_mfma_f32_16x16x32_bf16 v[46:49], v[142:145], v[106:109], v[46:49]\n" \
    "ds_read_b64_tr_b16 v[132:133], %[wp2] offset:2048\n" \
    "v_mfma_f32_16x16x32_bf16 v[66:69], v[106:109], v[146:149], v[66:69]\n" \
    "ds_read_b64_tr_b16 v[134:135], %[wp3] offset:0\n" \
    "v_mfma_f32_16x16x32_bf16 v[86:89], v[150:153], v[106:109], v[86:89]\n" \
    "ds_read_b64_tr_b16 v[136:137], %[wp3] offset:2048\n" \
    "v_mfma_f32_16x16x32_bf16 v[26:29], v[138:141], v[106:109], v[26:29]\n" \
    "ds_read_b128 v[106:109], %[ap0] offset:57344\n" \
    "s_waitcnt lgkmcnt(8)\n" \
    "v_mfma_f32_16x16x32_bf16 v[50:53], v[142:145], v[110:113], v[50:53]\n" \
    "ds_read_b64_tr_b16 v[122:123], %[wp0] offset:0\n" \
    "v_mfma_f32_16x16x32_bf16 v[70:73], v[110:113], v[146:149], v[70:73]\n" \
    "ds_read_b64_tr_b16 v[124:125], %[wp0] offset:2048\n" \
    "v_mfma_f32_16x16x32_bf16 v[90:93], v[150:153], v[110:113], v[90:93]\n" \
    "ds_read_b128 v[114:117], %[ap0] offset:61440\n" \
    "v_mfma_f32_16x16x32_bf16 v[30:33], v[138:141], v[110:113], v[30:33]\n" \
    "ds_read_b128 v[110:113], %[ap0] offset:59392\n" \
    "v_mfma_f32_16x16x32_bf16 v[54:57], v[142:145], v[118:121], v[54:57]\n" \
    "s_barrier\n" \
    "v_mfma_f32_16x16x32_bf16 v[74:77], v[118:121], v[146:149], v[74:77]\n" \
    "v_mfma_f32_16x16x32_bf16 v[94:97], v[150:153], v[118:121], v[94:97]\n" \
    "v_mfma_f32_16x16x32_bf16 v[34:37], v[138:141], v[118:121], v[34:37]\n" \
    "s_waitcnt lgkmcnt(8)\n" \
    "v_mfma_f32_16x16x32_bf16 v[42:45], v[126:129], v[102:105], v[42:45]\n" \
    "ds_read_b64_tr_b16 v[142:143], %[wp1] offset:34816\n" \
    "s_waitcnt lgkmcnt(8)\n" \
    "v_mfma_f32_16x16x32_bf16 v[62:65], v[102:105], v[130:133], v[62:65]\n" \
    "ds_read_b64_tr_b16 v[144:145], %[wp1] offset:36864\n" \
    "s_waitcnt lgkmcnt(7)\n" \
    "v_mfma_f32_16x16x32_bf16 v[82:85], v[134:137], v[102:105], v[82:85]\n" \
    "ds_read_b64_tr_b16 v[146:147], %[wp2] offset:34816\n" \
    "s_waitcnt lgkmcnt(5)\n" \
    "v_mfma_f32_16x16x32_bf16 v[22:25], v[122:125], v[102:105], v[22:25]\n" \
    "ds_read_b128 v[102:105], %[ap1] offset:55296\n" \
    "v_mfma_f32_16x16x32_bf16 v[46:49], v[126:129], v[106:109], v[46:49]\n" \
    "ds_read_b64_tr_b16 v[148:149], %[wp2] offset:36864\n" \
    "v_mfma_f32_16x16x32_bf16 v[66:69], v[106:109], v[130:133], v[66:69]\n" \
    "ds_read_b64_tr_b16 v[150:151], %[wp3] offset:34816\n" \
    "v_mfma_f32_16x16x32_bf16 v[86:89], v[134:137], v[106:109], v[86:89]\n" \
    "ds_read_b64_tr_b16 v[152:153], %[wp3] offset:36864\n" \
    "v_mfma_f32_16x16x32_bf16 v[26:29], v[122:125], v[106:109], v[26:29]\n" \
    "ds_read_b128 v[106:109], %[ap1] offset:57344\n" \
    "s_waitcnt lgkmcnt(8)\n" \
    "v_mfma_f32_16x16x32_bf16 v[50:53], v[126:129], v[110:113], v[50:53]\n" \
    "ds_read_b64_tr_b16 v[138:139], %[wp0] offset:34816\n" \
    "v_mfma_f32_16x16x32_bf16 v[70:73], v[110:113], v[130:133], v[70:73]\n" \
    "ds_read_b64_tr_b16 v[140:141], %[wp0] offset:36864\n" \
    "v_mfma_f32_16x16x32_bf16 v[90:93], v[134:137], v[110:113], v[90:93]\n" \
    "ds_read_b128 v[118:121], %[ap1] offset:61440\n" \
    "v_mfma_f32_16x16x32_bf16 v[30:33], v[122:125], v[110:113], v[30:33]\n" \
    "ds_read_b128 v[110:113], %[ap1] offset:59392\n" \
    "v_mfma_f32_16x16x32_bf16 v[54:57], v[126:129], v[114:117], v[54:57]\n" \
    "s_barrier\n" \
    "v_mfma_f32_16x16x32_bf16 v[74:77], v[114:117], v[130:133], v[74:77]\n" \
    "v_mfma_f32_16x16x32_bf16 v[94:97], v[134:137], v[114:117], v[94:97]\n" \
    "v_mfma_f32_16x16x32_bf16 v[34:37], v[122:125], v[114:117], v[34:37]\n" \
    "s_waitcnt lgkmcnt(8)\n" \
    "v_mfma_f32_16x16x32_bf16 v[42:45], v[142:145], v[102:105], v[42:45]\n" \
    "ds_read_b64_tr_b16 v[126:127], %[wp1] offset:51200\n" \
    "s_waitcnt lgkmcnt(8)\n" \
    "v_mfma_f32_16x16x32_bf16 v[62:65], v[102:105], v[146:149], v[62:65]\n" \
    "ds_read_b64_tr_b16 v[128:129], %[wp1] offset:53248\n" \
    "s_waitcnt lgkmcnt(7)\n" \
    "v_mfma_f32_16x16x32_bf16 v[82:85], v[150:153], v[102:105], v[82:85]\n" \
    "ds_read_b64_tr_b16 v[130:131], %[wp2] offset:51200\n" \
    "s_waitcnt lgkmcnt(5)\n" \
    "v_mfma_f32_16x16x32_bf16 v[22:25], v[138:141], v[102:105], v[22:25]\n" \
    "ds_read_b128 v[102:105], %[ap0] offset:2048\n" \
    "v_mfma_f32_16x16x32_bf16 v[46:49], v[142:145], v[106:109], v[46:49]\n" \
    "ds_read_b64_tr_b16 v[132:133], %[wp2] offset:53248\n" \
    "v_mfma_f32_16x16x32_bf16 v[66:69], v[106:109], v[146:149], v[66:69]\n" \
    "ds_read_b64_tr_b16 v[134:135], %[wp3] offset:51200\n" \
    "v_mfma_f32_16x16x32_bf16 v[86:89], v[150:153], v[106:109], v[86:89]\n" \
    "ds_read_b64_tr_b16 v[136:137], %[wp3] offset:53248\n" \
    "v_mfma_f32_16x16x32_bf16 v[26:29], v[138:141], v[106:109], v[26:29]\n" \
    "ds_read_b128 v[106:109], %[ap0] offset:4096\n" \
    "s_waitcnt lgkmcnt(8)\n" \
    "v_mfma_f32_16x16x32_bf16 v[50:53], v[142:145], v[110:113], v[50:53]\n" \
    "ds_read_b64_tr_b16 v[122:123], %[wp0] offset:51200\n" \
    "v_mfma_f32_16x16x32_bf16 v[70:73], v[110:113], v[146:149], v[70:73]\n" \
    "ds_read_b64_tr_b16 v[124:125], %[wp0] offset:53248\n" \
    "v_mfma_f32_16x16x32_bf16 v[90:93], v[150:153], v[110:113], v[90:93]\n" \
    "ds_read_b128 v[114:117], %[ap0] offset:8192\n" \
    "v_mfma_f32_16x16x32_bf16 v[30:33], v[138:141], v[110:113], v[30:33]\n" \
    "ds_read_b128 v[110:113], %[ap0] offset:6144\n" \
    "v_mfma_f32_16x16x32_bf16 v[54:57], v[142:145], v[118:121], v[54:57]\n" \
    "s_barrier\n" \
    "v_mfma_f32_16x16x32_bf16 v[74:77], v[118:121], v[146:149], v[74:77]\n" \
    "v_mfma_f32_16x16x32_bf16 v[94:97], v[150:153], v[118:121], v[94:97]\n" \
    "v_mfma_f32_16x16x32_bf16 v[34:37], v[138:141], v[118:121], v[34:37]\n" \
    "s_waitcnt lgkmcnt(8)\n" \
    "v_mfma_f32_16x16x32_bf16 v[42:45], v[126:129], v[102:105], v[42:45]\n" \
    "ds_read_b64_tr_b16 v[142:143], v155 offset:0\n" \
    "s_waitcnt lgkmcnt(8)\n" \
    "v_mfma_f32_16x16x32_bf16 v[62:65], v[102:105], v[130:133], v[62:65]\n" \
    "ds_read_b64_tr_b16 v[144:145], v155 offset:2048\n" \
    "s_waitcnt lgkmcnt(7)\n" \
    "v_mfma_f32_16x16x32_bf16 v[82:85], v[134:137], v[102:105], v[82:85]\n" \
    "ds_read_b64_tr_b16 v[146:147], v156 offset:0\n" \
    "s_waitcnt lgkmcnt(5)\n" \
    "v_mfma_f32_16x16x32_bf16 v[22:25], v[122:125], v[102:105], v[22:25]\n" \
    "ds_read_b128 v[102:105], %[ap1] offset:2048\n" \
    "v_mfma_f32_16x16x32_bf16 v[46:49], v[126:129], v[106:109], v[46:49]\n" \
    "ds_read_b64_tr_b16 v[148:149], v156 offset:2048\n" \
    "v_mfma_f32_16x16x32_bf16 v[66:69], v[106:109], v[130:133], v[66:69]\n" \
    "ds_read_b64_tr_b16 v[150:151], v157 offset:0\n" \
    "v_mfma_f32_16x16x32_bf16 v[86:89], v[134:137], v[106:109], v[86:89]\n" \
    "ds_read_b64_tr_b16 v[152:153], v157 offset:2048\n" \
    "v_mfma_f32_16x16x32_bf16 v[26:29], v[122:125], v[106:109], v[26:29]\n" \
    "ds_read_b128 v[106:109], %[ap1] offset:4096\n" \
    "s_waitcnt lgkmcnt(8)\n" \
    "v_mfma_f32_16x16x32_bf16 v[50:53], v[126:129], v[110:113], v[50:53]\n" \
    "ds_read_b64_tr_b16 v[138:139], v154 offset:0\n" \
    "v_mfma_f32_16x16x32_bf16 v[70:73], v[110:113], v[130:133], v[70:73]\n" \
    "ds_read_b64_tr_b16 v[140:141], v154 offset:2048\n" \
    "v_mfma_f32_16x16x32_bf16 v[90:93], v[134:137], v[110:113], v[90:93]\n" \
    "ds_read_b128 v[118:121], %[ap1] offset:8192\n" \
    "v_mfma_f32_16x16x32_bf16 v[30:33], v[122:125], v[110:113], v[30:33]\n" \
    "ds_read_b128 v[110:113], %[ap1] offset:6144\n" \
    "v_mfma_f32_16x16x32_bf16 v[54:57], v[126:129], v[114:117], v[54:57]\n" \
    "s_barrier\n" \
    "v_mfma_f32_16x16x32_bf16 v[74:77], v[114:117], v[130:133], v[74:77]\n" \
    "v_mfma_f32_16x16x32_bf16 v[94:97], v[134:137], v[114:117], v[94:97]\n" \
    "v_mfma_f32_16x16x32_bf16 v[34:37], v[122:125], v[114:117], v[34:37]\n" \
    "s_waitcnt lgkmcnt(8)\n" \
    "v_mfma_f32_16x16x32_bf16 v[42:45], v[142:145], v[102:105], v[42:45]\n" \
    "ds_read_b64_tr_b16 v[126:127], v155 offset:16384\n" \
    "s_waitcnt lgkmcnt(8)\n" \
    "v_mfma_f32_16x16x32_bf16 v[62:65], v[102:105], v[146:149], v[62:65]\n" \
    "ds_read_b64_tr_b16 v[128:129], v155 offset:18432\n" \
    "s_waitcnt lgkmcnt(7)\n" \
    "v_mfma_f32_16x16x32_bf16 v[82:85], v[150:153], v[102:105], v[82:85]\n" \
    "ds_read_b64_tr_b16 v[130:131], v156 offset:16384\n" \
    "s_waitcnt lgkmcnt(5)\n" \
    "v_mfma_f32_16x16x32_bf16 v[22:25], v[138:141], v[102:105], v[22:25]\n" \
    "ds_read_b128 v[102:105], %[ap0] offset:20480\n" \
    "v_mfma_f32_16x16x32_bf16 v[46:49], v[142:145], v[106:109], v[46:49]\n" \
    "ds_read_b64_tr_b16 v[132:133], v156 offset:18432\n" \
    "v_mfma_f32_16x16x32_bf16 v[66:69], v[106:109], v[146:149], v[66:69]\n" \
    "ds_read_b64_tr_b16 v[134:135], v157 offset:16384\n" \
    "v_mfma_f32_16x16x32_bf16 v[86:89], v[150:153], v[106:109], v[86:89]\n" \
    "ds_read_b64_tr_b16 v[136:137], v157 offset:18432\n" \
    "v_mfma_f32_16x16x32_bf16 v[26:29], v[138:141], v[106:109], v[26:29]\n" \
    "ds_read_b128 v[106:109], %[ap0] offset:22528\n" \
    "s_waitcnt lgkmcnt(8)\n" \
    "v_mfma_f32_16x16x32_bf16 v[50:53], v[142:145], v[110:113], v[50:53]\n" \
    "ds_read_b64_tr_b16 v[122:123], v154 offset:16384\n" \
    "v_mfma_f32_16x16x32_bf16 v[70:73], v[110:113], v[146:149], v[70:73]\n" \
    "ds_read_b64_tr_b16 v[124:125], v154 offset:18432\n" \
    "v_mfma_f32_16x16x32_bf16 v[90:93], v[150:153], v[110:113], v[90:93]\n" \
    "ds_read_b128 v[114:117], %[ap0] offset:26624\n" \
    "v_mfma_f32_16x16x32_bf16 v[30:33], v[138:141], v[110:113], v[30:33]\n" \
    "ds_read_b128 v[110:113], %[ap0] offset:24576\n" \
    "v_mfma_f32_16x16x32_bf16 v[54:57], v[142:145], v[118:121], v[54:57]\n" \
    "s_barrier\n" \
    "v_mfma_f32_16x16x32_bf16 v[74:77], v[118:121], v[146:149], v[74:77]\n" \
    "v_mfma_f32_16x16x32_bf16 v[94:97], v[150:153], v[118:121], v[94:97]\n" \
    "v_mfma_f32_16x16x32_bf16 v[34:37], v[138:141], v[118:121], v[34:37]\n" \
    "s_waitcnt lgkmcnt(8)\n" \
    "v_mfma_f32_16x16x32_bf16 v[42:45], v[126:129], v[102:105], v[42:45]\n" \
    "ds_read_b64_tr_b16 v[142:143], v155 offset:32768\n" \
    "s_waitcnt lgkmcnt(8)\n" \
    "v_mfma_f32_16x16x32_bf16 v[62:65], v[102:105], v[130:133], v[62:65]\n" \
    "ds_read_b64_tr_b16 v[144:145], v155 offset:34816\n" \
    "s_waitcnt lgkmcnt(7)\n" \
    "v_mfma_f32_16x16x32_bf16 v[82:85], v[134:137], v[102:105], v[82:85]\n" \
    "ds_read_b64_tr_b16 v[146:147], v156 offset:32768\n" \
    "s_waitcnt lgkmcnt(5)\n" \
    "v_mfma_f32_16x16x32_bf16 v[22:25], v[122:125], v[102:105], v[22:25]\n" \
    "ds_read_b128 v[102:105], %[ap1] offset:20480\n" \
    "v_mfma_f32_16x16x32_bf16 v[46:49], v[126:129], v[106:109], v[46:49]\n" \
    "ds_read_b64_tr_b16 v[148:149], v156 offset:34816\n" \
    "v_mfma_f32_16x16x32_bf16 v[66:69], v[106:109], v[130:133], v[66:69]\n" \
    "ds_read_b64_tr_b16 v[150:151], v157 offset:32768\n" \
    "v_mfma_f32_16x16x32_bf16 v[86:89], v[134:137], v[106:109], v[86:89]\n" \
    "ds_read_b64_tr_b16 v[152:153], v157 offset:34816\n" \
    "v_mfma_f32_16x16x32_bf16 v[26:29], v[122:125], v[106:109], v[26:29]\n" \
    "ds_read_b128 v[106:109], %[ap1] offset:22528\n" \
    "s_waitcnt lgkmcnt(8)\n" \
    "v_mfma_f32_16x16x32_bf16 v[50:53], v[126:129], v[110:113], v[50:53]\n" \
    "ds_read_b64_tr_b16 v[138:139], v154 offset:32768\n" \
    "v_mfma_f32_16x16x32_bf16 v[70:73], v[110:113], v[130:133], v[70:73]\n" \
    "ds_read_b64_tr_b16 v[140:141], v154 offset:34816\n" \
    "v_mfma_f32_16x16x32_bf16 v[90:93], v[134:137], v[110:113], v[90:93]\n" \
    "ds_read_b128 v[118:121], %[ap1] offset:26624\n" \
    "v_mfma_f32_16x16x32_bf16 v[30:33], v[122:125], v[110:113], v[30:33]\n" \
    "ds_read_b128 v[110:113], %[ap1] offset:24576\n" \
    "v_mov_b32 v161, 16\n" \
    "ds_write_b32 %[fr], v161\n" \
    "v_mfma_f32_16x16x32_bf16 v[54:57], v[126:129], v[114:117], v[54:57]\n" \
    "s_barrier\n" \
    "v_mfma_f32_16x16x32_bf16 v[74:77], v[114:117], v[130:133], v[74:77]\n" \
    "v_mfma_f32_16x16x32_bf16 v[94:97], v[134:137], v[114:117], v[94:97]\n" \
    "v_mfma_f32_16x16x32_bf16 v[34:37], v[122:125], v[114:117], v[34:37]\n" \
    "s_waitcnt lgkmcnt(9)\n" \
    "v_mfma_f32_16x16x32_bf16 v[42:45], v[142:145], v[102:105], v[42:45]\n" \
    "ds_read_b64_tr_b16 v[126:127], %[wp1] offset:0\n" \
    "s_waitcnt lgkmcnt(9)\n" \
    "v_mfma_f32_16x16x32_bf16 v[62:65], v[102:105], v[146:149], v[62:65]\n" \
    "ds_read_b64_tr_b16 v[128:129], %[wp1] offset:2048\n" \
    "s_waitcnt lgkmcnt(8)\n" \
    "v_mfma_f32_16x16x32_bf16 v[82:85], v[150:153], v[102:105], v[82:85]\n" \
    "ds_read_b64_tr_b16 v[130:131], %[wp2] offset:0\n" \
    "s_waitcnt lgkmcnt(6)\n" \
    "v_mfma_f32_16x16x32_bf16 v[22:25], v[138:141], v[102:105], v[22:25]\n" \
    "ds_read_b128 v[102:105], %[ap0] offset:55296\n" \
    "v_mfma_f32_16x16x32_bf16 v[46:49], v[142:145], v[106:109], v[46:49]\n" \
    "ds_read_b64_tr_b16 v[132:133], %[wp2] offset:2048\n" \
    "v_mfma_f32_16x16x32_bf16 v[66:69], v[106:109], v[146:149], v[66:69]\n" \
    "ds_read_b64_tr_b16 v[134:135], %[wp3] offset:0\n" \
    "v_mfma_f32_16x16x32_bf16 v[86:89], v[150:153], v[106:109], v[86:89]\n" \
    "ds_read_b64_tr_b16 v[136:137], %[wp3] offset:2048\n" \
    "v_mfma_f32_16x16x32_bf16 v[26:29], v[138:141], v[106:109], v[26:29]\n" \
    "ds_read_b128 v[106:109], %[ap0] offset:57344\n" \
    "s_waitcnt lgkmcnt(9)\n" \
    "v_mfma_f32_16x16x32_bf16 v[50:53], v[142:145], v[110:113], v[50:53]\n" \
    "ds_read_b64_tr_b16 v[122:123], %[wp0] offset:0\n" \
    "v_mfma_f32_16x16x32_bf16 v[70:73], v[110:113], v[146:149], v[70:73]\n" \
    "ds_read_b64_tr_b16 v[124:125], %[wp0] offset:2048\n" \
    "v_mfma_f32_16x16x32_bf16 v[90:93], v[150:153], v[110:113], v[90:93]\n" \
    "ds_read_b128 v[114:117], %[ap0] offset:61440\n" \
    "v_mfma_f32_16x16x32_bf16 v[30:33], v[138:141], v[110:113], v[30:33]\n" \
    "ds_read_b128 v[110:113], %[ap0] offset:59392\n" \
    "v_mov_b32 v161, 17\n" \
    "ds_write_b32 %[fr], v161\n" \
    "v_mfma_f32_16x16x32_bf16 v[54:57], v[142:145], v[118:121], v[54:57]\n" \
    "s_barrier\n" \
    "ds_read_b64_tr_b16 v[142:143], %[wp1] offset:34816\n" \
    "ds_read_b64_tr_b16 v[144:145], %[wp1] offset:36864\n" \
    "v_mfma_f32_16x16x32_bf16 v[74:77], v[118:121], v[146:149], v[74:77]\n" \
    "ds_read_b64_tr_b16 v[146:147], %[wp2] offset:34816\n" \
    "ds_read_b64_tr_b16 v[148:149], %[wp2] offset:36864\n" \
    "v_mfma_f32_16x16x32_bf16 v[94:97], v[150:153], v[118:121], v[94:97]\n" \
    "ds_read_b64_tr_b16 v[150:151], %[wp3] offset:34816\n" \
    "ds_read_b64_tr_b16 v[152:153], %[wp3] offset:36864\n" \
    "v_mfma_f32_16x16x32_bf16 v[34:37], v[138:141], v[118:121], v[34:37]\n" \
    "ds_read_b128 v[118:121], %[ap1] offset:61440\n" \
    "ds_read_b64_tr_b16 v[138:139], %[wp0] offset:34816\n" \
    "ds_read_b64_tr_b16 v[140:141], %[wp0] offset:36864\n" \
    "s_waitcnt lgkmcnt(11)\n" \
    "v_mfma_f32_16x16x32_bf16 v[54:57], v[126:129], v[114:117], v[54:57]\n" \
    "v_add_u32 %[wp0], %[tabc], %[l16]\n" \
    "v_cndmask_b32_e64 %[wp2], 0, -1, s[68:69]\n" \
    "v_mfma_f32_16x16x32_bf16 v[74:77], v[114:117], v[130:133], v[74:77]\n" \
    "v_add_u32 %[wp1], %[hxc], %[l16]\n" \
    "v_lshrrev_b32 %[wp3], 2, %[l16]\n" \
    "v_mfma_f32_16x16x32_bf16 v[94:97], v[134:137], v[114:117], v[94:97]\n" \
    "v_add_u32 %[wp3], %[hxd], %[wp3]\n" \
    "v_xor_b32 v158, 128, %[wp0]\n" \
    "v_mfma_f32_16x16x32_bf16 v[34:37], v[122:125], v[114:117], v[34:37]\n" \
    "s_waitcnt lgkmcnt(10)\n" \
    "v_mfma_f32_16x16x32_bf16 v[50:53], v[126:129], v[110:113], v[50:53]\n" \
    "v_mfma_f32_16x16x32_bf16 v[70:73], v[110:113], v[130:133], v[70:73]\n" \
    "v_mfma_f32_16x16x32_bf16 v[90:93], v[134:137], v[110:113], v[90:93]\n" \
    "v_mfma_f32_16x16x32_bf16 v[30:33], v[122:125], v[110:113], v[30:33]\n" \
    "ds_read_b128 v[110:113], %[ap1] offset:59392\n" \
    "s_waitcnt lgkmcnt(3)\n" \
    "v_mfma_f32_16x16x32_bf16 v[54:57], v[142:145], v[118:121], v[54:57]\n" \
    "v_cndmask_b32_e64 v154, %[zr], %[vxb], s[60:61]\n" \
    "v_cndmask_b32_e64 v155, %[zr], %[vxb], s[62:63]\n" \
    "v_mfma_f32_16x16x32_bf16 v[74:77], v[118:121], v[146:149], v[74:77]\n" \
    "v_cndmask_b32_e64 v156, %[zr], %[vxb], s[64:65]\n" \
    "v_cndmask_b32_e64 v157, %[zr], %[vxb], s[66:67]\n" \
    "v_mfma_f32_16x16x32_bf16 v[94:97], v[150:153], v[118:121], v[94:97]\n" \
    "v_mov_b32 v41, 0\n" \
    "s_waitcnt lgkmcnt(1)\n" \
    "v_mfma_f32_16x16x32_bf16 v[34:37], v[138:141], v[118:121], v[34:37]\n" \
    "v_mfma_f32_16x16x32_bf16 v[46:49], v[126:129], v[106:109], v[46:49]\n" \
    "v_mfma_f32_16x16x32_bf16 v[66:69], v[106:109], v[130:133], v[66:69]\n" \
    "v_cvt_pk_bf16_f32 v54, v54, v55\n" \
    "v_cvt_pk_bf16_f32 v55, v56, v57\n" \
    "v_mfma_f32_16x16x32_bf16 v[86:89], v[134:137], v[106:109], v[86:89]\n" \
    "v_cvt_pk_bf16_f32 v120, v74, v75\n" \
    "v_cvt_pk_bf16_f32 v121, v76, v77\n" \
    "v_mfma_f32_16x16x32_bf16 v[26:29], v[122:125], v[106:109], v[26:29]\n" \
    "ds_read_b128 v[106:109], %[ap1] offset:57344\n" \
    "v_cvt_pk_bf16_f32 v94, v94, v95\n" \
    "v_cvt_pk_bf16_f32 v56, v96, v97\n" \
    "ds_write2_b32 %[scw], v94, v56 offset0:128 offset1:129\n" \
    "s_waitcnt lgkmcnt(2)\n" \
    "v_mfma_f32_16x16x32_bf16 v[50:53], v[142:145], v[110:113], v[50:53]\n" \
    "v_permlane16_swap_b32 v94, v56\n" \
    "v_mfma_f32_16x16x32_bf16 v[70:73], v[110:113], v[146:149], v[70:73]\n" \
    "v_cvt_pk_bf16_f32 v34, v34, v35\n" \
    "v_cvt_pk_bf16_f32 v35, v36, v37\n" \
    "v_mfma_f32_16x16x32_bf16 v[90:93], v[150:153], v[110:113], v[90:93]\n" \
    "v_and_b32 v36, v94, %[wp2]\n" \
    "v_mov_b32 v37, 0\n" \
    "v_mov_b32 v57, 0\n" \
    "v_mfma_f32_16x16x32_bf16 v[30:33], v[138:141], v[110:113], v[30:33]\n" \
    "v_mfma_f32_16x16x32_bf16 v[42:45], v[126:129], v[102:105], v[42:45]\n" \
    "v_mfma_f32_16x16x32_bf16 v[62:65], v[102:105], v[130:133], v[62:65]\n" \
    "v_cvt_pk_bf16_f32 v50, v50, v51\n" \
    "v_cvt_pk_bf16_f32 v51, v52, v53\n" \
    "v_mfma_f32_16x16x32_bf16 v[82:85], v[134:137], v[102:105], v[82:85]\n" \
    "v_cvt_pk_bf16_f32 v118, v70, v71\n" \
    "v_cvt_pk_bf16_f32 v119, v72, v73\n" \
    "v_mfma_f32_16x16x32_bf16 v[22:25], v[122:125], v[102:105], v[22:25]\n" \
    "ds_read_b128 v[102:105], %[ap1] offset:55296\n" \
    "v_mov_b32 v161, 18\n" \
    "ds_write_b32 %[fr], v161\n" \
    "ds_read_b128 v[126:129], %[wp0] offset:1024\n" \
    "ds_read_b128 v[122:125], %[wp0]\n" \
    "ds_read_b128 v[134:137], v158 offset:1024\n" \
    "ds_read_b128 v[130:133], v158\n" \
    "v_cvt_pk_bf16_f32 v90, v90, v91\n" \
    "v_cvt_pk_bf16_f32 v52, v92, v93\n" \
    "ds_write2_b32 %[scw], v90, v52 offset0:96 offset1:97\n" \
    "s_waitcnt lgkmcnt(8)\n" \
    "v_mfma_f32_16x16x32_bf16 v[46:49], v[142:145], v[106:109], v[46:49]\n" \
    "v_permlane16_swap_b32 v90, v52\n" \
    "ds_read_b32 v160, %[fr] offset:128\n" \
    "v_mfma_f32_16x16x32_bf16 v[66:69], v[106:109], v[146:149], v[66:69]\n" \
    "v_cvt_pk_bf16_f32 v30, v30, v31\n" \
    "v_cvt_pk_bf16_f32 v31, v32, v33\n" \
    "v_mfma_f32_16x16x32_bf16 v[86:89], v[150:153], v[106:109], v[86:89]\n" \
    "v_and_b32_dpp v32, v90, %[wp2] row_ror:8 row_mask:0xf bank_mask:0xf\n" \
    "v_mov_b32 v33, 0\n" \
    "v_mov_b32 v53, 0\n" \
    "v_mfma_f32_16x16x32_bf16 v[26:29], v[138:141], v[106:109], v[26:29]\n" \
    "v_mov_b32_dpp v30, v30 row_ror:8 row_mask:0xf bank_mask:0xf\n" \
    "v_mov_b32_dpp v31, v31 row_ror:8 row_mask:0xf bank_mask:0xf\n" \
    "s_waitcnt lgkmcnt(7)\n" \
    "v_mfma_f32_16x16x32_bf16 v[42:45], v[142:145], v[102:105], v[42:45]\n" \
    "v_mfma_f32_16x16x32_bf16 v[62:65], v[102:105], v[146:149], v[62:65]\n" \
    "v_cvt_pk_bf16_f32 v46, v46, v47\n" \
    "v_cvt_pk_bf16_f32 v47, v48, v49\n" \
    "v_mfma_f32_16x16x32_bf16 v[82:85], v[150:153], v[102:105], v[82:85]\n" \
    "v_cvt_pk_bf16_f32 v116, v66, v67\n" \
    "v_cvt_pk_bf16_f32 v117, v68, v69\n" \
    "v_mfma_f32_16x16x32_bf16 v[22:25], v[138:141], v[102:105], v[22:25]\n" \
    "ds_read_b32 v159, %[zr] offset:1332\n" \
    "s_setprio 0\n" \
    "s_waitcnt lgkmcnt(1)\n" \
    "v_readfirstlane_b32 %[tmp], v160\n" \
    "s_cmp_lg_u32 %[tmp], 0\n" \
    "s_cbranch_scc1 Lhx_%=\n" \
    "s_mov_b32 %[cnt], 0\n" \
    "Lhxs_%=:\n" \
    "ds_read_b32 v160, %[fr] offset:128\n" \
    "s_add_u32 %[cnt], %[cnt], 1\n" \
    "s_waitcnt lgkmcnt(0)\n" \
    "v_readfirstlane_b32 %[tmp], v160\n" \
    "s_cmp_ge_u32 %[cnt], %[cap]\n" \
    "s_cbranch_scc1 Lhx_%=\n" \
    "s_cmp_eq_u32 %[tmp], 0\n" \
    "s_cbranch_scc1 Lhxs_%=\n" \
    "Lhx_%=:\n" \
    "ds_read_b64 v[38:39], %[wp1]\n" \
    "ds_read_b32 v40, %[wp3]\n" \
    "ds_read_b64 v[112:113], %[wp1] offset:8\n" \
    "v_mfma_f32_16x16x32_bf16 v[74:77], v[54:57], v[34:37], v[126:129]\n" \
    "v_cvt_pk_bf16_f32 v86, v86, v87\n" \
    "v_cvt_pk_bf16_f32 v48, v88, v89\n" \
    "ds_write2_b32 %[scw], v86, v48 offset0:64 offset1:65\n" \
    "v_mfma_f32_16x16x32_bf16 v[94:97], v[50:53], v[34:37], v[122:125]\n" \
    "v_cvt_pk_bf16_f32 v42, v42, v43\n" \
    "v_cvt_pk_bf16_f32 v43, v44, v45\n" \
    "v_permlane16_swap_b32 v86, v48\n" \
    "v_mfma_f32_16x16x32_bf16 v[70:73], v[50:53], v[30:33], v[134:137]\n" \
    "v_cvt_pk_bf16_f32 v26, v26, v27\n" \
    "v_cvt_pk_bf16_f32 v27, v28, v29\n" \
    "v_and_b32 v28, v86, %[wp2]\n" \
    "v_mov_b32 v29, 0\n" \
    "v_mov_b32 v49, 0\n" \
    "s_nop 1\n" \
    "v_mfma_f32_16x16x32_bf16 v[90:93], v[46:49], v[30:33], v[130:133]\n" \
    "v_cvt_pk_bf16_f32 v82, v82, v83\n" \
    "v_cvt_pk_bf16_f32 v44, v84, v85\n" \
    "ds_write2_b32 %[scw], v82, v44 offset0:32 offset1:33\n" \
    "v_cvt_pk_bf16_f32 v22, v22, v23\n" \
    "v_cvt_pk_bf16_f32 v23, v24, v25\n" \
    "v_mfma_f32_16x16x32_bf16 v[66:69], v[46:49], v[26:29], v[126:129]\n" \
    "v_mov_b32 v45, 0\n" \
    "v_permlane16_swap_b32 v82, v44\n" \
    "v_mov_b32_dpp v22, v22 row_ror:8 row_mask:0xf bank_mask:0xf\n" \
    "v_mov_b32_dpp v23, v23 row_ror:8 row_mask:0xf bank_mask:0xf\n" \
    "v_mfma_f32_16x16x32_bf16 v[86:89], v[42:45], v[26:29], v[122:125]\n" \
    "v_cvt_pk_bf16_f32 v114, v62, v63\n" \
    "v_cvt_pk_bf16_f32 v115, v64, v65\n" \
    "v_and_b32_dpp v24, v82, %[wp2] row_ror:8 row_mask:0xf bank_mask:0xf\n" \
    "v_mov_b32 v25, 0\n" \
    "v_maximum3_f32 v142, v70, v90, v90\n" \
    "s_nop 0\n" \
    "v_mfma_f32_16x16x32_bf16 v[62:65], v[42:45], v[22:25], v[134:137]\n" \
    "ds_read_b128 v[122:125], %[wp0] offset:2048\n" \
    "v_maximum3_f32 v143, v71, v91, v91\n" \
    "s_waitcnt lgkmcnt(4)\n" \
    "v_mfma_f32_16x16x32_bf16 v[82:85], v[38:41], v[22:25], v[130:133]\n" \
    "v_maximum3_f32 v144, v72, v92, v92\n" \
    "v_maximum3_f32 v145, v73, v93, v93\n" \
    "ds_read_b64_tr_b16 v[106:107], v157 offset:384\n" \
    "ds_read_b64_tr_b16 v[108:109], v157 offset:512\n" \
    "v_maximum3_f32 v142, v74, v94, v142\n" \
    "v_maximum3_f32 v143, v75, v95, v143\n" \
    "v_maximum3_f32 v144, v76, v96, v144\n" \
    "v_maximum3_f32 v145, v77, v97, v145\n" \
    "ds_read_b64_tr_b16 v[102:103], v156 offset:256\n" \
    "ds_read_b64_tr_b16 v[104:105], v156 offset:384\n" \
    "ds_read_b64_tr_b16 v[98:99], v155 offset:128\n" \
    "ds_read_b64_tr_b16 v[100:101], v155 offset:256\n" \
    "ds_read_b64_tr_b16 v[150:151], v154 offset:0\n" \
    "ds_read_b64_tr_b16 v[152:153], v154 offset:128\n" \
    "s_waitcnt lgkmcnt(8)\n" \
    "Lend_%=:\n" \
    "v_maximum3_f32 v148, v142, v143, v144\n" \
    "v_maximum3_f32 v148, v148, v145, v145\n" \
    "v_mov_b32 v149, v148\n" \
    "v_maximum3_f32 v138, v62, v82, v82\n" \
    "v_maximum3_f32 v139, v63, v83, v83\n" \
    "v_maximum3_f32 v140, v64, v84, v84\n" \
    "v_maximum3_f32 v141, v65, v85, v85\n" \
    "v_permlane16_swap_b32 v148, v149\n" \
    "v_maximum3_f32 v138, v66, v86, v138\n" \
    "v_maximum3_f32 v139, v67, v87, v139\n" \
    "v_maximum3_f32 v148, v148, v149, v149\n" \
    "v_maximum3_f32 v140, v68, v88, v140\n" \
    "v_maximum3_f32 v141, v69, v89, v141\n" \
    "v_mul_f32 v148, 0x3ea52b5b, v148\n" \
    "v_maximum3_f32 v146, v138, v139, v140\n" \
    "v_maximum3_f32 v146, v146, v141, v141\n" \
    "v_mov_b32 v147, v146\n" \
    "v_fma_f32 v142, v142, %[sc], -v148\n" \
    "v_fma_f32 v143, v143, %[sc], -v148\n" \
    "v_fma_f32 v144, v144, %[sc], -v148\n" \
    "v_fma_f32 v145, v145, %[sc], -v148\n" \
    "v_permlane16_swap_b32 v146, v147\n" \
    "v_exp_f32 v142, v142\n" \
    "v_exp_f32 v143, v143\n" \
    "v_maximum3_f32 v146, v146, v147, v147\n" \
    "v_exp_f32 v144, v144\n" \
    "v_exp_f32 v145, v145\n" \
    "v_mul_f32 v146, 0x3ea52b5b, v146\n" \
    "v_add_f32 v148, v142, v143\n" \
    "v_add_f32 v149, v144, v145\n" \
    "v_fma_f32 v138, v138, %[sc], -v146\n" \
    "v_fma_f32 v139, v139, %[sc], -v146\n" \
    "v_fma_f32 v140, v140, %[sc], -v146\n" \
    "v_fma_f32 v141, v141, %[sc], -v146\n" \
    "v_add_f32 v148, v148, v149\n" \
    "v_mov_b32 v149, v148\n" \
    "v_exp_f32 v138, v138\n" \
    "v_exp_f32 v139, v139\n" \
    "v_permlane16_swap_b32 v148, v149\n" \
    "v_exp_f32 v140, v140\n" \
    "v_add_f32 v148, v148, v149\n" \
    "v_exp_f32 v141, v141\n" \
    "v_rcp_f32 v148, v148\n" \
    "v_add_f32 v146, v138, v139\n" \
    "v_add_f32 v147, v140, v141\n" \
    "v_add_f32 v146, v146, v147\n" \
    "v_mov_b32 v147, v146\n" \
    "v_mul_f32 v142, v142, v148\n" \
    "v_mul_f32 v143, v143, v148\n" \
    "v_permlane16_swap_b32 v146, v147\n" \
    "v_mul_f32 v144, v144, v148\n" \
    "v_mul_f32 v145, v145, v148\n" \
    "v_add_f32 v146, v146, v147\n" \
    "v_cvt_pk_bf16_f32 v142, v142, v143\n" \
    "v_cvt_pk_bf16_f32 v143, v144, v145\n" \
    "v_rcp_f32 v146, v146\n" \
    "v_bitop3_b32 v34, v142, v122, v123 bitop3:0x80\n" \
    "v_bitop3_b32 v35, v143, v122, v124 bitop3:0x80\n" \
    "v_bitop3_b32 v36, v142, v122, v123 bitop3:0x40\n" \
    "v_bitop3_b32 v37, v143, v122, v124 bitop3:0x40\n" \
    "v_mul_f32 v138, v138, v146\n" \
    "v_mul_f32 v139, v139, v146\n" \
    "v_mfma_f32_16x16x32_bf16 v[74:77], v[118:121], v[34:37], 0\n" \
    "s_waitcnt lgkmcnt(6)\n" \
    "v_mfma_f32_16x16x32_bf16 v[94:97], v[106:109], v[34:37], 0\n" \
    "v_bitop3_b32 v30, v142, v122, v123 bitop3:0x20\n" \
    "v_bitop3_b32 v31, v143, v122, v124 bitop3:0x20\n" \
    "v_bitop3_b32 v32, v142, v122, v123 bitop3:0x10\n" \
    "v_bitop3_b32 v33, v143, v122, v124 bitop3:0x10\n" \
    "v_mul_f32 v140, v140, v146\n" \
    "v_mul_f32 v141, v141, v146\n" \
    "v_cvt_pk_bf16_f32 v138, v138, v139\n" \
    "v_cvt_pk_bf16_f32 v139, v140, v141\n" \
    "v_mfma_f32_16x16x32_bf16 v[70:73], v[116:119], v[30:33], 0\n" \
    "s_waitcnt lgkmcnt(4)\n" \
    "v_mfma_f32_16x16x32_bf16 v[94:97], v[102:105], v[30:33], v[94:97]\n" \
    "v_bitop3_b32 v26, v138, v122, v123 bitop3:0x80\n" \
    "v_bitop3_b32 v27, v139, v122, v124 bitop3:0x80\n" \
    "v_bitop3_b32 v28, v138, v122, v123 bitop3:0x40\n" \
    "v_bitop3_b32 v29, v139, v122, v124 bitop3:0x40\n" \
    "v_add_u32 v158, 40960, %[oo]\n" \
    "s_nop 0\n" \
    "v_mfma_f32_16x16x32_bf16 v[66:69], v[114:117], v[26:29], 0\n" \
    "s_waitcnt lgkmcnt(2)\n" \
    "v_mfma_f32_16x16x32_bf16 v[94:97], v[98:101], v[26:29], v[94:97]\n" \
    "v_bitop3_b32 v22, v138, v122, v123 bitop3:0x20\n" \
    "v_bitop3_b32 v23, v139, v122, v124 bitop3:0x20\n" \
    "v_bitop3_b32 v24, v138, v122, v123 bitop3:0x10\n" \
    "v_bitop3_b32 v25, v139, v122, v124 bitop3:0x10\n" \
    "s_nop 1\n" \
    "v_mfma_f32_16x16x32_bf16 v[62:65], v[112:115], v[22:25], 0\n" \
    "s_waitcnt lgkmcnt(0)\n" \
    "v_mfma_f32_16x16x32_bf16 v[94:97], v[150:153], v[22:25], v[94:97]\n" \
    "v_readfirstlane_b32 %[tmp], v159\n" \
    "s_cmp_lg_u32 %[tmp], 0\n" \
    "s_cbranch_scc1 Lse_%=\n" \
    "s_mov_b32 %[cnt], 0\n" \
    "Lses_%=:\n" \
    "ds_read_b32 v159, %[zr] offset:1332\n" \
    "s_add_u32 %[cnt], %[cnt], 1\n" \
    "s_waitcnt lgkmcnt(0)\n" \
    "v_readfirstlane_b32 %[tmp], v159\n" \
    "s_cmp_ge_u32 %[cnt], %[cap]\n" \
    "s_cbranch_scc1 Lse_%=\n" \
    "s_cmp_eq_u32 %[tmp], 0\n" \
    "s_cbranch_scc1 Lses_%=\n" \
    "Lse_%=:\n" \
    "v_cvt_pk_bf16_f32 v52, v74, v75\n" \
    "v_cvt_pk_bf16_f32 v53, v76, v77\n" \
    "v_cvt_pk_bf16_f32 v50, v70, v71\n" \
    "v_cvt_pk_bf16_f32 v51, v72, v73\n" \
    "v_cvt_pk_bf16_f32 v44, v66, v67\n" \
    "v_cvt_pk_bf16_f32 v45, v68, v69\n" \
    "v_permlane16_swap_b32 v50, v52\n" \
    "v_permlane16_swap_b32 v51, v53\n" \
    "v_cvt_pk_bf16_f32 v42, v62, v63\n" \
    "v_cvt_pk_bf16_f32 v43, v64, v65\n" \
    "global_store_dwordx4 v158, v[50:53], %[wsp] sc1\n" \
    "v_cvt_pk_bf16_f32 v94, v94, v95\n" \
    "v_cvt_pk_bf16_f32 v95, v96, v97\n" \
    "v_permlane16_swap_b32 v42, v44\n" \
    "v_permlane16_swap_b32 v43, v45\n" \
    "global_store_dwordx4 %[oo], v[42:45], %[wsp] sc1\n" \
    "global_store_dwordx2 %[ox], v[94:95], %[wsp] sc1\n" \
    "s_nop 1\n"
#define G_CLOB "v22", "v23", "v24", "v25", "v26", "v27", "v28", "v29", "v30", "v31", "v32", "v33", "v34", "v35", "v36", "v37", "v38", "v39", "v40", "v41", "v42", "v43", "v44", "v45", "v46", "v47", "v48", "v49", "v50", "v51", "v52", "v53", "v54", "v55", "v56", "v57", "v58", "v59", "v60", "v61", "v62", "v63", "v64", "v65", "v66", "v67", "v68", "v69", "v70", "v71", "v72", "v73", "v74", "v75", "v76", "v77", "v78", "v79", "v80", "v81", "v82", "v83", "v84", "v85", "v86", "v87", "v88", "v89", "v90", "v91", "v92", "v93", "v94", "v95", "v96", "v97", "v98", "v99", "v100", "v101", "v102", "v103", "v104", "v105", "v106", "v107", "v108", "v109", "v110", "v111", "v112", "v113", "v114", "v115", "v116", "v117", "v118", "v119", "v120", "v121", "v122", "v123", "v124", "v125", "v126", "v127", "v128", "v129", "v130", "v131", "v132", "v133", "v134", "v135", "v136", "v137", "v138", "v139", "v140", "v141", "v142", "v143", "v144", "v145", "v146", "v147", "v148", "v149", "v150", "v151", "v152", "v153", "v154", "v155", "v156", "v157", "v158", "v159", "v160", "v161", "s60", "s61", "s62", "s63", "s64", "s65", "s66", "s67", "s68", "s69", "s70", "s71", "vcc", "scc", "memory"
#define P_ASM \
    "s_mov_b32 %[cnt], 0\n" \
    "La_%=:\n" \
    "ds_read_b32 v157, %[ctl] offset:56\n" \
    "s_add_u32 %[cnt], %[cnt], 1\n" \
    "s_waitcnt lgkmcnt(0)\n" \
    "v_readfirstlane_b32 %[tmp], v157\n" \
    "s_cmp_ge_u32 %[tmp], %[won]\n" \
    "s_cbranch_scc1 Lax_%=\n" \
    "s_sleep 2\n" \
    "s_cmp_lt_u32 %[cnt], %[cap]\n" \
    "s_cbranch_scc1 La_%=\n" \
    "Lax_%=:\n" \
    "ds_read_b64_tr_b16 v[16:17], %[pc] offset:0\n" \
    "ds_read_b64_tr_b16 v[18:19], %[pd] offset:0\n" \
    "ds_read_b64_tr_b16 v[20:21], %[pc] offset:32\n" \
    "ds_read_b64_tr_b16 v[22:23], %[pd] offset:32\n" \
    "ds_read_b64_tr_b16 v[24:25], %[pc] offset:64\n" \
    "ds_read_b64_tr_b16 v[26:27], %[pd] offset:64\n" \
    "ds_read_b64_tr_b16 v[28:29], %[pc] offset:96\n" \
    "ds_read_b64_tr_b16 v[30:31], %[pd] offset:96\n" \
    "ds_read_b64_tr_b16 v[32:33], %[pc] offset:128\n" \
    "ds_read_b64_tr_b16 v[34:35], %[pd] offset:128\n" \
    "ds_read_b64_tr_b16 v[36:37], %[pc] offset:5120\n" \
    "ds_read_b64_tr_b16 v[38:39], %[pd] offset:5120\n" \
    "ds_read_b64_tr_b16 v[40:41], %[pc] offset:5152\n" \
    "ds_read_b64_tr_b16 v[42:43], %[pd] offset:5152\n" \
    "ds_read_b64_tr_b16 v[44:45], %[pc] offset:5184\n" \
    "ds_read_b64_tr_b16 v[46:47], %[pd] offset:5184\n" \
    "ds_read_b64_tr_b16 v[48:49], %[pc] offset:5216\n" \
    "ds_read_b64_tr_b16 v[50:51], %[pd] offset:5216\n" \
    "ds_read_b64_tr_b16 v[52:53], %[pc] offset:5248\n" \
    "ds_read_b64_tr_b16 v[54:55], %[pd] offset:5248\n" \
    "ds_read_b64_tr_b16 v[56:57], %[pc] offset:10240\n" \
    "ds_read_b64_tr_b16 v[58:59], %[pd] offset:10240\n" \
    "ds_read_b64_tr_b16 v[60:61], %[pc] offset:10272\n" \
    "ds_read_b64_tr_b16 v[62:63], %[pd] offset:10272\n" \
    "ds_read_b64_tr_b16 v[64:65], %[pc] offset:10304\n" \
    "ds_read_b64_tr_b16 v[66:67], %[pd] offset:10304\n" \
    "ds_read_b64_tr_b16 v[68:69], %[pc] offset:10336\n" \
    "ds_read_b64_tr_b16 v[70:71], %[pd] offset:10336\n" \
    "ds_read_b64_tr_b16 v[72:73], %[pc] offset:10368\n" \
    "ds_read_b64_tr_b16 v[74:75], %[pd] offset:10368\n" \
    "ds_read_b64_tr_b16 v[76:77], %[pc] offset:15360\n" \
    "ds_read_b64_tr_b16 v[78:79], %[pd] offset:15360\n" \
    "ds_read_b64_tr_b16 v[80:81], %[pc] offset:15392\n" \
    "ds_read_b64_tr_b16 v[82:83], %[pd] offset:15392\n" \
    "ds_read_b64_tr_b16 v[84:85], %[pc] offset:15424\n" \
    "ds_read_b64_tr_b16 v[86:87], %[pd] offset:15424\n" \
    "ds_read_b64_tr_b16 v[88:89], %[pc] offset:15456\n" \
    "ds_read_b64_tr_b16 v[90:91], %[pd] offset:15456\n" \
    "ds_read_b64_tr_b16 v[92:93], %[pc] offset:15488\n" \
    "ds_read_b64_tr_b16 v[94:95], %[pd] offset:15488\n" \
    "ds_read_b64_tr_b16 v[96:97], %[pc] offset:20480\n" \
    "ds_read_b64_tr_b16 v[98:99], %[pd] offset:20480\n" \
    "ds_read_b64_tr_b16 v[100:101], %[pc] offset:20512\n" \
    "ds_read_b64_tr_b16 v[102:103], %[pd] offset:20512\n" \
    "ds_read_b64_tr_b16 v[104:105], %[pc] offset:20544\n" \
    "ds_read_b64_tr_b16 v[106:107], %[pd] offset:20544\n" \
    "ds_read_b64_tr_b16 v[108:109], %[pc] offset:20576\n" \
    "ds_read_b64_tr_b16 v[110:111], %[pd] offset:20576\n" \
    "ds_read_b64_tr_b16 v[112:113], %[pc] offset:20608\n" \
    "ds_read_b64_tr_b16 v[114:115], %[pd] offset:20608\n" \
    "v_mov_b32 v157, 1\n" \
    "s_mov_b64 exec, 1\n" \
    "ds_add_u32 %[ctl], v157 offset:96\n" \
    "s_mov_b64 exec, -1\n" \
    "s_mov_b32 %[cnt], 0\n" \
    "Lb_%=:\n" \
    "ds_read_b32 v157, %[ctl] offset:100\n" \
    "s_add_u32 %[cnt], %[cnt], 1\n" \
    "s_waitcnt lgkmcnt(0)\n" \
    "v_readfirstlane_b32 %[tmp], v157\n" \
    "s_cmp_ge_u32 %[tmp], 1\n" \
    "s_cbranch_scc1 Lbx_%=\n" \
    "s_sleep 1\n" \
    "s_cmp_lt_u32 %[cnt], %[cap]\n" \
    "s_cbranch_scc1 Lb_%=\n" \
    "Lbx_%=:\n" \
    "s_mov_b32 %[cnt], 0\n" \
    "Lc_%=:\n" \
    "ds_read_b32 v157, %[ctl] offset:48\n" \
    "s_add_u32 %[cnt], %[cnt], 1\n" \
    "s_waitcnt lgkmcnt(0)\n" \
    "v_readfirstlane_b32 %[tmp], v157\n" \
    "s_cmp_ge_u32 %[tmp], %[ost]\n" \
    "s_cbranch_scc1 Lcx_%=\n" \
    "s_sleep 1\n" \
    "s_cmp_lt_u32 %[cnt], %[cap]\n" \
    "s_cbranch_scc1 Lc_%=\n" \
    "Lcx_%=:\n" \
    "s_mov_b32 %[cnt], 0\n" \
    "Ld_%=:\n" \
    "global_load_dword v156, %[wat], %[wsp] sc1\n" \
    "s_add_u32 %[cnt], %[cnt], 1\n" \
    "s_waitcnt vmcnt(0)\n" \
    "v_cmp_eq_u32 vcc, %[so], v156\n" \
    "s_mov_b32 %[wm], vcc_lo\n" \
    "s_and_b32 %[tmp], %[wm], 3\n" \
    "s_cbranch_scc0 Ldx_%=\n" \
    "s_sleep 4\n" \
    "s_cmp_lt_u32 %[cnt], %[cap]\n" \
    "s_cbranch_scc1 Ld_%=\n" \
    "Ldx_%=:\n" \
    "s_setprio 1\n" \
    "global_load_dwordx4 v[116:119], %[ofo], %[wsp] offset:0 sc1\n" \
    "global_load_dwordx4 v[120:123], %[ofo], %[wsp] offset:1024 sc1\n" \
    "global_load_dwordx4 v[124:127], %[ofo], %[wsp] offset:2048 sc1\n" \
    "global_load_dwordx4 v[128:131], %[ofo], %[wsp] offset:3072 sc1\n" \
    "global_load_dwordx2 v[132:133], %[of4], %[wsp] sc1\n" \
    "global_load_dwordx2 v[134:135], %[of4], %[wsp] offset:128 sc1\n" \
    "ds_read_b32 v157, %[ctl] offset:96\n" \
    "s_mov_b32 %[ah], 1\n" \
    "s_and_b32 %[tmp], %[wm], 12\n" \
    "s_cbranch_scc1 Ln_%=\n" \
    "s_waitcnt vmcnt(5)\n" \
    "v_mfma_f32_16x16x32_bf16 v[136:139], v[16:19], v[116:119], 0\n" \
    "v_mfma_f32_16x16x32_bf16 v[140:143], v[20:23], v[116:119], 0\n" \
    "v_mfma_f32_16x16x32_bf16 v[144:147], v[24:27], v[116:119], 0\n" \
    "v_mfma_f32_16x16x32_bf16 v[148:151], v[28:31], v[116:119], 0\n" \
    "v_mfma_f32_16x16x32_bf16 v[152:155], v[32:35], v[116:119], 0\n" \
    "global_load_dwordx4 v[116:119], %[ofo], %[ws2] offset:0 sc1\n" \
    "s_waitcnt vmcnt(5)\n" \
    "v_mfma_f32_16x16x32_bf16 v[136:139], v[36:39], v[120:123], v[136:139]\n" \
    "v_mfma_f32_16x16x32_bf16 v[140:143], v[40:43], v[120:123], v[140:143]\n" \
    "v_mfma_f32_16x16x32_bf16 v[144:147], v[44:47], v[120:123], v[144:147]\n" \
    "v_mfma_f32_16x16x32_bf16 v[148:151], v[48:51], v[120:123], v[148:151]\n" \
    "v_mfma_f32_16x16x32_bf16 v[152:155], v[52:55], v[120:123], v[152:155]\n" \
    "global_load_dwordx4 v[120:123], %[ofo], %[ws2] offset:1024 sc1\n" \
    "s_waitcnt vmcnt(5)\n" \
    "v_mfma_f32_16x16x32_bf16 v[136:139], v[56:59], v[124:127], v[136:139]\n" \
    "v_mfma_f32_16x16x32_bf16 v[140:143], v[60:63], v[124:127], v[140:143]\n" \
    "v_mfma_f32_16x16x32_bf16 v[144:147], v[64:67], v[124:127], v[144:147]\n" \
    "v_mfma_f32_16x16x32_bf16 v[148:151], v[68:71], v[124:127], v[148:151]\n" \
    "v_mfma_f32_16x16x32_bf16 v[152:155], v[72:75], v[124:127], v[152:155]\n" \
    "global_load_dwordx4 v[124:127], %[ofo], %[ws2] offset:2048 sc1\n" \
    "s_waitcnt vmcnt(5)\n" \
    "v_mfma_f32_16x16x32_bf16 v[136:139], v[76:79], v[128:131], v[136:139]\n" \
    "v_mfma_f32_16x16x32_bf16 v[140:143], v[80:83], v[128:131], v[140:143]\n" \
    "v_mfma_f32_16x16x32_bf16 v[144:147], v[84:87], v[128:131], v[144:147]\n" \
    "v_mfma_f32_16x16x32_bf16 v[148:151], v[88:91], v[128:131], v[148:151]\n" \
    "v_mfma_f32_16x16x32_bf16 v[152:155], v[92:95], v[128:131], v[152:155]\n" \
    "global_load_dwordx4 v[128:131], %[ofo], %[ws2] offset:3072 sc1\n" \
    "s_waitcnt vmcnt(4)\n" \
    "v_mfma_f32_16x16x32_bf16 v[136:139], v[96:99], v[132:135], v[136:139]\n" \
    "v_mfma_f32_16x16x32_bf16 v[140:143], v[100:103], v[132:135], v[140:143]\n" \
    "v_mfma_f32_16x16x32_bf16 v[144:147], v[104:107], v[132:135], v[144:147]\n" \
    "v_mfma_f32_16x16x32_bf16 v[148:151], v[108:111], v[132:135], v[148:151]\n" \
    "v_mfma_f32_16x16x32_bf16 v[152:155], v[112:115], v[132:135], v[152:155]\n" \
    "global_load_dwordx2 v[132:133], %[of4], %[ws2] sc1\n" \
    "global_load_dwordx2 v[134:135], %[of4], %[ws2] offset:128 sc1\n" \
    "s_branch Lj_%=\n" \
    "Ln_%=:\n" \
    "s_waitcnt vmcnt(5)\n" \
    "global_load_dword v156, %[wat], %[wsp] sc1\n" \
    "s_waitcnt vmcnt(6)\n" \
    "v_mfma_f32_16x16x32_bf16 v[136:139], v[16:19], v[116:119], 0\n" \
    "v_mfma_f32_16x16x32_bf16 v[140:143], v[20:23], v[116:119], 0\n" \
    "v_mfma_f32_16x16x32_bf16 v[144:147], v[24:27], v[116:119], 0\n" \
    "v_mfma_f32_16x16x32_bf16 v[148:151], v[28:31], v[116:119], 0\n" \
    "v_mfma_f32_16x16x32_bf16 v[152:155], v[32:35], v[116:119], 0\n" \
    "s_waitcnt vmcnt(5)\n" \
    "v_mfma_f32_16x16x32_bf16 v[136:139], v[36:39], v[120:123], v[136:139]\n" \
    "v_mfma_f32_16x16x32_bf16 v[140:143], v[40:43], v[120:123], v[140:143]\n" \
    "v_mfma_f32_16x16x32_bf16 v[144:147], v[44:47], v[120:123], v[144:147]\n" \
    "v_mfma_f32_16x16x32_bf16 v[148:151], v[48:51], v[120:123], v[148:151]\n" \
    "v_mfma_f32_16x16x32_bf16 v[152:155], v[52:55], v[120:123], v[152:155]\n" \
    "s_waitcnt vmcnt(4)\n" \
    "v_mfma_f32_16x16x32_bf16 v[136:139], v[56:59], v[124:127], v[136:139]\n" \
    "v_mfma_f32_16x16x32_bf16 v[140:143], v[60:63], v[124:127], v[140:143]\n" \
    "v_mfma_f32_16x16x32_bf16 v[144:147], v[64:67], v[124:127], v[144:147]\n" \
    "v_mfma_f32_16x16x32_bf16 v[148:151], v[68:71], v[124:127], v[148:151]\n" \
    "v_mfma_f32_16x16x32_bf16 v[152:155], v[72:75], v[124:127], v[152:155]\n" \
    "s_waitcnt vmcnt(3)\n" \
    "v_mfma_f32_16x16x32_bf16 v[136:139], v[76:79], v[128:131], v[136:139]\n" \
    "v_mfma_f32_16x16x32_bf16 v[140:143], v[80:83], v[128:131], v[140:143]\n" \
    "v_mfma_f32_16x16x32_bf16 v[144:147], v[84:87], v[128:131], v[144:147]\n" \
    "v_mfma_f32_16x16x32_bf16 v[148:151], v[88:91], v[128:131], v[148:151]\n" \
    "v_mfma_f32_16x16x32_bf16 v[152:155], v[92:95], v[128:131], v[152:155]\n" \
    "s_waitcnt vmcnt(1)\n" \
    "v_mfma_f32_16x16x32_bf16 v[136:139], v[96:99], v[132:135], v[136:139]\n" \
    "v_mfma_f32_16x16x32_bf16 v[140:143], v[100:103], v[132:135], v[140:143]\n" \
    "v_mfma_f32_16x16x32_bf16 v[144:147], v[104:107], v[132:135], v[144:147]\n" \
    "v_mfma_f32_16x16x32_bf16 v[148:151], v[108:111], v[132:135], v[148:151]\n" \
    "v_mfma_f32_16x16x32_bf16 v[152:155], v[112:115], v[132:135], v[152:155]\n" \
    "s_waitcnt vmcnt(0)\n" \
    "v_cmp_eq_u32 vcc, %[so], v156\n" \
    "s_mov_b32 %[wm], vcc_lo\n" \
    "s_mov_b32 %[ah], 0\n" \
    "s_and_b32 %[tmp], %[wm], 12\n" \
    "s_cbranch_scc1 Lj_%=\n" \
    "s_mov_b32 %[ah], 1\n" \
    "global_load_dwordx4 v[116:119], %[ofo], %[ws2] offset:0 sc1\n" \
    "global_load_dwordx4 v[120:123], %[ofo], %[ws2] offset:1024 sc1\n" \
    "global_load_dwordx4 v[124:127], %[ofo], %[ws2] offset:2048 sc1\n" \
    "global_load_dwordx4 v[128:131], %[ofo], %[ws2] offset:3072 sc1\n" \
    "global_load_dwordx2 v[132:133], %[of4], %[ws2] sc1\n" \
    "global_load_dwordx2 v[134:135], %[of4], %[ws2] offset:128 sc1\n" \
    "Lj_%=:\n" \
    "s_waitcnt lgkmcnt(0)\n" \
    "v_readfirstlane_b32 %[tmp], v157\n" \
    "s_cmp_ge_u32 %[tmp], 8\n" \
    "s_cbranch_scc1 Lex_%=\n" \
    "s_mov_b32 %[cnt], 0\n" \
    "Le_%=:\n" \
    "ds_read_b32 v157, %[ctl] offset:96\n" \
    "s_add_u32 %[cnt], %[cnt], 1\n" \
    "s_waitcnt lgkmcnt(0)\n" \
    "v_readfirstlane_b32 %[tmp], v157\n" \
    "s_cmp_ge_u32 %[tmp], 8\n" \
    "s_cbranch_scc1 Lex_%=\n" \
    "s_sleep 1\n" \
    "s_cmp_lt_u32 %[cnt], %[cap]\n" \
    "s_cbranch_scc1 Le_%=\n" \
    "Lex_%=:\n" \
    "s_nop 3\n" \
    "v_cmp_u_f32 vcc, v136, v136\n" \
    "s_cbranch_vccz Lfx_%=\n" \
    "s_waitcnt vmcnt(0)\n" \
    "s_mov_b32 %[ah], 0\n" \
    "s_mov_b32 %[cnt], 0\n" \
    "Lf_%=:\n" \
    "global_load_dwordx4 v[116:119], %[ofo], %[wsp] offset:0 sc1\n" \
    "global_load_dwordx4 v[120:123], %[ofo], %[wsp] offset:1024 sc1\n" \
    "global_load_dwordx4 v[124:127], %[ofo], %[wsp] offset:2048 sc1\n" \
    "global_load_dwordx4 v[128:131], %[ofo], %[wsp] offset:3072 sc1\n" \
    "global_load_dwordx2 v[132:133], %[of4], %[wsp] sc1\n" \
    "global_load_dwordx2 v[134:135], %[of4], %[wsp] offset:128 sc1\n" \
    "s_add_u32 %[cnt], %[cnt], 1\n" \
    "s_waitcnt vmcnt(0)\n" \
    "v_xor_b32 v157, %[so], v116\n" \
    "v_xor_b32 v158, %[so], v117\n" \
    "v_min_u32 v157, v157, v158\n" \
    "v_xor_b32 v158, %[so], v118\n" \
    "v_min_u32 v157, v157, v158\n" \
    "v_xor_b32 v158, %[so], v119\n" \
    "v_min_u32 v157, v157, v158\n" \
    "v_xor_b32 v158, %[so], v120\n" \
    "v_min_u32 v157, v157, v158\n" \
    "v_xor_b32 v158, %[so], v121\n" \
    "v_min_u32 v157, v157, v158\n" \
    "v_xor_b32 v158, %[so], v122\n" \
    "v_min_u32 v157, v157, v158\n" \
    "v_xor_b32 v158, %[so], v123\n" \
    "v_min_u32 v157, v157, v158\n" \
    "v_xor_b32 v158, %[so], v124\n" \
    "v_min_u32 v157, v157, v158\n" \
    "v_xor_b32 v158, %[so], v125\n" \
    "v_min_u32 v157, v157, v158\n" \
    "v_xor_b32 v158, %[so], v126\n" \
    "v_min_u32 v157, v157, v158\n" \
    "v_xor_b32 v158, %[so], v127\n" \
    "v_min_u32 v157, v157, v158\n" \
    "v_xor_b32 v158, %[so], v128\n" \
    "v_min_u32 v157, v157, v158\n" \
    "v_xor_b32 v158, %[so], v129\n" \
    "v_min_u32 v157, v157, v158\n" \
    "v_xor_b32 v158, %[so], v130\n" \
    "v_min_u32 v157, v157, v158\n" \
    "v_xor_b32 v158, %[so], v131\n" \
    "v_min_u32 v157, v157, v158\n" \
    "v_xor_b32 v158, %[so], v132\n" \
    "v_min_u32 v157, v157, v158\n" \
    "v_xor_b32 v158, %[so], v133\n" \
    "v_min_u32 v157, v157, v158\n" \
    "v_xor_b32 v158, %[so], v134\n" \
    "v_min_u32 v157, v157, v158\n" \
    "v_xor_b32 v158, %[so], v135\n" \
    "v_min_u32 v157, v157, v158\n" \
    "v_cmp_eq_u32 vcc, 0, v157\n" \
    "s_cmp_lt_u32 %[cnt], %[cap]\n" \
    "s_cbranch_vccz Lfy_%=\n" \
    "s_cbranch_scc1 Lf_%=\n" \
    "Lfy_%=:\n" \
    "s_waitcnt vmcnt(0)\n" \
    "v_mfma_f32_16x16x32_bf16 v[136:139], v[16:19], v[116:119], 0\n" \
    "v_mfma_f32_16x16x32_bf16 v[140:143], v[20:23], v[116:119], 0\n" \
    "v_mfma_f32_16x16x32_bf16 v[144:147], v[24:27], v[116:119], 0\n" \
    "v_mfma_f32_16x16x32_bf16 v[148:151], v[28:31], v[116:119], 0\n" \
    "v_mfma_f32_16x16x32_bf16 v[152:155], v[32:35], v[116:119], 0\n" \
    "s_waitcnt vmcnt(0)\n" \
    "v_mfma_f32_16x16x32_bf16 v[136:139], v[36:39], v[120:123], v[136:139]\n" \
    "v_mfma_f32_16x16x32_bf16 v[140:143], v[40:43], v[120:123], v[140:143]\n" \
    "v_mfma_f32_16x16x32_bf16 v[144:147], v[44:47], v[120:123], v[144:147]\n" \
    "v_mfma_f32_16x16x32_bf16 v[148:151], v[48:51], v[120:123], v[148:151]\n" \
    "v_mfma_f32_16x16x32_bf16 v[152:155], v[52:55], v[120:123], v[152:155]\n" \
    "s_waitcnt vmcnt(0)\n" \
    "v_mfma_f32_16x16x32_bf16 v[136:139], v[56:59], v[124:127], v[136:139]\n" \
    "v_mfma_f32_16x16x32_bf16 v[140:143], v[60:63], v[124:127], v[140:143]\n" \
    "v_mfma_f32_16x16x32_bf16 v[144:147], v[64:67], v[124:127], v[144:147]\n" \
    "v_mfma_f32_16x16x32_bf16 v[148:151], v[68:71], v[124:127], v[148:151]\n" \
    "v_mfma_f32_16x16x32_bf16 v[152:155], v[72:75], v[124:127], v[152:155]\n" \
    "s_waitcnt vmcnt(0)\n" \
    "v_mfma_f32_16x16x32_bf16 v[136:139], v[76:79], v[128:131], v[136:139]\n" \
    "v_mfma_f32_16x16x32_bf16 v[140:143], v[80:83], v[128:131], v[140:143]\n" \
    "v_mfma_f32_16x16x32_bf16 v[144:147], v[84:87], v[128:131], v[144:147]\n" \
    "v_mfma_f32_16x16x32_bf16 v[148:151], v[88:91], v[128:131], v[148:151]\n" \
    "v_mfma_f32_16x16x32_bf16 v[152:155], v[92:95], v[128:131], v[152:155]\n" \
    "s_waitcnt vmcnt(0)\n" \
    "v_mfma_f32_16x16x32_bf16 v[136:139], v[96:99], v[132:135], v[136:139]\n" \
    "v_mfma_f32_16x16x32_bf16 v[140:143], v[100:103], v[132:135], v[140:143]\n" \
    "v_mfma_f32_16x16x32_bf16 v[144:147], v[104:107], v[132:135], v[144:147]\n" \
    "v_mfma_f32_16x16x32_bf16 v[148:151], v[108:111], v[132:135], v[148:151]\n" \
    "v_mfma_f32_16x16x32_bf16 v[152:155], v[112:115], v[132:135], v[152:155]\n" \
    "s_nop 3\n" \
    "Lfx_%=:\n" \
    "v_mov_b32 v157, 1\n" \
    "ds_write_b128 %[xa0], v[136:139] offset:0\n" \
    "ds_write_b128 %[xa0], v[140:143] offset:1024\n" \
    "ds_write_b128 %[xa0], v[144:147] offset:2048\n" \
    "ds_write_b128 %[xa0], v[148:151] offset:3072\n" \
    "ds_write_b128 %[xa0], v[152:155] offset:4096\n" \
    "ds_write_b32 %[pf], v157 offset:0\n" \
    "s_cmp_lg_u32 %[ah], 0\n" \
    "s_cbranch_scc1 Lk_%=\n" \
    "s_and_b32 %[tmp], %[wm], 12\n" \
    "s_cbranch_scc0 Lhx_%=\n" \
    "s_mov_b32 %[cnt], 0\n" \
    "Lh_%=:\n" \
    "global_load_dword v156, %[wat], %[wsp] sc1\n" \
    "s_add_u32 %[cnt], %[cnt], 1\n" \
    "s_waitcnt vmcnt(0)\n" \
    "v_cmp_eq_u32 vcc, %[so], v156\n" \
    "s_mov_b32 %[wm], vcc_lo\n" \
    "s_and_b32 %[tmp], %[wm], 12\n" \
    "s_cbranch_scc0 Lhx_%=\n" \
    "s_sleep 4\n" \
    "s_cmp_lt_u32 %[cnt], %[cap]\n" \
    "s_cbranch_scc1 Lh_%=\n" \
    "Lhx_%=:\n" \
    "global_load_dwordx4 v[116:119], %[ofo], %[ws2] offset:0 sc1\n" \
    "global_load_dwordx4 v[120:123], %[ofo], %[ws2] offset:1024 sc1\n" \
    "global_load_dwordx4 v[124:127], %[ofo], %[ws2] offset:2048 sc1\n" \
    "global_load_dwordx4 v[128:131], %[ofo], %[ws2] offset:3072 sc1\n" \
    "global_load_dwordx2 v[132:133], %[of4], %[ws2] sc1\n" \
    "global_load_dwordx2 v[134:135], %[of4], %[ws2] offset:128 sc1\n" \
    "Lk_%=:\n" \
    "s_waitcnt vmcnt(5)\n" \
    "v_mfma_f32_16x16x32_bf16 v[136:139], v[16:19], v[116:119], 0\n" \
    "v_mfma_f32_16x16x32_bf16 v[140:143], v[20:23], v[116:119], 0\n" \
    "s_waitcnt vmcnt(4)\n" \
    "v_mfma_f32_16x16x32_bf16 v[136:139], v[36:39], v[120:123], v[136:139]\n" \
    "v_mfma_f32_16x16x32_bf16 v[140:143], v[40:43], v[120:123], v[140:143]\n" \
    "s_waitcnt vmcnt(3)\n" \
    "v_mfma_f32_16x16x32_bf16 v[136:139], v[56:59], v[124:127], v[136:139]\n" \
    "v_mfma_f32_16x16x32_bf16 v[140:143], v[60:63], v[124:127], v[140:143]\n" \
    "s_waitcnt vmcnt(2)\n" \
    "v_mfma_f32_16x16x32_bf16 v[136:139], v[76:79], v[128:131], v[136:139]\n" \
    "v_mfma_f32_16x16x32_bf16 v[140:143], v[80:83], v[128:131], v[140:143]\n" \
    "s_waitcnt vmcnt(0)\n" \
    "v_mfma_f32_16x16x32_bf16 v[136:139], v[96:99], v[132:135], v[136:139]\n" \
    "v_mfma_f32_16x16x32_bf16 v[140:143], v[100:103], v[132:135], v[140:143]\n" \
    "v_mfma_f32_16x16x32_bf16 v[144:147], v[24:27], v[116:119], 0\n" \
    "v_mfma_f32_16x16x32_bf16 v[144:147], v[44:47], v[120:123], v[144:147]\n" \
    "v_mfma_f32_16x16x32_bf16 v[144:147], v[64:67], v[124:127], v[144:147]\n" \
    "v_mfma_f32_16x16x32_bf16 v[144:147], v[84:87], v[128:131], v[144:147]\n" \
    "v_mfma_f32_16x16x32_bf16 v[144:147], v[104:107], v[132:135], v[144:147]\n" \
    "v_mov_b32 v157, 1\n" \
    "v_mfma_f32_16x16x32_bf16 v[148:151], v[28:31], v[116:119], 0\n" \
    "v_mfma_f32_16x16x32_bf16 v[148:151], v[48:51], v[120:123], v[148:151]\n" \
    "ds_write_b128 %[xa1], v[136:139] offset:0\n" \
    "v_mfma_f32_16x16x32_bf16 v[148:151], v[68:71], v[124:127], v[148:151]\n" \
    "v_cmp_u_f32 vcc, v136, v136\n" \
    "v_mfma_f32_16x16x32_bf16 v[148:151], v[88:91], v[128:131], v[148:151]\n" \
    "ds_write_b128 %[xa1], v[140:143] offset:1024\n" \
    "v_mfma_f32_16x16x32_bf16 v[148:151], v[108:111], v[132:135], v[148:151]\n" \
    "v_mfma_f32_16x16x32_bf16 v[152:155], v[32:35], v[116:119], 0\n" \
    "v_mfma_f32_16x16x32_bf16 v[152:155], v[52:55], v[120:123], v[152:155]\n" \
    "v_mfma_f32_16x16x32_bf16 v[152:155], v[72:75], v[124:127], v[152:155]\n" \
    "ds_write_b128 %[xa1], v[144:147] offset:2048\n" \
    "v_mfma_f32_16x16x32_bf16 v[152:155], v[92:95], v[128:131], v[152:155]\n" \
    "v_mfma_f32_16x16x32_bf16 v[152:155], v[112:115], v[132:135], v[152:155]\n" \
    "s_cbranch_vccnz Li_%=\n" \
    "s_nop 3\n" \
    "ds_write_b128 %[xa1], v[148:151] offset:3072\n" \
    "s_nop 2\n" \
    "ds_write_b128 %[xa1], v[152:155] offset:4096\n" \
    "ds_write_b32 %[pf], v157 offset:32\n" \
    "s_branch Lz_%=\n" \
    "Li_%=:\n" \
    "s_waitcnt vmcnt(0)\n" \
    "s_mov_b32 %[ah], 0\n" \
    "s_mov_b32 %[cnt], 0\n" \
    "Li2_%=:\n" \
    "global_load_dwordx4 v[116:119], %[ofo], %[ws2] offset:0 sc1\n" \
    "global_load_dwordx4 v[120:123], %[ofo], %[ws2] offset:1024 sc1\n" \
    "global_load_dwordx4 v[124:127], %[ofo], %[ws2] offset:2048 sc1\n" \
    "global_load_dwordx4 v[128:131], %[ofo], %[ws2] offset:3072 sc1\n" \
    "global_load_dwordx2 v[132:133], %[of4], %[ws2] sc1\n" \
    "global_load_dwordx2 v[134:135], %[of4], %[ws2] offset:128 sc1\n" \
    "s_add_u32 %[cnt], %[cnt], 1\n" \
    "s_waitcnt vmcnt(0)\n" \
    "v_xor_b32 v157, %[so], v116\n" \
    "v_xor_b32 v158, %[so], v117\n" \
    "v_min_u32 v157, v157, v158\n" \
    "v_xor_b32 v158, %[so], v118\n" \
    "v_min_u32 v157, v157, v158\n" \
    "v_xor_b32 v158, %[so], v119\n" \
    "v_min_u32 v157, v157, v158\n" \
    "v_xor_b32 v158, %[so], v120\n" \
    "v_min_u32 v157, v157, v158\n" \
    "v_xor_b32 v158, %[so], v121\n" \
    "v_min_u32 v157, v157, v158\n" \
    "v_xor_b32 v158, %[so], v122\n" \
    "v_min_u32 v157, v157, v158\n" \
    "v_xor_b32 v158, %[so], v123\n" \
    "v_min_u32 v157, v157, v158\n" \
    "v_xor_b32 v158, %[so], v124\n" \
    "v_min_u32 v157, v157, v158\n" \
    "v_xor_b32 v158, %[so], v125\n" \
    "v_min_u32 v157, v157, v158\n" \
    "v_xor_b32 v158, %[so], v126\n" \
    "v_min_u32 v157, v157, v158\n" \
    "v_xor_b32 v158, %[so], v127\n" \
    "v_min_u32 v157, v157, v158\n" \
    "v_xor_b32 v158, %[so], v128\n" \
    "v_min_u32 v157, v157, v158\n" \
    "v_xor_b32 v158, %[so], v129\n" \
    "v_min_u32 v157, v157, v158\n" \
    "v_xor_b32 v158, %[so], v130\n" \
    "v_min_u32 v157, v157, v158\n" \
    "v_xor_b32 v158, %[so], v131\n" \
    "v_min_u32 v157, v157, v158\n" \
    "v_xor_b32 v158, %[so], v132\n" \
    "v_min_u32 v157, v157, v158\n" \
    "v_xor_b32 v158, %[so], v133\n" \
    "v_min_u32 v157, v157, v158\n" \
    "v_xor_b32 v158, %[so], v134\n" \
    "v_min_u32 v157, v157, v158\n" \
    "v_xor_b32 v158, %[so], v135\n" \
    "v_min_u32 v157, v157, v158\n" \
    "v_cmp_eq_u32 vcc, 0, v157\n" \
    "s_cmp_lt_u32 %[cnt], %[cap]\n" \
    "s_cbranch_vccz Li2y_%=\n" \
    "s_cbranch_scc1 Li2_%=\n" \
    "Li2y_%=:\n" \
    "s_waitcnt vmcnt(0)\n" \
    "v_mfma_f32_16x16x32_bf16 v[136:139], v[16:19], v[116:119], 0\n" \
    "v_mfma_f32_16x16x32_bf16 v[140:143], v[20:23], v[116:119], 0\n" \
    "v_mfma_f32_16x16x32_bf16 v[144:147], v[24:27], v[116:119], 0\n" \
    "v_mfma_f32_16x16x32_bf16 v[148:151], v[28:31], v[116:119], 0\n" \
    "v_mfma_f32_16x16x32_bf16 v[152:155], v[32:35], v[116:119], 0\n" \
    "s_waitcnt vmcnt(0)\n" \
    "v_mfma_f32_16x16x32_bf16 v[136:139], v[36:39], v[120:123], v[136:139]\n" \
    "v_mfma_f32_16x16x32_bf16 v[140:143], v[40:43], v[120:123], v[140:143]\n" \
    "v_mfma_f32_16x16x32_bf16 v[144:147], v[44:47], v[120:123], v[144:147]\n" \
    "v_mfma_f32_16x16x32_bf16 v[148:151], v[48:51], v[120:123], v[148:151]\n" \
    "v_mfma_f32_16x16x32_bf16 v[152:155], v[52:55], v[120:123], v[152:155]\n" \
    "s_waitcnt vmcnt(0)\n" \
    "v_mfma_f32_16x16x32_bf16 v[136:139], v[56:59], v[124:127], v[136:139]\n" \
    "v_mfma_f32_16x16x32_bf16 v[140:143], v[60:63], v[124:127], v[140:143]\n" \
    "v_mfma_f32_16x16x32_bf16 v[144:147], v[64:67], v[124:127], v[144:147]\n" \
    "v_mfma_f32_16x16x32_bf16 v[148:151], v[68:71], v[124:127], v[148:151]\n" \
    "v_mfma_f32_16x16x32_bf16 v[152:155], v[72:75], v[124:127], v[152:155]\n" \
    "s_waitcnt vmcnt(0)\n" \
    "v_mfma_f32_16x16x32_bf16 v[136:139], v[76:79], v[128:131], v[136:139]\n" \
    "v_mfma_f32_16x16x32_bf16 v[140:143], v[80:83], v[128:131], v[140:143]\n" \
    "v_mfma_f32_16x16x32_bf16 v[144:147], v[84:87], v[128:131], v[144:147]\n" \
    "v_mfma_f32_16x16x32_bf16 v[148:151], v[88:91], v[128:131], v[148:151]\n" \
    "v_mfma_f32_16x16x32_bf16 v[152:155], v[92:95], v[128:131], v[152:155]\n" \
    "s_waitcnt vmcnt(0)\n" \
    "v_mfma_f32_16x16x32_bf16 v[136:139], v[96:99], v[132:135], v[136:139]\n" \
    "v_mfma_f32_16x16x32_bf16 v[140:143], v[100:103], v[132:135], v[140:143]\n" \
    "v_mfma_f32_16x16x32_bf16 v[144:147], v[104:107], v[132:135], v[144:147]\n" \
    "v_mfma_f32_16x16x32_bf16 v[148:151], v[108:111], v[132:135], v[148:151]\n" \
    "v_mfma_f32_16x16x32_bf16 v[152:155], v[112:115], v[132:135], v[152:155]\n" \
    "s_nop 3\n" \
    "v_mov_b32 v157, 1\n" \
    "s_nop 1\n" \
    "ds_write_b128 %[xa1], v[136:139] offset:0\n" \
    "ds_write_b128 %[xa1], v[140:143] offset:1024\n" \
    "ds_write_b128 %[xa1], v[144:147] offset:2048\n" \
    "ds_write_b128 %[xa1], v[148:151] offset:3072\n" \
    "ds_write_b128 %[xa1], v[152:155] offset:4096\n" \
    "ds_write_b32 %[pf], v157 offset:32\n" \
    "Lz_%=:\n" \
    "s_nop 0\n"
#define P_CLOB "v16", "v17", "v18", "v19", "v20", "v21", "v22", "v23", "v24", "v25", "v26", "v27", "v28", "v29", "v30", "v31", "v32", "v33", "v34", "v35", "v36", "v37", "v38", "v39", "v40", "v41", "v42", "v43", "v44", "v45", "v46", "v47", "v48", "v49", "v50", "v51", "v52", "v53", "v54", "v55", "v56", "v57", "v58", "v59", "v60", "v61", "v62", "v63", "v64", "v65", "v66", "v67", "v68", "v69", "v70", "v71", "v72", "v73", "v74", "v75", "v76", "v77", "v78", "v79", "v80", "v81", "v82", "v83", "v84", "v85", "v86", "v87", "v88", "v89", "v90", "v91", "v92", "v93", "v94", "v95", "v96", "v97", "v98", "v99", "v100", "v101", "v102", "v103", "v104", "v105", "v106", "v107", "v108", "v109", "v110", "v111", "v112", "v113", "v114", "v115", "v116", "v117", "v118", "v119", "v120", "v121", "v122", "v123", "v124", "v125", "v126", "v127", "v128", "v129", "v130", "v131", "v132", "v133", "v134", "v135", "v136", "v137", "v138", "v139", "v140", "v141", "v142", "v143", "v144", "v145", "v146", "v147", "v148", "v149", "v150", "v151", "v152", "v153", "v154", "v155", "v156", "v157", "v158", "v159", "vcc", "scc", "memory"
static_assert(C_WO - L_CTRL == 56 && C_OST - L_CTRL == 48 && C_BP - L_CTRL == 96 && C_WO0 - L_CTRL == 184 && C_QR2 - L_CTRL == 188 && C_SENT2 - L_CTRL == 100, "offsets in the output stream");
static_assert(L_CTRL - G_ZR == 1280 && C_HX - C_FREE == 144 && C_SENT - L_CTRL == 52, "offsets in the projection stream");
static_assert(C_HR - L_CTRL == 144 && C_RED - L_CTRL == 84 && C_XD - L_CTRL == 80 && C_SENT - L_CTRL == 52, "offsets in the conv stream");
typedef __attribute__((ext_vector_type(16))) unsigned u32x16;
typedef __attribute__((address_space(3))) const unsigned lds_cu32;
typedef __attribute__((address_space(3))) u32x2 lds_u32x2;
typedef __attribute__((address_space(3))) u32x4 lds_u32x4;
typedef __attribute__((address_space(3))) f32x4 lds_f32x4;
#define GPTR(T, lo, hi) ((T*)(__attribute__((address_space(1))) T*)(((u64)(hi) << 32) | (u64)(lo)))
__device__ __forceinline__ float add1(float a, float b) { float r; asm("v_add_f32 %0, %1, %2" : "=v"(r) : "v"(a), "v"(b)); return r; }
__device__ __forceinline__ float mul1(float a, float b) { float r; asm("v_mul_f32 %0, %1, %2" : "=v"(r) : "v"(a), "v"(b)); return r; }
__device__ __forceinline__ f32x4 add4(f32x4 a, f32x4 b) { return (f32x4){add1(a[0], b[0]), add1(a[1], b[1]), add1(a[2], b[2]), add1(a[3], b[3])}; }
__device__ __forceinline__ f32x4 mul4(f32x4 a, f32x4 b) { return (f32x4){mul1(a[0], b[0]), mul1(a[1], b[1]), mul1(a[2], b[2]), mul1(a[3], b[3])}; }
#if defined(__HIP_DEVICE_COMPILE__) && defined(NO_PK)
#define NOPK __attribute__((target("no-packed-fp32-ops")))
#else
#define NOPK
#endif
__global__ NOPK __launch_bounds__(768, 3) void fused_kernel(const u16* x_, const u16* kin_, const u16* vin_, const u16* shift_k_,
                                                       const u16* ssm_k_, const u16* ssm_D_, const u16* wqkv_, const u16* wo_,
                                                       const u16* gw_, char* ws_, u16* out_) {
    u32x16 ka0;
    u32x4 ka1;
    u32x2 ka2;
    asm volatile("s_load_dwordx16 %0, %3, 0\n\ts_load_dwordx4 %1, %3, 64\n\ts_load_dwordx2 %2, %3, 80"
                 : "=&s"(ka0), "=&s"(ka1), "=&s"(ka2) : "s"(__builtin_amdgcn_kernarg_segment_ptr()));
#define x GPTR(const u16, ka0[0], ka0[1])
#define kin GPTR(const u16, ka0[2], ka0[3])
#define vin GPTR(const u16, ka0[4], ka0[5])
#define shift_k GPTR(const u16, ka0[6], ka0[7])
#define ssm_k GPTR(const u16, ka0[8], ka0[9])
#define ssm_D GPTR(const u16, ka0[10], ka0[11])
#define wqkv GPTR(const u16, ka0[12], ka0[13])
#define wo GPTR(const u16, ka0[14], ka0[15])
#define gw GPTR(const u16, ka1[0], ka1[1])
#define ws GPTR(char, ka1[2], ka1[3])
#define out GPTR(u16, ka2[0], ka2[1])
#define wsr __builtin_amdgcn_make_buffer_rsrc(ws, 0, 0x4000000, 0x00020000)
    __shared__ __attribute__((aligned(16))) char smem[LDS_TOTAL];
    const int bid = __builtin_amdgcn_workgroup_id_x();
    const int tid = __builtin_amdgcn_workitem_id_x();
    int lane = tid & 63;
    const int wave = __builtin_amdgcn_readfirstlane(tid >> 6);
    const int xcd = bid & 7, r5 = bid >> 3, r = r5 & 15;
    const bool isG = r5 >= 16;
    const int hg = (xcd & 1) * 4 + (r & 3);
    const int rb2 = (xcd >> 1) * 4 + (r >> 2);
    const int cq = (xcd & 1) * 2 + (r & 1), rbv = (xcd >> 1) * 8 + (r >> 1);
    const int vid = cq * 32 + rbv;
    const int sid = rbv * 4 + cq;
    const int rbo = xcd * 4 + (r >> 3) * 2 + ((r5 >> 4) ^ GH ^ 1), oc = r5 & 7;
    const int w = wave & 3;
    const int sl = (w & 1) * 2 + (w >> 1);
    const int rtq = (rbo >> 1) * 8 + (w & 1) * 4 + (rbo & 1) * 2 + (w >> 1);
    const int g = lane >> 4, n16 = lane & 15, q4 = n16 >> 2, p4 = lane & 3;
    const unsigned lb = (unsigned)(size_t)(lds_char*)smem;
    u64 tag;
    {
        u64 id = llvm_dispatch_id();
        u64 dp = (u64)(size_t)__builtin_amdgcn_dispatch_ptr();
        tag = ((u64)((unsigned)id ^ 0x5bd1e995u) << 32) | (u64)((unsigned)(dp >> 6) ^ (unsigned)(id >> 32) ^ 0x9e3779b9u);
    }
    const unsigned tlo = (unsigned)tag, thi = (unsigned)(tag >> 32);
    if (wave == 0) {
        u64 pc;
        asm volatile("s_getpc_b64 %0" : "=s"(pc));
        pc &= ~255ull;
        const unsigned cl = lane * 32 + r5;
        if (cl < CODE_LINES) GLT(cl * 128, pc);
    }

#define WO_PATS() unsigned wpm, wpo, wpm4, wpo4; { \
        const int c0_ = w * 64 + lane, r0_ = (c0_ * 6554) >> 16, k0_ = r0_ ^ ((r0_ & 8) >> 1), b0_ = oc * 160 + (c0_ - r0_ * 10) * 16; \
        const int c4_ = 256 + lane, r4_ = (c4_ * 6554) >> 16, k4_ = r4_ ^ ((r4_ & 8) >> 1), b4_ = oc * 160 + (c4_ - r4_ * 10) * 16; \
        wpm = opaque((k0_ + ((k0_ >> 4) << 3)) * (D_DIM * 2) + b0_); wpo = opaque((20 * (k0_ >> 2) + (k0_ & 3) + ((~k0_ & 4) << 2)) * (D_DIM * 2) + b0_); \
        wpm4 = opaque((k4_ + 8) * (D_DIM * 2) + b4_); wpo4 = opaque((20 * (k4_ >> 2) + (k4_ & 3) + ((~k4_ & 4) << 2)) * (D_DIM * 2) + b4_); }
#define WO_ROW(ks) (((ks) / 5) * 160 + ((ks) % 5 < 4 ? 40 * ((ks) % 5) : 0))
#define WO_K(ks) GLS((ks) % 5 == 4 ? wpo : wpm, (const char*)wo + WO_ROW(ks) * (D_DIM * 2), lb + w * 1024, WO_POS(ks));
#define SEL4(a, b, c, d) (w == 0 ? (a) : w == 1 ? (b) : w == 2 ? (c) : (d))
#define WO_X(a, b, c, d) GLS(SEL4((a) % 5 == 4, (b) % 5 == 4, (c) % 5 == 4, (d) % 5 == 4) ? wpo4 : wpm4, \
        (const char*)wo + SEL4(WO_ROW(a), WO_ROW(b), WO_ROW(c), WO_ROW(d)) * (D_DIM * 2), lb + SEL4(WO_POS(a), WO_POS(b), WO_POS(c), WO_POS(d)), 4096);
#define WO_4(a, b, c, d) { WO_K(a) WO_K(b) WO_K(c) WO_K(d) WO_X(a, b, c, d) }
#define RD_FLAGS() ((lane & 31) < 8 ? WS_HF + ((rbo >> 1) * 8 + (lane & 7)) * 128 : WS_SF + ((lane & 31) < 24 ? oc * 16 + (lane & 31) - 8 : (lane & 3) * 32 + (rbo & ~1) + ((lane >> 2) & 1)) * 128)
    const int kr = KROT;
#define T_C(i) __builtin_amdgcn_readfirstlane((kr + (i)) >= 10 ? kr + (i) - 10 : kr + (i))
#define T_WV() ({ const int j_ = min(n16, 14); (unsigned)((r * 4 + g) * (NQKV * 2) + j_ * 128 + ((j_ >= 5) + (j_ >= 10) + (xcd & 1)) * 640); })
#define T_XV() ((unsigned)(((xcd >> 1) * 512 + r * 32 + (lane & 31)) * (D_DIM * 2)))
#define T_W(wv, i) GLT(wv, (const char*)wqkv + (size_t)T_C(i) * (64 * NQKV * 2))
#define T_X(xv, i) { const int c0_ = T_C(i), c1_ = T_C((i) + 1); GLT((xv) + (lane < 32 ? c0_ : c1_) * 128, x); }
#define T_PAIR(i) { T_W(wv, i); T_X(xv_, i); T_W(wv, (i) + 1); }

    unsigned e0;
    if (isG) {
        const unsigned t0_ = T_WV() + (kr + (w >> 1)) * (64 * NQKV * 2);
        const unsigned t1_ = ((xcd >> 1) * 512 + (w == 1 ? r * 32 + (lane & 31) : r - ((xcd >> 1) ? 16 : 0))) * (D_DIM * 2) + (w == 1 ? kr + (lane >> 5) : min(lane, 9)) * 128;
        e0 = (w & 1) ? t1_ : t0_;
    } else {
        const unsigned c = (wave >= 5 ? wave - 5 : wave) * 64 + lane, row = (c * 3277) >> 16;
        e0 = rbv * (64 * D_DIM * 2) + cq * 320 + row * (D_DIM * 2 - 320) + c * 16;
    }
    asm volatile("" : "+v"(e0));
    asm volatile("s_waitcnt lgkmcnt(0)" : "+s"(ka0), "+s"(ka1), "+s"(ka2) :: "memory");
    if (isG) {
        if ((wave >> 2) == 1) GLT(e0, (w & 1) ? x : wqkv);
        BARRIER();
    }
    u32x2 skw;
    if (!isG && wave < 10) {
        const int tens = wave >= 5, i5 = wave - 5 * tens;
        const unsigned lw = lb + (tens ? V_VST : V_KST) + i5 * 1024;
        const char* kb = (const char*)(tens ? vin : kin);
        __builtin_amdgcn_s_sleep(V_DELAY);
        GLSP(e0, kb, lw, 0);
        GLSP(e0, kb + 16 * D_DIM * 2, lw, 5152);
        GLSP(e0, kb + 32 * D_DIM * 2, lw, 2 * 5152);
        GLSP(e0, kb + 48 * D_DIM * 2, lw, 3 * 5152);
        asm volatile("global_load_dwordx2 %0, %1, %2" : "=v"(skw) : "v"((cq * 160 + wave * 16 + n16) * 8), "s"(shift_k) : "memory");
        if (wave == 0) { if (lane < 60) GLS(e0, kb - (rbv ? 3 * D_DIM * 2 : 0), lb, V_KST - 32 - 960); }
    }
    if (wave == 11) {
        if (lane < 16) *(u32x4*)(smem + L_CTRL + lane * 16) = (u32x4){0u, 0u, 0u, 0u};
        asm volatile("s_waitcnt lgkmcnt(0)" ::: "memory");
    }
#define Y_OFF (WS_Y + rbo * (D_DIM * 128) + oc * 10240 + w * 1024)
#define G_LOAD() float g1 = __uint_as_float(__builtin_amdgcn_raw_buffer_load_b32(wsr, (lane & 15) * 4, WS_G + rtq * 64, 16));
#define Y_FIRST() u32x4 yv[3]; { yv[0] = __builtin_amdgcn_raw_buffer_load_b128(wsr, lane * 16, Y_OFF, 16); yv[1] = __builtin_amdgcn_raw_buffer_load_b128(wsr, lane * 16, Y_OFF + 4096, 16); \
        yv[2] = __builtin_amdgcn_raw_buffer_load_b128(wsr, lane * 16, Y_OFF + (w < 2 ? 8192 : 0), 16); }
#define RELANE() const int lane_ = opaque(lane); const int g = lane_ >> 4, n16 = lane_ & 15, q4 = n16 >> 2, p4 = lane_ & 3; \
        const unsigned qa = lb + (isG ? G_Q : V_Q) + w * 2560 + n16 * 160 + 8 * g
#define SENT4(v) __any(min(min((v)[0] ^ S_O, (v)[1] ^ S_O), min((v)[2] ^ S_O, (v)[3] ^ S_O)) == 0u)
#define Q_READ(base) f32x4 gq[5]; { const int l_ = opaque(lane); _Pragma("unroll") for (int ct = 0; ct < 5; ++ct) { \
            const u32x2 q2 = *(const lds_u32x2*)(size_t)(lb + (base) + w * 2560 + (l_ & 15) * 160 + 8 * (l_ >> 4) + ct * 32); \
            gq[ct] = (f32x4){lo_f(q2[0]), hi_f(q2[0]), lo_f(q2[1]), hi_f(q2[1])}; } \
        asm volatile("" : "+v"(gq[0]), "+v"(gq[1]), "+v"(gq[2]), "+v"(gq[3]), "+v"(gq[4]) :: "memory"); }
#define FINAL() _Pragma("unroll") for (int ct = 0; ct < 5; ++ct) { \
            const f32x4 sb = *(const lds_f32x4*)(size_t)(pa + (10240 - h1) + ct * 1024) + *(const lds_f32x4*)(size_t)(pa + (10240 - h1) + 5120 + ct * 1024); \
            const f32x4 fin = gq[ct] + g0 * (sa[ct] + sb); \
            pk[ct][0] = pack2(fin[0], fin[1]); \
            pk[ct][1] = pack2(fin[2], fin[3]); }
#define X4(v) min(min((v)[0] ^ S_O, (v)[1] ^ S_O), min((v)[2] ^ S_O, (v)[3] ^ S_O))
#define P_STAGE() { \
        RELANE(); \
        const unsigned l_y = lb + (isG ? G_Y : V_Y); \
        unsigned yw = l_y + (w * 8 + (lane_ >> 3)) * Y_PITCH + (lane_ & 7) * 16, yw2 = yw + (w < 2 ? 64 * Y_PITCH : 0); \
        unsigned yr = l_y + (4 * g + q4) * Y_PITCH + sl * 32 + p4 * 8; \
        float g0 = 1.f - g1; \
        unsigned pa = lb + PART(sl) + (g * 16 + (((n16 >> 1) + (n16 & 1) * 8) ^ ((rtq & 1) ? 0 : 8))) * 16; \
        const auto outr = __builtin_amdgcn_make_buffer_rsrc(out, 0, S_LEN * D_DIM * 2, 0x00020000); \
        unsigned ob = ((rtq * 16 + n16) * D_DIM + oc * 80) * 2, ob2 = ob + (64 + 4 * g) * 2; \
        ob += ((g & 1) * 16 + (g >> 1) * 8) * 2; \
_Pragma("unroll") \
        for (int ct = 0; ct < 5; ++ct) gq[ct] = mul4(gq[ct], (f32x4){g1, g1, g1, g1}); \
        asm volatile("" : "+v"(gq[0]), "+v"(gq[1]), "+v"(gq[2]), "+v"(gq[3]), "+v"(gq[4]), "+v"(ob), "+v"(ob2), "+v"(yw), "+v"(yw2), "+v"(yr), "+v"(pa), "+v"(g0)); \
        SB(); \
_Pragma("nounroll") \
        for (int it = 0; it < SPIN_CAP; ++it) { \
            *(lds_u32x4*)(size_t)yw = yv[0]; \
            *(lds_u32x4*)(size_t)(yw + 32 * Y_PITCH) = yv[1]; \
            *(lds_u32x4*)(size_t)yw2 = yv[2]; \
            if (!__any(min(X4(yv[0]), min(X4(yv[1]), X4(yv[2]))) == 0u)) break; \
            __builtin_amdgcn_s_sleep(Y_SLEEP); \
            asm volatile("" ::: "memory"); \
            if (SENT4(yv[0])) yv[0] = __builtin_amdgcn_raw_buffer_load_b128(wsr, lane * 16, Y_OFF, 16); \
            if (SENT4(yv[1])) yv[1] = __builtin_amdgcn_raw_buffer_load_b128(wsr, lane * 16, Y_OFF + 4096, 16); \
            if (SENT4(yv[2])) yv[2] = __builtin_amdgcn_raw_buffer_load_b128(wsr, lane * 16, Y_OFF + (w < 2 ? 8192 : 0), 16); \
        } \
        asm volatile("" ::: "memory"); \
        lds_add(lb + C_Y, lane); \
        STAMP(8); \
_Pragma("nounroll") \
        for (int it = 0; it < SPIN_CAP; ++it) if (__builtin_amdgcn_readfirstlane(lds_ld32(lb + C_Y)) >= 4) break; \
        u32x2 y2[5]; \
_Pragma("unroll") \
        for (int ct = 0; ct < 5; ++ct) y2[ct] = __builtin_bit_cast(u32x2, TRRD((lds_char*)(size_t)(yr + ct * 16 * Y_PITCH))); \
_Pragma("unroll") \
        for (int ct = 0; ct < 5; ++ct) gq[ct] = mul4(gq[ct], (f32x4){lo_f(y2[ct][0]), hi_f(y2[ct][0]), lo_f(y2[ct][1]), hi_f(y2[ct][1])}); \
        asm volatile("" : "+v"(gq[0]), "+v"(gq[1]), "+v"(gq[2]), "+v"(gq[3]), "+v"(gq[4])); \
        STAMP(9); \
          \
        u32x4 pf; \
_Pragma("nounroll") \
        for (int it = 0; it < SPIN_CAP; ++it) { \
            pf = lds_v128(lb + C_PF + sl * 16); \
            if (__builtin_amdgcn_readfirstlane((pf[0] & pf[1]) | (pf[2] & pf[3]))) break; \
            __builtin_amdgcn_s_sleep(1); \
        } \
        const unsigned h1 = __builtin_amdgcn_readfirstlane(pf[0] & pf[1]) ? 0 : 10240; \
        f32x4 sa[5]; \
_Pragma("unroll") \
        for (int ct = 0; ct < 5; ++ct) sa[ct] = add4(*(const lds_f32x4*)(size_t)(pa + h1 + ct * 1024), *(const lds_f32x4*)(size_t)(pa + h1 + 5120 + ct * 1024)); \
        asm volatile("" : "+v"(sa[0]), "+v"(sa[1]), "+v"(sa[2]), "+v"(sa[3]), "+v"(sa[4])); \
_Pragma("nounroll") \
        for (int it = 0; it < SPIN_CAP; ++it) { \
            if (__builtin_amdgcn_readfirstlane((pf[0] & pf[1]) & (pf[2] & pf[3]))) break; \
            pf = lds_v128(lb + C_PF + sl * 16); \
        } \
        asm volatile("" ::: "memory"); \
        STAMP(13); \
        unsigned pk[5][2]; \
        FINAL() \
_Pragma("unroll") \
        for (int p = 0; p < 2; ++p) { \
            const auto r0 = __builtin_amdgcn_permlane16_swap(pk[2 * p][0], pk[2 * p + 1][0], false, false); \
            const auto r1 = __builtin_amdgcn_permlane16_swap(pk[2 * p][1], pk[2 * p + 1][1], false, false); \
            __builtin_amdgcn_raw_buffer_store_b128((u32x4){r0[0], r1[0], r0[1], r1[1]}, outr, ob + p * 64, 0, OUT_AUX); \
        } \
        __builtin_amdgcn_raw_buffer_store_b64((u32x2){pk[4][0], pk[4][1]}, outr, ob2, 0, OUT_AUX); \
        FLUSH(); \
        return; }

    if (isG) {
      if (wave >= 4) {
        const int half = (wave >> 2) - 1;
        if (half) __builtin_amdgcn_s_setprio(C1_PRIO); else __builtin_amdgcn_s_setprio(C0_PRIO);
        const int rb = rb2 * 2 + half;
        unsigned ap[2], wp[4];
#pragma unroll
        for (int sub = 0; sub < 2; ++sub) ap[sub] = lb + half * 8192 + ((n16 & 7) * 2 + (n16 >> 3)) * 128 + (((sub * 4 + g) ^ (n16 & 7)) * 16);
#pragma unroll
        for (int i = 0; i < 4; ++i) {
            const int part = i < 3 ? i : (p4 < 3 ? p4 : 2);
            const int Q = i < 3 ? 5 * w + (w & 1) + p4 : 5 * w + ((w & 1) ? 0 : 4);
            const int c2 = (Q >> 1) - 2 * (hg & 1);
            const int cc = ((unsigned)c2 < 8u ? part * 8 + c2 : 24 + part * 2 + (c2 & 1)) ^ (2 * ((g & 1) * 4 + q4));
            wp[i] = lb + W_OFF(0) + (8 * g + q4) * W_PITCH + cc * 16 + (Q & 1) * 8;
        }
        const int qi = n16;
        if (wave == 11) {
            f32x4 cm0, cm1;
            u32x4 psel = {(g >> 1) == (qi >> 3) ? 0xffffffffu : 0u, 0u, 0u, 0u};
#pragma unroll
            for (int e = 0; e < 4; ++e) {
                const bool own = (g & 1) * 4 + e <= (qi & 7), pm = (g >> 1) == (qi >> 3);
                cm0[e] = pm && !own ? 0.f : -3.0e38f;
                cm1[e] = pm && own ? 0.f : -3.0e38f;
                psel[1 + (e >> 1)] |= own ? 0u : (e & 1) ? 0xffff0000u : 0xffffu;
            }
            *(lds_f32x4*)(size_t)(lb + G_TAB + lane * 16) = cm0;
            *(lds_f32x4*)(size_t)(lb + G_TAB + 1024 + lane * 16) = cm1;
            *(lds_u32x4*)(size_t)(lb + G_TAB + 2048 + lane * 16) = psel;
            if (lane < 5) *(lds_u32x2*)(size_t)(lb + G_ZR + lane * 128) = (u32x2){0u, 0u};
        }
        const unsigned vxw = lb + G_VX + w * 1152;
        const unsigned scw = g == 2 ? vxw + half * 512 + qi * 8 : lb + G_VXD;
        const int hj = (hg & 1) * 4 + w;
        const unsigned oo = WS_O + ((rb * 4 + (g & 1)) * 20 + (hg >> 1) * 5 + (hj >> 1)) * 1024 + (((w & 1) * 2 + (g >> 1)) * 16 + qi) * 16;
        const unsigned ox = WS_O + ((rb * 4 + g) * 20 + (hg >> 1) * 5 + 4) * 1024 + hj * 128 + qi * 8;
        unsigned c0_, c1_, l16 = lane * 16;
        STAMP(6);
        CH(1);
        CSTAMP(0);
        asm volatile(G_ASM
                     : [cnt] "=&s"(c0_), [tmp] "=&s"(c1_), [wp0] "+v"(wp[0]), [wp1] "+v"(wp[1]), [wp2] "+v"(wp[2]), [wp3] "+v"(wp[3]), [l16] "+v"(l16)
                     : [ap0] "v"(ap[0]), [ap1] "v"(ap[1]), [scw] "v"(scw), [oo] "v"(oo), [ox] "v"(ox),
                       [vxb] "v"(vxw + half * 512 + (4 * g + q4) * 8), [zr] "v"(lb + G_ZR), [fr] "v"(lb + C_FREE + (wave - 4) * 4),
                       [tabc] "s"(lb + G_TAB), [hxc] "s"(lb + G_HX + w * 1024), [hxd] "s"(lb + G_HXO + w * 256),
                       [wsp] "s"(ws), [half] "s"(half), [rb2] "s"(rb2), [cap] "s"(SPIN_CAP), [sc] "s"(0x3ea52b5bu),
                       [thi] "s"(thi), [hfo] "s"(w == HINT_W ? WS_H + ((rb2 * 8 + hg) * 2 + half) * 128 : 0)
                     : G_CLOB);
        lane = l16 >> 4;
        CSTAMP(1);
        __builtin_amdgcn_s_setprio(T_PRIO);
        if (half) {
            CH(2);
            spin_ge(lb + C_SENT2, 1);
            G_LOAD();
            spin_ge(lb + C_WO, 4);
            spin_all(lb + C_FREE + (lane & 7) * 4, 18);
            Q_READ(G_Q);
            if (GY_DELAY > 0) __builtin_amdgcn_s_sleep(GY_DELAY);
            Y_FIRST();
            STAMP(4);
            P_STAGE();
        }
        lds_add(lb + C_OST, lane);
        STAMP(4);
      } else {
        __builtin_amdgcn_s_setprio(L_PRIO);
        unsigned ev[9];
        {
            const int hi = lane >> 5, pos = lane & 31;
#pragma unroll
            for (int v = 0; v < 2; ++v) {
                const int row = 2 * w + hi + 8 * v;
                int L = pos ^ (2 * (v * 4 + (row & 3)));
                L = L > 29 ? L - 2 : L;
                const int part = L < 24 ? L >> 3 : (L - 24) >> 1;
                const int chunk = L < 24 ? (L & 7) + 2 * (hg & 1) : (L & 1) + ((hg & 1) ? 0 : 8);
                ev[v] = row * (NQKV * 2) + chunk * 16 + part * 1280 + hg * 160;
                ev[v + 2] = ev[v] + 16 * NQKV * 2;
            }
            const int ch = (lane & 7) ^ ((4 * (w & 1) + g) & 7);
#pragma unroll
            for (int j = 0; j < 5; ++j) {
                int t = rb2 * 128 - 16 + 8 * w + (lane >> 3) + 32 * j;
                t = t < 0 ? 0 : t;
                t = t > S_LEN - 1 ? S_LEN - 1 : t;
                ev[4 + j] = t * (D_DIM * 2) + ch * 16;
            }
        }
        SB();
        CH(13);
        STAMP(5);
        if (w < 2) ring_issue<5, 0, ring_need(-1, 5)>(ev, x, wqkv, kr, lb + w * 1024); else ring_issue<4, 0, ring_need(-1, 4)>(ev, x, wqkv, kr, lb + w * 1024);
        SB();
        unsigned qf[3];
        WO_PATS();
#pragma unroll
        for (int j = 0; j < 3; ++j) { const int c = min(j * 64 + lane, 159); const int row = c / 10, ch = c - row * 10; qf[j] = opaque(row * (D_DIM * 2) + ch * 16); }
        const unsigned sfo = opaque(RD_FLAGS());
        const unsigned sf1 = opaque(WS_HF + (rb2 * 8 + (lane & 7)) * 128);
        const unsigned fra = opaque(lb + C_FREE + (lane & 7) * 4);
        SB();
        if (w < 2) {
            ring_issue<5, ring_need(-1, 5), ring_cum(-1, 5)>(ev, x, wqkv, kr, lb + w * 1024);
            waitvm<ring_cum(-1, 5) - ring_need(-1, 5)>();
            STAMP(6);
            BARRIER();
            ring_run<5, 0>(ev, x, wqkv, kr, lb + w * 1024);
        } else {
            ring_issue<4, ring_need(-1, 4), ring_cum(-1, 4)>(ev, x, wqkv, kr, lb + w * 1024);
            waitvm<ring_cum(-1, 4) - ring_need(-1, 4)>();
            BARRIER();
            ring_run<4, 0>(ev, x, wqkv, kr, lb + w * 1024);
        }
        STAMP(2);
#define WO_POS(ks) ((ks) < 10 ? G_R0 + (ks) * 5120 : G_R1 + ((ks) - 10) * 5120)
        static_assert(JL == 17, "last barrier");
        CH(3);
        WO_4(0, 1, 2, 3)
        WAIT_VM(9);
        BARRIER();
        WO_4(4, 5, 10, 11)
        WAIT_VM(10);
        BARRIER();
        __builtin_amdgcn_s_setprio(WO_PRIO);
        CH(4);
        u32x2 f1 = {0u, 0u};
#define SF1_ASK() if (w == 2) asm volatile("global_load_dwordx2 %0, %1, %2 sc1" : "=v"(f1) : "v"(sf1), "s"(ws) : "memory");
#define SF1_SEE() if (w == 2) { asm volatile("s_waitcnt vmcnt(5)" : "+v"(f1) :: "memory"); if (__all(f1[0] == tlo && f1[1] == thi)) lds_st32(lb + C_SENT, 1); }
        if (SF1_AT == 0) SF1_ASK()
        WO_4(12, 6, 7, 8)
        if (SF1_AT == 0) SF1_SEE()
        spin_all(fra, 16);
        if (SF1_AT == 1) SF1_ASK()
        WO_4(9, 13, 14, 15)
        if (SF1_AT == 1) SF1_SEE()
        WO_K(16)
        spin_all(fra, 17);
        WO_K(17) WO_K(18) WO_K(19) WO_X(16, 17, 18, 19)
#undef WO_POS
#pragma unroll
        for (int j = 0; j < 3; ++j)
            if (j < 2 || lane < 32) GLS(qf[j], (const char*)x + (size_t)(rtq * 16) * (D_DIM * 2) + oc * 160, lb + w * 2560, G_Q + j * 1024);
        STAMP(3);
        if (w == 2 && !__all(f1[0] == tlo && f1[1] == thi)) {
            poll_flags(wsr, sf1, tlo, thi);
            lds_st32(lb + C_SENT, 1);
        }
        u32x2 fv = {0u, 0u};
        if (w == 3) {
            asm volatile("global_load_dwordx2 %0, %1, %2 sc1" : "=v"(fv) : "v"(sfo), "s"(ws) : "memory");
            WAIT_VM(1);
        } else {
            WAIT_VM(0);
        }
        lds_add(lb + C_WO, lane);
        if (w == 3) {
            asm volatile("s_waitcnt vmcnt(0)" : "+v"(fv) :: "memory");
            if (!__all(fv[0] == tlo && fv[1] == thi)) poll_flags(wsr, sfo, tlo, thi);
            lds_st32(lb + C_SENT2, 1);
        }
        STAMP(4);
      }
    } else {
        __builtin_amdgcn_s_setprio(3);
        const int c5 = wave < 4 ? wave : 4;
        const int d = vid * 5 + c5;
        unsigned Dw = *(const __attribute__((address_space(4))) unsigned*)(size_t)(ssm_D + (d & ~1));
        unsigned hp0 = *(const __attribute__((address_space(4))) unsigned*)(size_t)(ssm_k + (vid * 5 + 4) * S_LEN + w * 512 - 2);
        BARRIER();
        if (wave <= 4) hp0 = 0;
        asm volatile("" : "+s"(Dw), "+s"(hp0));
#define WO_POS(ks) (V_WO + (ks) * 5120)
#define KV_DONE() do { if (lds_add_rtn(lb + C_KVD, lane) == 9) { \
            if (lane == 0) __hip_atomic_store((u64*)(ws + WS_XF + vid * 128), tag, __ATOMIC_RELAXED, __HIP_MEMORY_SCOPE_AGENT); lds_add(lb + C_KVD, lane); } } while (0)
        if (wave == 10) {
            const unsigned wv = T_WV(), xv_ = T_XV();
            T_PAIR(2);
            T_PAIR(4);
            if (WO_MALL) GLT((unsigned)((vid * 50 + min(lane, 49)) * 128), wo);
        }
        if (wave < 10) {
            const int m0 = (g & 1) * 2 + (g >> 1);
            const unsigned kp = opaque(lb + V_KST + (8 * m0 + q4) * 320 + (m0 >> 1) * 32 + wave * 32 + p4 * 8);
            const unsigned km = opaque(kp - 1280 - ((m0 & 1) ? 0 : 32));
            const unsigned sw_ = opaque(lb + V_STG + wave * 2048 + n16 * 128 + ((m0 + n16) & 7) * 16);
            const unsigned sr_ = opaque(lb + V_STG + wave * 2048 + (lane >> 3) * 128 + ((lane + (lane >> 3)) & 7) * 16);
            const unsigned e1 = opaque(WS_KV + (cq * 160 + wave * 16 + (lane >> 3)) * KV_PITCH + rbv * 128 + (lane & 7) * 16), e2 = opaque(e1 + 8 * KV_PITCH);
            asm volatile("s_waitcnt vmcnt(0)" : "+v"(skw) :: "memory");
            lds_add(lb + C_KV, lane);
            const f32x2 s0 = {lo_f(skw[0]), lo_f(skw[0])}, s1 = {hi_f(skw[0]), hi_f(skw[0])}, s2 = {lo_f(skw[1]), lo_f(skw[1])}, s3 = {hi_f(skw[1]), hi_f(skw[1])};
            spin_ge(lb + C_KV, 10);
            CH(5);
            STAMP(2);
            u32x2 kq[2][3], vq[2][2];
#pragma unroll
            for (int i = 0; i < 2; ++i) {
                kq[i][0] = __builtin_bit_cast(u32x2, TRRD((lds_char*)(size_t)(km + i * 10304)));
#pragma unroll
                for (int e = 0; e < 2; ++e) kq[i][e + 1] = __builtin_bit_cast(u32x2, TRRD((lds_char*)(size_t)(kp + i * 10304 + e * 1280)));
#pragma unroll
                for (int e = 0; e < 2; ++e) vq[i][e] = __builtin_bit_cast(u32x2, TRRD((lds_char*)(size_t)(kp + i * 10304 + (V_VST - V_KST) + e * 1280)));
            }
            if (rbv == 0 && m0 == 0) kq[0][0] = (u32x2){0u, 0u};
            f32x2 kf[12], res[8];
#pragma unroll
            for (int e = 1; e < 12; ++e) {
                const unsigned u0 = kq[0][e >> 2][(e >> 1) & 1], u1 = kq[1][e >> 2][(e >> 1) & 1];
                kf[e] = (e & 1) ? (f32x2){hi_f(u0), hi_f(u1)} : (f32x2){lo_f(u0), lo_f(u1)};
            }
#pragma unroll
            for (int e = 0; e < 8; ++e) {
                const unsigned u0 = vq[0][e >> 2][(e >> 1) & 1], u1 = vq[1][e >> 2][(e >> 1) & 1];
                f32x2 a = kf[e + 1] * s3;
                a = kf[e + 2] * s2 + a;
                a = kf[e + 3] * s1 + a;
                a = kf[e + 4] * s0 + a;
                res[e] = a * ((e & 1) ? (f32x2){hi_f(u0), hi_f(u1)} : (f32x2){lo_f(u0), lo_f(u1)});
            }
#pragma unroll
            for (int i = 0; i < 2; ++i) {
                u32x4 pk;
#pragma unroll
                for (int e = 0; e < 4; ++e) pk[e] = pack2(res[2 * e][i], res[2 * e + 1][i]);
                *(lds_vu32x4*)(size_t)(sw_ ^ (i * 64)) = pk;
            }
            const u32x4 pk0 = lds_v128(sr_), pk1 = lds_v128(sr_ + 1024);
            __builtin_amdgcn_raw_buffer_store_b128(pk0, wsr, e1, 0, 16);
            __builtin_amdgcn_raw_buffer_store_b128(pk1, wsr, e2, 0, 16);
            lds_add(lb + C_KVST, lane);
            STAMP(3);
        }
        if (wave >= 8) {
            const unsigned wv = T_WV(), xv_ = T_XV();
            WO_PATS();
            if (wave == 10) {
            } else if (wave == 11) {
                if (lane < 56) {
#pragma unroll
                    for (int c5 = 0; c5 < 5; ++c5)
                        *(lds_u32x4*)(size_t)(lb + (lane < 16 ? V_KV(c5) + lane * 16 : V_F + c5 * XC_BYTES + 15 * XC_ROW + 256 + (lane < 36 ? XC_COPY + (lane - 16) * 16 : XC_B + 4 + (lane - 36) * 16))) = (u32x4){0u, 0u, 0u, 0u};
                }
                const unsigned x4o = opaque(WS_KV + d * KV_PITCH + lane * 16);
                spin_ge(lb + C_KVD, 11);
                if (POLL_DELAY > 0) __builtin_amdgcn_s_sleep(POLL_DELAY);
                const unsigned xfo = WS_XF + (cq * 32 + (lane & 31)) * 128;
                const u32x2 fv = __builtin_amdgcn_raw_buffer_load_b64(wsr, xfo, 0, 16);
                if (!__all(fv[0] == tlo && fv[1] == thi)) {
                    lds_st32(lb + C_GO, 1);
                    poll_flags(wsr, xfo, tlo, thi);
                }
                asm volatile("s_mov_b32 m0, %2\n\ts_nop 0\n\tglobal_load_lds_dwordx4 %0, %1 sc1\n\tglobal_load_lds_dwordx4 %0, %1 offset:1024 sc1\n\t"
                             "global_load_lds_dwordx4 %0, %1 offset:2048 sc1\n\tglobal_load_lds_dwordx4 %0, %1 offset:3072 sc1" ::"v"(x4o), "s"(ws), "s"(lb + V_KV4 + 256) : "memory");
                lds_add(lb + C_KVR, lane);
                STAMP(2);
                WAIT_VM(0);
                lds_st32(lb + C_HR, 4);
                STAMP(3);
                spin_ge(lb + C_XD, 4);
                lds_st32(lb + C_GO, 1);
            } else {
                WAIT_VM(0);
                KV_DONE();
                STAMP(11);
                spin_ge(lb + OS_W, OS_N);
                CH(6); CH(16);
                const u32x4 sv = {S_O, S_O, S_O, S_O};
#pragma unroll
                for (int i = 0; i < 10; ++i) {
                    if (i >= SENT_PACE) waitvm<SENT_PACE - 1>();
                    __builtin_amdgcn_raw_buffer_store_b128(sv, wsr, WS_O + sid * 20480 + (wave & 1) * 10240 + lane * 16 + i * 1024, 0, 16);
                }
                WAIT_VM(0);
                if (lds_add_rtn(lb + C_YS, lane) == 1) {
                    if (lane == 0) __hip_atomic_store((u64*)(ws + WS_HF + sid * 128), tag, __ATOMIC_RELAXED, __HIP_MEMORY_SCOPE_AGENT);
                }
                STAMP(12);
            }
            if (wave == 10) {
                spin_ge(lb + GO_W, GO_N);
                T_PAIR(6);
                if (T4_EARLY) WAIT_VM(1); else spin_ge(lb + GO2_W, GO2_N);
                T_PAIR(8);
            }
            spin_mid(lb + C_XD, 4);
            CH(9);
            if (wave == WT_WAVE) {
#pragma unroll
                for (int i = 0; i < 4; ++i) {
                    const int l_ = r * 250 + min(i * 64 + lane, 249);
                    GLT((unsigned)((l_ < 800 ? l_ : l_ + 2400) * 128), wo);
                }
            }
            STAMP(5);
            u32x2 fv = {0u, 0u};
            const unsigned sfo = RD_FLAGS();
            if (r < 8) WO_4(2, 3, 4, 5) else WO_4(6, 7, 8, 9)
            if (WO_PACE >= 0) waitvm<(WO_PACE < 0 ? 0 : WO_PACE)>();
            if (r < 8) WO_4(6, 7, 8, 9) else WO_4(2, 3, 4, 5)
            WO_K(10) WO_K(11) WO_K(12)
            if (w < 3) WO_X(10, 11, 12, 12)
            STAMP(4);
            if (wave == SF_WAVE) {
                spin_mid(lb + SF_W, SF_N);
                asm volatile("global_load_dwordx2 %0, %1, %2 sc1" : "=v"(fv) : "v"(sfo), "s"(ws) : "memory");
                WAIT_VM(1);
            } else {
                WAIT_VM(0);
            }
            lds_add(lb + C_WO, lane);
            if (wave == SF_WAVE) {
                asm volatile("s_waitcnt vmcnt(0)" : "+v"(fv) :: "memory");
                if (!__all(fv[0] == tlo && fv[1] == thi)) poll_flags(wsr, sfo, tlo, thi);
                lds_st32(lb + C_SENT2, 1);
                STAMP(3);
            }
            STAMP(6);
        } else {
            const unsigned xb = lb + V_F + c5 * XC_BYTES;
            const unsigned hb = lb + V_KV(c5) + 256;
            const float Dd = (d & 1) ? hi_f(Dw) : lo_f(Dw);
            u32x4 hv[4];
            const unsigned l16 = lane * 16;
            {
                const char* hsrc = (const char*)ssm_k + (size_t)d * (S_LEN * 2) + (wave < 4 ? 0 : w * 1024);
                if (FILT_LATE) { WAIT_VM(0); KV_DONE(); if (FILT_LATE == 2) spin_ge(lb + C_KVD, 11); }
                spin_ge(lb + C_KVST, 10);
#define H_LD(i) asm volatile("global_load_dwordx4 %0, %1, %2 offset:%3" H_POL : "=v"(hv[i]) : "v"(l16), "s"(hsrc), "n"((i) * 1024) : "memory")
                H_LD(0);
                if (wave < 4) { H_LD(1); H_LD(2); H_LD(3); if (!FILT_LATE) WAIT_VM(4); } else if (!FILT_LATE) WAIT_VM(1);
                if (!FILT_LATE) KV_DONE();
                STAMP(4);
            }
            {
                const unsigned da = xb + (3 - g) * XC_ROW + (15 - n16) * 16 + (wave < 4 ? 0 : -w * (4 * XC_ROW));
                u32x4 ra[4], rb[4];
                asm volatile("s_waitcnt vmcnt(0)" : "+v"(hv[0]), "+v"(hv[1]), "+v"(hv[2]), "+v"(hv[3]) :: "memory");
#define H_PREV(i) __builtin_amdgcn_update_dpp((i) == 0 ? hp0 : __builtin_amdgcn_readlane(hv[(i) == 0 ? 0 : (i) - 1][3], 63), hv[i][3], 0x138, 0xf, 0xf, false)
#define BFI16(lo, hi) (((lo) & 0xffffu) | ((hi) & 0xffff0000u))
#define H_PREP(i) { ra[i] = (u32x4){__builtin_amdgcn_alignbit(hv[i][3], hv[i][3], 16), __builtin_amdgcn_alignbit(hv[i][2], hv[i][2], 16), __builtin_amdgcn_alignbit(hv[i][1], hv[i][1], 16), __builtin_amdgcn_alignbit(hv[i][0], hv[i][0], 16)}; \
                    rb[i] = (u32x4){BFI16(hv[i][3], hv[i][2]), BFI16(hv[i][2], hv[i][1]), BFI16(hv[i][1], hv[i][0]), BFI16(hv[i][0], H_PREV(i))}; \
                    *(lds_u32x4*)(size_t)(da + (3 - (i)) * (4 * XC_ROW) + XC_COPY) = ra[i]; *(lds_u32x4*)(size_t)(da + (3 - (i)) * (4 * XC_ROW) + XC_B + 4) = rb[i]; }
#define H_DUP(i) { *(lds_u32x4*)(size_t)(da + (3 - (i)) * (4 * XC_ROW) + XC_COPY - 32) = ra[i]; *(lds_u32x4*)(size_t)(da + (3 - (i)) * (4 * XC_ROW) + XC_B + 4 - 32) = rb[i]; }
                H_PREP(0)
                if (wave < 4) {
                    H_PREP(1) H_PREP(2) H_PREP(3)
                    if (n16 == 15) { H_DUP(0) H_DUP(1) H_DUP(2) if (g < 3) H_DUP(3) }
                } else {
                    if (n16 == 15 && (w < 3 || g < 3)) H_DUP(0)
                }
            }
            STAMP(9);
#define Y_ADDR(j0) const int l2_ = opaque(lane), gg = l2_ >> 4, mm = (l2_ & 3) | ((l2_ & 4) << 1) | ((l2_ & 8) >> 1); \
            const unsigned kp = hb + 2 * (256 * (j0) + 128 * (gg >> 1) + 8 * mm + 4 * (gg & 1)); \
            const int ts = 256 * ((j0) + (gg & 1)) + 128 * (gg >> 1) + 8 * mm; \
            const unsigned yo = WS_Y + d * 128 + ((ts >> 7) * 2 + ((ts >> 5) & 1)) * (D_DIM * 128) + (((ts >> 6) & 1) * 32 + (ts & 31)) * 2;
            if (wave < 4) {
                Y_ADDR(0)
                const int b1 = n16 >> 3, phi = n16 & 7;
                const unsigned xp = xb + ((phi & 1) ? XC_COPY - 4 : XC_B) + (1 - b1) * XC_ROW + 2 * (8 - 2 * (phi >> 1) + 8 * g);
                const unsigned hp = hb + 2 * (8 * (mm + g) - 120);
                const int sx4 = (4 - w) * XC_BYTES + (w >> 1) * XC_ROW + (w & 1) * 128, sh4 = V_KV4 - w * KV_BYTES + w * 128;
                unsigned cs[10];
                unsigned c0_, c1_, scr = lb + V_SCR + w * 8192 + l16;
                __builtin_amdgcn_s_setprio(CONV_PRIO);
                spin_ge(lb + C_KVR, 5);
                CH(8);
                STAMP(6);
                CSTAMP(0);
                asm volatile(CONV_ASM
                             : [cnt] "=&s"(c0_), [tmp] "=&s"(c1_), [scr] "+v"(scr)
                             : [xp] "v"(xp), [hp] "v"(hp), [sx4] "s"(sx4), [sh4] "s"(sh4), [kvl] "s"(hb), [kvr] "v"(kp),
                               [ctl] "v"(lb + L_CTRL), [dd] "s"(Dd), [cap] "s"(SPIN_CAP), [xo] "v"(WS_KV + d * KV_PITCH + l16), [wsp] "s"(ws), [yo] "v"(yo)
                             : CONV_CLOB);
                __builtin_amdgcn_s_setprio(3);
                lane = ((scr - lb - V_SCR) >> 4) & 63;
                CSTAMP(1);
                lds_add(lb + C_CONVN, lane);
                STAMP(12);
                unsigned qg[3];
#pragma unroll
                for (int j = 0; j < 3; ++j) { const int c = j * 64 + (j < 2 ? lane : lane & 31), row = (c * 6554) >> 16; qg[j] = row * (D_DIM * 2 - 160) + c * 16; }
                CH(11);
                spin_ge(lb + C_CONVN, 4);
#pragma unroll
                for (int j = 0; j < 3; ++j)
                    if (j < 2 || lane < 32) GLS(qg[j], (const char*)x + (size_t)(rtq * 16) * (D_DIM * 2) + oc * 160, lb + w * 2560, V_Q + j * 1024);
                spin_ge(lb + C_SENT2, 1);
                G_LOAD();
                asm volatile("s_waitcnt vmcnt(0)" : "+v"(g1) :: "memory");
                Q_READ(V_Q);
                if (VY_DELAY > 0) __builtin_amdgcn_s_sleep(VY_DELAY);
                Y_FIRST();
                STAMP(7);
                P_STAGE();
            } else {
                lds_add(lb + C_KVR, lane);
                Y_ADDR(2 * w)
                WO_PATS();
                {
                    const int grow = (xcd >> 1) * 512 + r * 32 + (xcd & 1) * 16 + w * 4 + g;
                    const unsigned go = opaque(grow * (D_DIM * 2) + n16 * 16);
                    if (GATE_LATE) spin_mid(lb + C_XD, 4);
                    u32x4 gx[5], gv[5];
#pragma unroll
                    for (int i = 0; i < 5; ++i) {
                        gx[i] = *(const u32x4*)((const char*)x + go + i * 256);
                        gv[i] = *(const u32x4*)((const char*)gw + n16 * 16 + i * 256);
                    }
                    float sa[4] = {0.f, 0.f, 0.f, 0.f};
#pragma unroll
                    for (int i = 0; i < 5; ++i)
#pragma unroll
                        for (int e = 0; e < 4; ++e) sa[e] = __builtin_fmaf(hi_f(gx[i][e]), hi_f(gv[i][e]), __builtin_fmaf(lo_f(gx[i][e]), lo_f(gv[i][e]), sa[e]));
                    float sacc = (sa[0] + sa[1]) + (sa[2] + sa[3]);
                    sacc += __builtin_amdgcn_update_dpp(0.f, sacc, 0x128, 0xf, 0xf, true);
                    sacc += __builtin_amdgcn_update_dpp(0.f, sacc, 0x124, 0xf, 0xf, true);
                    sacc += __builtin_amdgcn_update_dpp(0.f, sacc, 0x122, 0xf, 0xf, true);
                    sacc += __builtin_amdgcn_update_dpp(0.f, sacc, 0x121, 0xf, 0xf, true);
                    const float gres = rbf(__builtin_amdgcn_rcpf(1.f + __expf(-rbf(sacc))));
                    if (n16 == 0) __builtin_amdgcn_raw_buffer_store_b32(__float_as_uint(gres), wsr, WS_G + grow * 4, 0, 16);
                    spin_mid(lb + SENT_W, SENT_N);
                    CH(7); CH(17);
                    const u32x4 ones = {S_O, S_O, S_O, S_O};
#pragma unroll
                    for (int i = 0; i < 5; ++i) {
                        const int c_ = (w * 5 + i) * 64 + lane, b_ = (c_ * 1639) >> 16;
                        __builtin_amdgcn_raw_buffer_store_b128(ones, wsr, WS_Y + vid * 640 + b_ * (D_DIM * 128 - 640) + c_ * 16, 0, 16);
                    }
                    WAIT_VM(0);
                    if (lds_add_rtn(lb + C_SFC, lane) == 3) {
                        lds_st32(lb + C_SENT, 1);
                        if (lane == 0) __hip_atomic_store((u64*)(ws + WS_SF + vid * 128), tag, __ATOMIC_RELAXED, __HIP_MEMORY_SCOPE_AGENT);
                    }
                    STAMP(6);
                }
                spin_mid(lb + C_RED, 4);
                CH(10);
                STAMP(5);
                f32x4 acc[2];
#pragma unroll
                for (int e = 0; e < 2; ++e) {
                    const u32x2 kk = *(const lds_u32x2*)(size_t)(kp + e * 512);
                    acc[e] = mul4((f32x4){lo_f(kk[0]), hi_f(kk[0]), lo_f(kk[1]), hi_f(kk[1])}, (f32x4){Dd, Dd, Dd, Dd});
#pragma unroll
                    for (int o = 0; o < 4; ++o) acc[e] = add4(acc[e], *(const lds_f32x4*)(size_t)(lb + V_SCR + o * 8192 + (2 * w + e) * 1024 + l16));
                }
                lds_add(lb + C_RED2, lane);
                const auto r0 = __builtin_amdgcn_permlane16_swap(pack2(acc[0][0], acc[0][1]), pack2(acc[1][0], acc[1][1]), false, false);
                const auto r1 = __builtin_amdgcn_permlane16_swap(pack2(acc[0][2], acc[0][3]), pack2(acc[1][2], acc[1][3]), false, false);
                STAMP(7);
                spin_ge(lb + C_RED2, 4);
                WO_4(0, 1, 13, 14) WO_4(15, 16, 17, 18) WO_K(19)
                if (w == 0) WO_X(19, 19, 19, 19)
                if (__builtin_amdgcn_readfirstlane(lds_ld32(lb + C_SENT))) {
                    __builtin_amdgcn_raw_buffer_store_b128((u32x4){r0[0], r1[0], r0[1], r1[1]}, wsr, yo, 0, 16);
                    WAIT_VM(1);
                    lds_add(lb + C_WO, lane);
                } else {
                    WAIT_VM(0);
                    lds_add(lb + C_WO, lane);
                    spin_mid(lb + C_SENT, 1);
                    __builtin_amdgcn_raw_buffer_store_b128((u32x4){r0[0], r1[0], r0[1], r1[1]}, wsr, yo, 0, 16);
                }
            }
            STAMP(12);
        }
    }

    __builtin_amdgcn_s_setprio(T_PRIO);
    {
        RELANE();
        const int kh = (wave >> 2) == (isG ? 0 : 1), kq = kh * 2 + (w & 1), pp = w >> 1;
        const int rte = (rbo >> 1) * 8 + (rbo & 1) * 2 + pp;
        const unsigned ofo = WS_O + (rte * 20 + kq * 5) * 1024 + lane_ * 16;
        const int hsw = (g & 1) * 4;
        const unsigned lofs = lb + (8 * g + hsw + q4) * 160 + p4 * 8 + (w & 1) * 25600;
        const unsigned pc = lofs + (isG ? (kh ? G_R1 : G_R0) : V_WO + kh * 51200);
        const unsigned pd = pc + (4 - 2 * hsw) * 160;
        const unsigned ob4 = WS_O + (rte * 20 + kq * 5 + 4) * 1024;
        const unsigned wo_ = ob4 + (lane_ & 2) * 40960 + (lane_ & 1) * 512 + WATCH_J * 128;
        const unsigned xa = lb + kq * 5120 + lane_ * 16;
        unsigned c0_, c1_, c2_, c3_;
        CH(12);
        asm volatile(P_ASM
                     : [cnt] "=&s"(c0_), [tmp] "=&s"(c1_), [wm] "=&s"(c2_), [ah] "=&s"(c3_)
                     : [pc] "v"(pc), [pd] "v"(pd), [ofo] "v"(ofo), [of4] "v"(ob4 + g * 256 + n16 * 8), [wat] "v"(wo_), [xa0] "v"(xa + PART(pp)), [xa1] "v"(xa + PART(2 + pp)),
                       [pf] "v"(lb + C_PF + pp * 16 + kq * 4), [ctl] "v"(lb + L_CTRL), [wsp] "s"(ws), [ws2] "s"(ws + 4 * 20480), [won] "s"(isG ? 4 : 8),
                       [ost] "s"(isG ? G_WST : V_WST0), [cap] "s"(SPIN_CAP), [so] "s"(S_O), [thi] "s"(thi)
                     : P_CLOB);
        FLUSH();
    }
}

#undef x
#undef kin
#undef vin
#undef shift_k
#undef ssm_k
#undef ssm_D
#undef wqkv
#undef wo
#undef gw
#undef ws
#undef out
#undef wsr
extern "C" void kernel_launch(void* const* d_in, const int* in_sizes, int n_in, void* d_out, int out_size,
                              void* d_ws, size_t ws_size, hipStream_t stream) {
    const u16* q = (const u16*)d_in[0];
    const u16* k = (const u16*)d_in[1];
    const u16* v = (const u16*)d_in[2];
    const u16* shift_k = (const u16*)d_in[3];
    const u16* ssm_k = (const u16*)d_in[4];
    const u16* ssm_D = (const u16*)d_in[5];
    const u16* wqkv = (const u16*)d_in[7];
    const u16* wo = (const u16*)d_in[8];
    const u16* gw = (const u16*)d_in[9];
    fused_kernel<<<256, 768, 0, stream>>>(q, k, v, shift_k, ssm_k, ssm_D, wqkv, wo, gw, (char*)d_ws, (u16*)d_out);
}
```
